```python
import torch

def _softmax(x, axis=-1):
    return torch.softmax(x, dim=axis)
def _gelu(x):
    return torch.nn.functional.gelu(x, approximate="tanh")
def _silu(x):
    return torch.nn.functional.silu(x)
def _sigmoid(x):
    return torch.sigmoid(x)
def _layernorm(x, axis=-1, eps=1e-5):
    m = x.mean(dim=axis, keepdim=True)
    v = x.var(dim=axis, keepdim=True, unbiased=False)
    return (x - m) / torch.sqrt(v + eps)
def _scatter_add(tgt, idx, src, axis=0):
    return tgt.clone().index_add_(axis, idx, src)
def _segment_sum(src, seg, n):
    out = torch.zeros((n,) + src.shape[1:], dtype=src.dtype)
    return out.index_add_(0, seg, src)
def _pad(x, pw):
    flat = tuple(v for pair in reversed(pw) for v in pair)
    return torch.nn.functional.pad(x, flat)

kernel_name = 'interleave_layers__linear_kernel_hedgehog__grouped_kv_mqa__prompt'

def setup_inputs():

    _w = (lambda a: (lambda t: t.to(torch.bfloat16) if t.is_floating_point() else t)(torch.as_tensor(a)))
    def _A_setup():
        B, S, D, H = 1, 2048, 1024, 32
        Dh = D // H
        return {
            "x": _w((lambda *s: torch.randn(*s, dtype=torch.bfloat16) * 0.05)(B, S, D)),
            "wqkv": _w((lambda *s: torch.randn(*s, dtype=torch.bfloat16) * 0.2)(D, 3 * D)),
            "w_phi": _w((lambda *s: torch.randn(*s, dtype=torch.bfloat16) * 0.2)(H, Dh, Dh) * (Dh ** -0.5)),
            "wo": _w((lambda *s: torch.randn(*s, dtype=torch.bfloat16) * 0.05)(D, D)),
        }
    _A_in = _A_setup()

    def _B_setup():
        B, S, D, H = 1, 2048, 1024, 32
        Dh = D // H
        return {
            "x": _w((lambda *s: torch.randn(*s, dtype=torch.bfloat16) * 0.05)(B, S, D)),
            "wq": _w((lambda *s: torch.randn(*s, dtype=torch.bfloat16) * 0.1)(D, H * Dh)),
            "wk": _w((lambda *s: torch.randn(*s, dtype=torch.bfloat16) * 0.1)(D, Dh)),
            "wv": _w((lambda *s: torch.randn(*s, dtype=torch.bfloat16) * 0.05)(D, Dh)),
            "wo": _w((lambda *s: torch.randn(*s, dtype=torch.bfloat16) * 0.05)(H * Dh, D)),
        }
    _B_in = _B_setup()

    return {
        "A_x": _A_in["x"],
        "A_wqkv": _A_in["wqkv"],
        "A_w_phi": _A_in["w_phi"],
        "A_wo": _A_in["wo"],
        "B_x": _B_in["x"],
        "B_wq": _B_in["wq"],
        "B_wk": _B_in["wk"],
        "B_wv": _B_in["wv"],
        "B_wo": _B_in["wo"],
    }

def reference(A_x, A_wqkv, A_w_phi, A_wo, B_x, B_wq, B_wk, B_wv, B_wo):
    def _A_ref(x, wqkv, w_phi, wo):
        B, S, D = x.shape
        H = 32; Dh = D // H
        qkv = (lambda x, *d: x.permute(*d))((x @ wqkv).reshape(B, S, 3, H, Dh), 2, 0, 3, 1, 4)
        q, k, v = qkv[0], qkv[1], qkv[2]
        pq = torch.einsum("bhsd,hdf->bhsf", q, w_phi)
        pk = torch.einsum("bhsd,hdf->bhsf", k, w_phi)
        phi_q = torch.concatenate([_softmax(pq, axis=-1), _softmax(-pq, axis=-1)], axis=-1)
        phi_k = torch.concatenate([_softmax(pk, axis=-1), _softmax(-pk, axis=-1)], axis=-1)
        kv = torch.einsum("bhsf,bhsd->bhfd", phi_k, v)
        z  = torch.einsum("bhsf->bhf", phi_k)
        num = torch.einsum("bhqf,bhfd->bhqd", phi_q, kv)
        den = torch.einsum("bhqf,bhf->bhq", phi_q, z)[..., None] + 1e-6
        o = num / den
        return (lambda x, *d: x.permute(*d))(o, 0, 2, 1, 3).reshape(B, S, D) @ wo

    def _B_ref(x, wq, wk, wv, wo):
        B, S, D = x.shape
        H = 32
        Dh = D // H
        q = (lambda x, *d: x.permute(*d))((x @ wq).reshape(B, S, H, Dh), 0, 2, 1, 3)
        k = x @ wk
        v = x @ wv
        scores = torch.einsum("bhqd,bkd->bhqk", q, k) * (Dh ** -0.5)
        p = _softmax(scores, axis=-1)
        o = torch.einsum("bhqk,bkd->bhqd", p, v)
        return (lambda x, *d: x.permute(*d))(o, 0, 2, 1, 3).reshape(B, S, H * Dh) @ wo

    x = A_x
    x = x + _A_ref(_layernorm(x, axis=-1), A_wqkv, A_w_phi, A_wo)
    x = x + _B_ref(_layernorm(x, axis=-1), B_wq, B_wk, B_wv, B_wo)
    x = x + _A_ref(_layernorm(x, axis=-1), A_wqkv, A_w_phi, A_wo)
    x = x + _B_ref(_layernorm(x, axis=-1), B_wq, B_wk, B_wv, B_wo)
    return x
```

```cpp
#include <hip/hip_runtime.h>
#include <hip/hip_bf16.h>
#include <stdint.h>
#include <type_traits>

typedef __attribute__((ext_vector_type(8))) short bf16x8;
typedef __attribute__((ext_vector_type(4))) short s16x4;
typedef __attribute__((ext_vector_type(2))) __bf16 bf2_t;
typedef __attribute__((ext_vector_type(16))) float f32x16;
typedef __attribute__((ext_vector_type(4))) float f32x4;
typedef __attribute__((ext_vector_type(2))) float f32x2;
typedef __attribute__((ext_vector_type(4))) unsigned u32x4;
typedef __attribute__((ext_vector_type(2))) unsigned u32x2;
#define LAS __attribute__((address_space(3)))
#define DI __device__ __forceinline__
#define MFMA32(a, b, c) __builtin_amdgcn_mfma_f32_32x32x16_bf16((a), (b), (c), 0, 0, 0)
#define MFMA16(a, b, c) __builtin_amdgcn_mfma_f32_16x16x32_bf16((a), (b), (c), 0, 0, 0)
#define SBAR() __builtin_amdgcn_sched_barrier(0)

constexpr int S_ = 2048, D_ = 1024;
constexpr float LOG2E = 1.4426950408889634f;
constexpr float QSCALE = 0.17677669529663687f * LOG2E;

constexpr size_t MB = 1u << 20;
constexpr size_t WS_X0 = 0, WS_X1 = 4 * MB, WS_X2 = 8 * MB, WS_X3 = 12 * MB, WS_O = 16 * MB, WS_PHIQ = 20 * MB, WS_KVP = 28 * MB,
                 WS_Q = 32 * MB, WS_ZP = 36 * MB, WS_KVF = 36 * MB + 256 * 1024, WS_ST = 37 * MB, WS_CS = 38 * MB;
constexpr int CS_N = 3072 + 1024 + 64;

template <int I, int N, class F> DI void sfor(F&& f) { if constexpr (I < N) { f(std::integral_constant<int, I>{}); sfor<I + 1, N>(f); } }

DI unsigned cvtpk(float lo, float hi) { f32x2 v = {lo, hi}; return __builtin_bit_cast(unsigned, __builtin_convertvector(v, bf2_t)); }
DI float bflo(unsigned u) { return __uint_as_float(u << 16); }
DI float bfhi(unsigned u) { return __uint_as_float(u & 0xffff0000u); }
DI float rbf(float x) { return bflo(cvtpk(x, x)); }
DI float swap_add(float v) { auto r = __builtin_amdgcn_permlane32_swap(__float_as_uint(v), __float_as_uint(v), false, false); return __uint_as_float(r[0]) + __uint_as_float(r[1]); }
DI float swap_max(float v) { auto r = __builtin_amdgcn_permlane32_swap(__float_as_uint(v), __float_as_uint(v), false, false); return fmaxf(__uint_as_float(r[0]), __uint_as_float(r[1])); }
DI float swap_min(float v) { auto r = __builtin_amdgcn_permlane32_swap(__float_as_uint(v), __float_as_uint(v), false, false); return fminf(__uint_as_float(r[0]), __uint_as_float(r[1])); }
template <int S> DI bf16x8 pack8(const f32x16& v) {
  u32x4 p = {cvtpk(v[8 * S], v[8 * S + 1]), cvtpk(v[8 * S + 2], v[8 * S + 3]), cvtpk(v[8 * S + 4], v[8 * S + 5]), cvtpk(v[8 * S + 6], v[8 * S + 7])};
  return __builtin_bit_cast(bf16x8, p);
}
DI s16x4 trread(LAS const char* p) { return __builtin_amdgcn_ds_read_tr16_b64_v4i16((LAS s16x4*)p); }
DI bf16x8 cat8(s16x4 a, s16x4 b) { return __builtin_shufflevector(a, b, 0, 1, 2, 3, 4, 5, 6, 7); }
DI float dot2(unsigned a, unsigned b, float c) { return __builtin_amdgcn_fdot2_f32_bf16(__builtin_bit_cast(bf2_t, a), __builtin_bit_cast(bf2_t, b), c, false); }

template <bool AG, int OFF> DI void ldx(bf16x8& d, unsigned voff, const char* sb) {
  if constexpr (AG) asm volatile("global_load_dwordx4 %0, %1, %2 offset:%3" : "=a"(d) : "v"(voff), "s"(sb), "i"(OFF));
  else asm volatile("global_load_dwordx4 %0, %1, %2 offset:%3" : "=v"(d) : "v"(voff), "s"(sb), "i"(OFF));
}
template <int NA, int C> DI void issue_x(bf16x8 (&x)[64], unsigned voff, const char* xb) {
  const char* sb = xb + C * 8192 + 4096;
  sfor<0, 8>([&](auto i) { ldx<(C * 8 < NA), i * 1024 - 4096>(x[C * 8 + i], voff, sb); });
}
DI void dma4(unsigned lds, unsigned v0, unsigned v1, unsigned v2, unsigned v3, const char* sb) {
  unsigned keep;
  asm volatile("s_mov_b32 %0, m0\n\ts_mov_b32 m0, %1\n\ts_nop 0\n\t"
               "global_load_lds_dwordx4 %2, %6 offset:0\n\tglobal_load_lds_dwordx4 %3, %6 offset:1024\n\t"
               "global_load_lds_dwordx4 %4, %6 offset:2048\n\tglobal_load_lds_dwordx4 %5, %6 offset:3072\n\ts_mov_b32 m0, %0"
               : "=&s"(keep) : "s"(lds), "v"(v0), "v"(v1), "v"(v2), "v"(v3), "s"(sb) : "memory");
}
DI void dma2(unsigned lds, unsigned v, const char* sb) {
  unsigned keep;
  asm volatile("s_mov_b32 %0, m0\n\ts_mov_b32 m0, %1\n\ts_nop 0\n\t"
               "global_load_lds_dwordx4 %2, %3 offset:0\n\tglobal_load_lds_dwordx4 %2, %3 offset:1024\n\ts_mov_b32 m0, %0"
               : "=&s"(keep) : "s"(lds), "v"(v), "s"(sb) : "memory");
}
DI void dma1(unsigned lds, unsigned v, const char* sb) {
  unsigned keep;
  asm volatile("s_mov_b32 %0, m0\n\ts_mov_b32 m0, %1\n\ts_nop 0\n\tglobal_load_lds_dwordx4 %2, %3 offset:0\n\ts_mov_b32 m0, %0"
               : "=&s"(keep) : "s"(lds), "v"(v), "s"(sb) : "memory");
}
template <int N> DI void wait_bar() { asm volatile("s_waitcnt vmcnt(%0)\n\ts_barrier" ::"i"(N > 63 ? 63 : N) : "memory"); }
template <int NA, int C, int N> DI void wait_bar_x(bf16x8 (&x)[64]) {
  constexpr int n = N > 63 ? 63 : N;
  if constexpr (C * 8 < NA)
    asm volatile("s_waitcnt vmcnt(%8)\n\ts_barrier"
                 : "+a"(x[C * 8]), "+a"(x[C * 8 + 1]), "+a"(x[C * 8 + 2]), "+a"(x[C * 8 + 3]), "+a"(x[C * 8 + 4]), "+a"(x[C * 8 + 5]), "+a"(x[C * 8 + 6]), "+a"(x[C * 8 + 7])
                 : "i"(n) : "memory");
  else
    asm volatile("s_waitcnt vmcnt(%8)\n\ts_barrier"
                 : "+v"(x[C * 8]), "+v"(x[C * 8 + 1]), "+v"(x[C * 8 + 2]), "+v"(x[C * 8 + 3]), "+v"(x[C * 8 + 4]), "+v"(x[C * 8 + 5]), "+v"(x[C * 8 + 6]), "+v"(x[C * 8 + 7])
                 : "i"(n) : "memory");
}
DI void lds_bar() { asm volatile("s_waitcnt lgkmcnt(0)\n\ts_barrier" ::: "memory"); }

template <int RBX> DI void block_map(int& rb, int& cb) {
  const int x = blockIdx.x & 7, i = blockIdx.x >> 3;
  constexpr int CBX = 32 / RBX, GC = 16 / CBX;
  rb = RBX * (x / GC) + i / CBX; cb = CBX * (x % GC) + i % CBX;
}

DI void ln_stats(const f32x2* st, int token, int h, float& mean, float& rstd) {
  float s = 0.f, q = 0.f;
#pragma unroll
  for (int i = 0; i < 8; ++i) { const f32x2 v = st[(8 * h + i) * S_ + token]; s += v[0]; q += v[1]; }
  s = swap_add(s); q = swap_add(q);
  const float m = s * (1.f / D_);
  const float var = fmaxf(q * (1.f / D_) - m * m, 0.f);
  mean = rbf(m);
  rstd = 1.f / rbf(sqrtf(rbf(rbf(var) + 1e-5f)));
}

__global__ __launch_bounds__(256) void k_prep(const char* x, const char* wqkv, const char* wq, const char* wk, const char* wv, char* ws) {
  const int tid = threadIdx.x, lane = tid & 63, w = tid >> 6, r = lane & 31, h = lane >> 5;
  __shared__ float red[32][64];
  if (blockIdx.x < 256) {
    const int tt = blockIdx.x >> 2, cb = (blockIdx.x & 3) * 4 + w;
    const char* src = x + (size_t)(tt * 32 + r) * 2048 + cb * 128 + h * 16;
    u32x4 v[4];
#pragma unroll
    for (int i = 0; i < 4; ++i) v[i] = *(const u32x4*)(src + i * 32);
    float s = 0.f, q = 0.f;
#pragma unroll
    for (int i = 0; i < 4; ++i) {
#pragma unroll
      for (int e = 0; e < 4; ++e) { s = dot2(v[i][e], 0x3f803f80u, s); q = dot2(v[i][e], v[i][e], q); }
      auto r0 = __builtin_amdgcn_permlane32_swap(v[i][0], v[i][2], false, false);
      auto r1 = __builtin_amdgcn_permlane32_swap(v[i][1], v[i][3], false, false);
      u32x4 o = {r0[0], r1[0], r0[1], r1[1]};
      *(u32x4*)(ws + WS_X0 + ((size_t)(tt * 64 + cb * 4 + i) * 64 + lane) * 16) = o;
    }
    s = swap_add(s); q = swap_add(q);
    if (h == 0) ((f32x2*)(ws + WS_ST))[cb * S_ + tt * 32 + r] = f32x2{s, q};
  } else {
    const int u = blockIdx.x - 256, qr = u & 3, g = u >> 2;
    const char* src; int pitch;
    const int c8 = tid & 7, rr = tid >> 3;
    if (g < 48) { src = wqkv + g * 128 + c8 * 16; pitch = 6144; }
    else if (g < 64) { src = wq + (g - 48) * 128 + c8 * 16; pitch = 2048; }
    else { src = (c8 < 4 ? wk : wv) + (c8 & 3) * 16; pitch = 64; }
    src += (size_t)(qr * 256 + rr) * pitch;
    u32x4 v[8];
#pragma unroll
    for (int i = 0; i < 8; ++i) v[i] = *(const u32x4*)(src + (size_t)i * 32 * pitch);
    float a[8] = {};
#pragma unroll
    for (int i = 0; i < 8; ++i)
#pragma unroll
      for (int e = 0; e < 4; ++e) { a[2 * e] += bflo(v[i][e]); a[2 * e + 1] += bfhi(v[i][e]); }
#pragma unroll
    for (int e = 0; e < 8; ++e) red[rr][c8 * 8 + e] = a[e];
    __syncthreads();
    if (tid < 64) {
      float t = 0.f;
#pragma unroll
      for (int i = 0; i < 32; ++i) t += red[i][tid];
      ((float*)(ws + WS_CS))[qr * CS_N + g * 64 + tid] = t;
    }
  }
}

struct GemmLane {
  unsigned wv0, wv1, wv2, wv3;
  unsigned a0, a1;
};
DI GemmLane gemm_lane(int lane, int w, int pitch) {
  GemmLane g;
  const int row = lane >> 3, ch = (lane & 7) ^ (((row >> 1) & 1) << 2);
  g.wv0 = (unsigned)((32 * w + row) * pitch + ch * 16);
  g.wv1 = g.wv0 + 8 * pitch - 1024; g.wv2 = g.wv0 + 16 * pitch - 2048; g.wv3 = g.wv0 + 24 * pitch - 3072;
  const int h = lane >> 5, blk = (lane >> 4) & 1, q = (lane & 15) >> 2, p = lane & 3;
  g.a0 = (unsigned)((4 * h + q) * 128 + (((q >> 1) << 2) + 2 * blk + (p >> 1)) * 16 + 8 * (p & 1));
  g.a1 = g.a0 ^ 64u;
  return g;
}

template <int NW> struct WinPtr {
  unsigned b[NW];
  DI void init(unsigned a) { sfor<0, NW>([&](auto i) { b[i] = a + i * 65536u; asm volatile("" : "+v"(b[i])); }); }
  template <int OFF> DI bf16x8 frag() const {
    LAS const char* p = (LAS const char*)(uintptr_t)b[OFF >> 16] + (OFF & 65535);
    return cat8(trread(p), trread(p + 1024));
  }
};

template <bool FINAL>
__global__ __launch_bounds__(256, 1) void k_proj(const char* xin, const char* W, const char* res, char* xout, f32x2* st) {
  __shared__ __attribute__((aligned(16))) char smem[131072];
  constexpr int NA = 48;
  const int lane = threadIdx.x & 63, w = __builtin_amdgcn_readfirstlane(threadIdx.x >> 6), r = lane & 31, h = lane >> 5;
  int rb, cb; block_map<4>(rb, cb);
  const int tt = rb * 4 + w;
  const unsigned lds0 = (unsigned)(uintptr_t)(LAS char*)smem;
  const GemmLane g = gemm_lane(lane, w, 2048);
  const unsigned xv = lane * 16;
  const char* xb = xin + (size_t)tt * 65536;
  const char* wb = W + cb * 128;
  bf16x8 x[64], rs[4];
  { const char* rbp = res + (size_t)tt * 65536 + cb * 4096; sfor<0, 4>([&](auto i) { ldx<false, i * 1024>(rs[i], xv, rbp); }); }
  auto issue = [&](auto c) { dma4(lds0 + c * 16384 + w * 4096, g.wv0, g.wv1, g.wv2, g.wv3, wb + (size_t)c * 128 * 2048); issue_x<NA, c>(x, xv, xb); };
  sfor<0, 4>(issue);
  f32x16 acc0 = {}, acc1 = {};
  bf16x8 f0[3], f1[3];
  WinPtr<2> p0, p1; p0.init(lds0 + g.a0); p1.init(lds0 + g.a1);
  auto rd = [&](auto s) {
    if constexpr (s % 8 == 0) {
      constexpr int c = s / 8, issued = c + 4 > 8 ? 8 : c + 4;
      wait_bar_x<NA, c, 12 * (issued - c - 1)>(x);
      if constexpr (c + 4 < 8) issue(std::integral_constant<int, c + 4>{});
    }
    f0[s % 3] = p0.template frag<s * 2048>(); f1[s % 3] = p1.template frag<s * 2048>();
  };
  rd(std::integral_constant<int, 0>{}); rd(std::integral_constant<int, 1>{});
  sfor<0, 64>([&](auto s) {
    if constexpr (s + 2 < 64) rd(std::integral_constant<int, s + 2>{});
    SBAR();
    acc0 = MFMA32(f0[s % 3], x[s], acc0); acc1 = MFMA32(f1[s % 3], x[s], acc1);
    SBAR();
  });
  asm volatile("" : "+v"(rs[0]), "+v"(rs[1]), "+v"(rs[2]), "+v"(rs[3]));
  float s1 = 0.f, s2 = 0.f;
  u32x4 o[4];
  sfor<0, 4>([&](auto i) {
    const f32x16& a = (i < 2) ? acc0 : acc1;
    const u32x4 rr = __builtin_bit_cast(u32x4, rs[i]);
#pragma unroll
    for (int e = 0; e < 4; ++e) {
      o[i][e] = cvtpk(a[(i & 1) * 8 + 2 * e] + bflo(rr[e]), a[(i & 1) * 8 + 2 * e + 1] + bfhi(rr[e]));
      if (!FINAL) { s1 = dot2(o[i][e], 0x3f803f80u, s1); s2 = dot2(o[i][e], o[i][e], s2); }
    }
  });
  if constexpr (FINAL) {
    char* dst = xout + (size_t)(tt * 32 + r) * 2048 + cb * 128 + h * 16;
#pragma unroll
    for (int i = 0; i < 4; ++i) {
      auto r0 = __builtin_amdgcn_permlane32_swap(o[i][0], o[i][2], false, false);
      auto r1 = __builtin_amdgcn_permlane32_swap(o[i][1], o[i][3], false, false);
      *(u32x4*)(dst + i * 32) = u32x4{r0[0], r1[0], r0[1], r1[1]};
    }
  } else {
    char* dst = xout + (size_t)tt * 65536 + cb * 4096 + lane * 16;
#pragma unroll
    for (int i = 0; i < 4; ++i) *(u32x4*)(dst + i * 1024) = o[i];
    s1 = swap_add(s1); s2 = swap_add(s2);
    if (h == 0) st[cb * S_ + tt * 32 + r] = f32x2{s1, s2};
  }
}

DI void load_cs(const float* cs, int n0, int h, float (&c)[16]) {
#pragma unroll
  for (int gq = 0; gq < 4; ++gq) {
    f32x4 t = *(const f32x4*)(cs + n0 + 8 * gq + 4 * h);
#pragma unroll
    for (int p = 1; p < 4; ++p) t += *(const f32x4*)(cs + p * CS_N + n0 + 8 * gq + 4 * h);
#pragma unroll
    for (int e = 0; e < 4; ++e) c[4 * gq + e] = t[e];
  }
}

constexpr int A_NS = 9;
constexpr int a_last_w(int g) { return g < 3 ? g + 5 : (2 * g + 2 < g + 7 ? 2 * g + 2 : (g + 7 > 23 ? 23 : g + 7)); }
constexpr int a_ops_upto(int j) { return j < 8 ? 12 * (j + 1) : 96 + 4 * (j - 7); }
__global__ __launch_bounds__(256, 1) void k_qkvA(const char* xin, const char* W, const char* wphi, const f32x2* st, const float* cs, char* phiq, char* kvp, char* zp) {
  __shared__ __attribute__((aligned(16))) char smem[A_NS * 16384 + 4096 + 1024];
  constexpr int NA = 40;
  const int lane = threadIdx.x & 63, w = __builtin_amdgcn_readfirstlane(threadIdx.x >> 6), r = lane & 31, h = lane >> 5;
  int rb, cb; block_map<8>(rb, cb);
  const int tt = rb * 4 + w;
  const unsigned lds0 = (unsigned)(uintptr_t)(LAS char*)smem;
  const GemmLane g = gemm_lane(lane, w, 6144);
  const unsigned xv = lane * 16;
  const char* xb = xin + (size_t)tt * 65536;
  const char* wb = W + cb * 128;
  bf16x8 x[64];
  dma1(lds0 + A_NS * 16384 + w * 1024, xv, wphi + cb * 4096 + w * 1024);
  auto issue = [&](auto j) {
    constexpr int p = j / 8, c = j % 8, col = p == 0 ? 2048 : (p == 1 ? 0 : 4096);
    dma4(lds0 + (j % A_NS) * 16384 + w * 4096, g.wv0, g.wv1, g.wv2, g.wv3, wb + col + (size_t)c * 128 * 6144);
    if constexpr (j < 8) issue_x<NA, j>(x, xv, xb);
  };
  sfor<0, 5>(issue);
  f32x16 acc[6] = {};
  bf16x8 f0[3], f1[3];
  WinPtr<3> p0, p1; p0.init(lds0 + g.a0); p1.init(lds0 + g.a1);
  auto rd = [&](auto s) {
    constexpr int j = s / 8;
    if constexpr (s % 8 == 0) {
      constexpr int before = j == 0 ? 4 : a_last_w(j - 1), n = a_ops_upto(before) - a_ops_upto(j);
      if constexpr (j < 8) wait_bar_x<NA, j, n>(x); else wait_bar<n>();
      sfor<before + 1, a_last_w(j) + 1>(issue);
    }
    constexpr int off = (j % A_NS) * 16384 + (s % 8) * 2048;
    f0[s % 3] = p0.template frag<off>(); f1[s % 3] = p1.template frag<off>();
  };
  rd(std::integral_constant<int, 0>{}); rd(std::integral_constant<int, 1>{});
  sfor<0, 192>([&](auto s) {
    if constexpr (s + 2 < 192) rd(std::integral_constant<int, s + 2>{});
    SBAR();
    constexpr int p = s / 64;
    if constexpr (p < 2) { acc[2 * p] = MFMA32(f0[s % 3], x[s % 64], acc[2 * p]); acc[2 * p + 1] = MFMA32(f1[s % 3], x[s % 64], acc[2 * p + 1]); }
    else { acc[4] = MFMA32(x[s % 64], f0[s % 3], acc[4]); acc[5] = MFMA32(x[s % 64], f1[s % 3], acc[5]); }
    SBAR();
  });
  float mean, rstd; ln_stats(st, tt * 32 + r, h, mean, rstd);
  LAS f32x2* mr = (LAS f32x2*)((LAS char*)smem + A_NS * 16384 + 4096) + w * 32;
  if (h == 0) mr[r] = f32x2{mean, rstd};
  lds_bar();
  LAS char* pk_img = (LAS char*)smem;
  LAS char* v_img = (LAS char*)smem + 32768;
  const int blk = (lane >> 4) & 1, q4 = (lane & 15) >> 2, p4 = lane & 3;
  sfor<0, 4>([&](auto t) {
    constexpr int hd = t & 1; constexpr bool isq = t >= 2;
    float c[16]; load_cs(cs, (isq ? 0 : 1024) + cb * 64 + hd * 32, h, c);
    f32x16 v;
#pragma unroll
    for (int i = 0; i < 16; ++i) v[i] = (acc[t][i] - mean * c[i]) * rstd;
    LAS const char* wp = (LAS const char*)smem + A_NS * 16384 + hd * 2048 + (4 * h + q4) * 64 + blk * 32 + p4 * 8;
    f32x16 pz = {};
    pz = MFMA32(cat8(trread(wp), trread(wp + 512)), pack8<0>(v), pz);
    pz = MFMA32(cat8(trread(wp + 1024), trread(wp + 1536)), pack8<1>(v), pz);
    float mx = pz[0], mn = pz[0];
#pragma unroll
    for (int i = 1; i < 16; ++i) { mx = fmaxf(mx, pz[i]); mn = fminf(mn, pz[i]); }
    mx = swap_max(mx) * LOG2E; mn = swap_min(mn) * LOG2E;
    f32x16 ep, en; float sp = 0.f, sn = 0.f;
#pragma unroll
    for (int i = 0; i < 16; ++i) {
      ep[i] = __builtin_amdgcn_exp2f(__builtin_fmaf(pz[i], LOG2E, -mx)); sp += ep[i];
      en[i] = __builtin_amdgcn_exp2f(__builtin_fmaf(pz[i], -LOG2E, mn)); sn += en[i];
    }
    sp = __builtin_amdgcn_rcpf(swap_add(sp)); sn = __builtin_amdgcn_rcpf(swap_add(sn));
#pragma unroll
    for (int i = 0; i < 16; ++i) { ep[i] *= sp; en[i] *= sn; }
    if constexpr (isq) {
      char* dst = phiq + ((size_t)(tt * 32 + cb * 2 + hd) * 4) * 1024 + lane * 16;
      *(bf16x8*)(dst) = pack8<0>(ep); *(bf16x8*)(dst + 1024) = pack8<1>(ep);
      *(bf16x8*)(dst + 2048) = pack8<0>(en); *(bf16x8*)(dst + 3072) = pack8<1>(en);
    } else {
      LAS char* row = pk_img + hd * 16384 + (w * 32 + r) * 128 + h * 8;
#pragma unroll
      for (int gq = 0; gq < 4; ++gq) {
        *(LAS u32x2*)(row + ((gq ^ (r & 7)) << 4)) = u32x2{cvtpk(ep[4 * gq], ep[4 * gq + 1]), cvtpk(ep[4 * gq + 2], ep[4 * gq + 3])};
        *(LAS u32x2*)(row + (((4 + gq) ^ (r & 7)) << 4)) = u32x2{cvtpk(en[4 * gq], en[4 * gq + 1]), cvtpk(en[4 * gq + 2], en[4 * gq + 3])};
      }
    }
  });
  sfor<0, 2>([&](auto hd) {
    const int n = 2048 + cb * 64 + hd * 32 + r;
    const float c = cs[n] + cs[CS_N + n] + cs[2 * CS_N + n] + cs[3 * CS_N + n];
    f32x16 v;
#pragma unroll
    for (int i = 0; i < 16; ++i) { const f32x2 m = mr[(i & 3) + 8 * (i >> 2) + 4 * h]; v[i] = (acc[4 + hd][i] - m[0] * c) * m[1]; }
    LAS char* d = v_img + (hd * 8 + w * 2) * 1024 + lane * 16;
    *(LAS bf16x8*)d = pack8<0>(v); *(LAS bf16x8*)(d + 1024) = pack8<1>(v);
  });
  lds_bar();
  {
    const int hd = w >> 1, fh = w & 1;
    f32x16 kv = {}, z = {};
    const bf16x8 ones = {0x3f80, 0x3f80, 0x3f80, 0x3f80, 0x3f80, 0x3f80, 0x3f80, 0x3f80};
    const int ch = fh * 4 + 2 * blk + (p4 >> 1), r0 = 4 * h + q4;
    LAS const char* pa = pk_img + hd * 16384 + r0 * 128 + ((ch ^ r0) << 4) + 8 * (p4 & 1);
    LAS const char* pb = v_img + hd * 8192 + lane * 16;
#pragma unroll
    for (int s = 0; s < 8; ++s) {
      const bf16x8 a = cat8(trread(pa + s * 2048), trread(pa + s * 2048 + 1024));
      kv = MFMA32(a, *(LAS const bf16x8*)(pb + s * 1024), kv);
      z = MFMA32(a, ones, z);
    }
    const int u = (rb * 32 + cb * 2 + hd) * 2 + fh;
    char* dst = kvp + (size_t)u * 4096 + lane * 16;
#pragma unroll
    for (int gq = 0; gq < 4; ++gq) *(f32x4*)(dst + gq * 1024) = f32x4{kv[4 * gq], kv[4 * gq + 1], kv[4 * gq + 2], kv[4 * gq + 3]};
    if (r == 0) {
#pragma unroll
      for (int gq = 0; gq < 4; ++gq) *(f32x4*)(zp + (size_t)u * 128 + h * 64 + gq * 16) = f32x4{z[4 * gq], z[4 * gq + 1], z[4 * gq + 2], z[4 * gq + 3]};
    }
  }
}

__global__ __launch_bounds__(256) void k_oA(const char* phiq, const char* kvp, const char* zp, char* o) {
  __shared__ __attribute__((aligned(16))) f32x4 red[4][4][64];
  __shared__ __attribute__((aligned(16))) f32x4 zred[4][8];
  const int lane = threadIdx.x & 63, w = threadIdx.x >> 6, h = lane >> 5;
  const int head = (blockIdx.x & 7) * 4 + ((blockIdx.x >> 3) & 3), tb = blockIdx.x >> 5;
  bf16x8 pq[2][4];
#pragma unroll
  for (int t = 0; t < 2; ++t)
#pragma unroll
    for (int k = 0; k < 4; ++k) pq[t][k] = *(const bf16x8*)(phiq + ((size_t)((tb * 8 + w * 2 + t) * 32 + head) * 4 + k) * 1024 + lane * 16);
  {
    const int fh = w & 1, half = w >> 1;
    f32x4 a[4] = {}, zz = {};
#pragma unroll
    for (int i = 0; i < 8; ++i) {
      const int u = ((half * 8 + i) * 32 + head) * 2 + fh;
#pragma unroll
      for (int gq = 0; gq < 4; ++gq) a[gq] += *(const f32x4*)(kvp + (size_t)u * 4096 + gq * 1024 + lane * 16);
      zz += *(const f32x4*)(zp + (size_t)u * 128 + (lane & 7) * 16);
    }
#pragma unroll
    for (int gq = 0; gq < 4; ++gq) red[w][gq][lane] = a[gq];
    if (lane < 8) zred[w][lane] = zz;
  }
  __syncthreads();
  bf16x8 ka[4], za[4];
#pragma unroll
  for (int k = 0; k < 4; ++k) {
    const int fh = k >> 1, s = k & 1;
    const f32x4 a0 = red[fh][2 * s][lane] + red[fh + 2][2 * s][lane], a1 = red[fh][2 * s + 1][lane] + red[fh + 2][2 * s + 1][lane];
    const f32x4 z0 = zred[fh][h * 4 + 2 * s] + zred[fh + 2][h * 4 + 2 * s], z1 = zred[fh][h * 4 + 2 * s + 1] + zred[fh + 2][h * 4 + 2 * s + 1];
    ka[k] = __builtin_bit_cast(bf16x8, u32x4{cvtpk(a0[0], a0[1]), cvtpk(a0[2], a0[3]), cvtpk(a1[0], a1[1]), cvtpk(a1[2], a1[3])});
    za[k] = __builtin_bit_cast(bf16x8, u32x4{cvtpk(z0[0], z0[1]), cvtpk(z0[2], z0[3]), cvtpk(z1[0], z1[1]), cvtpk(z1[2], z1[3])});
  }
#pragma unroll
  for (int t = 0; t < 2; ++t) {
    f32x16 num = {}, den = {};
#pragma unroll
    for (int k = 0; k < 4; ++k) { num = MFMA32(ka[k], pq[t][k], num); den = MFMA32(za[k], pq[t][k], den); }
    const float inv = 1.f / (den[0] + 1e-6f);
#pragma unroll
    for (int i = 0; i < 16; ++i) num[i] *= inv;
    char* dst = o + ((size_t)(tb * 8 + w * 2 + t) * 64 + head * 2) * 1024 + lane * 16;
    *(bf16x8*)dst = pack8<0>(num); *(bf16x8*)(dst + 1024) = pack8<1>(num);
  }
}

template <int KV>
DI void qkvB_body(char* smem, const char* xin, const char* W, const char* wkv, const f32x2* st, const float* cs, char* qf, char* kvf, int rb, int cb) {
  constexpr int NA = 48, OPS = KV ? 13 : 12;
  const int lane = threadIdx.x & 63, w = __builtin_amdgcn_readfirstlane(threadIdx.x >> 6), r = lane & 31, h = lane >> 5;
  const int tt = rb * 4 + w, hf = (cb >> 2) & 1;
  const unsigned lds0 = (unsigned)(uintptr_t)(LAS char*)smem;
  const GemmLane g = gemm_lane(lane, w, 2048);
  const unsigned xv = lane * 16;
  const char* xb = xin + (size_t)tt * 65536;
  const char* wb = W + cb * 128;
  const unsigned kvv = (unsigned)((32 * w + (lane >> 1)) * 64 + hf * 32 + (lane & 1) * 16);
  bf16x8 x[64];
  auto issue = [&](auto c) {
    dma4(lds0 + c * 16384 + w * 4096, g.wv0, g.wv1, g.wv2, g.wv3, wb + (size_t)c * 128 * 2048);
    if constexpr (KV) dma1(lds0 + 131072 + c * 4096 + w * 1024, kvv, wkv + c * 128 * 64);
    issue_x<NA, c>(x, xv, xb);
  };
  sfor<0, 4>(issue);
  f32x16 acc0 = {}, acc1 = {}, acc2 = {};
  bf16x8 f0[3], f1[3], f2[3];
  WinPtr<2> p0, p1; p0.init(lds0 + g.a0); p1.init(lds0 + g.a1);
  unsigned a2 = lds0 + 131072 + (4 * h + ((lane & 15) >> 2)) * 32 + (lane & 3) * 8; asm volatile("" : "+v"(a2));
  LAS const char* p2 = (LAS const char*)(uintptr_t)a2;
  auto rd = [&](auto s) {
    if constexpr (s % 8 == 0) {
      constexpr int c = s / 8, issued = c + 4 > 8 ? 8 : c + 4;
      wait_bar_x<NA, c, OPS * (issued - c - 1)>(x);
      if constexpr (c + 4 < 8) issue(std::integral_constant<int, c + 4>{});
    }
    f0[s % 3] = p0.template frag<s * 2048>(); f1[s % 3] = p1.template frag<s * 2048>();
    if constexpr (KV) f2[s % 3] = cat8(trread(p2 + s * 512), trread(p2 + s * 512 + 256));
  };
  rd(std::integral_constant<int, 0>{}); rd(std::integral_constant<int, 1>{});
  sfor<0, 64>([&](auto s) {
    if constexpr (s + 2 < 64) rd(std::integral_constant<int, s + 2>{});
    SBAR();
    acc0 = MFMA32(f0[s % 3], x[s], acc0); acc1 = MFMA32(f1[s % 3], x[s], acc1);
    if constexpr (KV == 1) acc2 = MFMA32(f2[s % 3], x[s], acc2);
    if constexpr (KV == 2) acc2 = MFMA32(x[s], f2[s % 3], acc2);
    SBAR();
  });
  float mean, rstd; ln_stats(st, tt * 32 + r, h, mean, rstd);
  sfor<0, 2>([&](auto t) {
    float c[16]; load_cs(cs, 3072 + cb * 64 + t * 32, h, c);
    const f32x16& a = t == 0 ? acc0 : acc1;
    f32x16 v; const float sc = rstd * QSCALE;
#pragma unroll
    for (int i = 0; i < 16; ++i) v[i] = (a[i] - mean * c[i]) * sc;
    char* dst = qf + ((size_t)tt * 64 + cb * 4 + t * 2) * 1024 + lane * 16;
    *(bf16x8*)dst = pack8<0>(v); *(bf16x8*)(dst + 1024) = pack8<1>(v);
  });
  if constexpr (KV == 1) {
    float c[16]; load_cs(cs, 4096 + hf * 16, h, c);
    f32x16 v;
#pragma unroll
    for (int i = 0; i < 8; ++i) v[i] = (acc2[i] - mean * c[i]) * rstd;
    *(bf16x8*)(kvf + (size_t)tt * 4096 + hf * 1024 + lane * 16) = pack8<0>(v);
  }
  if constexpr (KV == 2) {
    lds_bar();
    LAS f32x2* mr = (LAS f32x2*)((LAS char*)smem) + w * 32;
    if (h == 0) mr[r] = f32x2{mean, rstd};
    const int n = 4096 + 32 + hf * 16 + (r & 15);
    const float c = cs[n] + cs[CS_N + n] + cs[2 * CS_N + n] + cs[3 * CS_N + n];
    f32x16 v;
#pragma unroll
    for (int i = 0; i < 16; ++i) { const f32x2 m = mr[(i & 3) + 8 * (i >> 2) + 4 * h]; v[i] = (acc2[i] - m[0] * c) * m[1]; }
    if (r < 16) {
      char* dst = kvf + (size_t)tt * 4096 + 2048 + (hf * 16 + r + 32 * h) * 16;
      *(bf16x8*)dst = pack8<0>(v); *(bf16x8*)(dst + 1024) = pack8<1>(v);
    }
  }
}
__global__ __launch_bounds__(256, 1) void k_qkvB(const char* xin, const char* wq, const char* wk, const char* wv, const f32x2* st, const float* cs, char* qf, char* kvf) {
  __shared__ __attribute__((aligned(16))) char smem[163840];
  int rb, cb; block_map<4>(rb, cb);
  if ((cb & 3) != 0) qkvB_body<0>(smem, xin, wq, wk, st, cs, qf, kvf, rb, cb);
  else if (cb < 8) qkvB_body<1>(smem, xin, wq, wk, st, cs, qf, kvf, rb, cb);
  else qkvB_body<2>(smem, xin, wq, wv, st, cs, qf, kvf, rb, cb);
}

__global__ __launch_bounds__(512, 2) void k_attn(const char* qf, const char* kvf, char* o) {
  __shared__ __attribute__((aligned(16))) char smem[131072 + 16];
  const int lane = threadIdx.x & 63, w = __builtin_amdgcn_readfirstlane(threadIdx.x >> 6);
  int rb, cb; block_map<4>(rb, cb);
  const int tt = rb * 4 + (w & 3), head = cb * 2 + (w >> 2);
  const unsigned lds0 = (unsigned)(uintptr_t)(LAS char*)smem;
  const unsigned xv = lane * 16;
  const char* qp = qf + ((size_t)tt * 64 + head * 2) * 1024 + lane * 16;
  const bf16x8 q0 = *(const bf16x8*)qp, q1 = *(const bf16x8*)(qp + 1024);
  LAS const char* kp = (LAS const char*)smem + lane * 16;
  LAS volatile int* flag = (LAS volatile int*)((LAS char*)smem + 131072);
  const short sv = (((lane >> 2) ^ (lane >> 4)) & 1) ? (short)0 : (short)0x3f80;
  const bf16x8 sel = {sv, sv, sv, sv, sv, sv, sv, sv};
  const f32x16 zero = {};
  if (threadIdx.x == 0) *flag = 0;
  float mref = 0.f;
  for (int attempt = 0; attempt < 2; ++attempt) {
    if (attempt == 1) {
      float mx = -INFINITY;
      for (int j = 0; j < 16; ++j) {
        lds_bar();
        dma2(lds0 + w * 2048, xv, kvf + j * 16384 + w * 2048);
        wait_bar<0>();
#pragma unroll
        for (int i = 0; i < 4; ++i) {
          f32x16 s = MFMA32(*(LAS const bf16x8*)(kp + i * 4096), q0, zero);
          s = MFMA32(*(LAS const bf16x8*)(kp + i * 4096 + 1024), q1, s);
#pragma unroll
          for (int e = 0; e < 16; ++e) mx = fmaxf(mx, s[e]);
        }
      }
      mref = swap_max(mx);
      lds_bar();
    }
#pragma unroll
    for (int j = 0; j < 8; ++j) dma2(lds0 + j * 16384 + w * 2048, xv, kvf + j * 16384 + w * 2048);
    wait_bar<14>();
    bf16x8 ka0, ka1, kb0, kb1, va0, va1, vb0, vb1;
    f32x16 sa, sb, negm, oacc = {};
    f32x4 lacc = {};
    ka0 = *(LAS const bf16x8*)(kp); ka1 = *(LAS const bf16x8*)(kp + 1024);
    kb0 = *(LAS const bf16x8*)(kp + 4096); kb1 = *(LAS const bf16x8*)(kp + 4096 + 1024);
    va0 = *(LAS const bf16x8*)(kp + 2048); va1 = *(LAS const bf16x8*)(kp + 3072);
    sa = MFMA32(ka0, q0, zero); sa = MFMA32(ka1, q1, sa);
    if (attempt == 0) {
      float mx = sa[0];
#pragma unroll
      for (int e = 1; e < 16; ++e) mx = fmaxf(mx, sa[e]);
      mref = swap_max(mx);
    }
#pragma unroll
    for (int e = 0; e < 16; ++e) { negm[e] = -mref; sa[e] -= mref; }
    asm volatile("" : "+v"(negm));
#define STEP(SC, SN, KN0, KN1, KR0, KR1, VC0, VC1, VR0, VR1, KOFF, VOFF) do { \
      KR0 = *(LAS const bf16x8*)(kp + (KOFF)); KR1 = *(LAS const bf16x8*)(kp + (KOFF) + 1024); \
      VR0 = *(LAS const bf16x8*)(kp + (VOFF) + 2048); VR1 = *(LAS const bf16x8*)(kp + (VOFF) + 3072); \
      SN = MFMA32(KN0, q0, negm); \
      _Pragma("unroll") for (int e = 0; e < 8; ++e) SC[e] = __builtin_amdgcn_exp2f(SC[e]); \
      SN = MFMA32(KN1, q1, SN); \
      const bf16x8 p0_ = pack8<0>(SC); \
      oacc = MFMA32(VC0, p0_, oacc); lacc = MFMA16(sel, p0_, lacc); \
      _Pragma("unroll") for (int e = 8; e < 16; ++e) SC[e] = __builtin_amdgcn_exp2f(SC[e]); \
      const bf16x8 p1_ = pack8<1>(SC); \
      oacc = MFMA32(VC1, p1_, oacc); lacc = MFMA16(sel, p1_, lacc); \
    } while (0)
    for (int j = 0; j < 16; ++j) {
      const int c = (j & 7) * 16384, n = ((j + 1) & 7) * 16384;
      STEP(sa, sb, kb0, kb1, ka0, ka1, va0, va1, vb0, vb1, c + 8192, c + 4096);
      STEP(sb, sa, ka0, ka1, kb0, kb1, vb0, vb1, va0, va1, c + 12288, c + 8192);
      if (j < 10) wait_bar<10>(); else wait_bar<0>();
      if (j >= 1 && j + 7 < 16) dma2(lds0 + ((j + 7) & 7) * 16384 + w * 2048, xv, kvf + (j + 7) * 16384 + w * 2048);
      STEP(sa, sb, kb0, kb1, ka0, ka1, va0, va1, vb0, vb1, n, c + 12288);
      STEP(sb, sa, ka0, ka1, kb0, kb1, vb0, vb1, va0, va1, n + 4096, n);
    }
#undef STEP
    const float l = lacc[0];
    bool bad = !(l < 1.2676506e30f);
    const float inv = 1.f / l;
#pragma unroll
    for (int e = 0; e < 16; ++e) { oacc[e] *= inv; bad |= !(fabsf(oacc[e]) < INFINITY); }
    if (attempt == 0 && __any(bad)) *flag = 1;
    lds_bar();
    if (attempt == 1 || *flag == 0) {
      char* dst = o + ((size_t)tt * 64 + head * 2) * 1024 + lane * 16;
      *(bf16x8*)dst = pack8<0>(oacc); *(bf16x8*)(dst + 1024) = pack8<1>(oacc);
      break;
    }
  }
}

extern "C" void kernel_launch(void* const* d_in, const int* in_sizes, int n_in, void* d_out, int out_size, void* d_ws, size_t ws_size, hipStream_t stream) {
  const char* x = (const char*)d_in[0];
  const char* a_wqkv = (const char*)d_in[1];
  const char* a_wphi = (const char*)d_in[2];
  const char* a_wo = (const char*)d_in[3];
  const char* b_wq = (const char*)d_in[5];
  const char* b_wk = (const char*)d_in[6];
  const char* b_wv = (const char*)d_in[7];
  const char* b_wo = (const char*)d_in[8];
  char* ws = (char*)d_ws;
  f32x2* st = (f32x2*)(ws + WS_ST);
  const float* cs = (const float*)(ws + WS_CS);
  k_prep<<<256 + 65 * 4, 256, 0, stream>>>(x, a_wqkv, b_wq, b_wk, b_wv, ws);
  const size_t xo[5] = {WS_X0, WS_X1, WS_X2, WS_X3, 0};
  for (int l = 0; l < 2; ++l) {
    char* xa = ws + xo[2 * l]; char* xb = ws + xo[2 * l + 1]; char* xc = ws + xo[2 * l + 2];
    k_qkvA<<<256, 256, 0, stream>>>(xa, a_wqkv, a_wphi, st, cs, ws + WS_PHIQ, ws + WS_KVP, ws + WS_ZP);
    k_oA<<<256, 256, 0, stream>>>(ws + WS_PHIQ, ws + WS_KVP, ws + WS_ZP, ws + WS_O);
    k_proj<false><<<256, 256, 0, stream>>>(ws + WS_O, a_wo, xa, xb, st);
    k_qkvB<<<256, 256, 0, stream>>>(xb, b_wq, b_wk, b_wv, st, cs, ws + WS_Q, ws + WS_KVF);
    k_attn<<<256, 512, 0, stream>>>(ws + WS_Q, ws + WS_KVF, ws + WS_O);
    if (l == 0) k_proj<false><<<256, 256, 0, stream>>>(ws + WS_O, b_wo, xb, xc, st);
    else k_proj<true><<<256, 256, 0, stream>>>(ws + WS_O, b_wo, xb, (char*)d_out, st);
  }
}
```

```cpp
#include <hip/hip_runtime.h>
#include <hip/hip_bf16.h>
#include <stdint.h>
#include <type_traits>

typedef __attribute__((ext_vector_type(8))) short bf16x8;
typedef __attribute__((ext_vector_type(4))) short s16x4;
typedef __attribute__((ext_vector_type(2))) __bf16 bf2_t;
typedef __attribute__((ext_vector_type(16))) float f32x16;
typedef __attribute__((ext_vector_type(4))) float f32x4;
typedef __attribute__((ext_vector_type(2))) float f32x2;
typedef __attribute__((ext_vector_type(4))) unsigned u32x4;
typedef __attribute__((ext_vector_type(2))) unsigned u32x2;
#define LAS __attribute__((address_space(3)))
#define DI __device__ __forceinline__
#define MFMA32(a, b, c) __builtin_amdgcn_mfma_f32_32x32x16_bf16((a), (b), (c), 0, 0, 0)
#define MFMA16(a, b, c) __builtin_amdgcn_mfma_f32_16x16x32_bf16((a), (b), (c), 0, 0, 0)
#define SBAR() __builtin_amdgcn_sched_barrier(0)

constexpr int S_ = 2048, D_ = 1024;
constexpr float LOG2E = 1.4426950408889634f;
constexpr float QSCALE = 0.17677669529663687f * LOG2E;

constexpr size_t MB = 1u << 20;
constexpr size_t WS_X0 = 0, WS_X1 = 4 * MB, WS_X2 = 8 * MB, WS_X3 = 12 * MB, WS_O = 16 * MB, WS_PHIQ = 20 * MB, WS_KVP = 28 * MB,
                 WS_Q = 32 * MB, WS_ZP = 36 * MB, WS_KVF = 36 * MB + 256 * 1024, WS_ST = 37 * MB, WS_CS = 38 * MB;
constexpr int CS_N = 3072 + 1024 + 64;

template <int I, int N, class F> DI void sfor(F&& f) { if constexpr (I < N) { f(std::integral_constant<int, I>{}); sfor<I + 1, N>(f); } }

DI unsigned cvtpk(float lo, float hi) { f32x2 v = {lo, hi}; return __builtin_bit_cast(unsigned, __builtin_convertvector(v, bf2_t)); }
DI float bflo(unsigned u) { return __uint_as_float(u << 16); }
DI float bfhi(unsigned u) { return __uint_as_float(u & 0xffff0000u); }
DI float rbf(float x) { return bflo(cvtpk(x, x)); }
DI float swap_add(float v) { auto r = __builtin_amdgcn_permlane32_swap(__float_as_uint(v), __float_as_uint(v), false, false); return __uint_as_float(r[0]) + __uint_as_float(r[1]); }
DI float swap_max(float v) { auto r = __builtin_amdgcn_permlane32_swap(__float_as_uint(v), __float_as_uint(v), false, false); return fmaxf(__uint_as_float(r[0]), __uint_as_float(r[1])); }
DI float swap_min(float v) { auto r = __builtin_amdgcn_permlane32_swap(__float_as_uint(v), __float_as_uint(v), false, false); return fminf(__uint_as_float(r[0]), __uint_as_float(r[1])); }
template <int S> DI bf16x8 pack8(const f32x16& v) {
  u32x4 p = {cvtpk(v[8 * S], v[8 * S + 1]), cvtpk(v[8 * S + 2], v[8 * S + 3]), cvtpk(v[8 * S + 4], v[8 * S + 5]), cvtpk(v[8 * S + 6], v[8 * S + 7])};
  return __builtin_bit_cast(bf16x8, p);
}
DI u32x4 unperm(u32x4 v) {
  auto r0 = __builtin_amdgcn_permlane32_swap(v[0], v[2], false, false);
  auto r1 = __builtin_amdgcn_permlane32_swap(v[1], v[3], false, false);
  return u32x4{r0[0], r1[0], r0[1], r1[1]};
}
DI s16x4 trread(LAS const char* p) { return __builtin_amdgcn_ds_read_tr16_b64_v4i16((LAS s16x4*)p); }
DI bf16x8 cat8(s16x4 a, s16x4 b) { return __builtin_shufflevector(a, b, 0, 1, 2, 3, 4, 5, 6, 7); }
DI float dot2(unsigned a, unsigned b, float c) { return __builtin_amdgcn_fdot2_f32_bf16(__builtin_bit_cast(bf2_t, a), __builtin_bit_cast(bf2_t, b), c, false); }

template <bool AG, int OFF> DI void ldx(bf16x8& d, unsigned voff, const char* sb) {
  if constexpr (AG) asm volatile("global_load_dwordx4 %0, %1, %2 offset:%3" : "=a"(d) : "v"(voff), "s"(sb), "i"(OFF));
  else asm volatile("global_load_dwordx4 %0, %1, %2 offset:%3" : "=v"(d) : "v"(voff), "s"(sb), "i"(OFF));
}
template <int NA, int C> DI void issue_x(bf16x8 (&x)[64], unsigned voff, const char* xb) {
  const char* sb = xb + C * 8192 + 4096;
  sfor<0, 8>([&](auto i) { ldx<(C * 8 < NA), i * 1024 - 4096>(x[C * 8 + i], voff, sb); });
}
DI void dma4(unsigned lds, unsigned v0, unsigned v1, unsigned v2, unsigned v3, const char* sb) {
  unsigned keep;
  asm volatile("s_mov_b32 %0, m0\n\ts_mov_b32 m0, %1\n\ts_nop 0\n\t"
               "global_load_lds_dwordx4 %2, %6 offset:0\n\tglobal_load_lds_dwordx4 %3, %6 offset:1024\n\t"
               "global_load_lds_dwordx4 %4, %6 offset:2048\n\tglobal_load_lds_dwordx4 %5, %6 offset:3072\n\ts_mov_b32 m0, %0"
               : "=&s"(keep) : "s"(lds), "v"(v0), "v"(v1), "v"(v2), "v"(v3), "s"(sb) : "memory");
}
DI void dma2(unsigned lds, unsigned v, const char* sb) {
  unsigned keep;
  asm volatile("s_mov_b32 %0, m0\n\ts_mov_b32 m0, %1\n\ts_nop 0\n\t"
               "global_load_lds_dwordx4 %2, %3 offset:0\n\tglobal_load_lds_dwordx4 %2, %3 offset:1024\n\ts_mov_b32 m0, %0"
               : "=&s"(keep) : "s"(lds), "v"(v), "s"(sb) : "memory");
}
DI void dma1(unsigned lds, unsigned v, const char* sb) {
  unsigned keep;
  asm volatile("s_mov_b32 %0, m0\n\ts_mov_b32 m0, %1\n\ts_nop 0\n\tglobal_load_lds_dwordx4 %2, %3 offset:0\n\ts_mov_b32 m0, %0"
               : "=&s"(keep) : "s"(lds), "v"(v), "s"(sb) : "memory");
}
template <int N> DI void wait_bar() { asm volatile("s_waitcnt vmcnt(%0)\n\ts_barrier" ::"i"(N > 63 ? 63 : N) : "memory"); }
template <int NA, int C, int N> DI void wait_bar_x(bf16x8 (&x)[64]) {
  constexpr int n = N > 63 ? 63 : N;
  if constexpr (C * 8 < NA)
    asm volatile("s_waitcnt vmcnt(%8)\n\ts_barrier"
                 : "+a"(x[C * 8]), "+a"(x[C * 8 + 1]), "+a"(x[C * 8 + 2]), "+a"(x[C * 8 + 3]), "+a"(x[C * 8 + 4]), "+a"(x[C * 8 + 5]), "+a"(x[C * 8 + 6]), "+a"(x[C * 8 + 7])
                 : "i"(n) : "memory");
  else
    asm volatile("s_waitcnt vmcnt(%8)\n\ts_barrier"
                 : "+v"(x[C * 8]), "+v"(x[C * 8 + 1]), "+v"(x[C * 8 + 2]), "+v"(x[C * 8 + 3]), "+v"(x[C * 8 + 4]), "+v"(x[C * 8 + 5]), "+v"(x[C * 8 + 6]), "+v"(x[C * 8 + 7])
                 : "i"(n) : "memory");
}
DI void lds_bar() { asm volatile("s_waitcnt lgkmcnt(0)\n\ts_barrier" ::: "memory"); }

template <int RBX> DI void block_map(int& rb, int& cb) {
  const int x = blockIdx.x & 7, i = blockIdx.x >> 3;
  constexpr int CBX = 32 / RBX, GC = 16 / CBX;
  rb = RBX * (x / GC) + i / CBX; cb = CBX * (x % GC) + i % CBX;
}

DI void ln_stats(const f32x2* st, int token, int h, float& mean, float& rstd) {
  float s = 0.f, q = 0.f;
#pragma unroll
  for (int i = 0; i < 8; ++i) { const f32x2 v = st[(8 * h + i) * S_ + token]; s += v[0]; q += v[1]; }
  s = swap_add(s); q = swap_add(q);
  const float m = s * (1.f / D_);
  const float var = fmaxf(q * (1.f / D_) - m * m, 0.f);
  mean = rbf(m);
  rstd = 1.f / rbf(sqrtf(rbf(rbf(var) + 1e-5f)));
}

__global__ __launch_bounds__(256) void k_prep(const char* x, const char* wqkv, const char* wq, const char* wk, const char* wv, char* ws) {
  const int tid = threadIdx.x, lane = tid & 63, w = tid >> 6, r = lane & 31, h = lane >> 5;
  __shared__ float red[32][64];
  if (blockIdx.x < 256) {
    const int tt = blockIdx.x >> 2, cb = (blockIdx.x & 3) * 4 + w;
    const char* src = x + (size_t)(tt * 32 + r) * 2048 + cb * 128 + h * 16;
    u32x4 v[4];
#pragma unroll
    for (int i = 0; i < 4; ++i) v[i] = *(const u32x4*)(src + i * 32);
    float s = 0.f, q = 0.f;
#pragma unroll
    for (int i = 0; i < 4; ++i) {
#pragma unroll
      for (int e = 0; e < 4; ++e) { s = dot2(v[i][e], 0x3f803f80u, s); q = dot2(v[i][e], v[i][e], q); }
      auto r0 = __builtin_amdgcn_permlane32_swap(v[i][0], v[i][2], false, false);
      auto r1 = __builtin_amdgcn_permlane32_swap(v[i][1], v[i][3], false, false);
      u32x4 o = {r0[0], r1[0], r0[1], r1[1]};
      *(u32x4*)(ws + WS_X0 + ((size_t)(tt * 64 + cb * 4 + i) * 64 + lane) * 16) = o;
    }
    s = swap_add(s); q = swap_add(q);
    if (h == 0) ((f32x2*)(ws + WS_ST))[cb * S_ + tt * 32 + r] = f32x2{s, q};
  } else {
    const int u = blockIdx.x - 256, qr = u & 3, g = u >> 2;
    const char* src; int pitch;
    const int c8 = tid & 7, rr = tid >> 3;
    if (g < 48) { src = wqkv + g * 128 + c8 * 16; pitch = 6144; }
    else if (g < 64) { src = wq + (g - 48) * 128 + c8 * 16; pitch = 2048; }
    else { src = (c8 < 4 ? wk : wv) + (c8 & 3) * 16; pitch = 64; }
    src += (size_t)(qr * 256 + rr) * pitch;
    u32x4 v[8];
#pragma unroll
    for (int i = 0; i < 8; ++i) v[i] = *(const u32x4*)(src + (size_t)i * 32 * pitch);
    float a[8] = {};
#pragma unroll
    for (int i = 0; i < 8; ++i)
#pragma unroll
      for (int e = 0; e < 4; ++e) { a[2 * e] += bflo(v[i][e]); a[2 * e + 1] += bfhi(v[i][e]); }
#pragma unroll
    for (int e = 0; e < 8; ++e) red[rr][c8 * 8 + e] = a[e];
    __syncthreads();
    if (tid < 64) {
      float t = 0.f;
#pragma unroll
      for (int i = 0; i < 32; ++i) t += red[i][tid];
      ((float*)(ws + WS_CS))[qr * CS_N + g * 64 + tid] = t;
    }
  }
}

struct GemmLane {
  unsigned wv0, wv1, wv2, wv3;
  unsigned a0, a1;
};
DI GemmLane gemm_lane(int lane, int w, int pitch) {
  GemmLane g;
  const int row = lane >> 3, ch = (lane & 7) ^ (((row >> 1) & 1) << 2);
  g.wv0 = (unsigned)((32 * w + row) * pitch + ch * 16);
  g.wv1 = g.wv0 + 8 * pitch - 1024; g.wv2 = g.wv0 + 16 * pitch - 2048; g.wv3 = g.wv0 + 24 * pitch - 3072;
  const int h = lane >> 5, blk = (lane >> 4) & 1, q = (lane & 15) >> 2, p = lane & 3;
  g.a0 = (unsigned)((4 * h + q) * 128 + (((q >> 1) << 2) + 2 * blk + (p >> 1)) * 16 + 8 * (p & 1));
  g.a1 = g.a0 ^ 64u;
  return g;
}

template <int NW> struct WinPtr {
  unsigned b[NW];
  DI void init(unsigned a) { sfor<0, NW>([&](auto i) { b[i] = a + i * 65536u; asm volatile("" : "+v"(b[i])); }); }
  template <int OFF> DI bf16x8 frag() const {
    LAS const char* p = (LAS const char*)(uintptr_t)b[OFF >> 16] + (OFF & 65535);
    return cat8(trread(p), trread(p + 1024));
  }
};

template <bool FINAL>
__global__ __launch_bounds__(256, 1) void k_proj(const char* xin, const char* W, const char* res, char* xout, f32x2* st) {
  __shared__ __attribute__((aligned(16))) char smem[131072];
  constexpr int NA = 48;
  const int lane = threadIdx.x & 63, w = __builtin_amdgcn_readfirstlane(threadIdx.x >> 6), r = lane & 31, h = lane >> 5;
  int rb, cb; block_map<4>(rb, cb);
  const int tt = rb * 4 + w;
  const unsigned lds0 = (unsigned)(uintptr_t)(LAS char*)smem;
  const GemmLane g = gemm_lane(lane, w, 2048);
  const unsigned xv = lane * 16;
  const char* xb = xin + (size_t)tt * 65536;
  const char* wb = W + cb * 128;
  bf16x8 x[64], rs[4];
  { const char* rbp = res + (size_t)tt * 65536 + cb * 4096; sfor<0, 4>([&](auto i) { ldx<false, i * 1024>(rs[i], xv, rbp); }); }
  auto issue = [&](auto c) { dma4(lds0 + c * 16384 + w * 4096, g.wv0, g.wv1, g.wv2, g.wv3, wb + (size_t)c * 128 * 2048); issue_x<NA, c>(x, xv, xb); };
  sfor<0, 4>(issue);
  f32x16 acc0 = {}, acc1 = {};
  bf16x8 f0[3], f1[3];
  WinPtr<2> p0, p1; p0.init(lds0 + g.a0); p1.init(lds0 + g.a1);
  auto rd = [&](auto s) {
    if constexpr (s % 8 == 0) {
      constexpr int c = s / 8, issued = c + 4 > 8 ? 8 : c + 4;
      wait_bar_x<NA, c, 12 * (issued - c - 1)>(x);
      if constexpr (c + 4 < 8) issue(std::integral_constant<int, c + 4>{});
    }
    f0[s % 3] = p0.template frag<s * 2048>(); f1[s % 3] = p1.template frag<s * 2048>();
  };
  rd(std::integral_constant<int, 0>{}); rd(std::integral_constant<int, 1>{});
  sfor<0, 64>([&](auto s) {
    if constexpr (s + 2 < 64) rd(std::integral_constant<int, s + 2>{});
    SBAR();
    acc0 = MFMA32(f0[s % 3], x[s], acc0); acc1 = MFMA32(f1[s % 3], x[s], acc1);
    SBAR();
  });
  asm volatile("" : "+v"(rs[0]), "+v"(rs[1]), "+v"(rs[2]), "+v"(rs[3]));
  float s1 = 0.f, s2 = 0.f;
  u32x4 o[4];
  sfor<0, 4>([&](auto i) {
    const f32x16& a = (i < 2) ? acc0 : acc1;
    const u32x4 rr = __builtin_bit_cast(u32x4, rs[i]);
#pragma unroll
    for (int e = 0; e < 4; ++e) {
      o[i][e] = cvtpk(a[(i & 1) * 8 + 2 * e] + bflo(rr[e]), a[(i & 1) * 8 + 2 * e + 1] + bfhi(rr[e]));
      if (!FINAL) { s1 = dot2(o[i][e], 0x3f803f80u, s1); s2 = dot2(o[i][e], o[i][e], s2); }
    }
  });
  if constexpr (FINAL) {
    char* dst = xout + (size_t)(tt * 32 + r) * 2048 + cb * 128 + h * 16;
#pragma unroll
    for (int i = 0; i < 4; ++i) {
      auto r0 = __builtin_amdgcn_permlane32_swap(o[i][0], o[i][2], false, false);
      auto r1 = __builtin_amdgcn_permlane32_swap(o[i][1], o[i][3], false, false);
      *(u32x4*)(dst + i * 32) = u32x4{r0[0], r1[0], r0[1], r1[1]};
    }
  } else {
    char* dst = xout + (size_t)tt * 65536 + cb * 4096 + lane * 16;
#pragma unroll
    for (int i = 0; i < 4; ++i) *(u32x4*)(dst + i * 1024) = o[i];
    s1 = swap_add(s1); s2 = swap_add(s2);
    if (h == 0) st[cb * S_ + tt * 32 + r] = f32x2{s1, s2};
  }
}

DI void load_cs(const float* cs, int n0, int h, float (&c)[16]) {
#pragma unroll
  for (int gq = 0; gq < 4; ++gq) {
    f32x4 t = *(const f32x4*)(cs + n0 + 8 * gq + 4 * h);
#pragma unroll
    for (int p = 1; p < 4; ++p) t += *(const f32x4*)(cs + p * CS_N + n0 + 8 * gq + 4 * h);
#pragma unroll
    for (int e = 0; e < 4; ++e) c[4 * gq + e] = t[e];
  }
}

constexpr int A_NS = 9;
constexpr int a_last_w(int g) { return g < 3 ? g + 5 : (2 * g + 2 < g + 7 ? 2 * g + 2 : (g + 7 > 23 ? 23 : g + 7)); }
constexpr int a_ops_upto(int j) { return j < 8 ? 12 * (j + 1) : 96 + 4 * (j - 7); }
__global__ __launch_bounds__(256, 1) void k_qkvA(const char* xin, const char* W, const char* wphi, const f32x2* st, const float* cs, char* phiq, char* kvp, char* zp) {
  __shared__ __attribute__((aligned(16))) char smem[A_NS * 16384 + 4096 + 1024];
  constexpr int NA = 40;
  const int lane = threadIdx.x & 63, w = __builtin_amdgcn_readfirstlane(threadIdx.x >> 6), r = lane & 31, h = lane >> 5;
  int rb, cb; block_map<8>(rb, cb);
  const int tt = rb * 4 + w;
  const unsigned lds0 = (unsigned)(uintptr_t)(LAS char*)smem;
  const GemmLane g = gemm_lane(lane, w, 6144);
  const unsigned xv = lane * 16;
  const char* xb = xin + (size_t)tt * 65536;
  const char* wb = W + cb * 128;
  bf16x8 x[64];
  dma1(lds0 + A_NS * 16384 + w * 1024, xv, wphi + cb * 4096 + w * 1024);
  auto issue = [&](auto j) {
    constexpr int p = j / 8, c = j % 8, col = p == 0 ? 2048 : (p == 1 ? 0 : 4096);
    dma4(lds0 + (j % A_NS) * 16384 + w * 4096, g.wv0, g.wv1, g.wv2, g.wv3, wb + col + (size_t)c * 128 * 6144);
    if constexpr (j < 8) issue_x<NA, j>(x, xv, xb);
  };
  sfor<0, 5>(issue);
  f32x16 acc[6] = {};
  bf16x8 f0[3], f1[3];
  WinPtr<3> p0, p1; p0.init(lds0 + g.a0); p1.init(lds0 + g.a1);
  auto rd = [&](auto s) {
    constexpr int j = s / 8;
    if constexpr (s % 8 == 0) {
      constexpr int before = j == 0 ? 4 : a_last_w(j - 1), n = a_ops_upto(before) - a_ops_upto(j);
      if constexpr (j < 8) wait_bar_x<NA, j, n>(x); else wait_bar<n>();
      sfor<before + 1, a_last_w(j) + 1>(issue);
    }
    constexpr int off = (j % A_NS) * 16384 + (s % 8) * 2048;
    f0[s % 3] = p0.template frag<off>(); f1[s % 3] = p1.template frag<off>();
  };
  rd(std::integral_constant<int, 0>{}); rd(std::integral_constant<int, 1>{});
  sfor<0, 192>([&](auto s) {
    if constexpr (s + 2 < 192) rd(std::integral_constant<int, s + 2>{});
    SBAR();
    constexpr int p = s / 64;
    if constexpr (p < 2) { acc[2 * p] = MFMA32(f0[s % 3], x[s % 64], acc[2 * p]); acc[2 * p + 1] = MFMA32(f1[s % 3], x[s % 64], acc[2 * p + 1]); }
    else { acc[4] = MFMA32(x[s % 64], f0[s % 3], acc[4]); acc[5] = MFMA32(x[s % 64], f1[s % 3], acc[5]); }
    SBAR();
  });
  float mean, rstd; ln_stats(st, tt * 32 + r, h, mean, rstd);
  LAS f32x2* mr = (LAS f32x2*)((LAS char*)smem + A_NS * 16384 + 4096) + w * 32;
  if (h == 0) mr[r] = f32x2{mean, rstd};
  lds_bar();
  LAS char* pk_img = (LAS char*)smem;
  LAS char* v_img = (LAS char*)smem + 32768;
  const int blk = (lane >> 4) & 1, q4 = (lane & 15) >> 2, p4 = lane & 3;
  sfor<0, 4>([&](auto t) {
    constexpr int hd = t & 1; constexpr bool isq = t >= 2;
    float c[16]; load_cs(cs, (isq ? 0 : 1024) + cb * 64 + hd * 32, h, c);
    f32x16 v;
#pragma unroll
    for (int i = 0; i < 16; ++i) v[i] = (acc[t][i] - mean * c[i]) * rstd;
    LAS const char* wp = (LAS const char*)smem + A_NS * 16384 + hd * 2048 + (4 * h + q4) * 64 + blk * 32 + p4 * 8;
    f32x16 pz = {};
    pz = MFMA32(cat8(trread(wp), trread(wp + 512)), pack8<0>(v), pz);
    pz = MFMA32(cat8(trread(wp + 1024), trread(wp + 1536)), pack8<1>(v), pz);
    float mx = pz[0], mn = pz[0];
#pragma unroll
    for (int i = 1; i < 16; ++i) { mx = fmaxf(mx, pz[i]); mn = fminf(mn, pz[i]); }
    mx = swap_max(mx) * LOG2E; mn = swap_min(mn) * LOG2E;
    f32x16 ep, en; float sp = 0.f, sn = 0.f;
#pragma unroll
    for (int i = 0; i < 16; ++i) {
      ep[i] = __builtin_amdgcn_exp2f(__builtin_fmaf(pz[i], LOG2E, -mx)); sp += ep[i];
      en[i] = __builtin_amdgcn_exp2f(__builtin_fmaf(pz[i], -LOG2E, mn)); sn += en[i];
    }
    sp = __builtin_amdgcn_rcpf(swap_add(sp)); sn = __builtin_amdgcn_rcpf(swap_add(sn));
#pragma unroll
    for (int i = 0; i < 16; ++i) { ep[i] *= sp; en[i] *= sn; }
    if constexpr (isq) {
      char* dst = phiq + ((size_t)(tt * 32 + cb * 2 + hd) * 4) * 1024 + lane * 16;
      *(bf16x8*)(dst) = pack8<0>(ep); *(bf16x8*)(dst + 1024) = pack8<1>(ep);
      *(bf16x8*)(dst + 2048) = pack8<0>(en); *(bf16x8*)(dst + 3072) = pack8<1>(en);
    } else {
      LAS char* row = pk_img + hd * 16384 + (w * 32 + r) * 128 + h * 8;
#pragma unroll
      for (int gq = 0; gq < 4; ++gq) {
        *(LAS u32x2*)(row + ((gq ^ (r & 7)) << 4)) = u32x2{cvtpk(ep[4 * gq], ep[4 * gq + 1]), cvtpk(ep[4 * gq + 2], ep[4 * gq + 3])};
        *(LAS u32x2*)(row + (((4 + gq) ^ (r & 7)) << 4)) = u32x2{cvtpk(en[4 * gq], en[4 * gq + 1]), cvtpk(en[4 * gq + 2], en[4 * gq + 3])};
      }
    }
  });
  sfor<0, 2>([&](auto hd) {
    const int n = 2048 + cb * 64 + hd * 32 + r;
    const float c = cs[n] + cs[CS_N + n] + cs[2 * CS_N + n] + cs[3 * CS_N + n];
    f32x16 v;
#pragma unroll
    for (int i = 0; i < 16; ++i) { const f32x2 m = mr[(i & 3) + 8 * (i >> 2) + 4 * h]; v[i] = (acc[4 + hd][i] - m[0] * c) * m[1]; }
    LAS char* d = v_img + (hd * 8 + w * 2) * 1024 + lane * 16;
    *(LAS bf16x8*)d = pack8<0>(v); *(LAS bf16x8*)(d + 1024) = pack8<1>(v);
  });
  lds_bar();
  {
    const int hd = w >> 1, fh = w & 1;
    f32x16 kv = {}, z = {};
    const bf16x8 ones = {0x3f80, 0x3f80, 0x3f80, 0x3f80, 0x3f80, 0x3f80, 0x3f80, 0x3f80};
    const int ch = fh * 4 + 2 * blk + (p4 >> 1), r0 = 4 * h + q4;
    LAS const char* pa = pk_img + hd * 16384 + r0 * 128 + ((ch ^ r0) << 4) + 8 * (p4 & 1);
    LAS const char* pb = v_img + hd * 8192 + lane * 16;
#pragma unroll
    for (int s = 0; s < 8; ++s) {
      const bf16x8 a = cat8(trread(pa + s * 2048), trread(pa + s * 2048 + 1024));
      kv = MFMA32(a, *(LAS const bf16x8*)(pb + s * 1024), kv);
      z = MFMA32(a, ones, z);
    }
    const int u = (rb * 32 + cb * 2 + hd) * 2 + fh;
    char* dst = kvp + (size_t)u * 4096 + lane * 16;
#pragma unroll
    for (int gq = 0; gq < 4; ++gq) *(f32x4*)(dst + gq * 1024) = f32x4{kv[4 * gq], kv[4 * gq + 1], kv[4 * gq + 2], kv[4 * gq + 3]};
    if (r == 0) {
#pragma unroll
      for (int gq = 0; gq < 4; ++gq) *(f32x4*)(zp + (size_t)u * 128 + h * 64 + gq * 16) = f32x4{z[4 * gq], z[4 * gq + 1], z[4 * gq + 2], z[4 * gq + 3]};
    }
  }
}

__global__ __launch_bounds__(256) void k_oA(const char* phiq, const char* kvp, const char* zp, char* o) {
  __shared__ __attribute__((aligned(16))) f32x4 red[4][4][64];
  __shared__ __attribute__((aligned(16))) f32x4 zred[4][8];
  const int lane = threadIdx.x & 63, w = threadIdx.x >> 6, h = lane >> 5;
  const int head = (blockIdx.x & 7) * 4 + ((blockIdx.x >> 3) & 3), tb = blockIdx.x >> 5;
  bf16x8 pq[2][4];
#pragma unroll
  for (int t = 0; t < 2; ++t)
#pragma unroll
    for (int k = 0; k < 4; ++k) pq[t][k] = *(const bf16x8*)(phiq + ((size_t)((tb * 8 + w * 2 + t) * 32 + head) * 4 + k) * 1024 + lane * 16);
  {
    const int fh = w & 1, half = w >> 1;
    f32x4 a[4] = {}, zz = {};
#pragma unroll
    for (int i = 0; i < 8; ++i) {
      const int u = ((half * 8 + i) * 32 + head) * 2 + fh;
#pragma unroll
      for (int gq = 0; gq < 4; ++gq) a[gq] += *(const f32x4*)(kvp + (size_t)u * 4096 + gq * 1024 + lane * 16);
      zz += *(const f32x4*)(zp + (size_t)u * 128 + (lane & 7) * 16);
    }
#pragma unroll
    for (int gq = 0; gq < 4; ++gq) red[w][gq][lane] = a[gq];
    if (lane < 8) zred[w][lane] = zz;
  }
  __syncthreads();
  bf16x8 ka[4], za[4];
#pragma unroll
  for (int k = 0; k < 4; ++k) {
    const int fh = k >> 1, s = k & 1;
    const f32x4 a0 = red[fh][2 * s][lane] + red[fh + 2][2 * s][lane], a1 = red[fh][2 * s + 1][lane] + red[fh + 2][2 * s + 1][lane];
    const f32x4 z0 = zred[fh][h * 4 + 2 * s] + zred[fh + 2][h * 4 + 2 * s], z1 = zred[fh][h * 4 + 2 * s + 1] + zred[fh + 2][h * 4 + 2 * s + 1];
    ka[k] = __builtin_bit_cast(bf16x8, u32x4{cvtpk(a0[0], a0[1]), cvtpk(a0[2], a0[3]), cvtpk(a1[0], a1[1]), cvtpk(a1[2], a1[3])});
    za[k] = __builtin_bit_cast(bf16x8, u32x4{cvtpk(z0[0], z0[1]), cvtpk(z0[2], z0[3]), cvtpk(z1[0], z1[1]), cvtpk(z1[2], z1[3])});
  }
#pragma unroll
  for (int t = 0; t < 2; ++t) {
    f32x16 num = {}, den = {};
#pragma unroll
    for (int k = 0; k < 4; ++k) { num = MFMA32(ka[k], pq[t][k], num); den = MFMA32(za[k], pq[t][k], den); }
    const float inv = 1.f / (den[0] + 1e-6f);
#pragma unroll
    for (int i = 0; i < 16; ++i) num[i] *= inv;
    char* dst = o + ((size_t)(tb * 8 + w * 2 + t) * 64 + head * 2) * 1024 + lane * 16;
    *(bf16x8*)dst = pack8<0>(num); *(bf16x8*)(dst + 1024) = pack8<1>(num);
  }
}

template <int KV>
DI void qkvB_body(char* smem, const char* xin, const char* W, const char* wkv, const f32x2* st, const float* cs, char* qf, char* kvf, int rb, int cb) {
  constexpr int NA = 48, OPS = KV ? 13 : 12;
  const int lane = threadIdx.x & 63, w = __builtin_amdgcn_readfirstlane(threadIdx.x >> 6), r = lane & 31, h = lane >> 5;
  const int tt = rb * 4 + w, hf = (cb >> 2) & 1;
  const unsigned lds0 = (unsigned)(uintptr_t)(LAS char*)smem;
  const GemmLane g = gemm_lane(lane, w, 2048);
  const unsigned xv = lane * 16;
  const char* xb = xin + (size_t)tt * 65536;
  const char* wb = W + cb * 128;
  const unsigned kvv = (unsigned)((32 * w + (lane >> 1)) * 64 + hf * 32 + (lane & 1) * 16);
  bf16x8 x[64];
  auto issue = [&](auto c) {
    dma4(lds0 + c * 16384 + w * 4096, g.wv0, g.wv1, g.wv2, g.wv3, wb + (size_t)c * 128 * 2048);
    if constexpr (KV) dma1(lds0 + 131072 + c * 4096 + w * 1024, kvv, wkv + c * 128 * 64);
    issue_x<NA, c>(x, xv, xb);
  };
  sfor<0, 4>(issue);
  f32x16 acc0 = {}, acc1 = {}, acc2 = {};
  bf16x8 f0[3], f1[3], f2[3];
  WinPtr<2> p0, p1; p0.init(lds0 + g.a0); p1.init(lds0 + g.a1);
  unsigned a2 = lds0 + 131072 + (4 * h + ((lane & 15) >> 2)) * 32 + (lane & 3) * 8; asm volatile("" : "+v"(a2));
  LAS const char* p2 = (LAS const char*)(uintptr_t)a2;
  auto rd = [&](auto s) {
    if constexpr (s % 8 == 0) {
      constexpr int c = s / 8, issued = c + 4 > 8 ? 8 : c + 4;
      wait_bar_x<NA, c, OPS * (issued - c - 1)>(x);
      if constexpr (c + 4 < 8) issue(std::integral_constant<int, c + 4>{});
    }
    f0[s % 3] = p0.template frag<s * 2048>(); f1[s % 3] = p1.template frag<s * 2048>();
    if constexpr (KV) f2[s % 3] = cat8(trread(p2 + s * 512), trread(p2 + s * 512 + 256));
  };
  rd(std::integral_constant<int, 0>{}); rd(std::integral_constant<int, 1>{});
  sfor<0, 64>([&](auto s) {
    if constexpr (s + 2 < 64) rd(std::integral_constant<int, s + 2>{});
    SBAR();
    acc0 = MFMA32(f0[s % 3], x[s], acc0); acc1 = MFMA32(f1[s % 3], x[s], acc1);
    if constexpr (KV == 1) acc2 = MFMA32(f2[s % 3], x[s], acc2);
    if constexpr (KV == 2) acc2 = MFMA32(x[s], f2[s % 3], acc2);
    SBAR();
  });
  float mean, rstd; ln_stats(st, tt * 32 + r, h, mean, rstd);
  sfor<0, 2>([&](auto t) {
    float c[16]; load_cs(cs, 3072 + cb * 64 + t * 32, h, c);
    const f32x16& a = t == 0 ? acc0 : acc1;
    f32x16 v; const float sc = rstd * QSCALE;
#pragma unroll
    for (int i = 0; i < 16; ++i) v[i] = (a[i] - mean * c[i]) * sc;
    char* dst = qf + ((size_t)tt * 64 + cb * 4 + t * 2) * 1024 + lane * 16;
    *(bf16x8*)dst = pack8<0>(v); *(bf16x8*)(dst + 1024) = pack8<1>(v);
  });
  if constexpr (KV == 1) {
    float c[16]; load_cs(cs, 4096 + hf * 16, h, c);
    f32x16 v;
#pragma unroll
    for (int i = 0; i < 8; ++i) v[i] = (acc2[i] - mean * c[i]) * rstd;
    *(bf16x8*)(kvf + (size_t)tt * 4096 + hf * 1024 + lane * 16) = pack8<0>(v);
  }
  if constexpr (KV == 2) {
    lds_bar();
    LAS f32x2* mr = (LAS f32x2*)((LAS char*)smem) + w * 32;
    if (h == 0) mr[r] = f32x2{mean, rstd};
    const int n = 4096 + 32 + hf * 16 + (r & 15);
    const float c = cs[n] + cs[CS_N + n] + cs[2 * CS_N + n] + cs[3 * CS_N + n];
    f32x16 v;
#pragma unroll
    for (int i = 0; i < 16; ++i) { const f32x2 m = mr[(i & 3) + 8 * (i >> 2) + 4 * h]; v[i] = (acc2[i] - m[0] * c) * m[1]; }
    const u32x4 o0 = unperm(__builtin_bit_cast(u32x4, pack8<0>(v))), o1 = unperm(__builtin_bit_cast(u32x4, pack8<1>(v)));
    if (r < 16) {
      char* dst = kvf + (size_t)tt * 4096 + 2048 + (hf * 16 + r + 32 * h) * 16;
      *(u32x4*)dst = o0; *(u32x4*)(dst + 1024) = o1;
    }
  }
}
__global__ __launch_bounds__(256, 1) void k_qkvB(const char* xin, const char* wq, const char* wk, const char* wv, const f32x2* st, const float* cs, char* qf, char* kvf) {
  __shared__ __attribute__((aligned(16))) char smem[163840];
  int rb, cb; block_map<4>(rb, cb);
  if ((cb & 3) != 0) qkvB_body<0>(smem, xin, wq, wk, st, cs, qf, kvf, rb, cb);
  else if (cb < 8) qkvB_body<1>(smem, xin, wq, wk, st, cs, qf, kvf, rb, cb);
  else qkvB_body<2>(smem, xin, wq, wv, st, cs, qf, kvf, rb, cb);
}

typedef __attribute__((ext_vector_type(8))) float f32x8;
#define LO8(v) __builtin_shufflevector(v, v, 0, 1, 2, 3, 4, 5, 6, 7)
#define HI8(v) __builtin_shufflevector(v, v, 8, 9, 10, 11, 12, 13, 14, 15)
#define A_EXP(x) asm volatile("v_exp_f32 %0, %0" : "+v"(x))
#define A_S0(d, k, q, c) asm volatile("v_mfma_f32_32x32x16_bf16 %0, %1, %2, %3" : "=&v"(d) : "v"(k), "v"(q), "v"(c))
#define A_S1(d, k, q) asm volatile("v_mfma_f32_32x32x16_bf16 %0, %1, %2, %0" : "+v"(d) : "v"(k), "v"(q))
#define A_PV(d, v, p) asm volatile("v_smfmac_f32_32x32x32_bf16 %0, %1, %2, %3" : "+v"(d) : "v"(v), "v"(p), "v"(idx))
#define A_L(d, p) asm volatile("v_smfmac_f32_16x16x64_bf16 %0, %1, %2, %3" : "+v"(d) : "v"(sel), "v"(p), "v"(idx))
#define A_RD(d, a, off) asm volatile("ds_read_b128 %0, %1 offset:" #off : "=v"(d) : "v"(a))
#define HALF(E, SN, K0, K1, Q0, Q1, NM, O, L, V0, V1, P, R0, R1, R2, R3) do { \
    A_S0(SN, K0, Q0, NM); R0; A_EXP(E[0]); A_EXP(E[1]); A_EXP(E[2]); \
    A_S1(SN, K1, Q1);     R1; A_EXP(E[3]); A_EXP(E[4]); A_EXP(E[5]); \
    A_PV(O, V0, LO8(P));  R2; A_EXP(E[6]); A_EXP(E[7]); A_EXP(E[8]); \
    A_PV(O, V1, HI8(P));  R3; A_EXP(E[9]); A_EXP(E[10]); A_EXP(E[11]); \
    A_L(L, LO8(P));           A_EXP(E[12]); A_EXP(E[13]); \
    A_L(L, HI8(P));           A_EXP(E[14]); A_EXP(E[15]); \
  } while (0)
#define A_LGKM(a, b, c, d) asm volatile("s_waitcnt lgkmcnt(0)" : "+v"(a), "+v"(b), "+v"(c), "+v"(d))
DI float max16(const f32x16& s) {
  float mx = s[0];
#pragma unroll
  for (int e = 1; e < 16; ++e) mx = fmaxf(mx, s[e]);
  return mx;
}
__global__ __launch_bounds__(256, 1) void k_attn(const char* qf, const char* kvf, char* o) {
  __shared__ __attribute__((aligned(16))) char smem[32768 + 16];
  const int lane = threadIdx.x & 63, w = __builtin_amdgcn_readfirstlane(threadIdx.x >> 6);
  int rb, cb; block_map<4>(rb, cb);
  const int tt = rb * 4 + w;
  const unsigned lds0 = (unsigned)(uintptr_t)(LAS char*)smem;
  const unsigned xv = lane * 16;
  const char* qp = qf + ((size_t)tt * 64 + cb * 4) * 1024;
  bf16x8 qa0, qa1, qb0, qb1;
  ldx<false, 0>(qa0, xv, qp); ldx<false, 1024>(qa1, xv, qp); ldx<false, 2048>(qb0, xv, qp); ldx<false, 3072>(qb1, xv, qp);
  LAS volatile int* flag = (LAS volatile int*)((LAS char*)smem + 32768);
  const short s0 = ((lane >> 2) & 1) ? (short)0 : (short)0x3f80, s1 = (short)0x3f80 - s0;
  bf16x8 sel = {s0, s0, s0, s0, s1, s1, s1, s1};
  unsigned idx = 0xddddddddu;
  asm volatile("" : "+v"(sel), "+v"(idx));
  const f32x16 zero = {};
  if (threadIdx.x == 0) *flag = 0;
  float mA = 0.f, mB = 0.f;
  const char* src = kvf + 2048 + w * 2048;
  for (int attempt = 0; attempt < 2; ++attempt) {
    if (attempt == 1) {
      mA = -INFINITY; mB = -INFINITY;
      for (int t = 0; t < 64; ++t) {
        const bf16x8 k0 = *(const bf16x8*)(kvf + t * 4096 + xv), k1 = *(const bf16x8*)(kvf + t * 4096 + 1024 + xv);
        f32x16 a = MFMA32(k0, qa0, zero); a = MFMA32(k1, qa1, a);
        f32x16 b = MFMA32(k0, qb0, zero); b = MFMA32(k1, qb1, b);
        mA = fmaxf(mA, max16(a)); mB = fmaxf(mB, max16(b));
      }
      mA = swap_max(mA); mB = swap_max(mB);
    }
    bf16x8 ka0, ka1, kb0, kb1, va0, va1, vb0 = {}, vb1 = {};
    ldx<false, 0>(ka0, xv, kvf); ldx<false, 1024>(ka1, xv, kvf);
#pragma unroll
    for (int j = 0; j < 3; ++j) dma2(lds0 + j * 8192 + w * 2048, xv, src + j * 8192);
    asm volatile("s_waitcnt vmcnt(6)" : "+v"(ka0), "+v"(ka1), "+v"(qa0), "+v"(qa1), "+v"(qb0), "+v"(qb1));
    f32x16 sa0, sa1, sb0, sb1 = {}, nma, nmb, oa = {}, ob = {};
    f32x4 la = {}, lb = {};
    sa0 = MFMA32(ka0, qa0, zero); sa0 = MFMA32(ka1, qa1, sa0);
    if (attempt == 0) {
      sb0 = MFMA32(ka0, qb0, zero); sb0 = MFMA32(ka1, qb1, sb0);
      mA = swap_max(max16(sa0)); mB = swap_max(max16(sb0));
    }
#pragma unroll
    for (int e = 0; e < 16; ++e) { nma[e] = -mA; nmb[e] = -mB; sa0[e] -= mA; }
    asm volatile("s_nop 1" : "+v"(nma), "+v"(nmb), "+v"(sa0), "+v"(sb1), "+v"(oa), "+v"(ob), "+v"(la), "+v"(lb), "+v"(vb0), "+v"(vb1));
    for (int j = 0; j < 32; ++j) {
      wait_bar<4>();
      dma2(lds0 + ((j + 3) & 3) * 8192 + w * 2048, xv, src + (j + 3) * 8192);
      const unsigned ra = lds0 + (j & 3) * 8192 + xv;
      HALF(sa0, sb0, ka0, ka1, qb0, qb1, nmb, ob, lb, vb0, vb1, sb1, A_RD(kb0, ra, 2048), A_RD(kb1, ra, 3072), A_RD(va0, ra, 0), A_RD(va1, ra, 1024));
      A_LGKM(kb0, kb1, va0, va1);
      HALF(sb0, sa1, kb0, kb1, qa0, qa1, nma, oa, la, va0, va1, sa0, , , , );
      HALF(sa1, sb1, kb0, kb1, qb0, qb1, nmb, ob, lb, va0, va1, sb0, A_RD(ka0, ra, 6144), A_RD(ka1, ra, 7168), A_RD(vb0, ra, 4096), A_RD(vb1, ra, 5120));
      A_LGKM(ka0, ka1, vb0, vb1);
      HALF(sb1, sa0, ka0, ka1, qa0, qa1, nma, oa, la, vb0, vb1, sa1, , , , );
    }
    asm volatile("s_nop 1" : "+v"(sb1));
    A_PV(ob, vb0, LO8(sb1)); A_PV(ob, vb1, HI8(sb1)); A_L(lb, LO8(sb1)); A_L(lb, HI8(sb1));
    asm volatile("s_nop 15\n\ts_waitcnt vmcnt(0)" : "+v"(oa), "+v"(ob), "+v"(la), "+v"(lb), "+v"(sa0) :: "memory");
    bool bad = !(la[0] < 1.2676506e30f) || !(lb[0] < 1.2676506e30f);
    const float ia = 1.f / la[0], ib = 1.f / lb[0];
#pragma unroll
    for (int e = 0; e < 16; ++e) { oa[e] *= ia; ob[e] *= ib; bad |= !(fabsf(oa[e]) < INFINITY) || !(fabsf(ob[e]) < INFINITY); }
    if (attempt == 0 && __any(bad)) *flag = 1;
    lds_bar();
    if (attempt == 1 || *flag == 0) {
      char* dst = o + ((size_t)tt * 64 + cb * 4) * 1024 + lane * 16;
      *(bf16x8*)dst = pack8<0>(oa); *(bf16x8*)(dst + 1024) = pack8<1>(oa);
      *(bf16x8*)(dst + 2048) = pack8<0>(ob); *(bf16x8*)(dst + 3072) = pack8<1>(ob);
      break;
    }
  }
}

extern "C" void kernel_launch(void* const* d_in, const int* in_sizes, int n_in, void* d_out, int out_size, void* d_ws, size_t ws_size, hipStream_t stream) {
  const char* x = (const char*)d_in[0];
  const char* a_wqkv = (const char*)d_in[1];
  const char* a_wphi = (const char*)d_in[2];
  const char* a_wo = (const char*)d_in[3];
  const char* b_wq = (const char*)d_in[5];
  const char* b_wk = (const char*)d_in[6];
  const char* b_wv = (const char*)d_in[7];
  const char* b_wo = (const char*)d_in[8];
  char* ws = (char*)d_ws;
  f32x2* st = (f32x2*)(ws + WS_ST);
  const float* cs = (const float*)(ws + WS_CS);
  k_prep<<<256 + 65 * 4, 256, 0, stream>>>(x, a_wqkv, b_wq, b_wk, b_wv, ws);
  const size_t xo[5] = {WS_X0, WS_X1, WS_X2, WS_X3, 0};
  for (int l = 0; l < 2; ++l) {
    char* xa = ws + xo[2 * l]; char* xb = ws + xo[2 * l + 1]; char* xc = ws + xo[2 * l + 2];
    k_qkvA<<<256, 256, 0, stream>>>(xa, a_wqkv, a_wphi, st, cs, ws + WS_PHIQ, ws + WS_KVP, ws + WS_ZP);
    k_oA<<<256, 256, 0, stream>>>(ws + WS_PHIQ, ws + WS_KVP, ws + WS_ZP, ws + WS_O);
    k_proj<false><<<256, 256, 0, stream>>>(ws + WS_O, a_wo, xa, xb, st);
    k_qkvB<<<256, 256, 0, stream>>>(xb, b_wq, b_wk, b_wv, st, cs, ws + WS_Q, ws + WS_KVF);
    k_attn<<<256, 256, 0, stream>>>(ws + WS_Q, ws + WS_KVF, ws + WS_O);
    if (l == 0) k_proj<false><<<256, 256, 0, stream>>>(ws + WS_O, b_wo, xb, xc, st);
    else k_proj<true><<<256, 256, 0, stream>>>(ws + WS_O, b_wo, xb, (char*)d_out, st);
  }
}
```

```cpp
#include <hip/hip_runtime.h>
#include <hip/hip_bf16.h>
#include <stdint.h>
#include <type_traits>

typedef __attribute__((ext_vector_type(8))) short bf16x8;
typedef __attribute__((ext_vector_type(4))) short s16x4;
typedef __attribute__((ext_vector_type(2))) __bf16 bf2_t;
typedef __attribute__((ext_vector_type(16))) float f32x16;
typedef __attribute__((ext_vector_type(4))) float f32x4;
typedef __attribute__((ext_vector_type(2))) float f32x2;
typedef __attribute__((ext_vector_type(4))) unsigned u32x4;
typedef __attribute__((ext_vector_type(2))) unsigned u32x2;
#define LAS __attribute__((address_space(3)))
#define DI __device__ __forceinline__
#define MFMA32(a, b, c) __builtin_amdgcn_mfma_f32_32x32x16_bf16((a), (b), (c), 0, 0, 0)
#define MFMA16(a, b, c) __builtin_amdgcn_mfma_f32_16x16x32_bf16((a), (b), (c), 0, 0, 0)
#define SBAR() __builtin_amdgcn_sched_barrier(0)

constexpr int S_ = 2048, D_ = 1024;
constexpr float LOG2E = 1.4426950408889634f;
constexpr float QSCALE = 0.17677669529663687f * LOG2E;

constexpr size_t MB = 1u << 20;
constexpr size_t WS_ACT = 0;
constexpr size_t WS_KVP = 32 * MB;
constexpr size_t WS_ZP = 36 * MB;
constexpr size_t WS_KVF = 37 * MB;
constexpr size_t WS_ST = 38 * MB;
constexpr size_t WS_CS = 39 * MB;
constexpr size_t WS_FLAG = 40 * MB;
constexpr int FL_ACT = 0, FL_KVP = 8 * 64 * 32, FL_KV = FL_KVP + 2 * 64 * 32, FL_WORDS = FL_KV + 2 * 256;
constexpr int CS_N = 3072 + 1024 + 64;

template <int I, int N, class F> DI void sfor(F&& f) { if constexpr (I < N) { f(std::integral_constant<int, I>{}); sfor<I + 1, N>(f); } }

DI unsigned cvtpk(float lo, float hi) { f32x2 v = {lo, hi}; return __builtin_bit_cast(unsigned, __builtin_convertvector(v, bf2_t)); }
DI float bflo(unsigned u) { return __uint_as_float(u << 16); }
DI float bfhi(unsigned u) { return __uint_as_float(u & 0xffff0000u); }
DI float rbf(float x) { return bflo(cvtpk(x, x)); }
DI float swap_add(float v) { auto r = __builtin_amdgcn_permlane32_swap(__float_as_uint(v), __float_as_uint(v), false, false); return __uint_as_float(r[0]) + __uint_as_float(r[1]); }
DI float swap_max(float v) { auto r = __builtin_amdgcn_permlane32_swap(__float_as_uint(v), __float_as_uint(v), false, false); return fmaxf(__uint_as_float(r[0]), __uint_as_float(r[1])); }
DI float swap_min(float v) { auto r = __builtin_amdgcn_permlane32_swap(__float_as_uint(v), __float_as_uint(v), false, false); return fminf(__uint_as_float(r[0]), __uint_as_float(r[1])); }
template <int S> DI bf16x8 pack8(const f32x16& v) {
  u32x4 p = {cvtpk(v[8 * S], v[8 * S + 1]), cvtpk(v[8 * S + 2], v[8 * S + 3]), cvtpk(v[8 * S + 4], v[8 * S + 5]), cvtpk(v[8 * S + 6], v[8 * S + 7])};
  return __builtin_bit_cast(bf16x8, p);
}
DI u32x4 unperm(u32x4 v) {
  auto r0 = __builtin_amdgcn_permlane32_swap(v[0], v[2], false, false);
  auto r1 = __builtin_amdgcn_permlane32_swap(v[1], v[3], false, false);
  return u32x4{r0[0], r1[0], r0[1], r1[1]};
}
DI s16x4 trread(LAS const char* p) { return __builtin_amdgcn_ds_read_tr16_b64_v4i16((LAS s16x4*)p); }
DI bf16x8 cat8(s16x4 a, s16x4 b) { return __builtin_shufflevector(a, b, 0, 1, 2, 3, 4, 5, 6, 7); }
DI float dot2(unsigned a, unsigned b, float c) { return __builtin_amdgcn_fdot2_f32_bf16(__builtin_bit_cast(bf2_t, a), __builtin_bit_cast(bf2_t, b), c, false); }

template <bool AG, int OFF> DI void ldx(bf16x8& d, unsigned voff, const char* sb) {
  if constexpr (AG) asm volatile("global_load_dwordx4 %0, %1, %2 offset:%3" : "=a"(d) : "v"(voff), "s"(sb), "i"(OFF));
  else asm volatile("global_load_dwordx4 %0, %1, %2 offset:%3" : "=v"(d) : "v"(voff), "s"(sb), "i"(OFF));
}
template <int NA, int C> DI void issue_x(bf16x8 (&x)[64], unsigned voff, const char* xb) {
  const char* sb = xb + C * 8192 + 4096;
  sfor<0, 8>([&](auto i) { ldx<(C * 8 < NA), i * 1024 - 4096>(x[C * 8 + i], voff, sb); });
}
DI void dma4(unsigned lds, unsigned v0, unsigned v1, unsigned v2, unsigned v3, const char* sb) {
  unsigned keep;
  asm volatile("s_mov_b32 %0, m0\n\ts_mov_b32 m0, %1\n\ts_nop 0\n\t"
               "global_load_lds_dwordx4 %2, %6 offset:0\n\tglobal_load_lds_dwordx4 %3, %6 offset:1024\n\t"
               "global_load_lds_dwordx4 %4, %6 offset:2048\n\tglobal_load_lds_dwordx4 %5, %6 offset:3072\n\ts_mov_b32 m0, %0"
               : "=&s"(keep) : "s"(lds), "v"(v0), "v"(v1), "v"(v2), "v"(v3), "s"(sb) : "memory");
}
DI void dma2(unsigned lds, unsigned v, const char* sb) {
  unsigned keep;
  asm volatile("s_mov_b32 %0, m0\n\ts_mov_b32 m0, %1\n\ts_nop 0\n\t"
               "global_load_lds_dwordx4 %2, %3 offset:0\n\tglobal_load_lds_dwordx4 %2, %3 offset:1024\n\ts_mov_b32 m0, %0"
               : "=&s"(keep) : "s"(lds), "v"(v), "s"(sb) : "memory");
}
DI void dma1(unsigned lds, unsigned v, const char* sb) {
  unsigned keep;
  asm volatile("s_mov_b32 %0, m0\n\ts_mov_b32 m0, %1\n\ts_nop 0\n\tglobal_load_lds_dwordx4 %2, %3 offset:0\n\ts_mov_b32 m0, %0"
               : "=&s"(keep) : "s"(lds), "v"(v), "s"(sb) : "memory");
}
template <int N> DI void wait_bar() { asm volatile("s_waitcnt vmcnt(%0)\n\ts_barrier" ::"i"(N > 63 ? 63 : N) : "memory"); }
template <int NA, int C, int N> DI void wait_bar_x(bf16x8 (&x)[64]) {
  constexpr int n = N > 63 ? 63 : N;
  if constexpr (C * 8 < NA)
    asm volatile("s_waitcnt vmcnt(%8)\n\ts_barrier"
                 : "+a"(x[C * 8]), "+a"(x[C * 8 + 1]), "+a"(x[C * 8 + 2]), "+a"(x[C * 8 + 3]), "+a"(x[C * 8 + 4]), "+a"(x[C * 8 + 5]), "+a"(x[C * 8 + 6]), "+a"(x[C * 8 + 7])
                 : "i"(n) : "memory");
  else
    asm volatile("s_waitcnt vmcnt(%8)\n\ts_barrier"
                 : "+v"(x[C * 8]), "+v"(x[C * 8 + 1]), "+v"(x[C * 8 + 2]), "+v"(x[C * 8 + 3]), "+v"(x[C * 8 + 4]), "+v"(x[C * 8 + 5]), "+v"(x[C * 8 + 6]), "+v"(x[C * 8 + 7])
                 : "i"(n) : "memory");
}
DI void lds_bar() { asm volatile("s_waitcnt lgkmcnt(0)\n\ts_barrier" ::: "memory"); }

template <bool AG, int OFF> DI void ldx1(bf16x8& d, unsigned voff, const char* sb) {
  if constexpr (AG) asm volatile("global_load_dwordx4 %0, %1, %2 offset:%3 sc1" : "=a"(d) : "v"(voff), "s"(sb), "i"(OFF));
  else asm volatile("global_load_dwordx4 %0, %1, %2 offset:%3 sc1" : "=v"(d) : "v"(voff), "s"(sb), "i"(OFF));
}
template <int OFF> DI void ld2_1(f32x2& d, unsigned voff, const char* sb) { asm volatile("global_load_dwordx2 %0, %1, %2 offset:%3 sc1" : "=v"(d) : "v"(voff), "s"(sb), "i"(OFF)); }
template <int OFF> DI void st4_1(u32x4 d, unsigned voff, char* sb) { asm volatile("global_store_dwordx4 %0, %1, %2 offset:%3 sc1\n\ts_nop 1" ::"v"(voff), "v"(d), "s"(sb), "i"(OFF) : "memory"); }
DI void st2_1(f32x2 d, unsigned voff, char* sb) { asm volatile("global_store_dwordx2 %0, %1, %2 sc1" ::"v"(voff), "v"(d), "s"(sb) : "memory"); }
template <int N> DI void waitv() { asm volatile("s_waitcnt vmcnt(%0)" ::"i"(N > 63 ? 63 : N) : "memory"); }
template <bool AG, int N> DI void waitx(bf16x8& d) {
  if constexpr (AG) asm volatile("s_waitcnt vmcnt(%1)" : "+a"(d) : "i"(N > 63 ? 63 : N)); else asm volatile("s_waitcnt vmcnt(%1)" : "+v"(d) : "i"(N > 63 ? 63 : N));
}
template <bool SW, bool XA, bool Z> DI void mm(f32x16& acc, const bf16x8& f, const bf16x8& x) {
  if constexpr (Z) {
    if constexpr (SW) { if constexpr (XA) asm volatile("v_mfma_f32_32x32x16_bf16 %0, %2, %1, 0" : "=&a"(acc) : "v"(f), "a"(x)); else asm volatile("v_mfma_f32_32x32x16_bf16 %0, %2, %1, 0" : "=&a"(acc) : "v"(f), "v"(x)); }
    else { if constexpr (XA) asm volatile("v_mfma_f32_32x32x16_bf16 %0, %1, %2, 0" : "=&a"(acc) : "v"(f), "a"(x)); else asm volatile("v_mfma_f32_32x32x16_bf16 %0, %1, %2, 0" : "=&a"(acc) : "v"(f), "v"(x)); }
  } else {
    if constexpr (SW) { if constexpr (XA) asm volatile("v_mfma_f32_32x32x16_bf16 %0, %2, %1, %0" : "+a"(acc) : "v"(f), "a"(x)); else asm volatile("v_mfma_f32_32x32x16_bf16 %0, %2, %1, %0" : "+a"(acc) : "v"(f), "v"(x)); }
    else { if constexpr (XA) asm volatile("v_mfma_f32_32x32x16_bf16 %0, %1, %2, %0" : "+a"(acc) : "v"(f), "a"(x)); else asm volatile("v_mfma_f32_32x32x16_bf16 %0, %1, %2, %0" : "+a"(acc) : "v"(f), "v"(x)); }
  }
}
DI void mm_done(f32x16& a, f32x16& b) { asm volatile("s_nop 15" : "+a"(a), "+a"(b)); }
template <class T> DI T opq_s(T v) { asm volatile("" : "+s"(v)); return v; }
template <class T> DI T opq_v(T v) { asm volatile("" : "+v"(v)); return v; }
DI void bar() { asm volatile("s_barrier" ::: "memory"); }
DI void set_flag(unsigned* flag) {
  unsigned z = 0, one = 1;
  asm volatile("s_mov_b64 exec, 1\n\tglobal_store_dword %0, %1, %2 sc1\n\ts_mov_b64 exec, -1" ::"v"(z), "v"(one), "s"(flag) : "memory");
}
DI void poll(const unsigned* base, unsigned voff) {
  for (int spin = 0; spin < (1 << 20); ++spin) {
    unsigned v;
    asm volatile("global_load_dword %0, %1, %2 sc1\n\ts_waitcnt vmcnt(0)" : "=v"(v) : "v"(voff), "s"(base) : "memory");
    if (__all(v == 1u)) break;
  }
}

template <int RBX> DI void block_map(int& rb, int& cb) {
  const int x = blockIdx.x & 7, i = blockIdx.x >> 3;
  constexpr int CBX = 32 / RBX, GC = 16 / CBX;
  rb = RBX * (x / GC) + i / CBX; cb = CBX * (x % GC) + i % CBX;
}

DI void ln_stats(const f32x2 (&p)[8], float& mean, float& rstd) {
  float s = 0.f, q = 0.f;
#pragma unroll
  for (int i = 0; i < 8; ++i) { s += p[i][0]; q += p[i][1]; }
  s = swap_add(s); q = swap_add(q);
  const float m = s * (1.f / D_);
  const float var = fmaxf(q * (1.f / D_) - m * m, 0.f);
  mean = rbf(m);
  rstd = 1.f / rbf(sqrtf(rbf(rbf(var) + 1e-5f)));
}
DI bf16x8 cs_frag(float c, bool on) {
  const float hi = rbf(c), mid = rbf(c - hi), lo = (c - hi) - mid;
  u32x4 v = {cvtpk(hi, mid), cvtpk(lo, 0.f), 0u, 0u};
  if (!on) { v[0] = 0u; v[1] = 0u; }
  return __builtin_bit_cast(bf16x8, v);
}
DI bf16x8 mean_frag(float mean) { u32x4 v = {cvtpk(-mean, -mean), cvtpk(-mean, 0.f), 0u, 0u}; return __builtin_bit_cast(bf16x8, v); }

__global__ __launch_bounds__(256) void k_prep(const char* x, const char* wqkv, const char* wq, const char* wk, const char* wv, char* ws) {
  const int tid = threadIdx.x, lane = tid & 63, w = tid >> 6, r = lane & 31, h = lane >> 5;
  __shared__ float red[32][64];
  if (blockIdx.x < 256) {
    const int tt = blockIdx.x >> 2, cb = (blockIdx.x & 3) * 4 + w;
    const char* src = x + (size_t)(tt * 32 + r) * 2048 + cb * 128 + h * 16;
    u32x4 v[4];
#pragma unroll
    for (int i = 0; i < 4; ++i) v[i] = *(const u32x4*)(src + i * 32);
    { const int f = blockIdx.x * 256 + tid; if (f < FL_WORDS) ((unsigned*)(ws + WS_FLAG))[f] = 0u; }
    float s = 0.f, q = 0.f;
#pragma unroll
    for (int i = 0; i < 4; ++i) {
#pragma unroll
      for (int e = 0; e < 4; ++e) { s = dot2(v[i][e], 0x3f803f80u, s); q = dot2(v[i][e], v[i][e], q); }
      *(u32x4*)(ws + WS_ACT + ((size_t)(tt * 64 + cb * 4 + i) * 64 + lane) * 16) = unperm(v[i]);
    }
    s = swap_add(s); q = swap_add(q);
    if (h == 0) ((f32x2*)(ws + WS_ST))[(tt * 16 + cb) * 32 + r] = f32x2{s, q};
  } else {
    const int u = blockIdx.x - 256, qr = u & 3, g = u >> 2;
    const char* src; int pitch;
    const int c8 = tid & 7, rr = tid >> 3;
    if (g < 48) { src = wqkv + g * 128 + c8 * 16; pitch = 6144; }
    else if (g < 64) { src = wq + (g - 48) * 128 + c8 * 16; pitch = 2048; }
    else { src = (c8 < 4 ? wk : wv) + (c8 & 3) * 16; pitch = 64; }
    src += (size_t)(qr * 256 + rr) * pitch;
    u32x4 v[8];
#pragma unroll
    for (int i = 0; i < 8; ++i) v[i] = *(const u32x4*)(src + (size_t)i * 32 * pitch);
    float a[8] = {};
#pragma unroll
    for (int i = 0; i < 8; ++i)
#pragma unroll
      for (int e = 0; e < 4; ++e) { a[2 * e] += bflo(v[i][e]); a[2 * e + 1] += bfhi(v[i][e]); }
#pragma unroll
    for (int e = 0; e < 8; ++e) red[rr][c8 * 8 + e] = a[e];
    __syncthreads();
    if (tid < 64) {
      float t = 0.f;
#pragma unroll
      for (int i = 0; i < 32; ++i) t += red[i][tid];
      ((float*)(ws + WS_CS))[qr * CS_N + g * 64 + tid] = t;
    }
  }
}

constexpr int SLOT = 16384, XL = 12;
struct WSrc { unsigned v0, v1, v2, v3; };
DI WSrc wsrc(int lane, int w, int pitch) {
  const int row = lane >> 3, ch = (lane & 7) ^ (((row >> 1) & 1) << 2);
  WSrc g; g.v0 = (unsigned)((32 * w + row) * pitch + ch * 16);
  g.v1 = g.v0 + 8 * pitch - 1024; g.v2 = g.v0 + 16 * pitch - 2048; g.v3 = g.v0 + 24 * pitch - 3072;
  return g;
}
struct WinPtr {
  unsigned b[3];
  DI void init(unsigned a) { sfor<0, 3>([&](auto i) { b[i] = a + i * 65536u; asm volatile("" : "+v"(b[i])); }); }
  template <int OFF> DI bf16x8 frag() const {
    LAS const char* p = (LAS const char*)(uintptr_t)b[OFF >> 16] + (OFF & 65535);
    return cat8(trread(p), trread(p + 1024));
  }
};
struct Ctx {
  int lane, w, r, h, rb, cb, tt;
  unsigned lds0, xv;
  WinPtr p0, p1;
  LAS char* smem;
  char* ws;
};
#define IC(n) std::integral_constant<int, (n)>{}
constexpr int slot_rot(int c) { return (c + 6) % 9; }
constexpr int slot_id(int c) { return c; }

template <bool ROT, int KV, bool NST, bool ZERO, int EXTRA, class FX>
DI void gemm64(const Ctx& c, const char* xb, const char* stp, f32x16& acc0, f32x16& acc1, f32x16& acc2, float& mean, float& rstd, FX&& extra) {
  bf16x8 x[64], f0[3], f1[3], f2[3];
  f32x2 sp[8];
  if constexpr (NST) sfor<0, 8>([&](auto i) { ld2_1<i * 256>(sp[i], (unsigned)(c.h * 2048 + c.r * 8), stp); });
  auto issue_x = [&](auto s) { ldx1<false, (s % 8) * 1024 - 4096>(x[s], c.xv, xb + (s / 8) * 8192 + 4096); };
  sfor<0, XL>(issue_x);
  bar();
  unsigned a2 = c.lds0 + 5 * SLOT + (4 * c.h + ((c.lane & 15) >> 2)) * 32 + (c.lane & 3) * 8, a3 = a2 + 4 * SLOT;
  if constexpr (KV) asm volatile("" : "+v"(a2), "+v"(a3));
  auto off = [](int s) constexpr { return (ROT ? slot_rot(s / 8) : s / 8) * SLOT + (s % 8) * 2048; };
  auto rd0 = [&](auto s) { f0[s % 3] = c.p0.template frag<off(s)>(); };
  auto rd1 = [&](auto s) { f1[s % 3] = c.p1.template frag<off(s)>(); };
  auto rd2 = [&](auto s) { LAS const char* p = (LAS const char*)(uintptr_t)(s < 32 ? a2 : a3) + (s % 32) * 512; f2[s % 3] = cat8(trread(p), trread(p + 256)); };
  rd0(IC(0)); rd1(IC(0)); if constexpr (KV) rd2(IC(0));
  rd0(IC(1)); rd1(IC(1)); if constexpr (KV) rd2(IC(1));
  sfor<0, 64>([&](auto s) {
    waitx<false, (s + XL <= 64 ? XL - 1 : 63 - s + EXTRA)>(x[s]);
    SBAR();
    mm<false, false, ZERO && s == 0>(acc0, f0[s % 3], x[s]);
    SBAR();
    if constexpr (s + 2 < 64) rd0(IC(s + 2));
    if constexpr (s + XL < 64) issue_x(IC(s + XL));
    SBAR();
    mm<false, false, ZERO && s == 0>(acc1, f1[s % 3], x[s]);
    SBAR();
    if constexpr (s + 2 < 64) rd1(IC(s + 2));
    if constexpr (KV) {
      SBAR();
      mm<KV == 2, false, s == 0>(acc2, f2[s % 3], x[s]);
      SBAR();
      if constexpr (s + 2 < 64) rd2(IC(s + 2));
    }
    if constexpr (s == 64 - XL) extra();
    if constexpr (NST && s == 8) {
      asm volatile("" : "+v"(sp[0]), "+v"(sp[1]), "+v"(sp[2]), "+v"(sp[3]), "+v"(sp[4]), "+v"(sp[5]), "+v"(sp[6]), "+v"(sp[7]));
      ln_stats(sp, mean, rstd);
    }
  });
  mm_done(acc0, acc1);
  if constexpr (KV) asm volatile("" : "+a"(acc2));
  bar();
}

DI void res_to_acc(const u32x4 (&xr)[4], f32x16& a0, f32x16& a1) {
#pragma unroll
  for (int e = 0; e < 4; ++e) {
    a0[2 * e] = bflo(xr[0][e]); a0[2 * e + 1] = bfhi(xr[0][e]); a0[8 + 2 * e] = bflo(xr[1][e]); a0[9 + 2 * e] = bfhi(xr[1][e]);
    a1[2 * e] = bflo(xr[2][e]); a1[2 * e + 1] = bfhi(xr[2][e]); a1[8 + 2 * e] = bflo(xr[3][e]); a1[9 + 2 * e] = bfhi(xr[3][e]);
  }
}
DI void store_x(const Ctx& c, const f32x16& a0, const f32x16& a1, u32x4 (&xr)[4], char* dst, char* stp) {
  xr[0] = __builtin_bit_cast(u32x4, pack8<0>(a0)); st4_1<0>(xr[0], c.xv, dst);
  xr[1] = __builtin_bit_cast(u32x4, pack8<1>(a0)); st4_1<1024>(xr[1], c.xv, dst);
  xr[2] = __builtin_bit_cast(u32x4, pack8<0>(a1)); st4_1<2048>(xr[2], c.xv, dst);
  xr[3] = __builtin_bit_cast(u32x4, pack8<1>(a1)); st4_1<3072>(xr[3], c.xv, dst);
  float s1 = 0.f, s2 = 0.f;
#pragma unroll
  for (int i = 0; i < 4; ++i)
#pragma unroll
    for (int e = 0; e < 4; ++e) { s1 = dot2(xr[i][e], 0x3f803f80u, s1); s2 = dot2(xr[i][e], xr[i][e], s2); }
  s1 = swap_add(s1); s2 = swap_add(s2);
  st2_1(f32x2{s1, s2}, (unsigned)(c.r * 8), stp);
}

DI void feature_map(const f32x16& t, float rstd, LAS const char* wp, f32x16& ep, f32x16& en) {
  f32x16 v;
#pragma unroll
  for (int i = 0; i < 16; ++i) v[i] = t[i] * rstd;
  f32x16 pz = {};
  pz = MFMA32(cat8(trread(wp), trread(wp + 512)), pack8<0>(v), pz);
  pz = MFMA32(cat8(trread(wp + 1024), trread(wp + 1536)), pack8<1>(v), pz);
  float mx = pz[0], mn = pz[0];
#pragma unroll
  for (int i = 1; i < 16; ++i) { mx = fmaxf(mx, pz[i]); mn = fminf(mn, pz[i]); }
  mx = swap_max(mx) * LOG2E; mn = swap_min(mn) * LOG2E;
  float sp = 0.f, sn = 0.f;
#pragma unroll
  for (int i = 0; i < 16; ++i) {
    ep[i] = __builtin_amdgcn_exp2f(__builtin_fmaf(pz[i], LOG2E, -mx)); sp += ep[i];
    en[i] = __builtin_amdgcn_exp2f(__builtin_fmaf(pz[i], -LOG2E, mn)); sn += en[i];
  }
  sp = __builtin_amdgcn_rcpf(swap_add(sp)); sn = __builtin_amdgcn_rcpf(swap_add(sn));
#pragma unroll
  for (int i = 0; i < 16; ++i) { ep[i] *= sp; en[i] *= sn; }
}

constexpr int a_slot(int j) { return j < 9 ? j : (j - 9) % 6; }
struct SchedA {
  int xw[64], ww[24];
  constexpr SchedA() : xw{}, ww{} {
    int pos = 8, xpos[64] = {}, wpos[28] = {}, stpos = 0;
    for (int s = 0; s < XL; ++s) xpos[s] = ++pos;
    for (int s = 0; s < 192; ++s) {
      if (s == 128) { pos += 3; stpos = pos; }
      if ((s + 2) % 8 == 0 && (s + 2) / 8 >= 5 && (s + 2) / 8 < 24) {
        const int g = (s + 2) / 8;
        ww[g] = g >= 9 ? pos - wpos[g] : 63;
        if (g == 18) { if (pos - stpos < ww[g]) ww[g] = pos - stpos; pos += 1; }
        if (g <= 22) { pos += 4; wpos[g + 4] = pos; }
      }
      if (s < 64) { xw[s] = pos - xpos[s]; if (s + XL < 64) xpos[s + XL] = ++pos; }
    }
  }
};

#define R_OA "v[64:79]"
#define R_OB "v[80:95]"
#define S0_(n, q, m) "v_mfma_f32_32x32x16_bf16 v[" #n ":" #n "+15], v[208:211], v[" #q ":" #q "+3], v[" #m ":" #m "+15]\n\t"
#define S1_(n, q) "v_mfma_f32_32x32x16_bf16 v[" #n ":" #n "+15], v[212:215], v[" #q "+4:" #q "+7], v[" #n ":" #n "+15]\n\t"
#define PV0_(o, p) "v_smfmac_f32_32x32x32_bf16 v[" #o ":" #o "+15], v[216:219], v[" #p ":" #p "+7], v236\n\t"
#define PV1_(o, p) "v_smfmac_f32_32x32x32_bf16 v[" #o ":" #o "+15], v[220:223], v[" #p "+8:" #p "+15], v236\n\t"
#define L0_(l, p) "v_smfmac_f32_16x16x64_bf16 v[" #l ":" #l "+3], v[224:227], v[" #p ":" #p "+7], v236\n\t"
#define L1_(l, p) "v_smfmac_f32_16x16x64_bf16 v[" #l ":" #l "+3], v[224:227], v[" #p "+8:" #p "+15], v236\n\t"
#define E_(e, i) "v_exp_f32 v[" #e "+" #i "], v[" #e "+" #i "]\n\t"
#define RD_(d, off) "ds_read_b128 v[" #d ":" #d "+3], v237 offset:" #off "\n\t"
#define HALF_(e, n, q, m, p, o, l, X0, X1, X2, X3, X4, Y2) \
  S0_(n, q, m) X0 E_(e, 0) E_(e, 1) E_(e, 2) S1_(n, q) X1 E_(e, 3) E_(e, 4) E_(e, 5) Y2 PV0_(o, p) X2 E_(e, 6) E_(e, 7) E_(e, 8) \
  PV1_(o, p) X3 E_(e, 9) E_(e, 10) E_(e, 11) L0_(l, p) X4 E_(e, 12) E_(e, 13) L1_(l, p) E_(e, 14) E_(e, 15)
#define LG2_ "s_waitcnt lgkmcnt(2)\n\t"
#define LG0_ "s_waitcnt lgkmcnt(0)\n\t"
#define STEP_(b, nb, kv, wv, wo) \
  "s_waitcnt vmcnt(4)\n\ts_barrier\n\t" \
  HALF_(128, 160, 200, 112, 176, 80, 232, RD_(208, b + 2048), RD_(212, b + 3072), RD_(216, b), RD_(220, b + 1024), "", "") LG2_ \
  HALF_(160, 144, 192, 96, 128, 64, 228, "s_add_u32 m0, s44, " #nb "\n\t", "global_load_lds_dwordx4 v" #kv ", s[40:41] sc1\n\t", "", \
        "global_load_lds_dwordx4 v" #kv ", s[40:41] offset:1024 sc1\n\t", "", LG0_) \
  HALF_(144, 176, 200, 112, 160, 80, 232, RD_(208, b + 6144), RD_(212, b + 7168), RD_(216, b + 4096), RD_(220, b + 5120), "", "") LG2_ \
  HALF_(176, 128, 192, 96, 144, 64, 228, "s_mov_b32 m0, s45\n\t", "global_load_lds_dwordx4 v" #wv ", s[42:43] offset:" #wo "\n\t", "", "", "", LG0_)
DI float max16(const f32x16& s) {
  float mx = s[0];
#pragma unroll
  for (int e = 1; e < 16; ++e) mx = fmaxf(mx, s[e]);
  return mx;
}
typedef __attribute__((ext_vector_type(8))) unsigned u32x8;
typedef __attribute__((ext_vector_type(16))) unsigned u32x16;

#define STAMP(i)

__global__ __launch_bounds__(256, 1) void k_main(const char* wqkv, const char* wphi, const char* woA, const char* wq, const char* wk, const char* wv, const char* woB,
                                                 char* ws, char* out) {
  __shared__ __attribute__((aligned(16))) char smem[163840];
  Ctx c;
  c.lane = threadIdx.x & 63; c.w = __builtin_amdgcn_readfirstlane(threadIdx.x >> 6); c.r = c.lane & 31; c.h = c.lane >> 5;
  block_map<8>(c.rb, c.cb);
  c.tt = c.rb * 4 + c.w; c.smem = (LAS char*)smem; c.lds0 = (unsigned)(uintptr_t)c.smem; c.xv = c.lane * 16; c.ws = ws;
  int lane = c.lane, w = c.w, r = c.r, h = c.h, rb = c.rb, cb = c.cb, tt = c.tt;
  int blk = (lane >> 4) & 1, q4 = (lane & 15) >> 2, p4 = lane & 3;
  { const unsigned a0 = (unsigned)((4 * h + q4) * 128 + (((q4 >> 1) << 2) + 2 * blk + (p4 >> 1)) * 16 + 8 * (p4 & 1));
    c.p0.init(c.lds0 + a0); c.p1.init(c.lds0 + (a0 ^ 64u)); }
  const WSrc g6 = wsrc(lane, w, 6144), g2 = wsrc(lane, w, 2048);
  unsigned* flags = (unsigned*)(ws + WS_FLAG);
  unsigned f16 = (lane & 15) * 4;
  int kvu = cb & 3 ? -1 : cb >> 2, hf = (cb >> 2) & 1;
  auto issue_a = [&](auto j) {
    constexpr int p = j / 8, col = p == 0 ? 2048 : (p == 1 ? 4096 : 0);
    dma4(c.lds0 + a_slot(j) * SLOT + w * 4096, g6.v0, g6.v1, g6.v2, g6.v3, wqkv + cb * 128 + col + (size_t)(j % 8) * 128 * 6144);
  };
  auto issue_w = [&](const char* W, int slot, int ch) { dma4(c.lds0 + slot * SLOT + w * 4096, g2.v0, g2.v1, g2.v2, g2.v3, W + cb * 128 + (size_t)ch * 128 * 2048); };
  auto issue_phi = [&]() { dma1(c.lds0 + 9 * SLOT + w * 1024, c.xv, wphi + cb * 4096 + w * 1024); };
  sfor<0, 9>(issue_a); issue_phi();
  float csk, csv, csq, csb, cse;
  { const float* cs = (const float*)(ws + WS_CS);
    auto ld = [&](int n) { return (cs[n] + cs[CS_N + n]) + (cs[2 * CS_N + n] + cs[3 * CS_N + n]); };
    csq = ld(cb * 64 + lane); csk = ld(1024 + cb * 64 + lane); csv = ld(2048 + cb * 64 + lane); csb = ld(3072 + cb * 64 + lane);
    cse = ld(4096 + (kvu >= 2 ? 32 : 0) + hf * 16 + (lane & 15)); }
  u32x4 xr[4];
#pragma unroll
  for (int i = 0; i < 4; ++i) xr[i] = *(const u32x4*)(ws + WS_ACT + (size_t)tt * 65536 + cb * 4096 + i * 1024 + lane * 16);
  asm volatile("" : "+v"(csk), "+v"(csv), "+v"(csq), "+v"(csb), "+v"(cse), "+v"(xr[0]), "+v"(xr[1]), "+v"(xr[2]), "+v"(xr[3]));

  char *act, *stt; unsigned* fact;
#define REFRESH() do { \
    asm volatile("" : "+s"(w), "+s"(cb), "+s"(rb), "+s"(ws), "+s"(wqkv), "+s"(wphi), "+s"(woA), "+s"(wq), "+s"(woB)); \
    asm volatile("" : "+v"(lane), "+v"(csk), "+v"(csv), "+v"(csq), "+v"(csb), "+v"(cse)); \
    r = lane & 31; h = lane >> 5; blk = (lane >> 4) & 1; q4 = (lane & 15) >> 2; p4 = lane & 3; tt = rb * 4 + w; f16 = (lane & 15) * 4; \
    kvu = cb & 3 ? -1 : cb >> 2; hf = (cb >> 2) & 1; flags = (unsigned*)(ws + WS_FLAG); \
    c.lane = lane; c.w = w; c.r = r; c.h = h; c.xv = lane * 16; \
    act = ws + WS_ACT + (size_t)l * 16 * MB + (size_t)tt * 65536;     \
    fact = flags + FL_ACT + (l * 4 * 64 + tt) * 32;                   \
    stt = ws + WS_ST + (size_t)l * 512 * 1024 + tt * 4096;            \
  } while (0)
  for (int l = 0; l < 2; ++l) {
    REFRESH();
    float mean, rstd;
    STAMP(0);
    {
      constexpr int NA = 40;
      constexpr SchedA SC;
      if (l > 0) poll(fact, f16);
      STAMP(1);
      bf16x8 x[64], f0[3], f1[3];
      f32x2 sp[8];
      sfor<0, 8>([&](auto i) { ld2_1<i * 256>(sp[i], (unsigned)(h * 2048 + r * 8), stt); });
      auto issue_x = [&](auto s) { ldx1<(s < NA), (s % 8) * 1024 - 4096>(x[s], c.xv, act + (s / 8) * 8192 + 4096); };
      sfor<0, XL>(issue_x);
      wait_bar<XL + 8>();
      f32x16 acc[6];
      char* kvp = ws + WS_KVP + (size_t)l * 2 * MB + (size_t)(cb * 4 + w) * 32768;
      char* zp = ws + WS_ZP + (size_t)l * 131072 + (cb * 4 + w) * 2048;
      unsigned* fkvp = flags + FL_KVP + (l * 64 + cb * 4 + w) * 32;
      auto off = [](int s) constexpr { return a_slot(s / 8) * SLOT + (s % 8) * 2048; };
      auto rd0 = [&](auto s) { f0[s % 3] = c.p0.template frag<off(s)>(); };
      auto rd1 = [&](auto s) { f1[s % 3] = c.p1.template frag<off(s)>(); };
      rd0(IC(0)); rd1(IC(0)); rd0(IC(1)); rd1(IC(1));
      auto step = [&](auto s) {
        if constexpr ((s + 2) % 8 == 0 && (s + 2) / 8 >= 5 && (s + 2) / 8 < 24) {
          constexpr int g = (s + 2) / 8;
          wait_bar<SC.ww[g]>();
          if constexpr (g == 18) set_flag(fkvp + rb);
          if constexpr (g <= 19) issue_a(IC(g + 4));
          else if constexpr (g <= 22) issue_w(woA, slot_rot(g - 20), g - 20);
        }
        if constexpr (s < 64) waitx<(s < NA), SC.xw[s < 64 ? s : 0]>(x[s % 64]);
        constexpr int p = s / 64;
        SBAR();
        mm<p == 1, (s % 64 < NA), s % 64 == 0>(acc[2 * p], f0[s % 3], x[s % 64]);
        SBAR();
        if constexpr (s + 2 < 192) rd0(IC(s + 2));
        if constexpr (s + XL < 64) issue_x(IC(s + XL));
        SBAR();
        mm<p == 1, (s % 64 < NA), s % 64 == 0>(acc[2 * p + 1], f1[s % 3], x[s % 64]);
        SBAR();
        if constexpr (s + 2 < 192) rd1(IC(s + 2));
        if constexpr (s == 8) {
          asm volatile("" : "+v"(sp[0]), "+v"(sp[1]), "+v"(sp[2]), "+v"(sp[3]), "+v"(sp[4]), "+v"(sp[5]), "+v"(sp[6]), "+v"(sp[7]));
          ln_stats(sp, mean, rstd);
        }
      };
      sfor<0, 128>(step);
      mm_done(acc[2], acc[3]);
      STAMP(2);
      REFRESH();
      const bf16x8 mf = mean_frag(mean);
      LAS char* pk_img = c.smem + 6 * SLOT;
      LAS char* v_img = c.smem + 8 * SLOT;
      LAS const char* wp = c.smem + 9 * SLOT + (4 * h + q4) * 64 + blk * 32 + p4 * 8;
      sfor<0, 2>([&](auto hd) {
        const f32x16 t = MFMA32(cs_frag(csk, h == hd), mf, acc[hd]);
        f32x16 ep, en; feature_map(t, rstd, wp + hd * 2048, ep, en);
        LAS char* row = pk_img + hd * 16384 + (w * 32 + r) * 128 + h * 8;
#pragma unroll
        for (int gq = 0; gq < 4; ++gq) {
          *(LAS u32x2*)(row + ((gq ^ (r & 7)) << 4)) = u32x2{cvtpk(ep[4 * gq], ep[4 * gq + 1]), cvtpk(ep[4 * gq + 2], ep[4 * gq + 3])};
          *(LAS u32x2*)(row + (((4 + gq) ^ (r & 7)) << 4)) = u32x2{cvtpk(en[4 * gq], en[4 * gq + 1]), cvtpk(en[4 * gq + 2], en[4 * gq + 3])};
        }
      });
      {
        float rs[16];
#pragma unroll
        for (int i = 0; i < 16; ++i) rs[i] = __shfl(rstd, (i & 3) + 8 * (i >> 2) + 4 * h);
        sfor<0, 2>([&](auto hd) {
          f32x16 t = MFMA32(mf, cs_frag(csv, h == hd), acc[2 + hd]);
#pragma unroll
          for (int i = 0; i < 16; ++i) t[i] *= rs[i];
          LAS char* d = v_img + (hd * 8 + w * 2) * 1024 + lane * 16;
          *(LAS bf16x8*)d = pack8<0>(t); *(LAS bf16x8*)(d + 1024) = pack8<1>(t);
        });
      }
      lds_bar();
      {
        const int hd = w >> 1, fh = w & 1;
        f32x16 kv = {}, z = {};
        const unsigned one2 = opq_v(0x3f803f80u);
        const bf16x8 ones = __builtin_bit_cast(bf16x8, u32x4{one2, one2, one2, one2});
        const int ch = fh * 4 + 2 * blk + (p4 >> 1), r0 = 4 * h + q4;
        LAS const char* pa = pk_img + hd * 16384 + r0 * 128 + ((ch ^ r0) << 4) + 8 * (p4 & 1);
        LAS const char* pb = v_img + hd * 8192 + lane * 16;
#pragma unroll
        for (int s = 0; s < 8; ++s) {
          const bf16x8 a = cat8(trread(pa + s * 2048), trread(pa + s * 2048 + 1024));
          kv = MFMA32(a, *(LAS const bf16x8*)(pb + s * 1024), kv);
          z = MFMA32(a, ones, z);
        }
        st4_1<0>(__builtin_bit_cast(u32x4, pack8<0>(kv)), c.xv, kvp + rb * 2048);
        st4_1<1024>(__builtin_bit_cast(u32x4, pack8<1>(kv)), c.xv, kvp + rb * 2048);
        f32x4 zz;
#pragma unroll
        for (int e = 0; e < 4; ++e) zz[e] = (p4 & 2) ? ((p4 & 1) ? z[12 + e] : z[8 + e]) : ((p4 & 1) ? z[4 + e] : z[e]);
        st4_1<0>(__builtin_bit_cast(u32x4, zz), (unsigned)(h * 64 + p4 * 16), zp + rb * 128);
      }
      STAMP(3);
      sfor<128, 192>(step);
      mm_done(acc[4], acc[5]);
      bar();
      STAMP(4);
      REFRESH();
      poll(fkvp, f16);
      STAMP(5);
      bf16x8 pp[32], zq[2];
      sfor<0, 32>([&](auto i) { ldx1<(i < 16), (i % 8) * 1024 - 4096>(pp[i], c.xv, kvp + (i / 8) * 8192 + 4096); });
      ldx1<false, 0>(zq[0], c.xv, zp); ldx1<false, 1024>(zq[1], c.xv, zp);
      sfor<3, 8>([&](auto ch) { issue_w(woA, slot_rot(ch), ch); });
      bf16x8 pq[2][4];
      sfor<0, 2>([&](auto hd) {
        const f32x16 t = MFMA32(cs_frag(csq, h == hd), mf, acc[4 + hd]);
        f32x16 ep, en; feature_map(t, rstd, wp + hd * 2048, ep, en);
        pq[hd][0] = pack8<0>(ep); pq[hd][1] = pack8<1>(ep); pq[hd][2] = pack8<0>(en); pq[hd][3] = pack8<1>(en);
      });
      STAMP(6);
      {
        bf16x8 sel0, sel1;
#pragma unroll
        for (int j = 0; j < 8; ++j) {
          const int kr = 8 * (j >> 2) + 4 * h + (j & 3);
          sel0[j] = r == kr ? (short)0x3f80 : (short)0; sel1[j] = r == 16 + kr ? (short)0x3f80 : (short)0;
        }
        f32x16 kv = {};
        sfor<0, 32>([&](auto i) {
          waitx<(i < 16), 31 - i + 2 + 20>(pp[i]);
          kv = MFMA32((i & 1) ? sel1 : sel0, pp[i], kv);
        });
        asm volatile("s_waitcnt vmcnt(20)" : "+v"(zq[0]), "+v"(zq[1]));
        f32x4 zs = __builtin_bit_cast(f32x4, zq[0]) + __builtin_bit_cast(f32x4, zq[1]);
#pragma unroll
        for (int e = 0; e < 4; ++e) { zs[e] += __shfl_xor(zs[e], 8); zs[e] += __shfl_xor(zs[e], 16); zs[e] = swap_add(zs[e]); }
        LAS char* sh = c.smem + 9 * SLOT + 4096;
        *(LAS bf16x8*)(sh + w * 2048 + lane * 16) = pack8<0>(kv); *(LAS bf16x8*)(sh + w * 2048 + 1024 + lane * 16) = pack8<1>(kv);
        if (lane < 8) *(LAS u32x2*)(sh + 8192 + w * 64 + lane * 8) = u32x2{cvtpk(zs[0], zs[1]), cvtpk(zs[2], zs[3])};
        lds_bar();
        char* dst = act + 4 * MB + cb * 4096;
        sfor<0, 2>([&](auto hd) {
          f32x16 num = {}, den = {};
#pragma unroll
          for (int k = 0; k < 4; ++k) {
            num = MFMA32(*(LAS const bf16x8*)(sh + (hd * 4 + k) * 1024 + lane * 16), pq[hd][k], num);
            den = MFMA32(*(LAS const bf16x8*)(sh + 8192 + (hd * 2 + (k >> 1)) * 64 + h * 32 + (k & 1) * 16), pq[hd][k], den);
          }
          const float inv = __builtin_amdgcn_rcpf(den[0] + 1e-6f);
#pragma unroll
          for (int i = 0; i < 16; ++i) num[i] *= inv;
          st4_1<hd * 2048>(__builtin_bit_cast(u32x4, pack8<0>(num)), c.xv, dst);
          st4_1<hd * 2048 + 1024>(__builtin_bit_cast(u32x4, pack8<1>(num)), c.xv, dst);
        });
        waitv<0>();
        set_flag(fact + 2048 + cb);
      }
    }
    STAMP(7);
    REFRESH();
    {
      f32x16 a0, a1, a2; res_to_acc(xr, a0, a1); asm volatile("" : "+a"(a0), "+a"(a1));
      poll(fact + 2048, f16);
      STAMP(8);
      const char* wkv = kvu >= 2 ? wv : wk;
      const unsigned kvv = (unsigned)((32 * w + (lane >> 1)) * 64 + hf * 32 + (lane & 1) * 16);
      gemm64<true, 0, false, false, 8>(c, act + 4 * MB, nullptr, a0, a1, a2, mean, rstd, [&]() {
        sfor<0, 8>([&](auto ch) { dma1(c.lds0 + (ch < 4 ? 5 : 9) * SLOT + (ch % 4) * 4096 + w * 1024, kvv, wkv + ch * 128 * 64); });
      });
      STAMP(9);
      store_x(c, a0, a1, xr, act + 8 * MB + cb * 4096, stt + 256 * 1024 + cb * 256);
      sfor<0, 3>([&](auto ch) { issue_w(wq, slot_rot(ch), ch); });
      waitv<12>();
      set_flag(fact + 2 * 2048 + cb);
      sfor<3, 8>([&](auto ch) { issue_w(wq, slot_rot(ch), ch); });
    }
    STAMP(10);
    REFRESH();
    u32x16 qq;
    char* kvf = ws + WS_KVF + (size_t)l * 512 * 1024;
    unsigned* fkv = flags + FL_KV + l * 256;
    {
      f32x16 a0, a1, a2 = {};
      poll(fact + 2 * 2048, f16);
      STAMP(11);
      const char* xb = act + 8 * MB; const char* stp = stt + 256 * 1024;
      if (kvu < 0) gemm64<true, 0, true, true, 0>(c, xb, stp, a0, a1, a2, mean, rstd, []() {});
      else if (kvu < 2) gemm64<true, 1, true, true, 0>(c, xb, stp, a0, a1, a2, mean, rstd, []() {});
      else gemm64<true, 2, true, true, 0>(c, xb, stp, a0, a1, a2, mean, rstd, []() {});
      STAMP(12);
      const bf16x8 mf = mean_frag(mean);
      if (kvu >= 0) {
        if (kvu < 2) {
          a2 = MFMA32(cs_frag(cse, h == 0), mf, a2);
#pragma unroll
          for (int i = 0; i < 8; ++i) a2[i] *= rstd;
          st4_1<0>(__builtin_bit_cast(u32x4, pack8<0>(a2)), c.xv, kvf + tt * 4096 + hf * 1024);
        } else {
          a2 = MFMA32(mf, cs_frag(cse, h == 0), a2);
#pragma unroll
          for (int i = 0; i < 16; ++i) a2[i] *= __shfl(rstd, (i & 3) + 8 * (i >> 2) + 4 * h);
          const unsigned vo = (unsigned)((hf * 16 + (r & 15) + 32 * h) * 16);
          st4_1<2048>(unperm(__builtin_bit_cast(u32x4, pack8<0>(a2))), vo, kvf + tt * 4096);
          st4_1<3072>(unperm(__builtin_bit_cast(u32x4, pack8<1>(a2))), vo, kvf + tt * 4096);
        }
      }
      a0 = MFMA32(cs_frag(csb, h == 0), mf, a0); a1 = MFMA32(cs_frag(csb, h == 1), mf, a1);
      const float sc = rstd * QSCALE;
#pragma unroll
      for (int i = 0; i < 8; ++i) { qq[i] = cvtpk(a0[2 * i] * sc, a0[2 * i + 1] * sc); qq[8 + i] = cvtpk(a1[2 * i] * sc, a1[2 * i + 1] * sc); }
      if (kvu >= 0) { waitv<0>(); set_flag(fkv + tt * 4 + kvu); }
    }
    STAMP(13);
    REFRESH();
    {
      poll(fkv + w * 64, (unsigned)(lane * 4));
      bar();
      STAMP(14);
      const bf16x8 qa0 = __builtin_bit_cast(bf16x8, __builtin_shufflevector(qq, qq, 0, 1, 2, 3)), qa1 = __builtin_bit_cast(bf16x8, __builtin_shufflevector(qq, qq, 4, 5, 6, 7));
      const bf16x8 qb0 = __builtin_bit_cast(bf16x8, __builtin_shufflevector(qq, qq, 8, 9, 10, 11)), qb1 = __builtin_bit_cast(bf16x8, __builtin_shufflevector(qq, qq, 12, 13, 14, 15));
      const f32x16 zero = {};
      float mA = 0.f, mB = 0.f;
      for (int attempt = 0; attempt < 2; ++attempt) {
        if (attempt == 1) {
          mA = -INFINITY; mB = -INFINITY;
          for (int t = 0; t < 64; ++t) {
            bf16x8 k0, k1; ldx1<false, 0>(k0, c.xv, kvf + t * 4096); ldx1<false, 1024>(k1, c.xv, kvf + t * 4096);
            asm volatile("s_waitcnt vmcnt(0)" : "+v"(k0), "+v"(k1));
            f32x16 a = MFMA32(k0, qa0, zero); a = MFMA32(k1, qa1, a);
            f32x16 b = MFMA32(k0, qb0, zero); b = MFMA32(k1, qb1, b);
            mA = fmaxf(mA, max16(a)); mB = fmaxf(mB, max16(b));
          }
          mA = swap_max(mA); mB = swap_max(mB);
        }
        bf16x8 k0, k1;
        ldx1<false, 0>(k0, c.xv, kvf); ldx1<false, 1024>(k1, c.xv, kvf);
        const char* ksrc = kvf + 2048 + w * 2048;
        const unsigned ring = c.lds0 + 8 * SLOT + w * 2048;
#pragma unroll
        for (int j = 0; j < 3; ++j) {
          unsigned keep;
          asm volatile("s_mov_b32 %0, m0\n\ts_mov_b32 m0, %1\n\ts_nop 0\n\tglobal_load_lds_dwordx4 %2, %3 sc1\n\tglobal_load_lds_dwordx4 %2, %3 offset:1024 sc1\n\ts_mov_b32 m0, %0"
                       : "=&s"(keep) : "s"(ring + j * 8192), "v"(c.xv), "s"(ksrc + j * 8192) : "memory");
        }
        asm volatile("s_waitcnt vmcnt(6)" : "+v"(k0), "+v"(k1));
        f32x16 sa0 = MFMA32(k0, qa0, zero); sa0 = MFMA32(k1, qa1, sa0);
        if (attempt == 0) {
          f32x16 sb = MFMA32(k0, qb0, zero); sb = MFMA32(k1, qb1, sb);
          mA = swap_max(max16(sa0)); mB = swap_max(max16(sb));
        }
        f32x16 nma, nmb, oa = {}, ob = {}, sb1 = {}, t0, t1;
#pragma unroll
        for (int e = 0; e < 16; ++e) { nma[e] = -mA; nmb[e] = -mB; sa0[e] -= mA; }
        const unsigned s0 = ((lane >> 2) & 1) ? 0u : 0x3f803f80u, s1 = 0x3f803f80u - s0;
        u32x16 kvr = {}, misc;
        { const u32x4 a = __builtin_bit_cast(u32x4, k0), b = __builtin_bit_cast(u32x4, k1);
#pragma unroll
          for (int e = 0; e < 4; ++e) { kvr[e] = a[e]; kvr[4 + e] = b[e]; } }
        misc = u32x16{s0, s0, s1, s1, 0u, 0u, 0u, 0u, 0u, 0u, 0u, 0u, 0xddddddddu, c.lds0 + 8 * SLOT + c.xv, 0u, 0u};
        const u32x8 offs = {c.xv, c.xv + 8192, c.xv + 16384, c.xv + 24576, g2.v0, g2.v1, g2.v2, g2.v3};
        const char* ks = ksrc + 3 * 8192; const char* wsr = woB + cb * 128;
        unsigned wm0 = c.lds0 + w * 4096, cnt = 8, keep;
        asm volatile(
            "s_mov_b32 %[keep], m0\n\ts_nop 1\n\t"
            "1:\n\t"
            STEP_(0, 24576, 240, 244, 0) STEP_(8192, 0, 241, 245, 1024) STEP_(16384, 8192, 242, 246, 2048) STEP_(24576, 16384, 243, 247, 3072)
            "s_add_u32 s40, s40, 0x8000\n\ts_addc_u32 s41, s41, 0\n\ts_add_u32 s42, s42, 0x40000\n\ts_addc_u32 s43, s43, 0\n\t"
            "s_add_u32 s45, s45, 0x4000\n\ts_sub_u32 %[cnt], %[cnt], 1\n\ts_cmp_lg_u32 %[cnt], 0\n\ts_cbranch_scc1 1b\n\t"
            PV0_(80, 176) PV1_(80, 176) L0_(232, 176) L1_(232, 176)
            "s_mov_b32 m0, %[keep]\n\ts_nop 15\n\t"
            : "+{v[64:79]}"(oa), "+{v[80:95]}"(ob), "+{v[128:143]}"(sa0), "=&{v[144:159]}"(t0), "=&{v[160:175]}"(t1), "+{v[176:191]}"(sb1),
              "+{v[208:223]}"(kvr), "+{v[224:239]}"(misc), "+{s[40:41]}"(ks), "+{s[42:43]}"(wsr), "+{s45}"(wm0), [cnt] "+s"(cnt), [keep] "=&s"(keep)
            : "{v[96:111]}"(nma), "{v[112:127]}"(nmb), "{v[192:207]}"(qq), "{v[240:247]}"(offs), "{s44}"(ring)
            : "scc", "memory");
        const float la = __uint_as_float(misc[4]), lb = __uint_as_float(misc[8]);
        bool bad = !(la < 1.2676506e30f) || !(lb < 1.2676506e30f);
        const float ia = __builtin_amdgcn_rcpf(la), ib = __builtin_amdgcn_rcpf(lb);
        float chk = 0.f;
#pragma unroll
        for (int e = 0; e < 16; ++e) { oa[e] *= ia; ob[e] *= ib; chk = fmaxf(chk, fmaxf(fabsf(oa[e]), fabsf(ob[e]))); }
        bad |= !(chk < INFINITY);
        char* dst = act + 12 * MB + cb * 4096;
        st4_1<0>(__builtin_bit_cast(u32x4, pack8<0>(oa)), c.xv, dst); st4_1<1024>(__builtin_bit_cast(u32x4, pack8<1>(oa)), c.xv, dst);
        st4_1<2048>(__builtin_bit_cast(u32x4, pack8<0>(ob)), c.xv, dst); st4_1<3072>(__builtin_bit_cast(u32x4, pack8<1>(ob)), c.xv, dst);
        wait_bar<0>();
        LAS volatile int* vote = (LAS volatile int*)(c.smem + 8 * SLOT);
        if (lane == 0) vote[w] = 0;
        if (attempt == 0 && bad) vote[w] = 1;
        lds_bar();
        const int redo = vote[0] | vote[1] | vote[2] | vote[3];
        if (!__builtin_amdgcn_readfirstlane(redo)) break;
        lds_bar();
      }
      set_flag(fact + 3 * 2048 + cb);
    }
    STAMP(15);
    REFRESH();
    {
      f32x16 a0, a1, a2; res_to_acc(xr, a0, a1); asm volatile("" : "+a"(a0), "+a"(a1));
      poll(fact + 3 * 2048, f16);
      STAMP(16);
      gemm64<false, 0, false, false, 5>(c, act + 12 * MB, nullptr, a0, a1, a2, mean, rstd, [&]() { issue_a(IC(8)); issue_phi(); });
      STAMP(17);
      if (l == 0) {
        store_x(c, a0, a1, xr, act + 16 * MB + cb * 4096, stt + 512 * 1024 + cb * 256);
        sfor<0, 3>(issue_a);
        waitv<12>();
        set_flag(fact + 4 * 2048 + cb);
        sfor<3, 8>(issue_a);
      } else {
        char* dst = out + (size_t)(tt * 32 + r) * 2048 + cb * 128 + h * 16;
        *(u32x4*)(dst) = unperm(__builtin_bit_cast(u32x4, pack8<0>(a0))); *(u32x4*)(dst + 32) = unperm(__builtin_bit_cast(u32x4, pack8<1>(a0)));
        *(u32x4*)(dst + 64) = unperm(__builtin_bit_cast(u32x4, pack8<0>(a1))); *(u32x4*)(dst + 96) = unperm(__builtin_bit_cast(u32x4, pack8<1>(a1)));
      }
    }
    STAMP(18);
  }
}

extern "C" void kernel_launch(void* const* d_in, const int* in_sizes, int n_in, void* d_out, int out_size, void* d_ws, size_t ws_size, hipStream_t stream) {
  char* ws = (char*)d_ws;
  k_prep<<<256 + 65 * 4, 256, 0, stream>>>((const char*)d_in[0], (const char*)d_in[1], (const char*)d_in[5], (const char*)d_in[6], (const char*)d_in[7], ws);
  k_main<<<256, 256, 0, stream>>>((const char*)d_in[1], (const char*)d_in[2], (const char*)d_in[3], (const char*)d_in[5], (const char*)d_in[6], (const char*)d_in[7],
                                  (const char*)d_in[8], ws, (char*)d_out);
}
```

```cpp
#include <hip/hip_runtime.h>
#include <hip/hip_bf16.h>
#include <stdint.h>
#include <type_traits>

typedef __attribute__((ext_vector_type(8))) short bf16x8;
typedef __attribute__((ext_vector_type(4))) short s16x4;
typedef __attribute__((ext_vector_type(2))) __bf16 bf2_t;
typedef __attribute__((ext_vector_type(16))) float f32x16;
typedef __attribute__((ext_vector_type(4))) float f32x4;
typedef __attribute__((ext_vector_type(2))) float f32x2;
typedef __attribute__((ext_vector_type(4))) unsigned u32x4;
typedef __attribute__((ext_vector_type(2))) unsigned u32x2;
#define LAS __attribute__((address_space(3)))
#define DI __device__ __forceinline__
#define MFMA32(a, b, c) __builtin_amdgcn_mfma_f32_32x32x16_bf16((a), (b), (c), 0, 0, 0)
#define MFMA16(a, b, c) __builtin_amdgcn_mfma_f32_16x16x32_bf16((a), (b), (c), 0, 0, 0)
#define SBAR() __builtin_amdgcn_sched_barrier(0)

constexpr int S_ = 2048, D_ = 1024;
constexpr float LOG2E = 1.4426950408889634f;
constexpr float QSCALE = 0.17677669529663687f * LOG2E;

constexpr size_t MB = 1u << 20;
constexpr size_t WS_ACT = 0;
constexpr size_t WS_KVP = 32 * MB;
constexpr size_t WS_ZP = 36 * MB;
constexpr size_t WS_KVF = 37 * MB;
constexpr size_t WS_ST = 38 * MB;
constexpr size_t WS_CS = 39 * MB;
constexpr size_t WS_FLAG = 40 * MB;
constexpr int FL_ACT = 0, FL_KVP = 8 * 64 * 32, FL_KV = FL_KVP + 2 * 64 * 32, FL_WORDS = FL_KV + 2 * 512;
constexpr int CS_N = 3072 + 1024 + 64;

template <int I, int N, class F> DI void sfor(F&& f) { if constexpr (I < N) { f(std::integral_constant<int, I>{}); sfor<I + 1, N>(f); } }

DI unsigned cvtpk(float lo, float hi) { f32x2 v = {lo, hi}; return __builtin_bit_cast(unsigned, __builtin_convertvector(v, bf2_t)); }
DI float bflo(unsigned u) { return __uint_as_float(u << 16); }
DI float bfhi(unsigned u) { return __uint_as_float(u & 0xffff0000u); }
DI float rbf(float x) { return bflo(cvtpk(x, x)); }
DI float swap_add(float v) { auto r = __builtin_amdgcn_permlane32_swap(__float_as_uint(v), __float_as_uint(v), false, false); return __uint_as_float(r[0]) + __uint_as_float(r[1]); }
DI float swap_max(float v) { auto r = __builtin_amdgcn_permlane32_swap(__float_as_uint(v), __float_as_uint(v), false, false); return fmaxf(__uint_as_float(r[0]), __uint_as_float(r[1])); }
DI float swap_min(float v) { auto r = __builtin_amdgcn_permlane32_swap(__float_as_uint(v), __float_as_uint(v), false, false); return fminf(__uint_as_float(r[0]), __uint_as_float(r[1])); }
template <int S> DI bf16x8 pack8(const f32x16& v) {
  u32x4 p = {cvtpk(v[8 * S], v[8 * S + 1]), cvtpk(v[8 * S + 2], v[8 * S + 3]), cvtpk(v[8 * S + 4], v[8 * S + 5]), cvtpk(v[8 * S + 6], v[8 * S + 7])};
  return __builtin_bit_cast(bf16x8, p);
}
DI u32x4 unperm(u32x4 v) {
  auto r0 = __builtin_amdgcn_permlane32_swap(v[0], v[2], false, false);
  auto r1 = __builtin_amdgcn_permlane32_swap(v[1], v[3], false, false);
  return u32x4{r0[0], r1[0], r0[1], r1[1]};
}
DI float lane_get(float v, int src) { return __uint_as_float(__builtin_amdgcn_ds_bpermute(src << 2, __float_as_uint(v))); }
DI s16x4 trread(LAS const char* p) { return __builtin_amdgcn_ds_read_tr16_b64_v4i16((LAS s16x4*)p); }
DI bf16x8 cat8(s16x4 a, s16x4 b) { return __builtin_shufflevector(a, b, 0, 1, 2, 3, 4, 5, 6, 7); }
DI float dot2(unsigned a, unsigned b, float c) { return __builtin_amdgcn_fdot2_f32_bf16(__builtin_bit_cast(bf2_t, a), __builtin_bit_cast(bf2_t, b), c, false); }

template <int OFF> DI void dma(unsigned lds, unsigned v, const char* sb) {
  asm volatile("s_mov_b32 m0, %0\n\ts_nop 0\n\tglobal_load_lds_dwordx4 %1, %2 offset:%3" ::"s"(lds), "v"(v), "s"(sb), "i"(OFF) : "memory");
}
template <int OFF> DI void dma_c(unsigned lds, unsigned v, const char* sb) {
  asm volatile("s_mov_b32 m0, %0\n\ts_nop 0\n\tglobal_load_lds_dwordx4 %1, %2 offset:%3 sc1" ::"s"(lds), "v"(v), "s"(sb), "i"(OFF) : "memory");
}
constexpr int cap63(int n) { return n > 63 ? 63 : n; }
template <int N> DI void wait_bar() { asm volatile("s_waitcnt vmcnt(%0)\n\ts_barrier" ::"i"(cap63(N)) : "memory"); }
DI void lds_bar() { asm volatile("s_waitcnt lgkmcnt(0)\n\ts_barrier" ::: "memory"); }
DI void all_bar() { asm volatile("s_waitcnt vmcnt(0) lgkmcnt(0)\n\ts_barrier" ::: "memory"); }
DI void bar() { asm volatile("s_barrier" ::: "memory"); }
template <bool AG, int OFF> DI void ldx1(bf16x8& d, unsigned voff, const char* sb) {
  if constexpr (AG) asm volatile("global_load_dwordx4 %0, %1, %2 offset:%3 sc1" : "=a"(d) : "v"(voff), "s"(sb), "i"(OFF));
  else asm volatile("global_load_dwordx4 %0, %1, %2 offset:%3 sc1" : "=v"(d) : "v"(voff), "s"(sb), "i"(OFF));
}
template <int OFF> DI void ld2_1(f32x2& d, unsigned voff, const char* sb) { asm volatile("global_load_dwordx2 %0, %1, %2 offset:%3 sc1" : "=v"(d) : "v"(voff), "s"(sb), "i"(OFF)); }
DI void ld1_1(unsigned& d, unsigned voff, const void* sb) { asm volatile("global_load_dword %0, %1, %2 sc1" : "=v"(d) : "v"(voff), "s"(sb) : "memory"); }
template <int OFF> DI void st4_1(u32x4 d, unsigned voff, char* sb) { asm volatile("global_store_dwordx4 %0, %1, %2 offset:%3 sc1\n\ts_nop 1" ::"v"(voff), "v"(d), "s"(sb), "i"(OFF) : "memory"); }
DI void st2_1(f32x2 d, unsigned voff, char* sb) { asm volatile("global_store_dwordx2 %0, %1, %2 sc1" ::"v"(voff), "v"(d), "s"(sb) : "memory"); }
DI void st1_1(float d, unsigned voff, char* sb) { asm volatile("global_store_dword %0, %1, %2 sc1" ::"v"(voff), "v"(d), "s"(sb) : "memory"); }
template <int N> DI void waitv() { asm volatile("s_waitcnt vmcnt(%0)" ::"i"(cap63(N)) : "memory"); }
template <bool AG, int N, class T> DI void waitx(T& d) {
  if constexpr (AG) asm volatile("s_waitcnt vmcnt(%1)" : "+a"(d) : "i"(cap63(N))); else asm volatile("s_waitcnt vmcnt(%1)" : "+v"(d) : "i"(cap63(N)));
}
template <bool SW, bool BA, bool Z, bool NOP = false> DI void mm(f32x16& acc, const bf16x8& a, const bf16x8& b) {
#define MM_(OPS, C, OUT, BC) do { if constexpr (NOP) asm volatile("s_nop 1\n\tv_mfma_f32_32x32x16_bf16 %0, " OPS ", " C : OUT(acc) : "v"(a), BC(b)); \
                                  else asm volatile("v_mfma_f32_32x32x16_bf16 %0, " OPS ", " C : OUT(acc) : "v"(a), BC(b)); } while (0)
  if constexpr (Z) {
    if constexpr (SW) { if constexpr (BA) MM_("%2, %1", "0", "=&v", "a"); else MM_("%2, %1", "0", "=&v", "v"); }
    else { if constexpr (BA) MM_("%1, %2", "0", "=&v", "a"); else MM_("%1, %2", "0", "=&v", "v"); }
  } else {
    if constexpr (SW) { if constexpr (BA) MM_("%2, %1", "%0", "+v", "a"); else MM_("%2, %1", "%0", "+v", "v"); }
    else { if constexpr (BA) MM_("%1, %2", "%0", "+v", "a"); else MM_("%1, %2", "%0", "+v", "v"); }
  }
#undef MM_
}
DI void mdone(f32x16& a) { asm volatile("s_nop 11" : "+v"(a)); }
DI void mdone(f32x16& a, f32x16& b) { asm volatile("s_nop 11" : "+v"(a), "+v"(b)); }
DI void mdone(f32x16& a, f32x16& b, f32x16& c, f32x16& d) { asm volatile("s_nop 11" : "+v"(a), "+v"(b), "+v"(c), "+v"(d)); }
template <class T> DI T opq_v(T v) { asm volatile("" : "+v"(v)); return v; }
DI void set_flag(unsigned* flag) {
  unsigned z = 0, one = 1;
  asm volatile("s_mov_b64 exec, 1\n\tglobal_store_dword %0, %1, %2 sc1\n\ts_mov_b64 exec, -1" ::"v"(z), "v"(one), "s"(flag) : "memory");
}
DI void poll(const unsigned* base, unsigned voff) {
  for (int spin = 0; spin < (1 << 15); ++spin) {
    unsigned v;
    asm volatile("global_load_dword %0, %1, %2 sc1\n\ts_waitcnt vmcnt(0)" : "=v"(v) : "v"(voff), "s"(base) : "memory");
    if (__all(v == 1u)) break;
  }
}
DI bool ones8(const bf16x8& a, const bf16x8& b) {
  const u32x4 x = __builtin_bit_cast(u32x4, a), y = __builtin_bit_cast(u32x4, b);
  return ((x[0] & x[1] & x[2] & x[3] & y[0] & y[1] & y[2] & y[3]) == 1u);
}

template <int RBX> DI void block_map(int& rb, int& cb) {
  const int x = blockIdx.x & 7, i = blockIdx.x >> 3;
  constexpr int CBX = 32 / RBX, GC = 16 / CBX;
  rb = RBX * (x / GC) + i / CBX; cb = CBX * (x % GC) + i % CBX;
}

DI void ln_stats(const f32x2 (&p)[8], float& mean, float& rstd) {
  float s = 0.f, q = 0.f;
#pragma unroll
  for (int i = 0; i < 8; ++i) { s += p[i][0]; q += p[i][1]; }
  s = swap_add(s); q = swap_add(q);
  const float m = s * (1.f / D_);
  const float var = fmaxf(q * (1.f / D_) - m * m, 0.f);
  mean = rbf(m);
  rstd = 1.f / rbf(sqrtf(rbf(rbf(var) + 1e-5f)));
}
DI bf16x8 cs_frag(float c, bool on) {
  const float hi = rbf(c), mid = rbf(c - hi), lo = (c - hi) - mid;
  u32x4 v = {cvtpk(hi, mid), cvtpk(lo, 0.f), 0u, 0u};
  if (!on) { v[0] = 0u; v[1] = 0u; }
  return __builtin_bit_cast(bf16x8, v);
}
DI bf16x8 mean_frag(float mean) { u32x4 v = {cvtpk(-mean, -mean), cvtpk(-mean, 0.f), 0u, 0u}; return __builtin_bit_cast(bf16x8, v); }

#define STAMP(i)

__global__ __launch_bounds__(256) void k_prep(const char* x, const char* wqkv, const char* wq, const char* wk, const char* wv, char* ws) {
  const int tid = threadIdx.x, lane = tid & 63, w = tid >> 6, r = lane & 31, h = lane >> 5;
  __shared__ float red[32][64];
  if (blockIdx.x < 256) {
    const int tt = blockIdx.x >> 2, cb = (blockIdx.x & 3) * 4 + w;
    const char* src = x + (size_t)(tt * 32 + r) * 2048 + cb * 128 + h * 16;
    u32x4 v[4];
#pragma unroll
    for (int i = 0; i < 4; ++i) v[i] = *(const u32x4*)(src + i * 32);
    { const int f = blockIdx.x * 256 + tid; if (f < FL_WORDS) ((unsigned*)(ws + WS_FLAG))[f] = 0u; }
    float s = 0.f, q = 0.f;
#pragma unroll
    for (int i = 0; i < 4; ++i) {
#pragma unroll
      for (int e = 0; e < 4; ++e) { s = dot2(v[i][e], 0x3f803f80u, s); q = dot2(v[i][e], v[i][e], q); }
      *(u32x4*)(ws + WS_ACT + ((size_t)(tt * 64 + cb * 4 + i) * 64 + lane) * 16) = unperm(v[i]);
    }
    s = swap_add(s); q = swap_add(q);
    if (h == 0) ((f32x2*)(ws + WS_ST))[(tt * 16 + cb) * 32 + r] = f32x2{s, q};
  } else {
    const int u = blockIdx.x - 256, qr = u & 3, g = u >> 2;
    const char* src; int pitch;
    const int c8 = tid & 7, rr = tid >> 3;
    if (g < 48) { src = wqkv + g * 128 + c8 * 16; pitch = 6144; }
    else if (g < 64) { src = wq + (g - 48) * 128 + c8 * 16; pitch = 2048; }
    else { src = (c8 < 4 ? wk : wv) + (c8 & 3) * 16; pitch = 64; }
    src += (size_t)(qr * 256 + rr) * pitch;
    u32x4 v[8];
#pragma unroll
    for (int i = 0; i < 8; ++i) v[i] = *(const u32x4*)(src + (size_t)i * 32 * pitch);
    float a[8] = {};
#pragma unroll
    for (int i = 0; i < 8; ++i)
#pragma unroll
      for (int e = 0; e < 4; ++e) { a[2 * e] += bflo(v[i][e]); a[2 * e + 1] += bfhi(v[i][e]); }
#pragma unroll
    for (int e = 0; e < 8; ++e) red[rr][c8 * 8 + e] = a[e];
    __syncthreads();
    if (tid < 64) {
      float t = 0.f;
#pragma unroll
      for (int i = 0; i < 32; ++i) t += red[i][tid];
      ((float*)(ws + WS_CS))[qr * CS_N + g * 64 + tid] = t;
    }
  }
}

constexpr int SLOT = 16384, XL = 12;
struct WSrc { unsigned v[4]; };
DI WSrc wsrc(int lane, int w, int pitch) {
  const int row = lane >> 3, ch = (lane & 7) ^ (((row >> 1) & 1) << 2);
  WSrc g; g.v[0] = (unsigned)((32 * w + row) * pitch + ch * 16);
  g.v[1] = g.v[0] + 8 * pitch - 1024; g.v[2] = g.v[0] + 16 * pitch - 2048; g.v[3] = g.v[0] + 24 * pitch - 3072;
  return g;
}
struct WinPtr {
  unsigned b[3];
  DI void init(unsigned a) { sfor<0, 3>([&](auto i) { b[i] = a + i * 65536u; asm volatile("" : "+v"(b[i])); }); }
  template <int OFF> DI bf16x8 frag() const {
    LAS const char* p = (LAS const char*)(uintptr_t)b[OFF >> 16] + (OFF & 65535);
    return cat8(trread(p), trread(p + 1024));
  }
};
struct Ctx { unsigned xv; WinPtr p0, p1; char* ws; };
#define IC(n) std::integral_constant<int, (n)>{}
constexpr int slot_a(int c) { constexpr int m[8] = {6, 0, 1, 2, 3, 7, 4, 5}; return m[c]; }
constexpr int slot_q(int c) { constexpr int m[8] = {8, 0, 1, 2, 3, 7, 4, 5}; return m[c]; }

template <int S> DI void ld_x(bf16x8 (&x)[64], const Ctx& c, const char* xb) { ldx1<false, (S % 8) * 1024 - 4096>(x[S], c.xv, xb + (S / 8) * 8192 + 4096); }
template <class H> constexpr int g64_wait(int s) {
  int n = 0;
  const int t0 = s - XL;
  if (t0 < 0) n += XL - 1 - s; else n += H::n(t0);
  for (int t = t0 < 0 ? 0 : t0 + 1; t < s; ++t) n += (t + XL < 64 ? 1 : 0) + H::n(t);
  return n;
}
template <bool MAPA, bool ZERO, class H, class FX>
DI void gemm64(const Ctx& c, bf16x8 (&x)[64], const char* xb, f32x16& acc0, f32x16& acc1, FX&& hook) {
  bf16x8 f0[3], f1[3];
  auto off = [](int s) constexpr { return (MAPA ? slot_a(s / 8) : s / 8) * SLOT + (s % 8) * 2048; };
  auto rd0 = [&](auto s) { f0[s % 3] = c.p0.template frag<off(s)>(); };
  auto rd1 = [&](auto s) { f1[s % 3] = c.p1.template frag<off(s)>(); };
  rd0(IC(0)); rd1(IC(0)); rd0(IC(1)); rd1(IC(1));
  sfor<0, 64>([&](auto s) {
    waitx<false, g64_wait<H>(s)>(x[s]);
    SBAR();
    mm<false, false, ZERO && s == 0>(acc0, f0[s % 3], x[s]);
    SBAR();
    if constexpr (s + 2 < 64) rd0(IC(s + 2));
    if constexpr (s + XL < 64) ld_x<s + XL>(x, c, xb);
    SBAR();
    mm<false, false, ZERO && s == 0>(acc1, f1[s % 3], x[s]);
    SBAR();
    if constexpr (s + 2 < 64) rd1(IC(s + 2));
    hook(s);
    SBAR();
  });
  mdone(acc0, acc1);
}
struct HookTail { static constexpr int n(int s) { return s >= 52 ? 1 : 0; } };
struct HookQ { static constexpr int n(int s) { return s == 52 ? 2 : 0; } };

DI void res_to_acc(const u32x4 (&xr)[4], f32x16& a0, f32x16& a1) {
#pragma unroll
  for (int e = 0; e < 4; ++e) {
    a0[2 * e] = bflo(xr[0][e]); a0[2 * e + 1] = bfhi(xr[0][e]); a0[8 + 2 * e] = bflo(xr[1][e]); a0[9 + 2 * e] = bfhi(xr[1][e]);
    a1[2 * e] = bflo(xr[2][e]); a1[2 * e + 1] = bfhi(xr[2][e]); a1[8 + 2 * e] = bflo(xr[3][e]); a1[9 + 2 * e] = bfhi(xr[3][e]);
  }
  asm volatile("" : "+v"(a0), "+v"(a1));
}
DI void store_x(const Ctx& c, int r, const f32x16& a0, const f32x16& a1, u32x4 (&xr)[4], char* dst, char* stp) {
  xr[0] = __builtin_bit_cast(u32x4, pack8<0>(a0)); st4_1<0>(xr[0], c.xv, dst);
  xr[1] = __builtin_bit_cast(u32x4, pack8<1>(a0)); st4_1<1024>(xr[1], c.xv, dst);
  xr[2] = __builtin_bit_cast(u32x4, pack8<0>(a1)); st4_1<2048>(xr[2], c.xv, dst);
  xr[3] = __builtin_bit_cast(u32x4, pack8<1>(a1)); st4_1<3072>(xr[3], c.xv, dst);
  float s1 = 0.f, s2 = 0.f, s3 = 0.f, s4 = 0.f;
#pragma unroll
  for (int i = 0; i < 4; ++i)
#pragma unroll
    for (int e = 0; e < 4; e += 2) {
      s1 = dot2(xr[i][e], 0x3f803f80u, s1); s2 = dot2(xr[i][e], xr[i][e], s2);
      s3 = dot2(xr[i][e + 1], 0x3f803f80u, s3); s4 = dot2(xr[i][e + 1], xr[i][e + 1], s4);
    }
  st2_1(f32x2{swap_add(s1 + s3), swap_add(s2 + s4)}, (unsigned)(r * 8), stp);
}
DI void pin8(f32x2 (&sp)[8]) { asm volatile("" : "+v"(sp[0]), "+v"(sp[1]), "+v"(sp[2]), "+v"(sp[3]), "+v"(sp[4]), "+v"(sp[5]), "+v"(sp[6]), "+v"(sp[7])); }

DI void hedge_exp(const f32x16& p, float sc, f32x16& ep, f32x16& en) {
  float mx = fmaxf(p[0], p[1]), mn = fminf(p[0], p[1]);
#pragma unroll
  for (int i = 2; i < 16; i += 2) { mx = fmaxf(fmaxf(mx, p[i]), p[i + 1]); mn = fminf(fminf(mn, p[i]), p[i + 1]); }
  mx = swap_max(mx) * sc; mn = swap_min(mn) * sc;
#pragma unroll
  for (int i = 0; i < 16; ++i) {
    ep[i] = __builtin_amdgcn_exp2f(__builtin_fmaf(p[i], sc, -mx));
    en[i] = __builtin_amdgcn_exp2f(__builtin_fmaf(p[i], -sc, mn));
  }
}

constexpr int a_xt(int s) {
  int t = s < 0 ? XL : (s < 15 ? XL + (3 * (s + 1)) / 2 : 34 + (2 * (s - 14)) / 3);
  if (s >= 15 && t < s + 10) t = s + 10;
  return t > 64 ? 64 : t;
}
struct SchedKV {
  int xw[64], ww[8];
  constexpr SchedKV() : xw{}, ww{} {
    int pos = 8, xpos[64] = {}, wpos[10] = {};
    for (int s = 0; s < XL; ++s) xpos[s] = ++pos;
    for (int s = 0; s < 64; ++s) {
      if ((s + 1) % 8 == 0 && (s + 1) / 8 < 8) { const int g = (s + 1) / 8; ww[g] = g >= 4 ? pos - wpos[g] : 63; }
      xw[s] = pos - xpos[s];
      for (int t = a_xt(s - 1); t < a_xt(s); ++t) xpos[t] = ++pos;
      if (s >= 15 && s <= 62) wpos[(s + 1) / 8 + 2] = ++pos;
      if (s >= 52 && s <= 56) ++pos;
    }
  }
};
constexpr int q_nd(int s) { return s < 6 ? 0 : (s < 14 ? 1 : (s < 46 ? ((s - 14) % 8 < 4 ? 1 : 0) : 0)); }
struct SchedQ {
  int ww[8], pw, lw[34];
  constexpr SchedQ() : ww{}, pw(0), lw{} {
    int pos = 3, stpos = 3, q6 = 0, q7 = 0, ppos = 0, lpos[34] = {};
    for (int s = 0; s < 64; ++s) {
      if ((s + 2) % 8 == 0 && (s + 2) / 8 < 8) {
        const int g = (s + 2) / 8;
        ww[g] = g == 2 ? pos - stpos : (g == 6 ? pos - q6 : (g == 7 ? pos - q7 : 63));
        if (g == 2) ++pos;
      }
      if (s == 46) pw = pos - ppos;
      pos += q_nd(s); if (s == 9) q6 = pos; if (s == 13) q7 = pos;
      if (s == 36) ppos = ++pos;
      if (s >= 46 && s < 63) { lpos[2 * (s - 46)] = ++pos; lpos[2 * (s - 46) + 1] = ++pos; }
    }
    pos += 16;
    for (int i = 0; i < 34; ++i) lw[i] = pos - lpos[i];
  }
};

#define R_OA "v[64:79]"
#define R_OB "v[80:95]"
#define S0_(n, q, m) "v_mfma_f32_32x32x16_bf16 v[" #n ":" #n "+15], v[208:211], v[" #q ":" #q "+3], v[" #m ":" #m "+15]\n\t"
#define S1_(n, q) "v_mfma_f32_32x32x16_bf16 v[" #n ":" #n "+15], v[212:215], v[" #q "+4:" #q "+7], v[" #n ":" #n "+15]\n\t"
#define PV0_(o, p) "v_smfmac_f32_32x32x32_bf16 v[" #o ":" #o "+15], v[216:219], v[" #p ":" #p "+7], v236\n\t"
#define PV1_(o, p) "v_smfmac_f32_32x32x32_bf16 v[" #o ":" #o "+15], v[220:223], v[" #p "+8:" #p "+15], v236\n\t"
#define L0_(l, p) "v_smfmac_f32_16x16x64_bf16 v[" #l ":" #l "+3], v[224:227], v[" #p ":" #p "+7], v236\n\t"
#define L1_(l, p) "v_smfmac_f32_16x16x64_bf16 v[" #l ":" #l "+3], v[224:227], v[" #p "+8:" #p "+15], v236\n\t"
#define E_(e, i) "v_exp_f32 v[" #e "+" #i "], v[" #e "+" #i "]\n\t"
#define RD_(d, off) "ds_read_b128 v[" #d ":" #d "+3], v237 offset:" #off "\n\t"
#define HALF_(e, n, q, m, p, o, l, X0, X1, X2, X3, X4, Y2) \
  S0_(n, q, m) X0 E_(e, 0) E_(e, 1) E_(e, 2) S1_(n, q) X1 E_(e, 3) E_(e, 4) E_(e, 5) Y2 PV0_(o, p) X2 E_(e, 6) E_(e, 7) E_(e, 8) \
  PV1_(o, p) X3 E_(e, 9) E_(e, 10) E_(e, 11) L0_(l, p) X4 E_(e, 12) E_(e, 13) L1_(l, p) E_(e, 14) E_(e, 15)
#define LG2_ "s_waitcnt lgkmcnt(2)\n\t"
#define LG0_ "s_waitcnt lgkmcnt(0)\n\t"
#define STEP_(b, nb, kv, wv, wo) \
  "s_waitcnt vmcnt(4)\n\ts_barrier\n\t" \
  HALF_(128, 160, 200, 112, 176, 80, 232, RD_(208, b + 2048), RD_(212, b + 3072), RD_(216, b), RD_(220, b + 1024), "", "") LG2_ \
  HALF_(160, 144, 192, 96, 128, 64, 228, "s_add_u32 m0, s44, " #nb "\n\t", "global_load_lds_dwordx4 v" #kv ", s[40:41] sc1\n\t", "", \
        "global_load_lds_dwordx4 v" #kv ", s[40:41] offset:1024 sc1\n\t", "", LG0_) \
  HALF_(144, 176, 200, 112, 160, 80, 232, RD_(208, b + 6144), RD_(212, b + 7168), RD_(216, b + 4096), RD_(220, b + 5120), "", "") LG2_ \
  HALF_(176, 128, 192, 96, 144, 64, 228, "s_mov_b32 m0, s45\n\t", "global_load_lds_dwordx4 v" #wv ", s[42:43] offset:" #wo "\n\t", "", "", "", LG0_)
DI float max16(const f32x16& s) {
  float mx = s[0];
#pragma unroll
  for (int e = 1; e < 16; ++e) mx = fmaxf(mx, s[e]);
  return mx;
}
typedef __attribute__((ext_vector_type(8))) unsigned u32x8;
typedef __attribute__((ext_vector_type(16))) unsigned u32x16;

__global__ __launch_bounds__(256, 1) void k_main(const char* wqkv, const char* wphi, const char* woA, const char* wq, const char* wk, const char* wv, const char* woB,
                                                 char* ws, char* out) {
  __shared__ __attribute__((aligned(16))) char smem_[163840];
  LAS char* const smem = (LAS char*)smem_;
  const unsigned lds0 = (unsigned)(uintptr_t)smem;
  Ctx c;
  int lane = threadIdx.x & 63, w = __builtin_amdgcn_readfirstlane(threadIdx.x >> 6), r = lane & 31, h = lane >> 5, rb, cb;
  block_map<4>(rb, cb);
  int tt = rb * 4 + w, blk = (lane >> 4) & 1, q4 = (lane & 15) >> 2, p4 = lane & 3;
  c.xv = lane * 16; c.ws = ws;
  { const unsigned a0 = (unsigned)((4 * h + q4) * 128 + (((q4 >> 1) << 2) + 2 * blk + (p4 >> 1)) * 16 + 8 * (p4 & 1));
    c.p0.init(lds0 + a0); c.p1.init(lds0 + (a0 ^ 64u)); }
  const WSrc g6 = wsrc(lane, w, 6144), g2 = wsrc(lane, w, 2048);
  unsigned* flags;
  auto pc_a = [&](int slot, int col, int ch, auto i) { dma<i * 1024>(lds0 + slot * SLOT + w * 4096, g6.v[i], wqkv + cb * 128 + col + (size_t)ch * 128 * 6144); };
  auto pc_kv = [&](auto g, auto i) { pc_a(2 * (g % 4) + i / 4, i < 4 ? 2048 : 4096, g, IC(i % 4)); };
  auto pc_q = [&](auto ch, auto i) { pc_a(slot_q(ch), 0, ch, i); };
  auto pc_w = [&](const char* W, int slot, int ch, auto i) { dma<i * 1024>(lds0 + slot * SLOT + w * 4096, g2.v[i], W + cb * 128 + (size_t)ch * 128 * 2048); };
  sfor<0, 32>([&](auto i) { pc_kv(IC(i / 8), IC(i % 8)); });
  float csk, csv, csq, csb, cse;
  { const float* cs = (const float*)(ws + WS_CS);
    auto ld = [&](int n) { return (cs[n] + cs[CS_N + n]) + (cs[2 * CS_N + n] + cs[3 * CS_N + n]); };
    csq = ld(cb * 64 + lane); csk = ld(1024 + cb * 64 + lane); csv = ld(2048 + cb * 64 + lane); csb = ld(3072 + cb * 64 + lane);
    cse = ld(4096 + (cb & 3) * 16 + (lane & 15)); }
  u32x4 xr[4];
#pragma unroll
  for (int i = 0; i < 4; ++i) xr[i] = *(const u32x4*)(ws + WS_ACT + (size_t)tt * 65536 + cb * 4096 + i * 1024 + lane * 16);
  asm volatile("" : "+v"(csk), "+v"(csv), "+v"(csq), "+v"(csb), "+v"(cse), "+v"(xr[0]), "+v"(xr[1]), "+v"(xr[2]), "+v"(xr[3]));
  char *act, *stt; unsigned* fact;
#define REFRESH() do { \
    asm volatile("" : "+s"(w), "+s"(cb), "+s"(rb), "+s"(ws), "+s"(wqkv), "+s"(wphi), "+s"(woA), "+s"(wq), "+s"(woB)); \
    asm volatile("" : "+v"(lane), "+v"(csk), "+v"(csv), "+v"(csq), "+v"(csb), "+v"(cse)); \
    r = lane & 31; h = lane >> 5; blk = (lane >> 4) & 1; q4 = (lane & 15) >> 2; p4 = lane & 3; tt = rb * 4 + w; \
    flags = (unsigned*)(ws + WS_FLAG); c.xv = lane * 16; \
    act = ws + WS_ACT + (size_t)l * 16 * MB + (size_t)tt * 65536;     \
    fact = flags + FL_ACT + (l * 4 * 64 + tt) * 32;                   \
    stt = ws + WS_ST + (size_t)l * 512 * 1024 + tt * 4096;            \
  } while (0)
  for (int l = 0; l < 2; ++l) {
    REFRESH();
    float mean, rstd;
    STAMP(0);
    {
      if (l > 0) poll(fact, (lane & 15) * 4);
      STAMP(1);
      bf16x8 x[64];
      f32x16 ak0, ak1, av0, av1;
      bf16x8 mfr;
      constexpr int NA = 60;
      auto issue_x = [&](auto s) { ldx1<(s < NA), (s % 8) * 1024 - 4096>(x[s], c.xv, act + (s / 8) * 8192 + 4096); };
      {
        constexpr SchedKV SC;
        f32x2 sp[8];
        sfor<0, 8>([&](auto i) { ld2_1<i * 256>(sp[i], (unsigned)(h * 2048 + r * 8), stt); });
        sfor<0, XL>(issue_x);
        wait_bar<XL + 8>();
        bf16x8 f[4][2];
        auto rd = [&](auto s, auto t) {
          constexpr int off = (2 * ((s / 8) % 4) + t / 2) * SLOT + (s % 8) * 2048;
          if constexpr (t % 2 == 0) f[t][s % 2] = c.p0.template frag<off>(); else f[t][s % 2] = c.p1.template frag<off>();
        };
        sfor<0, 4>([&](auto t) { rd(IC(0), t); });
        sfor<0, 64>([&](auto s) {
          if constexpr ((s + 1) % 8 == 0 && (s + 1) / 8 >= 2 && (s + 1) / 8 < 8) wait_bar<SC.ww[(s + 1) / 8]>();
          waitx<(s < NA), SC.xw[s]>(x[s]);
          SBAR();
          mm<false, (s < NA), s == 0>(ak0, f[0][s % 2], x[s]);
          SBAR();
          if constexpr (s + 1 < 64) rd(IC(s + 1), IC(0));
          if constexpr (a_xt(s - 1) < a_xt(s)) issue_x(IC(a_xt(s - 1)));
          SBAR();
          mm<false, (s < NA), s == 0>(ak1, f[1][s % 2], x[s]);
          SBAR();
          if constexpr (s + 1 < 64) rd(IC(s + 1), IC(1));
          if constexpr (a_xt(s - 1) + 1 < a_xt(s)) issue_x(IC(a_xt(s - 1) + 1));
          SBAR();
          mm<true, (s < NA), s == 0>(av0, f[2][s % 2], x[s]);
          SBAR();
          if constexpr (s + 1 < 64) rd(IC(s + 1), IC(2));
          if constexpr (s >= 15 && s <= 62) {
            constexpr int g = (s + 1) / 8 + 2, i = (s + 1) % 8;
            if constexpr (g < 8) pc_kv(IC(g), IC(i)); else pc_q(IC(2 * (g - 8) + 1 + i / 4), IC(i % 4));
          }
          SBAR();
          mm<true, (s < NA), s == 0>(av1, f[3][s % 2], x[s]);
          SBAR();
          if constexpr (s + 1 < 64) rd(IC(s + 1), IC(3));
          if constexpr (s >= 52 && s < 56) pc_q(IC(0), IC(s - 52));
          if constexpr (s == 56) dma<0>(lds0 + 9 * SLOT + w * 1024, c.xv, wphi + cb * 4096 + w * 1024);
          if constexpr (s == 4) { pin8(sp); ln_stats(sp, mean, rstd); mfr = mean_frag(mean); }
          if constexpr (s == 8) { mm<false, false, false, true>(ak0, cs_frag(csk, h == 0), mfr); mm<false, false, false, true>(ak1, cs_frag(csk, h == 1), mfr); }
          if constexpr (s == 10) { mm<true, false, false, true>(av0, cs_frag(csv, h == 0), mfr); mm<true, false, false, true>(av1, cs_frag(csv, h == 1), mfr); }
          SBAR();
        });
        mdone(ak0, ak1, av0, av1);
        wait_bar<6>();
      }
      STAMP(2);
      REFRESH();
      sfor<0, 4>([&](auto i) { pc_q(IC(5), i); });
      LAS char* pk_img = smem + 4 * SLOT;
      LAS char* v_img = smem + 6 * SLOT;
      char* kvp = ws + WS_KVP + (size_t)l * 2 * MB + (size_t)(cb * 4 + w) * 32768;
      char* zp = ws + WS_ZP + (size_t)l * 131072 + (cb * 4 + w) * 2048;
      unsigned* fkvp = flags + FL_KVP + (l * 64 + cb * 4 + w) * 32;
      const float sce = rstd * LOG2E;
      auto phi_mm = [&](const f32x16& t0, const f32x16& t1, f32x16& p0, f32x16& p1) {
        LAS const char* wp = smem + 9 * SLOT + (4 * h + q4) * 64 + blk * 32 + p4 * 8;
        const bf16x8 w00 = cat8(trread(wp), trread(wp + 512)), w01 = cat8(trread(wp + 1024), trread(wp + 1536));
        const bf16x8 w10 = cat8(trread(wp + 2048), trread(wp + 2560)), w11 = cat8(trread(wp + 3072), trread(wp + 3584));
        mm<false, false, true, true>(p0, w00, pack8<0>(t0)); mm<false, false, false, true>(p0, w01, pack8<1>(t0));
        mm<false, false, true, true>(p1, w10, pack8<0>(t1)); mm<false, false, false, true>(p1, w11, pack8<1>(t1));
      };
      {
        f32x16 p[2];
        phi_mm(ak0, ak1, p[0], p[1]);
        {
          LAS char* d = v_img + w * 2048 + lane * 16;
#pragma unroll
          for (int i = 0; i < 16; ++i) { const float rs = lane_get(rstd, (i & 3) + 8 * (i >> 2) + 4 * h); av0[i] *= rs; av1[i] *= rs; }
          *(LAS bf16x8*)d = pack8<0>(av0); *(LAS bf16x8*)(d + 1024) = pack8<1>(av0);
          *(LAS bf16x8*)(d + 8192) = pack8<0>(av1); *(LAS bf16x8*)(d + 9216) = pack8<1>(av1);
        }
        mdone(p[0], p[1]);
        sfor<0, 2>([&](auto hd) {
          f32x16 ep, en; hedge_exp(p[hd], sce, ep, en);
          float sp = 0.f, sn = 0.f;
#pragma unroll
          for (int i = 0; i < 16; ++i) { sp += ep[i]; sn += en[i]; }
          sp = __builtin_amdgcn_rcpf(swap_add(sp)); sn = __builtin_amdgcn_rcpf(swap_add(sn));
          LAS char* row = pk_img + hd * 16384 + (w * 32 + r) * 128 + h * 8;
#pragma unroll
          for (int gq = 0; gq < 4; ++gq) {
            *(LAS u32x2*)(row + ((gq ^ (r & 7)) << 4)) = u32x2{cvtpk(ep[4 * gq] * sp, ep[4 * gq + 1] * sp), cvtpk(ep[4 * gq + 2] * sp, ep[4 * gq + 3] * sp)};
            *(LAS u32x2*)(row + (((4 + gq) ^ (r & 7)) << 4)) = u32x2{cvtpk(en[4 * gq] * sn, en[4 * gq + 1] * sn), cvtpk(en[4 * gq + 2] * sn, en[4 * gq + 3] * sn)};
          }
        });
      }
      all_bar();
      {
        const int hd = w >> 1, fh = w & 1;
        f32x16 kv, z;
        const unsigned one2 = opq_v(0x3f803f80u);
        const bf16x8 ones = __builtin_bit_cast(bf16x8, u32x4{one2, one2, one2, one2});
        const int ch = fh * 4 + 2 * blk + (p4 >> 1), r0 = 4 * h + q4;
        LAS const char* pa = pk_img + hd * 16384 + r0 * 128 + ((ch ^ r0) << 4) + 8 * (p4 & 1);
        LAS const char* pb = v_img + hd * 8192 + lane * 16;
        bf16x8 a[8], b[8];
#pragma unroll
        for (int s = 0; s < 8; ++s) { a[s] = cat8(trread(pa + s * 2048), trread(pa + s * 2048 + 1024)); b[s] = *(LAS const bf16x8*)(pb + s * 1024); }
        SBAR();
        sfor<0, 8>([&](auto s) { mm<false, false, s == 0>(kv, a[s], b[s]); mm<false, false, s == 0, s == 0>(z, ones, a[s]); });
        mdone(kv, z);
        st4_1<0>(__builtin_bit_cast(u32x4, pack8<0>(kv)), c.xv, kvp + rb * 2048);
        st4_1<1024>(__builtin_bit_cast(u32x4, pack8<1>(kv)), c.xv, kvp + rb * 2048);
        st1_1(z[0], (unsigned)(r * 4), zp + rb * 128);
      }
      STAMP(3);
      f32x16 aq0, aq1;
      bf16x8 pp[32], zq[2];
      constexpr SchedQ SQ;
      {
        bf16x8 f0[3], f1[3];
        unsigned fv;
        auto off = [](int s) constexpr { return slot_q(s / 8) * SLOT + (s % 8) * 2048; };
        auto rd0 = [&](auto s) { f0[s % 3] = c.p0.template frag<off(s)>(); };
        auto rd1 = [&](auto s) { f1[s % 3] = c.p1.template frag<off(s)>(); };
        auto ldp = [&](auto i) {
          if constexpr (i < 32) ldx1<true, (i % 8) * 1024 - 4096>(pp[i], c.xv, kvp + (i / 8) * 8192 + 4096); else ldx1<false, (i - 32) * 1024>(zq[i - 32], c.xv, zp);
        };
        rd0(IC(0)); rd1(IC(0)); rd0(IC(1)); rd1(IC(1));
        sfor<0, 64>([&](auto s) {
          if constexpr ((s + 2) % 8 == 0 && (s + 2) / 8 < 8) {
            wait_bar<SQ.ww[(s + 2) / 8]>();
            if constexpr ((s + 2) / 8 == 2) set_flag(fkvp + rb);
          }
          if constexpr (s == 46) { waitx<false, SQ.pw>(fv); if (!__all(fv == 1u)) poll(fkvp, (lane & 15) * 4); }
          SBAR();
          mm<false, (s < NA), s == 0>(aq0, f0[s % 3], x[s]);
          SBAR();
          if constexpr (s + 2 < 64) rd0(IC(s + 2));
          if constexpr (q_nd(s)) {
            if constexpr (s < 14) pc_q(IC(6 + (s - 6) / 4), IC((s - 6) % 4)); else pc_w(woA, slot_a((s - 14) / 8), (s - 14) / 8, IC((s - 14) % 8));
          }
          if constexpr (s == 36) ld1_1(fv, (lane & 15) * 4, fkvp);
          if constexpr (s >= 46 && s < 63) ldp(IC(2 * (s - 46)));
          SBAR();
          mm<false, (s < NA), s == 0>(aq1, f1[s % 3], x[s]);
          SBAR();
          if constexpr (s + 2 < 64) rd1(IC(s + 2));
          if constexpr (s >= 46 && s < 63) ldp(IC(2 * (s - 46) + 1));
          if constexpr (s == 2) { mm<false, false, false, true>(aq0, cs_frag(csq, h == 0), mfr); mm<false, false, false, true>(aq1, cs_frag(csq, h == 1), mfr); }
          SBAR();
        });
        mdone(aq0, aq1);
        bar();
      }
      STAMP(4);
      REFRESH();
      sfor<0, 16>([&](auto i) { pc_w(woA, slot_a(4 + i / 4), 4 + i / 4, IC(i % 4)); });
      bf16x8 pq[2][4];
      {
        f32x16 p[2];
        phi_mm(aq0, aq1, p[0], p[1]);
        mdone(p[0], p[1]);
        sfor<0, 2>([&](auto hd) {
          f32x16 ep, en; hedge_exp(p[hd], sce, ep, en);
          pq[hd][0] = pack8<0>(ep); pq[hd][1] = pack8<1>(ep); pq[hd][2] = pack8<0>(en); pq[hd][3] = pack8<1>(en);
        });
        asm volatile("" : "+v"(pq[0][0]), "+v"(pq[0][1]), "+v"(pq[0][2]), "+v"(pq[0][3]), "+v"(pq[1][0]), "+v"(pq[1][1]), "+v"(pq[1][2]), "+v"(pq[1][3]));
      }
      STAMP(5);
      {
        u32x4 s0, s1;
#pragma unroll
        for (int e = 0; e < 4; ++e) {
          const int kr = 8 * (e >> 1) + 4 * h + 2 * (e & 1), rr = r & 15;
          const unsigned v = rr == kr ? 0x3f80u : (rr == kr + 1 ? 0x3f800000u : 0u);
          s0[e] = r < 16 ? v : 0u; s1[e] = r < 16 ? 0u : v;
        }
        const bf16x8 sel0 = __builtin_bit_cast(bf16x8, s0), sel1 = __builtin_bit_cast(bf16x8, s1);
        f32x16 kv;
        sfor<0, 32>([&](auto i) { waitx<true, SQ.lw[i]>(pp[i]); mm<false, true, i == 0, i < 2>(kv, (i & 1) ? sel1 : sel0, pp[i]); });
        asm volatile("s_waitcnt vmcnt(%2)" : "+v"(zq[0]), "+v"(zq[1]) : "i"(SQ.lw[33]));
        f32x4 zs = __builtin_bit_cast(f32x4, zq[0]) + __builtin_bit_cast(f32x4, zq[1]);
#pragma unroll
        for (int e = 0; e < 4; ++e) { zs[e] += lane_get(zs[e], lane ^ 8); zs[e] += lane_get(zs[e], lane ^ 16); zs[e] = swap_add(zs[e]); }
        LAS char* sh = smem + 9 * SLOT + 4096;
        if (lane < 8) *(LAS u32x2*)(sh + 8192 + w * 64 + (lane & 1) * 32 + (lane >> 2) * 16 + ((lane >> 1) & 1) * 8) = u32x2{cvtpk(zs[0], zs[1]), cvtpk(zs[2], zs[3])};
        else if (lane < 16) *(LAS u32x2*)(sh + 8192 + 256 + w * 64 + (lane & 7) * 8) = u32x2{0x3f803f80u, 0x3f803f80u};
        mdone(kv);
        *(LAS bf16x8*)(sh + w * 2048 + lane * 16) = pack8<0>(kv); *(LAS bf16x8*)(sh + w * 2048 + 1024 + lane * 16) = pack8<1>(kv);
        lds_bar();
        STAMP(6);
        char* dst = act + 4 * MB + cb * 4096;
        LAS const char* zb = sh + 8192 + (r & 1) * 256 + h * 32;
        sfor<0, 2>([&](auto hd) {
          bf16x8 ka[4], za[4];
#pragma unroll
          for (int k = 0; k < 4; ++k) { ka[k] = *(LAS const bf16x8*)(sh + (hd * 4 + k) * 1024 + lane * 16); za[k] = *(LAS const bf16x8*)(zb + (hd * 2 + (k >> 1)) * 64 + (k & 1) * 16); }
          f32x16 nm[2], dn[2];
          sfor<0, 4>([&](auto k) { mm<false, false, k % 2 == 0>(dn[k / 2], za[k], pq[hd][k]); });
          sfor<0, 4>([&](auto k) { mm<false, false, k % 2 == 0>(nm[k / 2], ka[k], pq[hd][k]); });
          mdone(nm[0], nm[1], dn[0], dn[1]);
          const float a = __builtin_amdgcn_rcpf(dn[0][1]), b = __builtin_amdgcn_rcpf(dn[1][1]);
          const float inv = __builtin_amdgcn_rcpf(dn[0][0] * a + dn[1][0] * b + 1e-6f);
          const float ai = a * inv, bi = b * inv;
          f32x16 o;
#pragma unroll
          for (int i = 0; i < 16; ++i) o[i] = nm[0][i] * ai + nm[1][i] * bi;
          st4_1<hd * 2048>(__builtin_bit_cast(u32x4, pack8<0>(o)), c.xv, dst);
          st4_1<hd * 2048 + 1024>(__builtin_bit_cast(u32x4, pack8<1>(o)), c.xv, dst);
        });
        waitv<0>();
        set_flag(fact + 2048 + cb);
      }
    }
    STAMP(7);
    REFRESH();
    {
      f32x16 a0, a1; res_to_acc(xr, a0, a1);
      poll(fact + 2048, (lane & 15) * 4);
      STAMP(8);
      bf16x8 x[64];
      const char* xb = act + 4 * MB;
      sfor<0, XL>([&](auto s) { ld_x<s>(x, c, xb); });
      bar();
      const char* wkv = ((cb & 2) ? wv : wk) + w * 256 * 64;
      const unsigned kvv = (unsigned)((lane >> 1) * 64 + (cb & 1) * 32 + (lane & 1) * 16);
      gemm64<true, false, HookTail>(c, x, xb, a0, a1, [&](auto s) {
        if constexpr (s == 33) bar();
        if constexpr (s >= 52 && s < 60) dma<0>(lds0 + 8 * SLOT + w * 8192 + (s - 52) * 1024, kvv, wkv + (s - 52) * 32 * 64);
        if constexpr (s >= 60) pc_w(wq, slot_a(0), 0, IC(s - 60));
      });
      STAMP(9);
      store_x(c, r, a0, a1, xr, act + 8 * MB + cb * 4096, stt + 256 * 1024 + cb * 256);
      sfor<4, 12>([&](auto i) { pc_w(wq, slot_a(i / 4), i / 4, IC(i % 4)); });
      waitv<8>();
      set_flag(fact + 2 * 2048 + cb);
      bar();
      sfor<12, 32>([&](auto i) { pc_w(wq, slot_a(i / 4), i / 4, IC(i % 4)); });
    }
    STAMP(10);
    REFRESH();
    u32x16 qq;
    char* kvf = ws + WS_KVF + (size_t)l * 512 * 1024;
    bf16x8 k0, k1;
    {
      unsigned* fkv = flags + FL_KV + l * 512;
      const int du = (cb >> 2) - w, ty = cb & 3;
      poll(fact + 2 * 2048 - w * 32, (unsigned)((lane & 15) * 4 + (blk ? cb >> 2 : w) * 128));
      STAMP(11);
      const char* xb = act + 8 * MB; const char* stp = stt + 256 * 1024;
      bf16x8 x[64], xu[16], fu[16];
      f32x2 su[8], sp[8];
      sfor<0, 16>([&](auto i) { ldx1<true, (i % 8) * 1024 - 4096>(xu[i], c.xv, xb + (ptrdiff_t)du * 65536 + w * 16384 + (i / 8) * 8192 + 4096); });
      sfor<0, 8>([&](auto i) { ld2_1<i * 256>(su[i], (unsigned)(h * 2048 + r * 8), stp + du * 4096); });
      sfor<0, 8>([&](auto i) { ld2_1<i * 256>(sp[i], (unsigned)(h * 2048 + r * 8), stp); });
      sfor<0, XL>([&](auto s) { ld_x<s>(x, c, xb); });
      LAS char* reg = smem + 8 * SLOT;
      { LAS const char* p = reg + w * 8192 + (4 * h + q4) * 32 + p4 * 8;
#pragma unroll
        for (int i = 0; i < 16; ++i) fu[i] = cat8(trread(p + i * 512), trread(p + i * 512 + 256)); }
      f32x16 au, av;
      sfor<0, 16>([&](auto i) { waitx<true, 15 - i + 16 + XL>(xu[i]); mm<false, true, i == 0>(au, fu[i], xu[i]); mm<true, true, i == 0>(av, fu[i], xu[i]); });
      float mu, ru;
      waitv<8 + XL>(); pin8(su); ln_stats(su, mu, ru);
      { const bf16x8 cf = cs_frag(cse, h == 0 && w == 0), mf = mean_frag(mu);
        mm<false, false, false, true>(au, cf, mf); mm<true, false, false>(av, cf, mf); }
      mdone(au, av);
      if (ty >= 2) au = av;
#pragma unroll
      for (int gq = 0; gq < 4; ++gq) *(LAS f32x4*)(reg + w * 8192 + gq * 1024 + lane * 16) = f32x4{au[4 * gq], au[4 * gq + 1], au[4 * gq + 2], au[4 * gq + 3]};
      lds_bar();
      if (w < (ty < 2 ? 1 : 2)) {
        f32x4 lo = {}, hi = {};
#pragma unroll
        for (int j = 0; j < 4; ++j) { lo += *(LAS const f32x4*)(reg + j * 8192 + w * 2048 + lane * 16); hi += *(LAS const f32x4*)(reg + j * 8192 + w * 2048 + 1024 + lane * 16); }
        char* dst = kvf + (tt + du) * 4096;
        if (ty < 2) {
          lo *= ru; hi *= ru;
          st4_1<0>(u32x4{cvtpk(lo[0], lo[1]), cvtpk(lo[2], lo[3]), cvtpk(hi[0], hi[1]), cvtpk(hi[2], hi[3])}, c.xv, dst + ty * 1024);
        } else {
#pragma unroll
          for (int e = 0; e < 4; ++e) { lo[e] *= lane_get(ru, 16 * w + 4 * h + e); hi[e] *= lane_get(ru, 16 * w + 8 + 4 * h + e); }
          const unsigned vo = (unsigned)(((ty & 1) * 16 + (r & 15) + 32 * h) * 16);
          st4_1<2048>(unperm(u32x4{cvtpk(lo[0], lo[1]), cvtpk(lo[2], lo[3]), cvtpk(hi[0], hi[1]), cvtpk(hi[2], hi[3])}), vo, dst + w * 1024);
        }
      }
      STAMP(12);
      f32x16 a0, a1;
      bf16x8 fa, fb;
      gemm64<true, true, HookQ>(c, x, xb, a0, a1, [&](auto s) {
        if constexpr (s == 2) { pin8(sp); ln_stats(sp, mean, rstd); }
        if constexpr (s == 6) { const bf16x8 mf = mean_frag(mean); mm<false, false, false, true>(a0, cs_frag(csb, h == 0), mf); mm<false, false, false, true>(a1, cs_frag(csb, h == 1), mf); }
        if constexpr (s == 16) { if (w < 2) set_flag(fkv + (tt + du) * 8 + ty * 2 + w); }
        if constexpr (s == 33) bar();
        if constexpr (s == 52) { ldx1<false, 0>(fa, c.xv, (const char*)fkv); ldx1<false, 1024>(fb, c.xv, (const char*)fkv); }
      });
      STAMP(13);
      asm volatile("s_waitcnt vmcnt(0)" : "+v"(fa), "+v"(fb)::"memory");
      for (int spin = 0; spin < (1 << 15) && !__all(ones8(fa, fb)); ++spin) {
        __builtin_amdgcn_s_sleep(4);
        ldx1<false, 0>(fa, c.xv, (const char*)fkv); ldx1<false, 1024>(fb, c.xv, (const char*)fkv);
        asm volatile("s_waitcnt vmcnt(0)" : "+v"(fa), "+v"(fb)::"memory");
      }
      const float sc = rstd * QSCALE;
      ldx1<false, 0>(k0, c.xv, kvf); ldx1<false, 1024>(k1, c.xv, kvf);
      sfor<0, 6>([&](auto i) { dma_c<(i % 2) * 1024>(lds0 + 8 * SLOT + w * 2048 + (i / 2) * 8192, c.xv, kvf + 2048 + w * 2048 + (i / 2) * 8192); });
#pragma unroll
      for (int i = 0; i < 8; ++i) { qq[i] = cvtpk(a0[2 * i] * sc, a0[2 * i + 1] * sc); qq[8 + i] = cvtpk(a1[2 * i] * sc, a1[2 * i + 1] * sc); }
      asm volatile("s_waitcnt vmcnt(6)" : "+v"(k0), "+v"(k1), "+v"(qq));
    }
    STAMP(14);
    REFRESH();
    {
      const bf16x8 qa0 = __builtin_bit_cast(bf16x8, __builtin_shufflevector(qq, qq, 0, 1, 2, 3)), qa1 = __builtin_bit_cast(bf16x8, __builtin_shufflevector(qq, qq, 4, 5, 6, 7));
      const bf16x8 qb0 = __builtin_bit_cast(bf16x8, __builtin_shufflevector(qq, qq, 8, 9, 10, 11)), qb1 = __builtin_bit_cast(bf16x8, __builtin_shufflevector(qq, qq, 12, 13, 14, 15));
      float mA = 0.f, mB = 0.f;
      const char* ksrc = kvf + 2048 + w * 2048;
      const unsigned ring = lds0 + 8 * SLOT + w * 2048;
      for (int attempt = 0; attempt < 2; ++attempt) {
        f32x16 sa0, sb;
        if (attempt == 1) {
          mA = -INFINITY; mB = -INFINITY;
          for (int t = 0; t < 64; ++t) {
            ldx1<false, 0>(k0, c.xv, kvf + t * 4096); ldx1<false, 1024>(k1, c.xv, kvf + t * 4096);
            asm volatile("s_waitcnt vmcnt(0)" : "+v"(k0), "+v"(k1));
            mm<false, false, true>(sa0, k0, qa0); mm<false, false, false>(sa0, k1, qa1); mm<false, false, true>(sb, k0, qb0); mm<false, false, false>(sb, k1, qb1);
            mdone(sa0, sb);
            mA = fmaxf(mA, max16(sa0)); mB = fmaxf(mB, max16(sb));
          }
          mA = swap_max(mA); mB = swap_max(mB);
          ldx1<false, 0>(k0, c.xv, kvf); ldx1<false, 1024>(k1, c.xv, kvf);
          sfor<0, 6>([&](auto i) { dma_c<(i % 2) * 1024>(ring + (i / 2) * 8192, c.xv, ksrc + (i / 2) * 8192); });
          asm volatile("s_waitcnt vmcnt(6)" : "+v"(k0), "+v"(k1));
        }
        mm<false, false, true, true>(sa0, k0, qa0); mm<false, false, false>(sa0, k1, qa1); mm<false, false, true>(sb, k0, qb0); mm<false, false, false>(sb, k1, qb1);
        mdone(sa0, sb);
        if (attempt == 0) { mA = swap_max(max16(sa0)); mB = swap_max(max16(sb)); }
        f32x16 nma, nmb, oa = {}, ob = {}, sb1 = {}, t0, t1;
#pragma unroll
        for (int e = 0; e < 16; ++e) { nma[e] = -mA; nmb[e] = -mB; sa0[e] -= mA; }
        const unsigned s0 = ((lane >> 2) & 1) ? 0u : 0x3f803f80u, s1 = 0x3f803f80u - s0;
        u32x16 kvr = {}, misc;
        { const u32x4 a = __builtin_bit_cast(u32x4, k0), b = __builtin_bit_cast(u32x4, k1);
#pragma unroll
          for (int e = 0; e < 4; ++e) { kvr[e] = a[e]; kvr[4 + e] = b[e]; } }
        misc = u32x16{s0, s0, s1, s1, 0u, 0u, 0u, 0u, 0u, 0u, 0u, 0u, 0xddddddddu, lds0 + 8 * SLOT + c.xv, 0u, 0u};
        const u32x8 offs = {c.xv, c.xv + 8192, c.xv + 16384, c.xv + 24576, g2.v[0], g2.v[1], g2.v[2], g2.v[3]};
        const char* ks = ksrc + 3 * 8192; const char* wsr = woB + cb * 128;
        unsigned wm0 = lds0 + w * 4096, cnt = 8;
        asm volatile(
            "s_nop 1\n\t"
            "1:\n\t"
            STEP_(0, 24576, 240, 244, 0) STEP_(8192, 0, 241, 245, 1024) STEP_(16384, 8192, 242, 246, 2048) STEP_(24576, 16384, 243, 247, 3072)
            "s_add_u32 s40, s40, 0x8000\n\ts_addc_u32 s41, s41, 0\n\ts_add_u32 s42, s42, 0x40000\n\ts_addc_u32 s43, s43, 0\n\t"
            "s_add_u32 s45, s45, 0x4000\n\ts_sub_u32 %[cnt], %[cnt], 1\n\ts_cmp_lg_u32 %[cnt], 0\n\ts_cbranch_scc1 1b\n\t"
            PV0_(80, 176) PV1_(80, 176) L0_(232, 176) L1_(232, 176)
            "s_nop 15\n\t"
            : "+{v[64:79]}"(oa), "+{v[80:95]}"(ob), "+{v[128:143]}"(sa0), "=&{v[144:159]}"(t0), "=&{v[160:175]}"(t1), "+{v[176:191]}"(sb1),
              "+{v[208:223]}"(kvr), "+{v[224:239]}"(misc), "+{s[40:41]}"(ks), "+{s[42:43]}"(wsr), "+{s45}"(wm0), [cnt] "+s"(cnt)
            : "{v[96:111]}"(nma), "{v[112:127]}"(nmb), "{v[192:207]}"(qq), "{v[240:247]}"(offs), "{s44}"(ring)
            : "scc", "memory");
        const float la = __uint_as_float(misc[4]), lb = __uint_as_float(misc[8]);
        const float ia = __builtin_amdgcn_rcpf(la), ib = __builtin_amdgcn_rcpf(lb);
#pragma unroll
        for (int e = 0; e < 16; ++e) { oa[e] *= ia; ob[e] *= ib; }
        char* dst = act + 12 * MB + cb * 4096;
        st4_1<0>(__builtin_bit_cast(u32x4, pack8<0>(oa)), c.xv, dst); st4_1<1024>(__builtin_bit_cast(u32x4, pack8<1>(oa)), c.xv, dst);
        st4_1<2048>(__builtin_bit_cast(u32x4, pack8<0>(ob)), c.xv, dst); st4_1<3072>(__builtin_bit_cast(u32x4, pack8<1>(ob)), c.xv, dst);
        float chk = 0.f;
#pragma unroll
        for (int e = 0; e < 16; ++e) chk = __builtin_fmaf(oa[e], 0.f, __builtin_fmaf(ob[e], 0.f, chk));
        const bool bad = !(la < 1.2676506e30f) || !(lb < 1.2676506e30f) || !(chk == 0.f);
        wait_bar<4>();
        LAS volatile int* vote = (LAS volatile int*)(smem + 8 * SLOT);
        if (lane == 0) vote[w] = 0;
        if (attempt == 0 && bad) vote[w] = 1;
        lds_bar();
        const int redo = vote[0] | vote[1] | vote[2] | vote[3];
        if (!__builtin_amdgcn_readfirstlane(redo)) break;
        lds_bar();
      }
      waitv<0>();
      set_flag(fact + 3 * 2048 + cb);
    }
    STAMP(15);
    REFRESH();
    {
      f32x16 a0, a1; res_to_acc(xr, a0, a1);
      poll(fact + 3 * 2048, (lane & 15) * 4);
      STAMP(16);
      bf16x8 x[64];
      const char* xb = act + 12 * MB;
      sfor<0, XL>([&](auto s) { ld_x<s>(x, c, xb); });
      bar();
      gemm64<false, false, HookTail>(c, x, xb, a0, a1, [&](auto s) {
        if constexpr (s == 33) bar();
        if constexpr (s >= 52) pc_kv(IC((s - 52) / 8), IC((s - 52) % 8));
      });
      STAMP(17);
      if (l == 0) {
        store_x(c, r, a0, a1, xr, act + 16 * MB + cb * 4096, stt + 512 * 1024 + cb * 256);
        sfor<12, 16>([&](auto i) { pc_kv(IC(i / 8), IC(i % 8)); });
        waitv<4>();
        set_flag(fact + 4 * 2048 + cb);
        bar();
        sfor<16, 32>([&](auto i) { pc_kv(IC(i / 8), IC(i % 8)); });
      } else {
        char* dst = out + (size_t)(tt * 32 + r) * 2048 + cb * 128 + h * 16;
        *(u32x4*)(dst) = unperm(__builtin_bit_cast(u32x4, pack8<0>(a0))); *(u32x4*)(dst + 32) = unperm(__builtin_bit_cast(u32x4, pack8<1>(a0)));
        *(u32x4*)(dst + 64) = unperm(__builtin_bit_cast(u32x4, pack8<0>(a1))); *(u32x4*)(dst + 96) = unperm(__builtin_bit_cast(u32x4, pack8<1>(a1)));
      }
    }
    STAMP(18);
  }
}

extern "C" void kernel_launch(void* const* d_in, const int* in_sizes, int n_in, void* d_out, int out_size, void* d_ws, size_t ws_size, hipStream_t stream) {
  char* ws = (char*)d_ws;
  k_prep<<<256 + 65 * 4, 256, 0, stream>>>((const char*)d_in[0], (const char*)d_in[1], (const char*)d_in[5], (const char*)d_in[6], (const char*)d_in[7], ws);
  k_main<<<256, 256, 0, stream>>>((const char*)d_in[1], (const char*)d_in[2], (const char*)d_in[3], (const char*)d_in[5], (const char*)d_in[6], (const char*)d_in[7],
                                  (const char*)d_in[8], ws, (char*)d_out);
}
```

```cpp
#include <hip/hip_runtime.h>
#include <hip/hip_bf16.h>
#include <stdint.h>
#include <type_traits>

typedef __attribute__((ext_vector_type(8))) short bf16x8;
typedef __attribute__((ext_vector_type(4))) short s16x4;
typedef __attribute__((ext_vector_type(2))) __bf16 bf2_t;
typedef __attribute__((ext_vector_type(16))) float f32x16;
typedef __attribute__((ext_vector_type(4))) float f32x4;
typedef __attribute__((ext_vector_type(2))) float f32x2;
typedef __attribute__((ext_vector_type(4))) unsigned u32x4;
typedef __attribute__((ext_vector_type(2))) unsigned u32x2;
#define LAS __attribute__((address_space(3)))
#define DI __device__ __forceinline__
#define MFMA32(a, b, c) __builtin_amdgcn_mfma_f32_32x32x16_bf16((a), (b), (c), 0, 0, 0)
#define MFMA16(a, b, c) __builtin_amdgcn_mfma_f32_16x16x32_bf16((a), (b), (c), 0, 0, 0)
#define SBAR() __builtin_amdgcn_sched_barrier(0)

constexpr int S_ = 2048, D_ = 1024;
constexpr float LOG2E = 1.4426950408889634f;
constexpr float QSCALE = 0.17677669529663687f * LOG2E;

constexpr size_t MB = 1u << 20, KB = 1024;
constexpr size_t STG = 256 * KB;
constexpr size_t WS_COL = 32 * MB;
constexpr size_t C_WT = 0;
constexpr size_t C_KVP = 768 * KB;
constexpr size_t C_ZP = 1024 * KB;
constexpr size_t C_CS = 1040 * KB;
constexpr size_t WS_ST = 64 * MB;
constexpr size_t WS_KVF = WS_ST + 1024 * KB;
constexpr size_t WS_FLAG = WS_ST + 1664 * KB;
constexpr size_t WS_EPOCH = WS_ST + 1920 * KB;
constexpr int FL_ACT = 0, FL_KVP = 8 * 64 * 128, FL_KV = FL_KVP + 2 * 64 * 128, FL_W = FL_KV + 2 * 2048;
typedef unsigned long long u64;

template <int I, int N, class F> DI void sfor(F&& f) { if constexpr (I < N) { f(std::integral_constant<int, I>{}); sfor<I + 1, N>(f); } }

DI unsigned cvtpk(float lo, float hi) { f32x2 v = {lo, hi}; return __builtin_bit_cast(unsigned, __builtin_convertvector(v, bf2_t)); }
DI float bflo(unsigned u) { return __uint_as_float(u << 16); }
DI float bfhi(unsigned u) { return __uint_as_float(u & 0xffff0000u); }
DI float rbf(float x) { return bflo(cvtpk(x, x)); }
DI float swap_add(float v) { auto r = __builtin_amdgcn_permlane32_swap(__float_as_uint(v), __float_as_uint(v), false, false); return __uint_as_float(r[0]) + __uint_as_float(r[1]); }
DI float swap_max(float v) { auto r = __builtin_amdgcn_permlane32_swap(__float_as_uint(v), __float_as_uint(v), false, false); return fmaxf(__uint_as_float(r[0]), __uint_as_float(r[1])); }
DI float swap_min(float v) { auto r = __builtin_amdgcn_permlane32_swap(__float_as_uint(v), __float_as_uint(v), false, false); return fminf(__uint_as_float(r[0]), __uint_as_float(r[1])); }
template <int S> DI bf16x8 pack8(const f32x16& v) {
  u32x4 p = {cvtpk(v[8 * S], v[8 * S + 1]), cvtpk(v[8 * S + 2], v[8 * S + 3]), cvtpk(v[8 * S + 4], v[8 * S + 5]), cvtpk(v[8 * S + 6], v[8 * S + 7])};
  return __builtin_bit_cast(bf16x8, p);
}
DI u32x4 unperm(u32x4 v) {
  auto r0 = __builtin_amdgcn_permlane32_swap(v[0], v[2], false, false);
  auto r1 = __builtin_amdgcn_permlane32_swap(v[1], v[3], false, false);
  return u32x4{r0[0], r1[0], r0[1], r1[1]};
}
DI float lane_get(float v, int src) { return __uint_as_float(__builtin_amdgcn_ds_bpermute(src << 2, __float_as_uint(v))); }
DI s16x4 trread(LAS const char* p) { return __builtin_amdgcn_ds_read_tr16_b64_v4i16((LAS s16x4*)p); }
DI bf16x8 cat8(s16x4 a, s16x4 b) { return __builtin_shufflevector(a, b, 0, 1, 2, 3, 4, 5, 6, 7); }
DI float dot2(unsigned a, unsigned b, float c) { return __builtin_amdgcn_fdot2_f32_bf16(__builtin_bit_cast(bf2_t, a), __builtin_bit_cast(bf2_t, b), c, false); }

template <int OFF> DI void dma(unsigned lds, unsigned v, const char* sb) {
  asm volatile("s_mov_b32 m0, %0\n\ts_nop 0\n\tglobal_load_lds_dwordx4 %1, %2 offset:%3" ::"s"(lds), "v"(v), "s"(sb), "i"(OFF) : "memory");
}
template <int OFF> DI void dma_c(unsigned lds, unsigned v, const char* sb) {
  asm volatile("s_mov_b32 m0, %0\n\ts_nop 0\n\tglobal_load_lds_dwordx4 %1, %2 offset:%3 sc1" ::"s"(lds), "v"(v), "s"(sb), "i"(OFF) : "memory");
}
constexpr int cap63(int n) { return n > 63 ? 63 : n; }
template <int N> DI void wait_bar() { asm volatile("s_waitcnt vmcnt(%0)\n\ts_barrier" ::"i"(cap63(N)) : "memory"); }
DI void lds_bar() { asm volatile("s_waitcnt lgkmcnt(0)\n\ts_barrier" ::: "memory"); }
DI void all_bar() { asm volatile("s_waitcnt vmcnt(0) lgkmcnt(0)\n\ts_barrier" ::: "memory"); }
DI void bar() { asm volatile("s_barrier" ::: "memory"); }
template <bool AG, int OFF> DI void ldx1(bf16x8& d, unsigned voff, const char* sb) {
  if constexpr (AG) asm volatile("global_load_dwordx4 %0, %1, %2 offset:%3 sc1" : "=a"(d) : "v"(voff), "s"(sb), "i"(OFF));
  else asm volatile("global_load_dwordx4 %0, %1, %2 offset:%3 sc1" : "=v"(d) : "v"(voff), "s"(sb), "i"(OFF));
}
template <int OFF> DI void ld2_1(f32x2& d, unsigned voff, const char* sb) { asm volatile("global_load_dwordx2 %0, %1, %2 offset:%3 sc1" : "=v"(d) : "v"(voff), "s"(sb), "i"(OFF)); }
DI void ld1_1(unsigned& d, unsigned voff, const void* sb) { asm volatile("global_load_dword %0, %1, %2 sc1" : "=v"(d) : "v"(voff), "s"(sb) : "memory"); }
template <int OFF> DI void st4_1(u32x4 d, unsigned voff, char* sb) { asm volatile("global_store_dwordx4 %0, %1, %2 offset:%3 sc1\n\ts_nop 1" ::"v"(voff), "v"(d), "s"(sb), "i"(OFF) : "memory"); }
DI void st2_1(f32x2 d, unsigned voff, char* sb) { asm volatile("global_store_dwordx2 %0, %1, %2 sc1" ::"v"(voff), "v"(d), "s"(sb) : "memory"); }
DI void st1_1(float d, unsigned voff, char* sb) { asm volatile("global_store_dword %0, %1, %2 sc1" ::"v"(voff), "v"(d), "s"(sb) : "memory"); }
template <int N> DI void waitv() { asm volatile("s_waitcnt vmcnt(%0)" ::"i"(cap63(N)) : "memory"); }
template <bool AG, int N, class T> DI void waitx(T& d) {
  if constexpr (AG) asm volatile("s_waitcnt vmcnt(%1)" : "+a"(d) : "i"(cap63(N))); else asm volatile("s_waitcnt vmcnt(%1)" : "+v"(d) : "i"(cap63(N)));
}
template <bool SW, bool BA, bool Z, bool NOP = false> DI void mm(f32x16& acc, const bf16x8& a, const bf16x8& b) {
#define MM_(OPS, C, OUT, BC) do { if constexpr (NOP) asm volatile("s_nop 1\n\tv_mfma_f32_32x32x16_bf16 %0, " OPS ", " C : OUT(acc) : "v"(a), BC(b)); \
                                  else asm volatile("v_mfma_f32_32x32x16_bf16 %0, " OPS ", " C : OUT(acc) : "v"(a), BC(b)); } while (0)
  if constexpr (Z) {
    if constexpr (SW) { if constexpr (BA) MM_("%2, %1", "0", "=&v", "a"); else MM_("%2, %1", "0", "=&v", "v"); }
    else { if constexpr (BA) MM_("%1, %2", "0", "=&v", "a"); else MM_("%1, %2", "0", "=&v", "v"); }
  } else {
    if constexpr (SW) { if constexpr (BA) MM_("%2, %1", "%0", "+v", "a"); else MM_("%2, %1", "%0", "+v", "v"); }
    else { if constexpr (BA) MM_("%1, %2", "%0", "+v", "a"); else MM_("%1, %2", "%0", "+v", "v"); }
  }
#undef MM_
}
DI void mdone(f32x16& a) { asm volatile("s_nop 11" : "+v"(a)); }
DI void mdone(f32x16& a, f32x16& b) { asm volatile("s_nop 11" : "+v"(a), "+v"(b)); }
DI void mdone(f32x16& a, f32x16& b, f32x16& c, f32x16& d) { asm volatile("s_nop 11" : "+v"(a), "+v"(b), "+v"(c), "+v"(d)); }
template <class T> DI T opq_v(T v) { asm volatile("" : "+v"(v)); return v; }
template <int OFF> DI void ldx0(bf16x8& d, unsigned voff, const char* sb) { asm volatile("global_load_dwordx4 %0, %1, %2 offset:%3" : "=v"(d) : "v"(voff), "s"(sb), "i"(OFF)); }
template <int OFF> DI void ds_write_a(unsigned addr, const bf16x8& v) { asm volatile("ds_write_b128 %0, %1 offset:%2" ::"v"(addr), "a"(v), "i"(OFF) : "memory"); }
template <int N, class T> DI void wait2(T& a, T& b) { asm volatile("s_waitcnt vmcnt(%2)" : "+v"(a), "+v"(b) : "i"(cap63(N))); }
DI void set_flag(char* flag, u64 tag) {
  unsigned z = 0;
  asm volatile("s_mov_b64 exec, 1\n\tglobal_atomic_swap_x2 %0, %1, %2 sc1\n\ts_mov_b64 exec, -1" ::"v"(z), "v"(tag), "s"(flag) : "memory");
}
DI void ld8_1(u64& d, unsigned voff, const char* sb) { asm volatile("global_load_dwordx2 %0, %1, %2 sc1" : "=v"(d) : "v"(voff), "s"(sb) : "memory"); }
template <int NL> DI void rd_flag(u64& v, unsigned voff, const char* sb) {
  const u64 zero = 0;
  if constexpr (NL == 64) asm volatile("global_atomic_or_x2 %0, %1, %3, %2 sc0 sc1" : "+v"(v) : "v"(voff), "s"(sb), "v"(zero) : "memory");
  else asm volatile("s_mov_b64 exec, %4\n\tglobal_atomic_or_x2 %0, %1, %3, %2 sc0 sc1\n\ts_mov_b64 exec, -1" : "+v"(v) : "v"(voff), "s"(sb), "v"(zero), "s"((1ull << (NL & 63)) - 1) : "memory");
}
template <int NL> DI void poll(const char* base, unsigned voff, u64 tag) {
  for (int spin = 0; spin < (1 << 15); ++spin) {
    u64 v = tag;
    rd_flag<NL>(v, voff, base);
    waitx<false, 0>(v);
    if (__all(v == tag)) break;
  }
  asm volatile("" ::: "memory");
}

template <int RBX> DI void block_map(int& rb, int& cb) {
  const int x = blockIdx.x & 7, i = blockIdx.x >> 3;
  constexpr int CBX = 32 / RBX, GC = 16 / CBX;
  rb = RBX * (x / GC) + i / CBX; cb = CBX * (x % GC) + i % CBX;
}

DI f32x4 sum4(const bf16x8 (&p)[4]) {
  return (__builtin_bit_cast(f32x4, p[0]) + __builtin_bit_cast(f32x4, p[1])) + (__builtin_bit_cast(f32x4, p[2]) + __builtin_bit_cast(f32x4, p[3]));
}
DI void pin4(bf16x8 (&p)[4]) { asm volatile("" : "+v"(p[0]), "+v"(p[1]), "+v"(p[2]), "+v"(p[3])); }
DI void ln_stats(const bf16x8 (&p)[4], float& mean, float& rstd) {
  const f32x4 t = sum4(p);
  const float m = swap_add(t[0] + t[2]) * (1.f / D_);
  const float var = fmaxf(swap_add(t[1] + t[3]) * (1.f / D_) - m * m, 0.f);
  mean = rbf(m);
  rstd = __builtin_amdgcn_rcpf(rbf(__builtin_amdgcn_sqrtf(rbf(rbf(var) + 1e-5f))));
}
DI bf16x8 cs_frag(float c) {
  const float hi = rbf(c), mid = rbf(c - hi), lo = (c - hi) - mid;
  return __builtin_bit_cast(bf16x8, u32x4{cvtpk(hi, mid), cvtpk(lo, 0.f), 0u, 0u});
}
DI float cs_sum(const bf16x8 (&p)[4]) { const f32x4 t = sum4(p); return (t[0] + t[1]) + (t[2] + t[3]); }
DI bf16x8 mean_frag(float mean, bool on) { const float m = on ? -mean : 0.f; return __builtin_bit_cast(bf16x8, u32x4{cvtpk(m, m), cvtpk(m, 0.f), 0u, 0u}); }

#define STAMP(i)

constexpr int SLOT = 16384, XL = 12;
struct WinPtr {
  unsigned b[3];
  DI void init(unsigned a) { sfor<0, 3>([&](auto i) { b[i] = a + i * 65536u; asm volatile("" : "+v"(b[i])); }); }
  template <int OFF> DI bf16x8 frag() const { return *(LAS const bf16x8*)((LAS const char*)(uintptr_t)b[OFF >> 16] + (OFF & 65535)); }
};
struct Ctx { unsigned xv; WinPtr p; };
#define IC(n) std::integral_constant<int, (n)>{}
constexpr int slot_a(int c) { constexpr int m[8] = {6, 0, 1, 2, 3, 7, 4, 5}; return m[c]; }
constexpr int slot_q(int c) { constexpr int m[8] = {8, 0, 1, 2, 3, 7, 4, 5}; return m[c]; }

template <int S> DI void ld_x(bf16x8 (&x)[64], const Ctx& c, const char* xb) { ldx1<false, (S % 8) * 1024 - 4096>(x[S], c.xv, xb + (S / 8) * 8192 + 4096); }
template <class H> constexpr int g64_wait(int s) {
  int n = 0;
  const int t0 = s - XL;
  if (t0 < 0) n += XL - 1 - s; else n += H::n(t0);
  for (int t = t0 < 0 ? 0 : t0 + 1; t < s; ++t) n += (t + XL < 64 ? 1 : 0) + H::n(t);
  return n;
}
template <bool MAPA, bool ZERO, class H, class FX>
DI void gemm64(const Ctx& c, bf16x8 (&x)[64], const char* xb, f32x16& acc0, f32x16& acc1, FX&& hook) {
  bf16x8 f0[3], f1[3];
  auto off = [](int s) constexpr { return (MAPA ? slot_a(s / 8) : s / 8) * SLOT + (s % 8) * 2048; };
  auto rd0 = [&](auto s) { f0[s % 3] = c.p.template frag<off(s)>(); };
  auto rd1 = [&](auto s) { f1[s % 3] = c.p.template frag<off(s) + 1024>(); };
  rd0(IC(0)); rd1(IC(0)); rd0(IC(1)); rd1(IC(1));
  sfor<0, 64>([&](auto s) {
    waitx<false, g64_wait<H>(s)>(x[s]);
    SBAR();
    mm<false, false, ZERO && s == 0>(acc0, f0[s % 3], x[s]);
    SBAR();
    if constexpr (s + 2 < 64) rd0(IC(s + 2));
    if constexpr (s + XL < 64) ld_x<s + XL>(x, c, xb);
    SBAR();
    mm<false, false, ZERO && s == 0>(acc1, f1[s % 3], x[s]);
    SBAR();
    if constexpr (s + 2 < 64) rd1(IC(s + 2));
    hook(s);
    SBAR();
  });
  mdone(acc0, acc1);
}
struct HookP6 { static constexpr int n(int s) { return s >= 52 ? 1 : 0; } };
struct HookP3 { static constexpr int n(int s) { return s >= 52 && s < 60 ? 1 : 0; } };
struct HookP4 { static constexpr int n(int s) { return s == 52 || (s >= 8 && s < 12) ? 1 : 0; } };

DI void res_to_acc(const u32x4 (&xr)[4], f32x16& a0, f32x16& a1) {
#pragma unroll
  for (int e = 0; e < 4; ++e) {
    a0[2 * e] = bflo(xr[0][e]); a0[2 * e + 1] = bfhi(xr[0][e]); a0[8 + 2 * e] = bflo(xr[1][e]); a0[9 + 2 * e] = bfhi(xr[1][e]);
    a1[2 * e] = bflo(xr[2][e]); a1[2 * e + 1] = bfhi(xr[2][e]); a1[8 + 2 * e] = bflo(xr[3][e]); a1[9 + 2 * e] = bfhi(xr[3][e]);
  }
  asm volatile("" : "+v"(a0), "+v"(a1));
}
DI void store_stats(int r, const u32x4 (&xr)[4], char* stp) {
  float s1 = 0.f, s2 = 0.f, s3 = 0.f, s4 = 0.f;
#pragma unroll
  for (int i = 0; i < 4; ++i)
#pragma unroll
    for (int e = 0; e < 4; e += 2) {
      s1 = dot2(xr[i][e], 0x3f803f80u, s1); s2 = dot2(xr[i][e], xr[i][e], s2);
      s3 = dot2(xr[i][e + 1], 0x3f803f80u, s3); s4 = dot2(xr[i][e + 1], xr[i][e + 1], s4);
    }
  st2_1(f32x2{swap_add(s1 + s3), swap_add(s2 + s4)}, (unsigned)(r * 16), stp);
}
DI void store_x(const Ctx& c, int r, const f32x16& a0, const f32x16& a1, u32x4 (&xr)[4], char* dst, char* stp) {
  xr[0] = __builtin_bit_cast(u32x4, pack8<0>(a0)); st4_1<0>(xr[0], c.xv, dst);
  xr[1] = __builtin_bit_cast(u32x4, pack8<1>(a0)); st4_1<1024>(xr[1], c.xv, dst);
  xr[2] = __builtin_bit_cast(u32x4, pack8<0>(a1)); st4_1<2048>(xr[2], c.xv, dst);
  xr[3] = __builtin_bit_cast(u32x4, pack8<1>(a1)); st4_1<3072>(xr[3], c.xv, dst);
  store_stats(r, xr, stp);
}

template <class FX> DI void hedge_exp(const f32x16& p, float sc, f32x16& ep, f32x16& en, FX&& hook) {
  float mx = fmaxf(p[0], p[1]), mn = fminf(p[0], p[1]);
#pragma unroll
  for (int i = 2; i < 16; i += 2) { mx = fmaxf(fmaxf(mx, p[i]), p[i + 1]); mn = fminf(fminf(mn, p[i]), p[i + 1]); }
  mx = swap_max(mx) * sc; mn = swap_min(mn) * sc;
  sfor<0, 16>([&](auto i) {
    SBAR();
    hook(i);
    constexpr int e = i;
    float a = __builtin_amdgcn_exp2f(__builtin_fmaf(p[e], sc, -mx)), b = __builtin_amdgcn_exp2f(__builtin_fmaf(p[e], -sc, mn));
    asm volatile("" : "+v"(a), "+v"(b));
    ep[e] = a; en[e] = b;
  });
  SBAR();
}

constexpr int a_xt(int s) {
  int t = s < 0 ? XL : (s < 15 ? XL + (3 * (s + 1)) / 2 : 34 + (2 * (s - 14)) / 3);
  if (s >= 15 && t < s + 10) t = s + 10;
  return t > 64 ? 64 : t;
}
struct SchedKV {
  int xw[64], ww[8], fw, sw, pw;
  constexpr SchedKV(bool first) : xw{}, ww{}, fw(0), sw(0), pw(0) {
    int pos = 0, xpos[64] = {}, wpos[8] = {}, fpos = 0, spos = 0, ppos = 0;
    for (int s = 0; s < XL; ++s) xpos[s] = ++pos;
    if (first) ++pos;
    for (int s = 0; s < 64; ++s) {
      if ((s + 1) % 8 == 0 && (s + 1) / 8 < 8) ww[(s + 1) / 8] = pos - wpos[(s + 1) / 8];
      if (s == 60) sw = pos - spos;
      xw[s] = pos - xpos[s];
      for (int t = a_xt(s - 1); t < a_xt(s); ++t) xpos[t] = ++pos;
      if ((s < 15 && first) || (s >= 15 && s <= 46)) wpos[(s + 1) / 8 + 2] = ++pos;
      if (s >= 48 && s <= 52) { ++pos; if (s == 51) spos = pos; if (s == 52) ppos = pos; }
      if (s == 56) fpos = ++pos;
    }
    pw = pos - ppos; fw = pos - fpos;
  }
};
constexpr int QP = 30, QC = 42;
constexpr int q_nd(int s) { return s < 6 ? 0 : (s < 14 ? 1 : (s < 42 ? ((s - 14) % 8 < 4 ? 1 : 0) : 0)); }
struct SchedQ {
  int ww[8], pw, lw[33];
  constexpr SchedQ() : ww{}, pw(0), lw{} {
    int pos = 3, stpos = 3, q6 = 0, q7 = 0, ppos = 0, lpos[34] = {};
    for (int s = 0; s < 64; ++s) {
      if ((s + 2) % 8 == 0 && (s + 2) / 8 < 8) {
        const int g = (s + 2) / 8;
        ww[g] = g == 2 ? pos - stpos : (g == 6 ? pos - q6 : (g == 7 ? pos - q7 : 63));
        if (g == 2) ++pos;
      }
      if (s == QC) pw = pos - ppos;
      pos += q_nd(s); if (s == 9) q6 = pos; if (s == 13) q7 = pos;
      if (s == QP) ppos = ++pos;
      if (s >= QC && s < QC + 17) { lpos[2 * (s - QC)] = ++pos; lpos[2 * (s - QC) + 1] = ++pos; }
    }
    for (int i = 0; i < 32; ++i) { lw[i] = pos - lpos[i]; pos += i & 1; }
    lw[32] = pos - lpos[33];
  }
};

#define R_OA "v[64:79]"
#define R_OB "v[80:95]"
#define S0_(n, q, m) "v_mfma_f32_32x32x16_bf16 v[" #n ":" #n "+15], v[208:211], v[" #q ":" #q "+3], v[" #m ":" #m "+15]\n\t"
#define S1_(n, q) "v_mfma_f32_32x32x16_bf16 v[" #n ":" #n "+15], v[212:215], v[" #q "+4:" #q "+7], v[" #n ":" #n "+15]\n\t"
#define PV0_(o, p) "v_smfmac_f32_32x32x32_bf16 v[" #o ":" #o "+15], v[216:219], v[" #p ":" #p "+7], v236\n\t"
#define PV1_(o, p) "v_smfmac_f32_32x32x32_bf16 v[" #o ":" #o "+15], v[220:223], v[" #p "+8:" #p "+15], v236\n\t"
#define L0_(l, p) "v_smfmac_f32_16x16x64_bf16 v[" #l ":" #l "+3], v[224:227], v[" #p ":" #p "+7], v236\n\t"
#define L1_(l, p) "v_smfmac_f32_16x16x64_bf16 v[" #l ":" #l "+3], v[224:227], v[" #p "+8:" #p "+15], v236\n\t"
#define E_(e, i) "v_exp_f32 v[" #e "+" #i "], v[" #e "+" #i "]\n\t"
#define RD_(d, off) "ds_read_b128 v[" #d ":" #d "+3], v237 offset:" #off "\n\t"
#define HALF_(e, n, q, m, p, o, l, X0, X1, X2, X3, X4, Y2) \
  S0_(n, q, m) X0 E_(e, 0) E_(e, 1) E_(e, 2) S1_(n, q) X1 E_(e, 3) E_(e, 4) E_(e, 5) Y2 PV0_(o, p) X2 E_(e, 6) E_(e, 7) E_(e, 8) \
  PV1_(o, p) X3 E_(e, 9) E_(e, 10) E_(e, 11) L0_(l, p) X4 E_(e, 12) E_(e, 13) L1_(l, p) E_(e, 14) E_(e, 15)
#define LG2_ "s_waitcnt lgkmcnt(2)\n\t"
#define LG0_ "s_waitcnt lgkmcnt(0)\n\t"
#define STEP_(b, nb, kv, wv, wo) \
  "s_waitcnt vmcnt(4)\n\ts_barrier\n\t" \
  HALF_(128, 160, 200, 112, 176, 80, 232, RD_(208, b + 2048), RD_(212, b + 3072), RD_(216, b), RD_(220, b + 1024), "", "") LG2_ \
  HALF_(160, 144, 192, 96, 128, 64, 228, "s_add_u32 m0, s44, " #nb "\n\t", "global_load_lds_dwordx4 v" #kv ", s[40:41] sc1\n\t", "", \
        "global_load_lds_dwordx4 v" #kv ", s[40:41] offset:1024 sc1\n\t", "", LG0_) \
  HALF_(144, 176, 200, 112, 160, 80, 232, RD_(208, b + 6144), RD_(212, b + 7168), RD_(216, b + 4096), RD_(220, b + 5120), "", "") LG2_ \
  HALF_(176, 128, 192, 96, 144, 64, 228, "s_mov_b32 m0, s45\n\t", "global_load_lds_dwordx4 v" #wv ", s[42:43] offset:" #wo " sc1\n\t", "", "", "", LG0_)
DI float max16(const f32x16& s) {
  float mx = s[0];
#pragma unroll
  for (int e = 1; e < 16; ++e) mx = fmaxf(mx, s[e]);
  return mx;
}
typedef __attribute__((ext_vector_type(8))) unsigned u32x8;
typedef __attribute__((ext_vector_type(16))) unsigned u32x16;

__global__ __launch_bounds__(256, 1) void k_main(const char* xin, const char* wqkv, const char* wphi, const char* woA, const char* wq, const char* wk, const char* wv,
                                                 const char* woB, char* ws, char* out) {
  __shared__ __attribute__((aligned(16))) char smem_[163840];
  LAS char* const smem = (LAS char*)smem_;
  const unsigned lds0 = (unsigned)(uintptr_t)smem;
  Ctx c;
  int lane = threadIdx.x & 63, w = __builtin_amdgcn_readfirstlane(threadIdx.x >> 6), r = lane & 31, h = lane >> 5, rb, cb;
  block_map<4>(rb, cb);
  int tt = rb * 4 + w, blk = (lane >> 4) & 1, q4 = (lane & 15) >> 2, p4 = lane & 3;
  c.xv = lane * 16; c.p.init(lds0 + c.xv);
  char* flags = ws + WS_FLAG;
  char* col = ws + WS_COL + (size_t)cb * 2 * MB;
  u64 tag;
  u32x4 xr[4];
  float csk, csv;
  auto wt = [&](int m, int ch) { return col + C_WT + m * 131072 + ch * SLOT + w * 4096; };
  auto pc_w = [&](int m, int slot, int ch, auto i) { dma_c<i * 1024>(lds0 + slot * SLOT + w * 4096, c.xv, wt(m, ch)); };
  auto pc_kv = [&](auto g, auto i) { pc_w(1 + i / 4, 2 * (g % 4) + i / 4, g, IC(i % 4)); };
  auto pc_q = [&](auto ch, auto i) { pc_w(0, slot_q(ch), ch, i); };
  auto ld_cs = [&](bf16x8& d, int m, auto i) { ldx1<false, i * 1024>(d, c.xv, col + C_CS + m * 4096); };
  auto raw_in = [&](auto j, int slot) {
    const int hi = rb >> 3, pitch = (j == 0 || (j == 1 && !hi)) ? 6144 : 2048, row = lane >> 3;
    const char* src = (j == 0 ? wqkv + 2048 + hi * 2048 : (j == 1 ? (hi ? woA : wqkv) : (hi ? woB : wq))) + cb * 128 + (size_t)(rb & 7) * 128 * pitch;
    const unsigned v0 = (unsigned)((32 * w + row) * pitch + ((lane & 7) ^ (((row >> 1) & 1) << 2)) * 16);
    sfor<0, 4>([&](auto i) { dma<i * 1024>(lds0 + slot * SLOT + w * 4096, v0 + i * (8 * pitch - 1024), src); });
  };
  auto raw_out = [&](auto j, int slot) {
    const int hi = rb >> 3, ch = rb & 7, m = j == 0 ? 1 + hi : (j == 1 ? 3 * hi : 4 + hi);
    LAS const char* p = smem + slot * SLOT + (w >> 1) * 8192 + (4 * h + q4) * 128 + (((((q4 >> 1) ^ w) & 1) << 2) + 2 * blk + (p4 >> 1)) * 16 + 8 * (p4 & 1);
    char* dst = col + C_WT + m * 131072 + ch * SLOT + (w >> 1) * 8192 + (w & 1) * 1024 + 4096;
    float cs = 0.f;
    sfor<0, 4>([&](auto s) {
      const u32x4 f = __builtin_bit_cast(u32x4, cat8(trread(p + s * 2048), trread(p + s * 2048 + 1024)));
      st4_1<s * 2048 - 4096>(f, c.xv, dst);
#pragma unroll
      for (int e = 0; e < 4; ++e) cs = dot2(f[e], 0x3f803f80u, cs);
    });
    st1_1(swap_add(cs), (unsigned)(r * 16), col + C_CS + m * 4096 + (ch >> 1) * 1024 + (w & 1) * 512 + ((ch & 1) * 2 + (w >> 1)) * 4);
  };
  {
    [[maybe_unused]] const int l = 0;
    STAMP(30);
    raw_in(IC(0), 7);
    u64 ep; ld8_1(ep, 0u, ws + WS_EPOCH);
    unsigned tch;
    ld1_1(tch, (unsigned)(h ? WS_COL + cb * 2 * MB + 1536 * KB : rb * 2 * MB + 7 * STG + w * 65536 + cb * 4096), ws);
    bf16x8 xt[4];
    sfor<0, 4>([&](auto i) { ldx0<i * 32>(xt[i], (unsigned)(r * 2048 + h * 16), xin + (size_t)tt * 65536 + cb * 128); });
    wait_bar<6>();
    raw_out(IC(0), 7);
    asm volatile("s_waitcnt vmcnt(5)" : "+v"(tch), "+v"(ep), "+v"(xt[0]), "+v"(xt[1]), "+v"(xt[2]), "+v"(xt[3]));
    { char* dst = ws + (size_t)rb * 2 * MB + w * 65536 + cb * 4096;
      sfor<0, 4>([&](auto i) { xr[i] = unperm(__builtin_bit_cast(u32x4, xt[i])); st4_1<i * 1024>(xr[i], c.xv, dst); });
      store_stats(r, xr, ws + WS_ST + tt * 4096 + (cb >> 1) * 512 + (cb & 1) * 8); }
    { u64 z = ((u64)__builtin_amdgcn_readfirstlane((unsigned)(ep >> 32)) << 32 | __builtin_amdgcn_readfirstlane((unsigned)ep)) + 0x9E3779B97F4A7C15ull;
      z = (z ^ (z >> 30)) * 0xBF58476D1CE4E5B9ull; z = (z ^ (z >> 27)) * 0x94D049BB133111EBull; tag = z ^ (z >> 31); }
    waitv<0>();
    set_flag(flags + FL_ACT + tt * 128 + cb * 8, tag);
    bar();
    if (w == 0) set_flag(flags + FL_W + cb * 128 + rb * 8, tag);
    poll<32>(flags, (unsigned)((lane & 15) * 8 + (blk ? FL_W + cb * 128 : FL_ACT + tt * 128)), tag);
    bf16x8 ck[4], cv[4];
    sfor<0, 4>([&](auto i) { ld_cs(ck[i], 1, i); }); sfor<0, 4>([&](auto i) { ld_cs(cv[i], 2, i); });
    sfor<0, 16>([&](auto i) { pc_kv(IC(i / 8), IC(i % 8)); });
    waitv<16>(); pin4(ck); pin4(cv);
    csk = cs_sum(ck); csv = cs_sum(cv);
    STAMP(31);
  }
  char *act, *stt, *fact;
#define REFRESH() do { \
    asm volatile("" : "+s"(w), "+s"(cb), "+s"(rb), "+s"(ws), "+s"(tag)); \
    asm volatile("" : "+v"(lane), "+v"(csk), "+v"(csv)); \
    r = lane & 31; h = lane >> 5; blk = (lane >> 4) & 1; q4 = (lane & 15) >> 2; p4 = lane & 3; tt = rb * 4 + w; \
    flags = ws + WS_FLAG; col = ws + WS_COL + (size_t)cb * 2 * MB; c.xv = lane * 16; c.p.init(lds0 + c.xv); \
    act = ws + (size_t)rb * 2 * MB + l * 4 * STG + w * 65536;     \
    fact = flags + FL_ACT + (l * 4 * 64 + tt) * 128;                  \
    stt = ws + WS_ST + (size_t)l * 512 * 1024 + tt * 4096;            \
  } while (0)
  for (int l = 0; l < 2; ++l) {
    REFRESH();
    float mean, rstd, csb;
    bf16x8 park[32];
    STAMP(0);
    {
      if (l > 0) poll<16>(fact, lane * 8, tag);
      STAMP(1);
      constexpr int NA = 60;
      bf16x8 x[64];
      f32x16 ak0, ak1, av0, av1;
      auto issue_x = [&](auto s) { ldx1<(s < NA), (s % 8) * 1024 - 4096>(x[s], c.xv, act + (s / 8) * 8192 + 4096); };
      {
        constexpr SchedKV SC(false), S1(true);
        bf16x8 sp[4], f[4][2], ck, cv;
        u64 f1;
        sfor<0, XL>(issue_x);
        wait_bar<XL>();
        if (l == 0) pc_kv(IC(2), IC(0));
        auto rd = [&](auto s, auto t) { f[t][s % 2] = c.p.template frag<(2 * ((s / 8) % 4) + t / 2) * SLOT + (s % 8) * 2048 + (t % 2) * 1024>(); };
        sfor<0, 4>([&](auto t) { rd(IC(0), t); });
        sfor<0, 64>([&](auto s) {
          if constexpr ((s + 1) % 8 == 0 && (s + 1) / 8 >= 2 && (s + 1) / 8 < 8) wait_bar<((s + 1) / 8 < 4 ? S1.ww[(s + 1) / 8] : SC.ww[(s + 1) / 8])>();
          if constexpr (s == 47) { if (l == 0 && w == 0) set_flag(flags + FL_W + 2048 + cb * 128 + rb * 8, tag); }
          if constexpr (s == 60) { waitv<SC.sw>(); pin4(sp); }
          waitx<(s < NA), SC.xw[s]>(x[s]);
          SBAR();
          mm<false, (s < NA), s == 0>(ak0, f[0][s % 2], x[s]);
          SBAR();
          if constexpr (s + 1 < 64) rd(IC(s + 1), IC(0));
          if constexpr (a_xt(s - 1) < a_xt(s)) issue_x(IC(a_xt(s - 1)));
          if constexpr (s == 60) ln_stats(sp, mean, rstd);
          SBAR();
          mm<false, (s < NA), s == 0>(ak1, f[1][s % 2], x[s]);
          SBAR();
          if constexpr (s + 1 < 64) rd(IC(s + 1), IC(1));
          if constexpr (a_xt(s - 1) + 1 < a_xt(s)) issue_x(IC(a_xt(s - 1) + 1));
          if constexpr (s == 58) ck = opq_v(cs_frag(csk));
          SBAR();
          mm<true, (s < NA), s == 0>(av0, f[2][s % 2], x[s]);
          SBAR();
          if constexpr (s + 1 < 64) rd(IC(s + 1), IC(2));
          if constexpr (s < 15) { if (l == 0) pc_kv(IC((s + 1) / 8 + 2), IC((s + 1) % 8)); }
          if constexpr (s >= 15 && s <= 46) pc_kv(IC((s + 1) / 8 + 2), IC((s + 1) % 8));
          if constexpr (s == 58) cv = opq_v(cs_frag(csv));
          SBAR();
          mm<true, (s < NA), s == 0>(av1, f[3][s % 2], x[s]);
          SBAR();
          if constexpr (s + 1 < 64) rd(IC(s + 1), IC(3));
          if constexpr (s == 1) { if (l == 0) raw_in(IC(1), 8); }
          if constexpr (s == 4) { if (l == 0) raw_in(IC(2), 9); }
          if constexpr (s == 24) { if (l == 0) raw_out(IC(1), 8); }
          if constexpr (s == 26) { if (l == 0) raw_out(IC(2), 9); }
          if constexpr (s == 56) { f1 = tag; rd_flag<16>(f1, lane * 8, flags + FL_W + 2048 + cb * 128); }
          if constexpr (s >= 48 && s < 52) ldx1<false, (s - 48) * 512>(sp[s - 48], (unsigned)(h * 2048 + r * 16), stt);
          if constexpr (s == 52) dma<0>(lds0 + 9 * SLOT + w * 1024, c.xv, wphi + cb * 4096 + w * 1024);
          if constexpr (s == 62) {
            const bf16x8 m0 = mean_frag(mean, h == 0), m1 = mean_frag(mean, h == 1);
            mm<false, false, false, true>(ak0, ck, m0); mm<false, false, false, true>(ak1, ck, m1);
            mm<true, false, false>(av0, cv, m0); mm<true, false, false>(av1, cv, m1);
          }
          SBAR();
        });
        mdone(ak0, ak1, av0, av1);
        waitx<false, SC.fw>(f1);
        if (!__all(f1 == tag)) poll<16>(flags + FL_W + 2048 + cb * 128, lane * 8, tag);
        bar();
      }
      STAMP(2);
      REFRESH();
      LAS char* pk_img = smem + 4 * SLOT;
      LAS char* v_img = smem + 6 * SLOT;
      char* kvp = col + C_KVP + l * 131072 + w * 32768;
      char* zp = col + C_ZP + l * 8192 + w * 2048;
      char* fkvp = flags + FL_KVP + (l * 64 + cb * 4 + w) * 128;
      const float sce = rstd * LOG2E;
      auto phi_mm = [&](const f32x16& t0, const f32x16& t1, f32x16& p0, f32x16& p1) {
        LAS const char* wp = smem + 9 * SLOT + (4 * h + q4) * 64 + blk * 32 + p4 * 8;
        const bf16x8 w00 = cat8(trread(wp), trread(wp + 512)), w01 = cat8(trread(wp + 1024), trread(wp + 1536));
        const bf16x8 w10 = cat8(trread(wp + 2048), trread(wp + 2560)), w11 = cat8(trread(wp + 3072), trread(wp + 3584));
        mm<false, false, true, true>(p0, w00, pack8<0>(t0)); mm<false, false, false, true>(p0, w01, pack8<1>(t0));
        mm<false, false, true, true>(p1, w10, pack8<0>(t1)); mm<false, false, false, true>(p1, w11, pack8<1>(t1));
      };
      bf16x8 cq[4], cw[4];
      {
        auto side = [&](auto i) {
          SBAR();
          if constexpr (i < 24) pc_q(IC(i / 4), IC(i % 4)); else if constexpr (i < 28) ld_cs(cq[i - 24], 0, IC(i - 24)); else ld_cs(cw[i - 28], 4, IC(i - 28));
          SBAR();
        };
        f32x16 p[2];
        side(IC(0)); side(IC(1));
        phi_mm(ak0, ak1, p[0], p[1]);
        side(IC(2)); side(IC(3));
        {
          LAS char* d = v_img + w * 2048 + lane * 16;
          float rs[16];
#pragma unroll
          for (int i = 0; i < 16; ++i) rs[i] = lane_get(rstd, (i & 3) + 8 * (i >> 2) + 4 * h);
          side(IC(4));
#pragma unroll
          for (int i = 0; i < 16; ++i) av0[i] *= rs[i];
          *(LAS bf16x8*)d = pack8<0>(av0); *(LAS bf16x8*)(d + 1024) = pack8<1>(av0);
          side(IC(5));
#pragma unroll
          for (int i = 0; i < 16; ++i) av1[i] *= rs[i];
          *(LAS bf16x8*)(d + 8192) = pack8<0>(av1); *(LAS bf16x8*)(d + 9216) = pack8<1>(av1);
        }
        asm volatile("" : "+v"(p[0]), "+v"(p[1]));
        sfor<0, 2>([&](auto hd) {
          f32x16 ep, en;
          hedge_exp(p[hd], sce, ep, en, [&](auto i) { if constexpr (i % 2 == 0) side(IC(6 + hd * 13 + i / 2)); });
          float sp = 0.f, sn = 0.f;
#pragma unroll
          for (int i = 0; i < 16; ++i) { sp += ep[i]; sn += en[i]; }
          side(IC(14 + hd * 13));
          sp = __builtin_amdgcn_rcpf(swap_add(sp)); sn = __builtin_amdgcn_rcpf(swap_add(sn));
          LAS char* row = pk_img + hd * 16384 + (w * 32 + r) * 128 + h * 8;
          sfor<0, 4>([&](auto g) {
            constexpr int gq = g;
            *(LAS u32x2*)(row + ((gq ^ (r & 7)) << 4)) = u32x2{cvtpk(ep[4 * gq] * sp, ep[4 * gq + 1] * sp), cvtpk(ep[4 * gq + 2] * sp, ep[4 * gq + 3] * sp)};
            *(LAS u32x2*)(row + (((4 + gq) ^ (r & 7)) << 4)) = u32x2{cvtpk(en[4 * gq] * sn, en[4 * gq + 1] * sn), cvtpk(en[4 * gq + 2] * sn, en[4 * gq + 3] * sn)};
            side(IC(15 + hd * 13 + gq));
          });
        });
      }
      asm volatile("s_waitcnt vmcnt(8) lgkmcnt(0)\n\ts_barrier" ::: "memory");
      {
        const int hd = w >> 1, fh = w & 1;
        f32x16 kv, z;
        const unsigned one2 = opq_v(0x3f803f80u);
        const bf16x8 ones = __builtin_bit_cast(bf16x8, u32x4{one2, one2, one2, one2});
        const int ch = fh * 4 + 2 * blk + (p4 >> 1), r0 = 4 * h + q4;
        LAS const char* pa = pk_img + hd * 16384 + r0 * 128 + ((ch ^ r0) << 4) + 8 * (p4 & 1);
        LAS const char* pb = v_img + hd * 8192 + lane * 16;
        bf16x8 a[8], b[8];
#pragma unroll
        for (int s = 0; s < 8; ++s) { a[s] = cat8(trread(pa + s * 2048), trread(pa + s * 2048 + 1024)); b[s] = *(LAS const bf16x8*)(pb + s * 1024); }
        SBAR();
        sfor<0, 8>([&](auto s) { mm<false, false, s == 0>(kv, a[s], b[s]); });
        sfor<0, 8>([&](auto s) {
          mm<false, false, s == 0, s == 0>(z, ones, a[s]);
          if constexpr (s == 1) { mdone(kv); st4_1<0>(__builtin_bit_cast(u32x4, pack8<0>(kv)), c.xv, kvp + rb * 2048); }
          if constexpr (s == 2) st4_1<1024>(__builtin_bit_cast(u32x4, pack8<1>(kv)), c.xv, kvp + rb * 2048);
          if constexpr (s == 3) { waitv<2>(); pin4(cq); pin4(cw); cq[0] = cs_frag(cs_sum(cq)); pin4(cq); }
          if constexpr (s == 5) { csb = opq_v(cs_sum(cw)); }
          SBAR();
        });
        mdone(z);
        st1_1(z[0], (unsigned)(r * 4), zp + rb * 128);
      }
      STAMP(3);
      f32x16 aq0, aq1;
      bf16x8 pp[32], zq[2];
      constexpr SchedQ SQ;
      auto ld_park = [&](auto i) { ldx1<true, (i % 4) * 1024>(park[i], c.xv, wt(4, i / 4)); };
      {
        bf16x8 f0[3], f1[3];
        u64 fv;
        auto off = [](int s) constexpr { return slot_q(s / 8) * SLOT + (s % 8) * 2048; };
        auto rd0 = [&](auto s) { f0[s % 3] = c.p.template frag<off(s)>(); };
        auto rd1 = [&](auto s) { f1[s % 3] = c.p.template frag<off(s) + 1024>(); };
        auto ldp = [&](auto i) {
          if constexpr (i < 32) ldx1<true, (i % 8) * 1024 - 4096>(pp[i], c.xv, kvp + (i / 8) * 8192 + 4096); else ldx1<false, (i - 32) * 1024>(zq[i - 32], c.xv, zp);
        };
        rd0(IC(0)); rd1(IC(0)); rd0(IC(1)); rd1(IC(1));
        sfor<0, 64>([&](auto s) {
          if constexpr ((s + 2) % 8 == 0 && (s + 2) / 8 < 8) {
            wait_bar<SQ.ww[(s + 2) / 8]>();
            if constexpr ((s + 2) / 8 == 2) set_flag(fkvp + rb * 8, tag);
          }
          if constexpr (s == QC) { waitx<false, SQ.pw>(fv); if (!__all(fv == tag)) poll<16>(fkvp, lane * 8, tag); }
          SBAR();
          mm<false, (s < NA), s == 0>(aq0, f0[s % 3], x[s]);
          SBAR();
          if constexpr (s + 2 < 64) rd0(IC(s + 2));
          if constexpr (q_nd(s)) {
            if constexpr (s < 14) pc_q(IC(6 + (s - 6) / 4), IC((s - 6) % 4)); else pc_w(3, slot_a((s - 14) / 8), (s - 14) / 8, IC((s - 14) % 8));
          }
          if constexpr (s == QP) { fv = tag; rd_flag<16>(fv, lane * 8, fkvp); }
          if constexpr (s >= QC && s < QC + 17) ldp(IC(2 * (s - QC)));
          SBAR();
          mm<false, (s < NA), s == 0>(aq1, f1[s % 3], x[s]);
          SBAR();
          if constexpr (s + 2 < 64) rd1(IC(s + 2));
          if constexpr (s >= QC && s < QC + 17) ldp(IC(2 * (s - QC) + 1));
          if constexpr (s == 2) { mm<false, false, false, true>(aq0, cq[0], mean_frag(mean, h == 0)); mm<false, false, false, true>(aq1, cq[0], mean_frag(mean, h == 1)); }
          SBAR();
        });
        mdone(aq0, aq1);
        bar();
      }
      STAMP(4);
      REFRESH();
      bf16x8 pq[2][4];
      f32x16 kv;
      {
        f32x16 p[2];
        phi_mm(aq0, aq1, p[0], p[1]);
        u32x4 s0, s1;
#pragma unroll
        for (int e = 0; e < 4; ++e) {
          const int kr = 8 * (e >> 1) + 4 * h + 2 * (e & 1), rr = r & 15;
          const unsigned v = rr == kr ? 0x3f80u : (rr == kr + 1 ? 0x3f800000u : 0u);
          s0[e] = r < 16 ? v : 0u; s1[e] = r < 16 ? 0u : v;
        }
        const bf16x8 sel0 = opq_v(__builtin_bit_cast(bf16x8, s0)), sel1 = opq_v(__builtin_bit_cast(bf16x8, s1));
        mdone(p[0], p[1]);
        sfor<0, 2>([&](auto hd) {
          f32x16 ep, en;
          hedge_exp(p[hd], sce, ep, en, [&](auto e) {
            constexpr int i = hd * 16 + e;
            waitx<true, SQ.lw[i]>(pp[i]);
            mm<false, true, i == 0>(kv, (i & 1) ? sel1 : sel0, pp[i]);
            if constexpr (i & 1) pc_w(3, slot_a(4 + i / 8), 4 + i / 8, IC((i / 2) % 4));
          });
          pq[hd][0] = pack8<0>(ep); pq[hd][1] = pack8<1>(ep); pq[hd][2] = pack8<0>(en); pq[hd][3] = pack8<1>(en);
          pin4(pq[hd]);
        });
      }
      STAMP(5);
      {
        asm volatile("s_waitcnt vmcnt(%2)" : "+v"(zq[0]), "+v"(zq[1]) : "i"(SQ.lw[32]));
        LAS char* sh = smem + 9 * SLOT + 4096;
        ld_park(IC(0)); ld_park(IC(1));
        mdone(kv);
        *(LAS bf16x8*)(sh + w * 2048 + lane * 16) = pack8<0>(kv); *(LAS bf16x8*)(sh + w * 2048 + 1024 + lane * 16) = pack8<1>(kv);
        f32x4 zs = __builtin_bit_cast(f32x4, zq[0]) + __builtin_bit_cast(f32x4, zq[1]);
#pragma unroll
        for (int e = 0; e < 4; ++e) zs[e] += lane_get(zs[e], lane ^ 8);
        ld_park(IC(2)); ld_park(IC(3));
#pragma unroll
        for (int e = 0; e < 4; ++e) zs[e] += lane_get(zs[e], lane ^ 16);
        ld_park(IC(4)); ld_park(IC(5));
#pragma unroll
        for (int e = 0; e < 4; ++e) zs[e] = swap_add(zs[e]);
        if (lane < 8) *(LAS u32x2*)(sh + 8192 + w * 64 + (lane & 1) * 32 + (lane >> 2) * 16 + ((lane >> 1) & 1) * 8) = u32x2{cvtpk(zs[0], zs[1]), cvtpk(zs[2], zs[3])};
        else if (lane < 24) *(LAS unsigned*)(sh + 8192 + 256 + w * 64 + (lane - 8) * 4) = opq_v(0x3f803f80u);
        ld_park(IC(6)); ld_park(IC(7));
        lds_bar();
        STAMP(6);
        char* dst = act + STG + cb * 4096;
        LAS const char* zb = sh + 8192 + (r & 1) * 256 + h * 32;
        bf16x8 ka[8], za[8];
#pragma unroll
        for (int k = 0; k < 8; ++k) { za[k] = *(LAS const bf16x8*)(zb + (k >> 1) * 64 + (k & 1) * 16); ka[k] = *(LAS const bf16x8*)(sh + k * 1024 + lane * 16); }
        f32x16 nm[4], dn[2];
        float ai[2], bi[2];
        auto coef = [&](auto hd) {
          const float a = __builtin_amdgcn_rcpf(dn[0][1]), b = __builtin_amdgcn_rcpf(dn[1][1]);
          const float inv = __builtin_amdgcn_rcpf(dn[0][0] * a + dn[1][0] * b + 1e-6f);
          ai[hd] = opq_v(a * inv); bi[hd] = opq_v(b * inv);
        };
        auto fin = [&](auto hd) {
          f32x16 o;
#pragma unroll
          for (int i = 0; i < 16; ++i) o[i] = nm[2 * hd][i] * ai[hd] + nm[2 * hd + 1][i] * bi[hd];
          st4_1<hd * 2048>(__builtin_bit_cast(u32x4, pack8<0>(o)), c.xv, dst);
          st4_1<hd * 2048 + 1024>(__builtin_bit_cast(u32x4, pack8<1>(o)), c.xv, dst);
        };
        ld_park(IC(8)); ld_park(IC(9));
        sfor<0, 4>([&](auto k) { mm<false, false, k % 2 == 0>(dn[k / 2], za[k], pq[0][k]); if constexpr (k % 2) ld_park(IC(10 + k / 2)); });
        sfor<0, 4>([&](auto k) { mm<false, false, k % 2 == 0>(nm[k / 2], ka[k], pq[0][k]); if constexpr (k % 2) ld_park(IC(12 + k / 2)); });
        mdone(dn[0], dn[1]); coef(IC(0));
        sfor<0, 4>([&](auto k) { mm<false, false, k % 2 == 0>(dn[k / 2], za[4 + k], pq[1][k]); if constexpr (k % 2) ld_park(IC(14 + k / 2)); });
        sfor<0, 4>([&](auto k) { mm<false, false, k % 2 == 0>(nm[2 + k / 2], ka[4 + k], pq[1][k]); if constexpr (k % 2) ld_park(IC(16 + k / 2)); });
        asm volatile("" : "+v"(nm[0]), "+v"(nm[1]));
        fin(IC(0));
        mdone(dn[0], dn[1], nm[2], nm[3]); coef(IC(1));
        fin(IC(1));
        sfor<18, 28>(ld_park);
        waitv<10>();
        set_flag(fact + 8192 + cb * 8, tag);
        sfor<28, 32>(ld_park);
      }
    }
    STAMP(7);
    REFRESH();
    {
      f32x16 a0, a1; res_to_acc(xr, a0, a1);
      poll<16>(fact + 8192, lane * 8, tag);
      sfor<0, 32>([&](auto i) { waitx<true, 63>(park[i]); });
      STAMP(8);
      bf16x8 x[64];
      const char* xb = act + STG;
      sfor<0, XL>([&](auto s) { ld_x<s>(x, c, xb); });
      bar();
      const char* wkv = ((cb & 2) ? wv : wk) + w * 256 * 64;
      const unsigned kvv = (unsigned)((lane >> 1) * 64 + (cb & 1) * 32 + (lane & 1) * 16);
      const unsigned wa = lds0 + w * 4096 + c.xv, wb = wa + 65536;
      auto unpark = [&](auto i) {
        constexpr int off = slot_a(i / 4) * SLOT + (i % 4) * 1024;
        ds_write_a<(off & 65535)>(off < 65536 ? wa : wb, park[i]);
      };
      gemm64<true, false, HookP3>(c, x, xb, a0, a1, [&](auto s) {
        if constexpr (s == 33) bar();
        if constexpr (s >= 36 && s < 52) unpark(IC(s - 36));
        if constexpr (s >= 52 && s < 60) dma<0>(lds0 + 8 * SLOT + w * 8192 + (s - 52) * 1024, kvv, wkv + (s - 52) * 32 * 64);
      });
      STAMP(9);
      store_x(c, r, a0, a1, xr, act + 2 * STG + cb * 4096, stt + 256 * 1024 + (cb >> 1) * 512 + (cb & 1) * 8);
      bar();
      sfor<16, 32>(unpark);
      waitv<0>();
      set_flag(fact + 2 * 8192 + cb * 8, tag);
    }
    STAMP(10);
    REFRESH();
    u32x16 qq;
    char* kvf = ws + WS_KVF + l * 320 * KB;
    bf16x8 k0, k1;
    {
      char* fkv = flags + FL_KV + l * 2048;
      const int du = (cb >> 2) - w, ty = cb & 3;
      LAS char* reg = smem + 8 * SLOT;
      bf16x8 fu[16], cu, cs;
      { LAS const char* p = reg + w * 8192 + (4 * h + q4) * 32 + p4 * 8;
        float t0 = 0.f, t1 = 0.f;
#pragma unroll
        for (int i = 0; i < 16; ++i) {
          fu[i] = cat8(trread(p + i * 512), trread(p + i * 512 + 256));
          const u32x4 f = __builtin_bit_cast(u32x4, fu[i]);
          t0 = dot2(f[0], 0x3f803f80u, t0); t1 = dot2(f[1], 0x3f803f80u, t1); t0 = dot2(f[2], 0x3f803f80u, t0); t1 = dot2(f[3], 0x3f803f80u, t1);
        }
        cu = opq_v(cs_frag(swap_add(t0 + t1))); cs = opq_v(cs_frag(csb)); }
      poll<32>(fact + 2 * 8192 - w * 128, (unsigned)((lane & 15) * 8 + (blk ? cb >> 2 : w) * 128), tag);
      STAMP(11);
      const char* xb = act + 2 * STG; const char* stp = stt + 256 * 1024;
      bf16x8 x[64], xu[16], su[4], sp[4];
      sfor<0, 16>([&](auto i) { ldx1<true, (i % 8) * 1024 - 4096>(xu[i], c.xv, xb + (ptrdiff_t)du * 65536 + w * 16384 + (i / 8) * 8192 + 4096); });
      sfor<0, 4>([&](auto i) { ldx1<false, i * 512>(su[i], (unsigned)(h * 2048 + r * 16), stp + du * 4096); });
      f32x16 au, av;
      sfor<0, 16>([&](auto i) {
        waitx<true, 15 - i + 4 + (i < XL ? i : XL)>(xu[i]);
        mm<false, true, i == 0>(au, fu[i], xu[i]);
        if constexpr (i < XL) ld_x<i>(x, c, xb);
        mm<true, true, i == 0>(av, fu[i], xu[i]);
      });
      lds_bar();
      STAMP(12);
      f32x16 a0, a1;
      u64 fa;
      float mu, ru;
      gemm64<true, true, HookP4>(c, x, xb, a0, a1, [&](auto s) {
        if constexpr (s == 0) { pin4(su); ln_stats(su, mu, ru); }
        if constexpr (s == 1) { const bf16x8 mf = mean_frag(mu, h == 0); mm<false, false, false, true>(au, cu, mf); mm<true, false, false>(av, cu, mf); }
        if constexpr (s == 4) {
          asm volatile("" : "+v"(au), "+v"(av));
          if (ty >= 2) au = av;
#pragma unroll
          for (int gq = 0; gq < 4; ++gq) *(LAS f32x4*)(reg + w * 8192 + gq * 1024 + lane * 16) = f32x4{au[4 * gq], au[4 * gq + 1], au[4 * gq + 2], au[4 * gq + 3]};
        }
        if constexpr (s == 6) {
          lds_bar();
          if (w == 0) {
            char* dst = kvf + (tt + du) * 4096;
            auto frag = [&](auto fs, f32x4& lo, f32x4& hi) {
              lo = f32x4{}; hi = f32x4{};
#pragma unroll
              for (int j = 0; j < 4; ++j) { lo += *(LAS const f32x4*)(reg + j * 8192 + fs * 2048 + lane * 16); hi += *(LAS const f32x4*)(reg + j * 8192 + fs * 2048 + 1024 + lane * 16); }
            };
            f32x4 lo, hi;
            if (ty < 2) {
              frag(IC(0), lo, hi); lo *= ru; hi *= ru;
              st4_1<0>(u32x4{cvtpk(lo[0], lo[1]), cvtpk(lo[2], lo[3]), cvtpk(hi[0], hi[1]), cvtpk(hi[2], hi[3])}, c.xv, dst + ty * 1024);
            } else {
              const unsigned vo = (unsigned)(((ty & 1) * 16 + (r & 15) + 32 * h) * 16);
              sfor<0, 2>([&](auto fs) {
                frag(fs, lo, hi);
#pragma unroll
                for (int e = 0; e < 4; ++e) { lo[e] *= lane_get(ru, 16 * fs + 4 * h + e); hi[e] *= lane_get(ru, 16 * fs + 8 + 4 * h + e); }
                st4_1<2048 + fs * 1024>(unperm(u32x4{cvtpk(lo[0], lo[1]), cvtpk(lo[2], lo[3]), cvtpk(hi[0], hi[1]), cvtpk(hi[2], hi[3])}), vo, dst);
              });
            }
          }
        }
        if constexpr (s >= 8 && s < 12) ldx1<false, (s - 8) * 512>(sp[s - 8], (unsigned)(h * 2048 + r * 16), stp);
        if constexpr (s == 7 + XL) { if (w == 0) set_flag(fkv + ((tt + du) * 4 + ty) * 8, tag); }
        if constexpr (s == 12 + XL) { pin4(sp); ln_stats(sp, mean, rstd); }
        if constexpr (s == 14 + XL) { mm<false, false, false, true>(a0, cs, mean_frag(mean, h == 0)); mm<false, false, false, true>(a1, cs, mean_frag(mean, h == 1)); }
        if constexpr (s == 52) ld8_1(fa, lane * 8, fkv + w * 512);
      });
      waitx<false, 0>(fa);
      for (int spin = 0; spin < (1 << 15) && !__all(fa == tag); ++spin) { __builtin_amdgcn_s_sleep(8); ld8_1(fa, lane * 8, fkv + w * 512); waitx<false, 0>(fa); }
      bar();
      ldx1<false, 0>(k0, c.xv, kvf); ldx1<false, 1024>(k1, c.xv, kvf);
      sfor<0, 6>([&](auto i) { dma_c<(i % 2) * 1024>(lds0 + 8 * SLOT + w * 2048 + (i / 2) * 8192, c.xv, kvf + 2048 + w * 2048 + (i / 2) * 8192); });
      STAMP(13);
      const float sc = rstd * QSCALE;
#pragma unroll
      for (int i = 0; i < 8; ++i) { qq[i] = cvtpk(a0[2 * i] * sc, a0[2 * i + 1] * sc); qq[8 + i] = cvtpk(a1[2 * i] * sc, a1[2 * i + 1] * sc); }
      asm volatile("s_waitcnt vmcnt(6)" : "+v"(k0), "+v"(k1), "+v"(qq));
    }
    STAMP(14);
    REFRESH();
    {
      const bf16x8 qa0 = __builtin_bit_cast(bf16x8, __builtin_shufflevector(qq, qq, 0, 1, 2, 3)), qa1 = __builtin_bit_cast(bf16x8, __builtin_shufflevector(qq, qq, 4, 5, 6, 7));
      const bf16x8 qb0 = __builtin_bit_cast(bf16x8, __builtin_shufflevector(qq, qq, 8, 9, 10, 11)), qb1 = __builtin_bit_cast(bf16x8, __builtin_shufflevector(qq, qq, 12, 13, 14, 15));
      float mA = 0.f, mB = 0.f;
      const char* ksrc = kvf + 2048 + w * 2048;
      const unsigned ring = lds0 + 8 * SLOT + w * 2048;
      for (int attempt = 0; attempt < 2; ++attempt) {
        f32x16 sa0, sb;
        if (attempt == 1) {
          mA = -INFINITY; mB = -INFINITY;
          for (int t = 0; t < 64; ++t) {
            ldx1<false, 0>(k0, c.xv, kvf + t * 4096); ldx1<false, 1024>(k1, c.xv, kvf + t * 4096);
            asm volatile("s_waitcnt vmcnt(0)" : "+v"(k0), "+v"(k1));
            mm<false, false, true>(sa0, k0, qa0); mm<false, false, false>(sa0, k1, qa1); mm<false, false, true>(sb, k0, qb0); mm<false, false, false>(sb, k1, qb1);
            mdone(sa0, sb);
            mA = fmaxf(mA, max16(sa0)); mB = fmaxf(mB, max16(sb));
          }
          mA = swap_max(mA); mB = swap_max(mB);
          ldx1<false, 0>(k0, c.xv, kvf); ldx1<false, 1024>(k1, c.xv, kvf);
          sfor<0, 6>([&](auto i) { dma_c<(i % 2) * 1024>(ring + (i / 2) * 8192, c.xv, ksrc + (i / 2) * 8192); });
          asm volatile("s_waitcnt vmcnt(6)" : "+v"(k0), "+v"(k1));
        }
        mm<false, false, true, true>(sa0, k0, qa0); mm<false, false, false>(sa0, k1, qa1); mm<false, false, true>(sb, k0, qb0); mm<false, false, false>(sb, k1, qb1);
        mdone(sa0, sb);
        if (attempt == 0) { mA = swap_max(max16(sa0)); mB = swap_max(max16(sb)); }
        f32x16 nma, nmb, oa = {}, ob = {}, sb1 = {}, t0, t1;
#pragma unroll
        for (int e = 0; e < 16; ++e) { nma[e] = -mA; nmb[e] = -mB; sa0[e] -= mA; }
        const unsigned o2 = opq_v(0x3f803f80u), s0 = ((lane >> 2) & 1) ? 0u : o2, s1 = o2 - s0;
        u32x16 kvr = {}, misc;
        { const u32x4 a = __builtin_bit_cast(u32x4, k0), b = __builtin_bit_cast(u32x4, k1);
#pragma unroll
          for (int e = 0; e < 4; ++e) { kvr[e] = a[e]; kvr[4 + e] = b[e]; } }
        misc = u32x16{s0, s0, s1, s1, 0u, 0u, 0u, 0u, 0u, 0u, 0u, 0u, 0xddddddddu, lds0 + 8 * SLOT + c.xv, 0u, 0u};
        const u32x4 offs = {c.xv, c.xv + 8192, c.xv + 16384, c.xv + 24576};
        const char* ks = ksrc + 3 * 8192; const char* wsr = wt(5, 0);
        unsigned wm0 = lds0 + w * 4096, cnt = 8;
        asm volatile(
            "s_nop 1\n\t"
            "1:\n\t"
            STEP_(0, 24576, 240, 240, 0) STEP_(8192, 0, 241, 240, 1024) STEP_(16384, 8192, 242, 240, 2048) STEP_(24576, 16384, 243, 240, 3072)
            "s_add_u32 s40, s40, 0x8000\n\ts_addc_u32 s41, s41, 0\n\ts_add_u32 s42, s42, 0x4000\n\ts_addc_u32 s43, s43, 0\n\t"
            "s_add_u32 s45, s45, 0x4000\n\ts_sub_u32 %[cnt], %[cnt], 1\n\ts_cmp_lg_u32 %[cnt], 0\n\ts_cbranch_scc1 1b\n\t"
            PV0_(80, 176) PV1_(80, 176) L0_(232, 176) L1_(232, 176)
            "s_nop 15\n\t"
            : "+{v[64:79]}"(oa), "+{v[80:95]}"(ob), "+{v[128:143]}"(sa0), "=&{v[144:159]}"(t0), "=&{v[160:175]}"(t1), "+{v[176:191]}"(sb1),
              "+{v[208:223]}"(kvr), "+{v[224:239]}"(misc), "+{s[40:41]}"(ks), "+{s[42:43]}"(wsr), "+{s45}"(wm0), [cnt] "+s"(cnt)
            : "{v[96:111]}"(nma), "{v[112:127]}"(nmb), "{v[192:207]}"(qq), "{v[240:243]}"(offs), "{s44}"(ring)
            : "scc", "memory");
        const float la = __uint_as_float(misc[4]), lb = __uint_as_float(misc[8]);
        const float ia = __builtin_amdgcn_rcpf(la), ib = __builtin_amdgcn_rcpf(lb);
#pragma unroll
        for (int e = 0; e < 16; ++e) { oa[e] *= ia; ob[e] *= ib; }
        char* dst = act + 3 * STG + cb * 4096;
        st4_1<0>(__builtin_bit_cast(u32x4, pack8<0>(oa)), c.xv, dst); st4_1<1024>(__builtin_bit_cast(u32x4, pack8<1>(oa)), c.xv, dst);
        st4_1<2048>(__builtin_bit_cast(u32x4, pack8<0>(ob)), c.xv, dst); st4_1<3072>(__builtin_bit_cast(u32x4, pack8<1>(ob)), c.xv, dst);
        float chk = 0.f;
#pragma unroll
        for (int e = 0; e < 16; ++e) chk = __builtin_fmaf(oa[e], 0.f, __builtin_fmaf(ob[e], 0.f, chk));
        const bool bad = !(la < 1.2676506e30f) || !(lb < 1.2676506e30f) || !(chk == 0.f);
        wait_bar<4>();
        LAS volatile int* vote = (LAS volatile int*)(smem + 8 * SLOT);
        if (lane == 0) vote[w] = 0;
        if (attempt == 0 && bad) vote[w] = 1;
        lds_bar();
        const int redo = vote[0] | vote[1] | vote[2] | vote[3];
        if (!__builtin_amdgcn_readfirstlane(redo)) break;
        lds_bar();
      }
      waitv<0>();
      set_flag(fact + 3 * 8192 + cb * 8, tag);
    }
    STAMP(15);
    REFRESH();
    {
      f32x16 a0, a1; res_to_acc(xr, a0, a1);
      poll<16>(fact + 3 * 8192, lane * 8, tag);
      STAMP(16);
      bf16x8 x[64];
      const char* xb = act + 3 * STG;
      sfor<0, XL>([&](auto s) { ld_x<s>(x, c, xb); });
      bar();
      gemm64<false, false, HookP6>(c, x, xb, a0, a1, [&](auto s) {
        if constexpr (s == 33) bar();
        if constexpr (s >= 52) pc_kv(IC((s - 52) / 8), IC((s - 52) % 8));
      });
      STAMP(17);
      if (l == 0) {
        store_x(c, r, a0, a1, xr, act + 4 * STG + cb * 4096, stt + 512 * 1024 + (cb >> 1) * 512 + (cb & 1) * 8);
        bar();
        sfor<12, 24>([&](auto i) { pc_kv(IC(i / 8), IC(i % 8)); });
        waitv<12>();
        set_flag(fact + 4 * 8192 + cb * 8, tag);
        sfor<24, 32>([&](auto i) { pc_kv(IC(i / 8), IC(i % 8)); });
      } else {
        char* dst = out + (size_t)(tt * 32 + r) * 2048 + cb * 128 + h * 16;
        *(u32x4*)(dst) = unperm(__builtin_bit_cast(u32x4, pack8<0>(a0))); *(u32x4*)(dst + 32) = unperm(__builtin_bit_cast(u32x4, pack8<1>(a0)));
        *(u32x4*)(dst + 64) = unperm(__builtin_bit_cast(u32x4, pack8<0>(a1))); *(u32x4*)(dst + 96) = unperm(__builtin_bit_cast(u32x4, pack8<1>(a1)));
        if (tt == 0 && cb == 0) set_flag(ws + WS_EPOCH, tag);
      }
    }
    STAMP(18);
  }
}

extern "C" void kernel_launch(void* const* d_in, const int* in_sizes, int n_in, void* d_out, int out_size, void* d_ws, size_t ws_size, hipStream_t stream) {
  k_main<<<256, 256, 0, stream>>>((const char*)d_in[0], (const char*)d_in[1], (const char*)d_in[2], (const char*)d_in[3], (const char*)d_in[5], (const char*)d_in[6],
                                  (const char*)d_in[7], (const char*)d_in[8], (char*)d_ws, (char*)d_out);
}
```

```cpp
#include <hip/hip_runtime.h>
#include <hip/hip_bf16.h>
#include <stdint.h>
#include <type_traits>

typedef __attribute__((ext_vector_type(8))) short bf16x8;
typedef __attribute__((ext_vector_type(4))) short s16x4;
typedef __attribute__((ext_vector_type(2))) __bf16 bf2_t;
typedef __attribute__((ext_vector_type(16))) float f32x16;
typedef __attribute__((ext_vector_type(4))) float f32x4;
typedef __attribute__((ext_vector_type(2))) float f32x2;
typedef __attribute__((ext_vector_type(4))) unsigned u32x4;
typedef __attribute__((ext_vector_type(2))) unsigned u32x2;
#define LAS __attribute__((address_space(3)))
#define DI __device__ __forceinline__
#define MFMA32(a, b, c) __builtin_amdgcn_mfma_f32_32x32x16_bf16((a), (b), (c), 0, 0, 0)
#define MFMA16(a, b, c) __builtin_amdgcn_mfma_f32_16x16x32_bf16((a), (b), (c), 0, 0, 0)
#define SBAR() __builtin_amdgcn_sched_barrier(0)

constexpr int S_ = 2048, D_ = 1024;
constexpr float LOG2E = 1.4426950408889634f;
constexpr float QSCALE = 0.17677669529663687f * LOG2E;

constexpr size_t MB = 1u << 20, KB = 1024;
constexpr size_t STG = 256 * KB;
constexpr size_t WS_COL = 32 * MB;
constexpr size_t C_WT = 0;
constexpr size_t C_KVP = 768 * KB;
constexpr size_t C_ZP = 1024 * KB;
constexpr size_t C_CS = 1040 * KB;
constexpr size_t WS_ST = 64 * MB;
constexpr size_t WS_KVF = WS_ST + 1024 * KB;
constexpr size_t WS_FLAG = WS_ST + 1664 * KB;
constexpr size_t WS_EPOCH = WS_ST + 1920 * KB;
constexpr int FL_ACT = 0, FL_KVP = 8 * 64 * 128, FL_KV = FL_KVP + 2 * 64 * 128, FL_W = FL_KV + 2 * 2048;
typedef unsigned long long u64;

template <int I, int N, class F> DI void sfor(F&& f) { if constexpr (I < N) { f(std::integral_constant<int, I>{}); sfor<I + 1, N>(f); } }

DI unsigned cvtpk(float lo, float hi) { f32x2 v = {lo, hi}; return __builtin_bit_cast(unsigned, __builtin_convertvector(v, bf2_t)); }
DI float bflo(unsigned u) { return __uint_as_float(u << 16); }
DI float bfhi(unsigned u) { return __uint_as_float(u & 0xffff0000u); }
DI float rbf(float x) { return bflo(cvtpk(x, x)); }
DI float swap_add(float v) { auto r = __builtin_amdgcn_permlane32_swap(__float_as_uint(v), __float_as_uint(v), false, false); return __uint_as_float(r[0]) + __uint_as_float(r[1]); }
DI float swap_max(float v) { auto r = __builtin_amdgcn_permlane32_swap(__float_as_uint(v), __float_as_uint(v), false, false); return fmaxf(__uint_as_float(r[0]), __uint_as_float(r[1])); }
DI float swap_min(float v) { auto r = __builtin_amdgcn_permlane32_swap(__float_as_uint(v), __float_as_uint(v), false, false); return fminf(__uint_as_float(r[0]), __uint_as_float(r[1])); }
template <int S> DI bf16x8 pack8(const f32x16& v) {
  u32x4 p = {cvtpk(v[8 * S], v[8 * S + 1]), cvtpk(v[8 * S + 2], v[8 * S + 3]), cvtpk(v[8 * S + 4], v[8 * S + 5]), cvtpk(v[8 * S + 6], v[8 * S + 7])};
  return __builtin_bit_cast(bf16x8, p);
}
DI u32x4 unperm(u32x4 v) {
  auto r0 = __builtin_amdgcn_permlane32_swap(v[0], v[2], false, false);
  auto r1 = __builtin_amdgcn_permlane32_swap(v[1], v[3], false, false);
  return u32x4{r0[0], r1[0], r0[1], r1[1]};
}
DI float lane_get(float v, int src) { return __uint_as_float(__builtin_amdgcn_ds_bpermute(src << 2, __float_as_uint(v))); }
DI s16x4 trread(LAS const char* p) { return __builtin_amdgcn_ds_read_tr16_b64_v4i16((LAS s16x4*)p); }
DI bf16x8 cat8(s16x4 a, s16x4 b) { return __builtin_shufflevector(a, b, 0, 1, 2, 3, 4, 5, 6, 7); }
DI float dot2(unsigned a, unsigned b, float c) { return __builtin_amdgcn_fdot2_f32_bf16(__builtin_bit_cast(bf2_t, a), __builtin_bit_cast(bf2_t, b), c, false); }

template <int OFF> DI void dma(unsigned lds, unsigned v, const char* sb) {
  asm volatile("s_mov_b32 m0, %0\n\ts_nop 0\n\tglobal_load_lds_dwordx4 %1, %2 offset:%3" ::"s"(lds), "v"(v), "s"(sb), "i"(OFF) : "memory");
}
template <int OFF> DI void dma_c(unsigned lds, unsigned v, const char* sb) {
  asm volatile("s_mov_b32 m0, %0\n\ts_nop 0\n\tglobal_load_lds_dwordx4 %1, %2 offset:%3 sc1" ::"s"(lds), "v"(v), "s"(sb), "i"(OFF) : "memory");
}
constexpr int cap63(int n) { return n > 63 ? 63 : n; }
template <int N> DI void wait_bar() { asm volatile("s_waitcnt vmcnt(%0)\n\ts_barrier" ::"i"(cap63(N)) : "memory"); }
DI void lds_bar() { asm volatile("s_waitcnt lgkmcnt(0)\n\ts_barrier" ::: "memory"); }
DI void all_bar() { asm volatile("s_waitcnt vmcnt(0) lgkmcnt(0)\n\ts_barrier" ::: "memory"); }
DI void bar() { asm volatile("s_barrier" ::: "memory"); }
template <bool AG, int OFF> DI void ldx1(bf16x8& d, unsigned voff, const char* sb) {
  if constexpr (AG) asm volatile("global_load_dwordx4 %0, %1, %2 offset:%3 sc1" : "=a"(d) : "v"(voff), "s"(sb), "i"(OFF));
  else asm volatile("global_load_dwordx4 %0, %1, %2 offset:%3 sc1" : "=v"(d) : "v"(voff), "s"(sb), "i"(OFF));
}
template <int OFF> DI void ld2_1(f32x2& d, unsigned voff, const char* sb) { asm volatile("global_load_dwordx2 %0, %1, %2 offset:%3 sc1" : "=v"(d) : "v"(voff), "s"(sb), "i"(OFF)); }
DI void ld1_1(unsigned& d, unsigned voff, const void* sb) { asm volatile("global_load_dword %0, %1, %2 sc1" : "=v"(d) : "v"(voff), "s"(sb) : "memory"); }
template <int OFF> DI void st4_1(u32x4 d, unsigned voff, char* sb) { asm volatile("global_store_dwordx4 %0, %1, %2 offset:%3 sc1\n\ts_nop 1" ::"v"(voff), "v"(d), "s"(sb), "i"(OFF) : "memory"); }
DI void st2_1(f32x2 d, unsigned voff, char* sb) { asm volatile("global_store_dwordx2 %0, %1, %2 sc1" ::"v"(voff), "v"(d), "s"(sb) : "memory"); }
DI void st1_1(float d, unsigned voff, char* sb) { asm volatile("global_store_dword %0, %1, %2 sc1" ::"v"(voff), "v"(d), "s"(sb) : "memory"); }
template <int N> DI void waitv() { asm volatile("s_waitcnt vmcnt(%0)" ::"i"(cap63(N)) : "memory"); }
template <bool AG, int N, class T> DI void waitx(T& d) {
  if constexpr (AG) asm volatile("s_waitcnt vmcnt(%1)" : "+a"(d) : "i"(cap63(N))); else asm volatile("s_waitcnt vmcnt(%1)" : "+v"(d) : "i"(cap63(N)));
}
template <bool SW, bool BA, bool Z, bool NOP = false> DI void mm(f32x16& acc, const bf16x8& a, const bf16x8& b) {
#define MM_(OPS, C, OUT, BC) do { if constexpr (NOP) asm volatile("s_nop 1\n\tv_mfma_f32_32x32x16_bf16 %0, " OPS ", " C : OUT(acc) : "v"(a), BC(b)); \
                                  else asm volatile("v_mfma_f32_32x32x16_bf16 %0, " OPS ", " C : OUT(acc) : "v"(a), BC(b)); } while (0)
  if constexpr (Z) {
    if constexpr (SW) { if constexpr (BA) MM_("%2, %1", "0", "=&v", "a"); else MM_("%2, %1", "0", "=&v", "v"); }
    else { if constexpr (BA) MM_("%1, %2", "0", "=&v", "a"); else MM_("%1, %2", "0", "=&v", "v"); }
  } else {
    if constexpr (SW) { if constexpr (BA) MM_("%2, %1", "%0", "+v", "a"); else MM_("%2, %1", "%0", "+v", "v"); }
    else { if constexpr (BA) MM_("%1, %2", "%0", "+v", "a"); else MM_("%1, %2", "%0", "+v", "v"); }
  }
#undef MM_
}
DI void mdone(f32x16& a) { asm volatile("s_nop 11" : "+v"(a)); }
DI void mdone(f32x16& a, f32x16& b) { asm volatile("s_nop 11" : "+v"(a), "+v"(b)); }
DI void mdone(f32x16& a, f32x16& b, f32x16& c, f32x16& d) { asm volatile("s_nop 11" : "+v"(a), "+v"(b), "+v"(c), "+v"(d)); }
template <class T> DI T opq_v(T v) { asm volatile("" : "+v"(v)); return v; }
template <int OFF> DI void ldx0(bf16x8& d, unsigned voff, const char* sb) { asm volatile("global_load_dwordx4 %0, %1, %2 offset:%3" : "=v"(d) : "v"(voff), "s"(sb), "i"(OFF)); }
template <int OFF> DI void ds_write_a(unsigned addr, const bf16x8& v) { asm volatile("ds_write_b128 %0, %1 offset:%2" ::"v"(addr), "a"(v), "i"(OFF) : "memory"); }
template <int N, class T> DI void wait2(T& a, T& b) { asm volatile("s_waitcnt vmcnt(%2)" : "+v"(a), "+v"(b) : "i"(cap63(N))); }
DI void set_flag(char* flag, u64 tag) {
  unsigned z = 0;
  asm volatile("s_mov_b64 exec, 1\n\tglobal_atomic_swap_x2 %0, %1, %2 sc1\n\ts_mov_b64 exec, -1" ::"v"(z), "v"(tag), "s"(flag) : "memory");
}
DI void ld8_1(u64& d, unsigned voff, const char* sb) { asm volatile("global_load_dwordx2 %0, %1, %2 sc1" : "=v"(d) : "v"(voff), "s"(sb) : "memory"); }
template <int NL> DI void rd_flag(u64& v, unsigned voff, const char* sb) {
  const u64 zero = 0;
  if constexpr (NL == 64) asm volatile("global_atomic_or_x2 %0, %1, %3, %2 sc0 sc1" : "+v"(v) : "v"(voff), "s"(sb), "v"(zero) : "memory");
  else asm volatile("s_mov_b64 exec, %4\n\tglobal_atomic_or_x2 %0, %1, %3, %2 sc0 sc1\n\ts_mov_b64 exec, -1" : "+v"(v) : "v"(voff), "s"(sb), "v"(zero), "s"((1ull << (NL & 63)) - 1) : "memory");
}
template <int NL> DI void poll(const char* base, unsigned voff, u64 tag) {
  for (int spin = 0; spin < (1 << 15); ++spin) {
    u64 v = tag;
    rd_flag<NL>(v, voff, base);
    waitx<false, 0>(v);
    if (__all(v == tag)) break;
  }
  asm volatile("" ::: "memory");
}

template <int RBX> DI void block_map(int& rb, int& cb) {
  const int x = blockIdx.x & 7, i = blockIdx.x >> 3;
  constexpr int CBX = 32 / RBX, GC = 16 / CBX;
  rb = RBX * (x / GC) + i / CBX; cb = CBX * (x % GC) + i % CBX;
}

DI f32x4 sum4(const bf16x8 (&p)[4]) {
  return (__builtin_bit_cast(f32x4, p[0]) + __builtin_bit_cast(f32x4, p[1])) + (__builtin_bit_cast(f32x4, p[2]) + __builtin_bit_cast(f32x4, p[3]));
}
DI void pin4(bf16x8 (&p)[4]) { asm volatile("" : "+v"(p[0]), "+v"(p[1]), "+v"(p[2]), "+v"(p[3])); }
DI void ln_stats(const bf16x8 (&p)[4], float& mean, float& rstd) {
  const f32x4 t = sum4(p);
  const float m = swap_add(t[0] + t[2]) * (1.f / D_);
  const float var = fmaxf(swap_add(t[1] + t[3]) * (1.f / D_) - m * m, 0.f);
  mean = rbf(m);
  rstd = __builtin_amdgcn_rcpf(rbf(__builtin_amdgcn_sqrtf(rbf(rbf(var) + 1e-5f))));
}
DI bf16x8 cs_frag(float c) {
  const float hi = rbf(c), mid = rbf(c - hi), lo = (c - hi) - mid;
  return __builtin_bit_cast(bf16x8, u32x4{cvtpk(hi, mid), cvtpk(lo, 0.f), 0u, 0u});
}
DI float cs_sum(const bf16x8 (&p)[4]) { const f32x4 t = sum4(p); return (t[0] + t[1]) + (t[2] + t[3]); }
DI bf16x8 mean_frag(float mean, bool on) { const float m = on ? -mean : 0.f; return __builtin_bit_cast(bf16x8, u32x4{cvtpk(m, m), cvtpk(m, 0.f), 0u, 0u}); }

#define STAMP(i)

constexpr int SLOT = 16384, XL = 12;
struct WinPtr {
  unsigned b[3];
  DI void init(unsigned a) { sfor<0, 3>([&](auto i) { b[i] = a + i * 65536u; asm volatile("" : "+v"(b[i])); }); }
  template <int OFF> DI bf16x8 frag() const { return *(LAS const bf16x8*)((LAS const char*)(uintptr_t)b[OFF >> 16] + (OFF & 65535)); }
};
struct Ctx { unsigned xv; WinPtr p; };
#define IC(n) std::integral_constant<int, (n)>{}
constexpr int slot_a(int c) { constexpr int m[8] = {6, 0, 1, 2, 3, 7, 4, 5}; return m[c]; }
constexpr int slot_q(int c) { constexpr int m[8] = {8, 0, 1, 2, 3, 7, 4, 5}; return m[c]; }

template <int S> DI void ld_x(bf16x8 (&x)[64], const Ctx& c, const char* xb) { ldx1<false, (S % 8) * 1024 - 4096>(x[S], c.xv, xb + (S / 8) * 8192 + 4096); }
template <class H> constexpr int g64_wait(int s) {
  int n = 0;
  const int t0 = s - XL;
  if (t0 < 0) n += XL - 1 - s; else n += H::n(t0);
  for (int t = t0 < 0 ? 0 : t0 + 1; t < s; ++t) n += (t + XL < 64 ? 1 : 0) + H::n(t);
  return n;
}
template <bool MAPA, bool ZERO, class H, class FX>
DI void gemm64(const Ctx& c, bf16x8 (&x)[64], const char* xb, f32x16& acc0, f32x16& acc1, FX&& hook) {
  bf16x8 f0[3], f1[3];
  auto off = [](int s) constexpr { return (MAPA ? slot_a(s / 8) : s / 8) * SLOT + (s % 8) * 2048; };
  auto rd0 = [&](auto s) { f0[s % 3] = c.p.template frag<off(s)>(); };
  auto rd1 = [&](auto s) { f1[s % 3] = c.p.template frag<off(s) + 1024>(); };
  rd0(IC(0)); rd1(IC(0)); rd0(IC(1)); rd1(IC(1));
  sfor<0, 64>([&](auto s) {
    waitx<false, g64_wait<H>(s)>(x[s]);
    SBAR();
    mm<false, false, ZERO && s == 0>(acc0, f0[s % 3], x[s]);
    SBAR();
    if constexpr (s + 2 < 64) rd0(IC(s + 2));
    if constexpr (s + XL < 64) ld_x<s + XL>(x, c, xb);
    SBAR();
    mm<false, false, ZERO && s == 0>(acc1, f1[s % 3], x[s]);
    SBAR();
    if constexpr (s + 2 < 64) rd1(IC(s + 2));
    hook(s);
    SBAR();
  });
  mdone(acc0, acc1);
}
struct HookP6 { static constexpr int n(int s) { return s >= 52 ? 1 : 0; } };
struct HookP3 { static constexpr int n(int s) { return s >= 52 && s < 60 ? 1 : 0; } };
struct HookP4 { static constexpr int n(int s) { return s == 52 || (s >= 8 && s < 12) ? 1 : 0; } };

DI void res_to_acc(const u32x4 (&xr)[4], f32x16& a0, f32x16& a1) {
#pragma unroll
  for (int e = 0; e < 4; ++e) {
    a0[2 * e] = bflo(xr[0][e]); a0[2 * e + 1] = bfhi(xr[0][e]); a0[8 + 2 * e] = bflo(xr[1][e]); a0[9 + 2 * e] = bfhi(xr[1][e]);
    a1[2 * e] = bflo(xr[2][e]); a1[2 * e + 1] = bfhi(xr[2][e]); a1[8 + 2 * e] = bflo(xr[3][e]); a1[9 + 2 * e] = bfhi(xr[3][e]);
  }
  asm volatile("" : "+v"(a0), "+v"(a1));
}
DI void store_stats(int r, const u32x4 (&xr)[4], char* stp) {
  float s1 = 0.f, s2 = 0.f, s3 = 0.f, s4 = 0.f;
#pragma unroll
  for (int i = 0; i < 4; ++i)
#pragma unroll
    for (int e = 0; e < 4; e += 2) {
      s1 = dot2(xr[i][e], 0x3f803f80u, s1); s2 = dot2(xr[i][e], xr[i][e], s2);
      s3 = dot2(xr[i][e + 1], 0x3f803f80u, s3); s4 = dot2(xr[i][e + 1], xr[i][e + 1], s4);
    }
  st2_1(f32x2{swap_add(s1 + s3), swap_add(s2 + s4)}, (unsigned)(r * 16), stp);
}
DI void store_x(const Ctx& c, int r, const f32x16& a0, const f32x16& a1, u32x4 (&xr)[4], char* dst, char* stp) {
  xr[0] = __builtin_bit_cast(u32x4, pack8<0>(a0)); st4_1<0>(xr[0], c.xv, dst);
  xr[1] = __builtin_bit_cast(u32x4, pack8<1>(a0)); st4_1<1024>(xr[1], c.xv, dst);
  xr[2] = __builtin_bit_cast(u32x4, pack8<0>(a1)); st4_1<2048>(xr[2], c.xv, dst);
  xr[3] = __builtin_bit_cast(u32x4, pack8<1>(a1)); st4_1<3072>(xr[3], c.xv, dst);
  store_stats(r, xr, stp);
}

template <class FX> DI void hedge_exp(const f32x16& p, float sc, f32x16& ep, f32x16& en, FX&& hook) {
  float mx = fmaxf(p[0], p[1]), mn = fminf(p[0], p[1]);
#pragma unroll
  for (int i = 2; i < 16; i += 2) { mx = fmaxf(fmaxf(mx, p[i]), p[i + 1]); mn = fminf(fminf(mn, p[i]), p[i + 1]); }
  mx = swap_max(mx) * sc; mn = swap_min(mn) * sc;
  sfor<0, 16>([&](auto i) {
    SBAR();
    hook(i);
    constexpr int e = i;
    float a = __builtin_amdgcn_exp2f(__builtin_fmaf(p[e], sc, -mx)), b = __builtin_amdgcn_exp2f(__builtin_fmaf(p[e], -sc, mn));
    asm volatile("" : "+v"(a), "+v"(b));
    ep[e] = a; en[e] = b;
  });
  SBAR();
}

constexpr int a_xt(int s) {
  int t = s < 0 ? XL : (s < 15 ? XL + (3 * (s + 1)) / 2 : 34 + (2 * (s - 14)) / 3);
  if (s >= 15 && t < s + 10) t = s + 10;
  return t > 64 ? 64 : t;
}
struct SchedKV {
  int xw[64], ww[8], fw, sw, pw;
  constexpr SchedKV(bool first) : xw{}, ww{}, fw(0), sw(0), pw(0) {
    int pos = 0, xpos[64] = {}, wpos[8] = {}, fpos = 0, spos = 0, ppos = 0;
    for (int s = 0; s < XL; ++s) xpos[s] = ++pos;
    if (first) ++pos;
    for (int s = 0; s < 64; ++s) {
      if ((s + 1) % 8 == 0 && (s + 1) / 8 < 8) ww[(s + 1) / 8] = pos - wpos[(s + 1) / 8];
      if (s == 60) sw = pos - spos;
      xw[s] = pos - xpos[s];
      for (int t = a_xt(s - 1); t < a_xt(s); ++t) xpos[t] = ++pos;
      if ((s < 15 && first) || (s >= 15 && s <= 46)) wpos[(s + 1) / 8 + 2] = ++pos;
      if (s >= 48 && s <= 52) { ++pos; if (s == 51) spos = pos; if (s == 52) ppos = pos; }
      if (s == 56) fpos = ++pos;
    }
    pw = pos - ppos; fw = pos - fpos;
  }
};
constexpr int QP = 30, QC = 42;
constexpr int q_nd(int s) { return s < 6 ? 0 : (s < 14 ? 1 : (s < 42 ? ((s - 14) % 8 < 4 ? 1 : 0) : 0)); }
struct SchedQ {
  int ww[8], pw, lw[33];
  constexpr SchedQ() : ww{}, pw(0), lw{} {
    int pos = 3, stpos = 3, q6 = 0, q7 = 0, ppos = 0, lpos[34] = {};
    for (int s = 0; s < 64; ++s) {
      if ((s + 2) % 8 == 0 && (s + 2) / 8 < 8) {
        const int g = (s + 2) / 8;
        ww[g] = g == 2 ? pos - stpos : (g == 6 ? pos - q6 : (g == 7 ? pos - q7 : 63));
        if (g == 2) ++pos;
      }
      if (s == QC) pw = pos - ppos;
      pos += q_nd(s); if (s == 9) q6 = pos; if (s == 13) q7 = pos;
      if (s == QP) ppos = ++pos;
      if (s >= QC && s < QC + 17) { lpos[2 * (s - QC)] = ++pos; lpos[2 * (s - QC) + 1] = ++pos; }
    }
    for (int i = 0; i < 32; ++i) { lw[i] = pos - lpos[i]; pos += i & 1; }
    lw[32] = pos - lpos[33];
  }
};

#define R_OA "v[64:79]"
#define R_OB "v[80:95]"
#define S0_(n, q, m) "v_mfma_f32_32x32x16_bf16 v[" #n ":" #n "+15], v[208:211], v[" #q ":" #q "+3], v[" #m ":" #m "+15]\n\t"
#define S1_(n, q) "v_mfma_f32_32x32x16_bf16 v[" #n ":" #n "+15], v[212:215], v[" #q "+4:" #q "+7], v[" #n ":" #n "+15]\n\t"
#define PV0_(o, p) "v_smfmac_f32_32x32x32_bf16 v[" #o ":" #o "+15], v[216:219], v[" #p ":" #p "+7], v236\n\t"
#define PV1_(o, p) "v_smfmac_f32_32x32x32_bf16 v[" #o ":" #o "+15], v[220:223], v[" #p "+8:" #p "+15], v236\n\t"
#define L0_(l, p) "v_smfmac_f32_16x16x64_bf16 v[" #l ":" #l "+3], v[224:227], v[" #p ":" #p "+7], v236\n\t"
#define L1_(l, p) "v_smfmac_f32_16x16x64_bf16 v[" #l ":" #l "+3], v[224:227], v[" #p "+8:" #p "+15], v236\n\t"
#define E_(e, i) "v_exp_f32 v[" #e "+" #i "], v[" #e "+" #i "]\n\t"
#define RD_(d, off) "ds_read_b128 v[" #d ":" #d "+3], v237 offset:" #off "\n\t"
#define HALF_(e, n, q, m, p, o, l, X0, X1, X2, X3, X4, Y2) \
  S0_(n, q, m) X0 E_(e, 0) E_(e, 1) E_(e, 2) S1_(n, q) X1 E_(e, 3) E_(e, 4) E_(e, 5) Y2 PV0_(o, p) X2 E_(e, 6) E_(e, 7) E_(e, 8) \
  PV1_(o, p) X3 E_(e, 9) E_(e, 10) E_(e, 11) L0_(l, p) X4 E_(e, 12) E_(e, 13) L1_(l, p) E_(e, 14) E_(e, 15)
#define LG2_ "s_waitcnt lgkmcnt(2)\n\t"
#define LG0_ "s_waitcnt lgkmcnt(0)\n\t"
#define STEP_(b, nb, kv, wv, wo) \
  "s_waitcnt vmcnt(4)\n\ts_barrier\n\t" \
  HALF_(128, 160, 200, 112, 176, 80, 232, RD_(208, b + 2048), RD_(212, b + 3072), RD_(216, b), RD_(220, b + 1024), "", "") LG2_ \
  HALF_(160, 144, 192, 96, 128, 64, 228, "s_add_u32 m0, s44, " #nb "\n\t", "global_load_lds_dwordx4 v" #kv ", s[40:41] sc1\n\t", "", \
        "global_load_lds_dwordx4 v" #kv ", s[40:41] offset:1024 sc1\n\t", "", LG0_) \
  HALF_(144, 176, 200, 112, 160, 80, 232, RD_(208, b + 6144), RD_(212, b + 7168), RD_(216, b + 4096), RD_(220, b + 5120), "", "") LG2_ \
  HALF_(176, 128, 192, 96, 144, 64, 228, "s_mov_b32 m0, s45\n\t", "global_load_lds_dwordx4 v" #wv ", s[42:43] offset:" #wo " sc1\n\t", "", "", "", LG0_)
DI float max16(const f32x16& s) {
  float mx = s[0];
#pragma unroll
  for (int e = 1; e < 16; ++e) mx = fmaxf(mx, s[e]);
  return mx;
}
typedef __attribute__((ext_vector_type(8))) unsigned u32x8;
typedef __attribute__((ext_vector_type(16))) unsigned u32x16;

__global__ __launch_bounds__(256, 1) void k_main(const char* xin, const char* wqkv, const char* wphi, const char* woA, const char* wq, const char* wk, const char* wv,
                                                 const char* woB, char* ws, char* out) {
  __shared__ __attribute__((aligned(16))) char smem_[163840];
  LAS char* const smem = (LAS char*)smem_;
  const unsigned lds0 = (unsigned)(uintptr_t)smem;
  Ctx c;
  int lane = threadIdx.x & 63, w = __builtin_amdgcn_readfirstlane(threadIdx.x >> 6), r = lane & 31, h = lane >> 5, rb, cb;
  block_map<4>(rb, cb);
  int tt = rb * 4 + w, blk = (lane >> 4) & 1, q4 = (lane & 15) >> 2, p4 = lane & 3;
  c.xv = lane * 16; c.p.init(lds0 + c.xv);
  char* flags = ws + WS_FLAG;
  char* col = ws + WS_COL + (size_t)cb * 2 * MB;
  u64 tag;
  u32x4 xr[4];
  float csk, csv;
  auto wt = [&](int m, int ch) { return col + C_WT + m * 131072 + ch * SLOT + w * 4096; };
  auto pc_w = [&](int m, int slot, int ch, auto i) { dma_c<i * 1024>(lds0 + slot * SLOT + w * 4096, c.xv, wt(m, ch)); };
  auto pc_kv = [&](auto g, auto i) { pc_w(1 + i / 4, 2 * (g % 4) + i / 4, g, IC(i % 4)); };
  auto pc_q = [&](auto ch, auto i) { pc_w(0, slot_q(ch), ch, i); };
  auto ld_cs = [&](bf16x8& d, int m, auto i) { ldx1<false, i * 1024>(d, c.xv, col + C_CS + m * 4096); };
  auto raw_in = [&](auto j, int slot) {
    const int hi = rb >> 3, pitch = (j == 0 || (j == 1 && !hi)) ? 6144 : 2048, row = lane >> 3;
    const char* src = (j == 0 ? wqkv + 2048 + hi * 2048 : (j == 1 ? (hi ? woA : wqkv) : (hi ? woB : wq))) + cb * 128 + (size_t)(rb & 7) * 128 * pitch;
    const unsigned v0 = (unsigned)((32 * w + row) * pitch + ((lane & 7) ^ (((row >> 1) & 1) << 2)) * 16);
    sfor<0, 4>([&](auto i) { dma<i * 1024>(lds0 + slot * SLOT + w * 4096, v0 + i * (8 * pitch - 1024), src); });
  };
  auto raw_out = [&](auto j, int slot) {
    const int hi = rb >> 3, ch = rb & 7, m = j == 0 ? 1 + hi : (j == 1 ? 3 * hi : 4 + hi);
    LAS const char* p = smem + slot * SLOT + (w >> 1) * 8192 + (4 * h + q4) * 128 + (((((q4 >> 1) ^ w) & 1) << 2) + 2 * blk + (p4 >> 1)) * 16 + 8 * (p4 & 1);
    char* dst = col + C_WT + m * 131072 + ch * SLOT + (w >> 1) * 8192 + (w & 1) * 1024 + 4096;
    float cs = 0.f;
    sfor<0, 4>([&](auto s) {
      const u32x4 f = __builtin_bit_cast(u32x4, cat8(trread(p + s * 2048), trread(p + s * 2048 + 1024)));
      st4_1<s * 2048 - 4096>(f, c.xv, dst);
#pragma unroll
      for (int e = 0; e < 4; ++e) cs = dot2(f[e], 0x3f803f80u, cs);
    });
    st1_1(swap_add(cs), (unsigned)(r * 16), col + C_CS + m * 4096 + (ch >> 1) * 1024 + (w & 1) * 512 + ((ch & 1) * 2 + (w >> 1)) * 4);
  };
  {
    [[maybe_unused]] const int l = 0;
    STAMP(30);
    raw_in(IC(0), 7);
    u64 ep; ld8_1(ep, 0u, ws + WS_EPOCH);
    unsigned tch;
    ld1_1(tch, (unsigned)(h ? WS_COL + cb * 2 * MB + 1536 * KB : rb * 2 * MB + 7 * STG + w * 65536 + cb * 4096), ws);
    bf16x8 xt[4];
    sfor<0, 4>([&](auto i) { ldx0<i * 32>(xt[i], (unsigned)(r * 2048 + h * 16), xin + (size_t)tt * 65536 + cb * 128); });
    bf16x8 cf;
    { const char* pc; asm volatile("s_getpc_b64 %0" : "=s"(pc));
      const int i = (blockIdx.x >> 3) * 4 + w;
      ldx0<0>(cf, c.xv, pc + (i % 46) * 1024); }
    wait_bar<7>();
    raw_out(IC(0), 7);
    asm volatile("s_waitcnt vmcnt(6)" : "+v"(tch), "+v"(ep), "+v"(xt[0]), "+v"(xt[1]), "+v"(xt[2]), "+v"(xt[3]));
    { char* dst = ws + (size_t)rb * 2 * MB + w * 65536 + cb * 4096;
      sfor<0, 4>([&](auto i) { xr[i] = unperm(__builtin_bit_cast(u32x4, xt[i])); st4_1<i * 1024>(xr[i], c.xv, dst); });
      store_stats(r, xr, ws + WS_ST + tt * 4096 + (cb >> 1) * 512 + (cb & 1) * 8); }
    { u64 z = ((u64)__builtin_amdgcn_readfirstlane((unsigned)(ep >> 32)) << 32 | __builtin_amdgcn_readfirstlane((unsigned)ep)) + 0x9E3779B97F4A7C15ull;
      z = (z ^ (z >> 30)) * 0xBF58476D1CE4E5B9ull; z = (z ^ (z >> 27)) * 0x94D049BB133111EBull; tag = z ^ (z >> 31); }
    waitx<false, 0>(cf);
    set_flag(flags + FL_ACT + tt * 128 + cb * 8, tag);
    bar();
    if (w == 0) set_flag(flags + FL_W + cb * 128 + rb * 8, tag);
    poll<32>(flags, (unsigned)((lane & 15) * 8 + (blk ? FL_W + cb * 128 : FL_ACT + tt * 128)), tag);
    bf16x8 ck[4], cv[4];
    sfor<0, 4>([&](auto i) { ld_cs(ck[i], 1, i); }); sfor<0, 4>([&](auto i) { ld_cs(cv[i], 2, i); });
    sfor<0, 16>([&](auto i) { pc_kv(IC(i / 8), IC(i % 8)); });
    waitv<16>(); pin4(ck); pin4(cv);
    csk = cs_sum(ck); csv = cs_sum(cv);
    STAMP(31);
  }
  char *act, *stt, *fact;
#define REFRESH() do { \
    asm volatile("" : "+s"(w), "+s"(cb), "+s"(rb), "+s"(ws), "+s"(tag)); \
    asm volatile("" : "+v"(lane), "+v"(csk), "+v"(csv)); \
    r = lane & 31; h = lane >> 5; blk = (lane >> 4) & 1; q4 = (lane & 15) >> 2; p4 = lane & 3; tt = rb * 4 + w; \
    flags = ws + WS_FLAG; col = ws + WS_COL + (size_t)cb * 2 * MB; c.xv = lane * 16; c.p.init(lds0 + c.xv); \
    act = ws + (size_t)rb * 2 * MB + l * 4 * STG + w * 65536;     \
    fact = flags + FL_ACT + (l * 4 * 64 + tt) * 128;                  \
    stt = ws + WS_ST + (size_t)l * 512 * 1024 + tt * 4096;            \
  } while (0)
  for (int l = 0; l < 2; ++l) {
    REFRESH();
    float mean, rstd, csb;
    bf16x8 park[32];
    STAMP(0);
    {
      if (l > 0) poll<16>(fact, lane * 8, tag);
      STAMP(1);
      constexpr int NA = 60;
      bf16x8 x[64];
      f32x16 ak0, ak1, av0, av1;
      auto issue_x = [&](auto s) { ldx1<(s < NA), (s % 8) * 1024 - 4096>(x[s], c.xv, act + (s / 8) * 8192 + 4096); };
      {
        constexpr SchedKV SC(false), S1(true);
        bf16x8 sp[4], f[4][2], ck, cv;
        u64 f1;
        sfor<0, XL>(issue_x);
        wait_bar<XL>();
        if (l == 0) pc_kv(IC(2), IC(0));
        auto rd = [&](auto s, auto t) { f[t][s % 2] = c.p.template frag<(2 * ((s / 8) % 4) + t / 2) * SLOT + (s % 8) * 2048 + (t % 2) * 1024>(); };
        sfor<0, 4>([&](auto t) { rd(IC(0), t); });
        sfor<0, 64>([&](auto s) {
          if constexpr ((s + 1) % 8 == 0 && (s + 1) / 8 >= 2 && (s + 1) / 8 < 8) wait_bar<((s + 1) / 8 < 4 ? S1.ww[(s + 1) / 8] : SC.ww[(s + 1) / 8])>();
          if constexpr (s == 47) { if (l == 0 && w == 0) set_flag(flags + FL_W + 2048 + cb * 128 + rb * 8, tag); }
          if constexpr (s == 60) { waitv<SC.sw>(); pin4(sp); }
          waitx<(s < NA), SC.xw[s]>(x[s]);
          SBAR();
          mm<false, (s < NA), s == 0>(ak0, f[0][s % 2], x[s]);
          SBAR();
          if constexpr (s + 1 < 64) rd(IC(s + 1), IC(0));
          if constexpr (a_xt(s - 1) < a_xt(s)) issue_x(IC(a_xt(s - 1)));
          if constexpr (s == 60) ln_stats(sp, mean, rstd);
          SBAR();
          mm<false, (s < NA), s == 0>(ak1, f[1][s % 2], x[s]);
          SBAR();
          if constexpr (s + 1 < 64) rd(IC(s + 1), IC(1));
          if constexpr (a_xt(s - 1) + 1 < a_xt(s)) issue_x(IC(a_xt(s - 1) + 1));
          if constexpr (s == 58) ck = opq_v(cs_frag(csk));
          SBAR();
          mm<true, (s < NA), s == 0>(av0, f[2][s % 2], x[s]);
          SBAR();
          if constexpr (s + 1 < 64) rd(IC(s + 1), IC(2));
          if constexpr (s < 15) { if (l == 0) pc_kv(IC((s + 1) / 8 + 2), IC((s + 1) % 8)); }
          if constexpr (s >= 15 && s <= 46) pc_kv(IC((s + 1) / 8 + 2), IC((s + 1) % 8));
          if constexpr (s == 58) cv = opq_v(cs_frag(csv));
          SBAR();
          mm<true, (s < NA), s == 0>(av1, f[3][s % 2], x[s]);
          SBAR();
          if constexpr (s + 1 < 64) rd(IC(s + 1), IC(3));
          if constexpr (s == 1) { if (l == 0) raw_in(IC(1), 8); }
          if constexpr (s == 4) { if (l == 0) raw_in(IC(2), 9); }
          if constexpr (s == 24) { if (l == 0) raw_out(IC(1), 8); }
          if constexpr (s == 26) { if (l == 0) raw_out(IC(2), 9); }
          if constexpr (s == 56) { f1 = tag; rd_flag<16>(f1, lane * 8, flags + FL_W + 2048 + cb * 128); }
          if constexpr (s >= 48 && s < 52) ldx1<false, (s - 48) * 512>(sp[s - 48], (unsigned)(h * 2048 + r * 16), stt);
          if constexpr (s == 52) dma<0>(lds0 + 9 * SLOT + w * 1024, c.xv, wphi + cb * 4096 + w * 1024);
          if constexpr (s == 62) {
            const bf16x8 m0 = mean_frag(mean, h == 0), m1 = mean_frag(mean, h == 1);
            mm<false, false, false, true>(ak0, ck, m0); mm<false, false, false, true>(ak1, ck, m1);
            mm<true, false, false>(av0, cv, m0); mm<true, false, false>(av1, cv, m1);
          }
          SBAR();
        });
        mdone(ak0, ak1, av0, av1);
        waitx<false, SC.fw>(f1);
        if (!__all(f1 == tag)) poll<16>(flags + FL_W + 2048 + cb * 128, lane * 8, tag);
        bar();
      }
      STAMP(2);
      REFRESH();
      LAS char* pk_img = smem + 4 * SLOT;
      LAS char* v_img = smem + 6 * SLOT;
      char* kvp = col + C_KVP + l * 131072 + w * 32768;
      char* zp = col + C_ZP + l * 8192 + w * 2048;
      char* fkvp = flags + FL_KVP + (l * 64 + cb * 4 + w) * 128;
      const float sce = rstd * LOG2E;
      auto phi_mm = [&](const f32x16& t0, const f32x16& t1, f32x16& p0, f32x16& p1) {
        LAS const char* wp = smem + 9 * SLOT + (4 * h + q4) * 64 + blk * 32 + p4 * 8;
        const bf16x8 w00 = cat8(trread(wp), trread(wp + 512)), w01 = cat8(trread(wp + 1024), trread(wp + 1536));
        const bf16x8 w10 = cat8(trread(wp + 2048), trread(wp + 2560)), w11 = cat8(trread(wp + 3072), trread(wp + 3584));
        mm<false, false, true, true>(p0, w00, pack8<0>(t0)); mm<false, false, false, true>(p0, w01, pack8<1>(t0));
        mm<false, false, true, true>(p1, w10, pack8<0>(t1)); mm<false, false, false, true>(p1, w11, pack8<1>(t1));
      };
      bf16x8 cq[4], cw[4];
      {
        auto side = [&](auto i) {
          SBAR();
          if constexpr (i < 24) pc_q(IC(i / 4), IC(i % 4)); else if constexpr (i < 28) ld_cs(cq[i - 24], 0, IC(i - 24)); else ld_cs(cw[i - 28], 4, IC(i - 28));
          SBAR();
        };
        f32x16 p[2];
        side(IC(0)); side(IC(1));
        phi_mm(ak0, ak1, p[0], p[1]);
        side(IC(2)); side(IC(3));
        {
          LAS char* d = v_img + w * 2048 + lane * 16;
          float rs[16];
#pragma unroll
          for (int i = 0; i < 16; ++i) rs[i] = lane_get(rstd, (i & 3) + 8 * (i >> 2) + 4 * h);
          side(IC(4));
#pragma unroll
          for (int i = 0; i < 16; ++i) av0[i] *= rs[i];
          *(LAS bf16x8*)d = pack8<0>(av0); *(LAS bf16x8*)(d + 1024) = pack8<1>(av0);
          side(IC(5));
#pragma unroll
          for (int i = 0; i < 16; ++i) av1[i] *= rs[i];
          *(LAS bf16x8*)(d + 8192) = pack8<0>(av1); *(LAS bf16x8*)(d + 9216) = pack8<1>(av1);
        }
        asm volatile("" : "+v"(p[0]), "+v"(p[1]));
        sfor<0, 2>([&](auto hd) {
          f32x16 ep, en;
          hedge_exp(p[hd], sce, ep, en, [&](auto i) { if constexpr (i % 2 == 0) side(IC(6 + hd * 13 + i / 2)); });
          float sp = 0.f, sn = 0.f;
#pragma unroll
          for (int i = 0; i < 16; ++i) { sp += ep[i]; sn += en[i]; }
          side(IC(14 + hd * 13));
          sp = __builtin_amdgcn_rcpf(swap_add(sp)); sn = __builtin_amdgcn_rcpf(swap_add(sn));
          LAS char* row = pk_img + hd * 16384 + (w * 32 + r) * 128 + h * 8;
          sfor<0, 4>([&](auto g) {
            constexpr int gq = g;
            *(LAS u32x2*)(row + ((gq ^ (r & 7)) << 4)) = u32x2{cvtpk(ep[4 * gq] * sp, ep[4 * gq + 1] * sp), cvtpk(ep[4 * gq + 2] * sp, ep[4 * gq + 3] * sp)};
            *(LAS u32x2*)(row + (((4 + gq) ^ (r & 7)) << 4)) = u32x2{cvtpk(en[4 * gq] * sn, en[4 * gq + 1] * sn), cvtpk(en[4 * gq + 2] * sn, en[4 * gq + 3] * sn)};
            side(IC(15 + hd * 13 + gq));
          });
        });
      }
      asm volatile("s_waitcnt vmcnt(8) lgkmcnt(0)\n\ts_barrier" ::: "memory");
      {
        const int hd = w >> 1, fh = w & 1;
        f32x16 kv, z;
        const unsigned one2 = opq_v(0x3f803f80u);
        const bf16x8 ones = __builtin_bit_cast(bf16x8, u32x4{one2, one2, one2, one2});
        const int ch = fh * 4 + 2 * blk + (p4 >> 1), r0 = 4 * h + q4;
        LAS const char* pa = pk_img + hd * 16384 + r0 * 128 + ((ch ^ r0) << 4) + 8 * (p4 & 1);
        LAS const char* pb = v_img + hd * 8192 + lane * 16;
        bf16x8 a[8], b[8];
#pragma unroll
        for (int s = 0; s < 8; ++s) { a[s] = cat8(trread(pa + s * 2048), trread(pa + s * 2048 + 1024)); b[s] = *(LAS const bf16x8*)(pb + s * 1024); }
        SBAR();
        sfor<0, 8>([&](auto s) { mm<false, false, s == 0>(kv, a[s], b[s]); });
        sfor<0, 8>([&](auto s) {
          mm<false, false, s == 0, s == 0>(z, ones, a[s]);
          if constexpr (s == 1) { mdone(kv); st4_1<0>(__builtin_bit_cast(u32x4, pack8<0>(kv)), c.xv, kvp + rb * 2048); }
          if constexpr (s == 2) st4_1<1024>(__builtin_bit_cast(u32x4, pack8<1>(kv)), c.xv, kvp + rb * 2048);
          if constexpr (s == 3) { waitv<2>(); pin4(cq); pin4(cw); cq[0] = cs_frag(cs_sum(cq)); pin4(cq); }
          if constexpr (s == 5) { csb = opq_v(cs_sum(cw)); }
          SBAR();
        });
        mdone(z);
        st1_1(z[0], (unsigned)(r * 4), zp + rb * 128);
      }
      STAMP(3);
      f32x16 aq0, aq1;
      bf16x8 pp[32], zq[2];
      constexpr SchedQ SQ;
      auto ld_park = [&](auto i) { ldx1<true, (i % 4) * 1024>(park[i], c.xv, wt(4, i / 4)); };
      {
        bf16x8 f0[3], f1[3];
        u64 fv;
        auto off = [](int s) constexpr { return slot_q(s / 8) * SLOT + (s % 8) * 2048; };
        auto rd0 = [&](auto s) { f0[s % 3] = c.p.template frag<off(s)>(); };
        auto rd1 = [&](auto s) { f1[s % 3] = c.p.template frag<off(s) + 1024>(); };
        auto ldp = [&](auto i) {
          if constexpr (i < 32) ldx1<true, (i % 8) * 1024 - 4096>(pp[i], c.xv, kvp + (i / 8) * 8192 + 4096); else ldx1<false, (i - 32) * 1024>(zq[i - 32], c.xv, zp);
        };
        rd0(IC(0)); rd1(IC(0)); rd0(IC(1)); rd1(IC(1));
        sfor<0, 64>([&](auto s) {
          if constexpr ((s + 2) % 8 == 0 && (s + 2) / 8 < 8) {
            wait_bar<SQ.ww[(s + 2) / 8]>();
            if constexpr ((s + 2) / 8 == 2) set_flag(fkvp + rb * 8, tag);
          }
          if constexpr (s == QC) { waitx<false, SQ.pw>(fv); if (!__all(fv == tag)) poll<16>(fkvp, lane * 8, tag); }
          SBAR();
          mm<false, (s < NA), s == 0>(aq0, f0[s % 3], x[s]);
          SBAR();
          if constexpr (s + 2 < 64) rd0(IC(s + 2));
          if constexpr (q_nd(s)) {
            if constexpr (s < 14) pc_q(IC(6 + (s - 6) / 4), IC((s - 6) % 4)); else pc_w(3, slot_a((s - 14) / 8), (s - 14) / 8, IC((s - 14) % 8));
          }
          if constexpr (s == QP) { fv = tag; rd_flag<16>(fv, lane * 8, fkvp); }
          if constexpr (s >= QC && s < QC + 17) ldp(IC(2 * (s - QC)));
          SBAR();
          mm<false, (s < NA), s == 0>(aq1, f1[s % 3], x[s]);
          SBAR();
          if constexpr (s + 2 < 64) rd1(IC(s + 2));
          if constexpr (s >= QC && s < QC + 17) ldp(IC(2 * (s - QC) + 1));
          if constexpr (s == 2) { mm<false, false, false, true>(aq0, cq[0], mean_frag(mean, h == 0)); mm<false, false, false, true>(aq1, cq[0], mean_frag(mean, h == 1)); }
          SBAR();
        });
        mdone(aq0, aq1);
        bar();
      }
      STAMP(4);
      REFRESH();
      bf16x8 pq[2][4];
      f32x16 kv;
      {
        f32x16 p[2];
        phi_mm(aq0, aq1, p[0], p[1]);
        u32x4 s0, s1;
#pragma unroll
        for (int e = 0; e < 4; ++e) {
          const int kr = 8 * (e >> 1) + 4 * h + 2 * (e & 1), rr = r & 15;
          const unsigned v = rr == kr ? 0x3f80u : (rr == kr + 1 ? 0x3f800000u : 0u);
          s0[e] = r < 16 ? v : 0u; s1[e] = r < 16 ? 0u : v;
        }
        const bf16x8 sel0 = opq_v(__builtin_bit_cast(bf16x8, s0)), sel1 = opq_v(__builtin_bit_cast(bf16x8, s1));
        mdone(p[0], p[1]);
        sfor<0, 2>([&](auto hd) {
          f32x16 ep, en;
          hedge_exp(p[hd], sce, ep, en, [&](auto e) {
            constexpr int i = hd * 16 + e;
            waitx<true, SQ.lw[i]>(pp[i]);
            mm<false, true, i == 0>(kv, (i & 1) ? sel1 : sel0, pp[i]);
            if constexpr (i & 1) pc_w(3, slot_a(4 + i / 8), 4 + i / 8, IC((i / 2) % 4));
          });
          pq[hd][0] = pack8<0>(ep); pq[hd][1] = pack8<1>(ep); pq[hd][2] = pack8<0>(en); pq[hd][3] = pack8<1>(en);
          pin4(pq[hd]);
        });
      }
      STAMP(5);
      {
        asm volatile("s_waitcnt vmcnt(%2)" : "+v"(zq[0]), "+v"(zq[1]) : "i"(SQ.lw[32]));
        LAS char* sh = smem + 9 * SLOT + 4096;
        ld_park(IC(0)); ld_park(IC(1));
        mdone(kv);
        *(LAS bf16x8*)(sh + w * 2048 + lane * 16) = pack8<0>(kv); *(LAS bf16x8*)(sh + w * 2048 + 1024 + lane * 16) = pack8<1>(kv);
        f32x4 zs = __builtin_bit_cast(f32x4, zq[0]) + __builtin_bit_cast(f32x4, zq[1]);
#pragma unroll
        for (int e = 0; e < 4; ++e) zs[e] += lane_get(zs[e], lane ^ 8);
        ld_park(IC(2)); ld_park(IC(3));
#pragma unroll
        for (int e = 0; e < 4; ++e) zs[e] += lane_get(zs[e], lane ^ 16);
        ld_park(IC(4)); ld_park(IC(5));
#pragma unroll
        for (int e = 0; e < 4; ++e) zs[e] = swap_add(zs[e]);
        if (lane < 8) *(LAS u32x2*)(sh + 8192 + w * 64 + (lane & 1) * 32 + (lane >> 2) * 16 + ((lane >> 1) & 1) * 8) = u32x2{cvtpk(zs[0], zs[1]), cvtpk(zs[2], zs[3])};
        else if (lane < 24) *(LAS unsigned*)(sh + 8192 + 256 + w * 64 + (lane - 8) * 4) = opq_v(0x3f803f80u);
        ld_park(IC(6)); ld_park(IC(7));
        lds_bar();
        STAMP(6);
        char* dst = act + STG + cb * 4096;
        LAS const char* zb = sh + 8192 + (r & 1) * 256 + h * 32;
        bf16x8 ka[8], za[8];
#pragma unroll
        for (int k = 0; k < 8; ++k) { za[k] = *(LAS const bf16x8*)(zb + (k >> 1) * 64 + (k & 1) * 16); ka[k] = *(LAS const bf16x8*)(sh + k * 1024 + lane * 16); }
        f32x16 nm[4], dn[2];
        float ai[2], bi[2];
        auto coef = [&](auto hd) {
          const float a = __builtin_amdgcn_rcpf(dn[0][1]), b = __builtin_amdgcn_rcpf(dn[1][1]);
          const float inv = __builtin_amdgcn_rcpf(dn[0][0] * a + dn[1][0] * b + 1e-6f);
          ai[hd] = opq_v(a * inv); bi[hd] = opq_v(b * inv);
        };
        auto fin = [&](auto hd) {
          f32x16 o;
#pragma unroll
          for (int i = 0; i < 16; ++i) o[i] = nm[2 * hd][i] * ai[hd] + nm[2 * hd + 1][i] * bi[hd];
          st4_1<hd * 2048>(__builtin_bit_cast(u32x4, pack8<0>(o)), c.xv, dst);
          st4_1<hd * 2048 + 1024>(__builtin_bit_cast(u32x4, pack8<1>(o)), c.xv, dst);
        };
        ld_park(IC(8)); ld_park(IC(9));
        sfor<0, 4>([&](auto k) { mm<false, false, k % 2 == 0>(dn[k / 2], za[k], pq[0][k]); if constexpr (k % 2) ld_park(IC(10 + k / 2)); });
        sfor<0, 4>([&](auto k) { mm<false, false, k % 2 == 0>(nm[k / 2], ka[k], pq[0][k]); if constexpr (k % 2) ld_park(IC(12 + k / 2)); });
        mdone(dn[0], dn[1]); coef(IC(0));
        sfor<0, 4>([&](auto k) { mm<false, false, k % 2 == 0>(dn[k / 2], za[4 + k], pq[1][k]); if constexpr (k % 2) ld_park(IC(14 + k / 2)); });
        sfor<0, 4>([&](auto k) { mm<false, false, k % 2 == 0>(nm[2 + k / 2], ka[4 + k], pq[1][k]); if constexpr (k % 2) ld_park(IC(16 + k / 2)); });
        asm volatile("" : "+v"(nm[0]), "+v"(nm[1]));
        fin(IC(0));
        mdone(dn[0], dn[1], nm[2], nm[3]); coef(IC(1));
        fin(IC(1));
        sfor<18, 28>(ld_park);
        waitv<10>();
        set_flag(fact + 8192 + cb * 8, tag);
        sfor<28, 32>(ld_park);
      }
    }
    STAMP(7);
    REFRESH();
    {
      f32x16 a0, a1; res_to_acc(xr, a0, a1);
      poll<16>(fact + 8192, lane * 8, tag);
      sfor<0, 32>([&](auto i) { waitx<true, 63>(park[i]); });
      STAMP(8);
      bf16x8 x[64];
      const char* xb = act + STG;
      sfor<0, XL>([&](auto s) { ld_x<s>(x, c, xb); });
      bar();
      const char* wkv = ((cb & 2) ? wv : wk) + w * 256 * 64;
      const unsigned kvv = (unsigned)((lane >> 1) * 64 + (cb & 1) * 32 + (lane & 1) * 16);
      const unsigned wa = lds0 + w * 4096 + c.xv, wb = wa + 65536;
      auto unpark = [&](auto i) {
        constexpr int off = slot_a(i / 4) * SLOT + (i % 4) * 1024;
        ds_write_a<(off & 65535)>(off < 65536 ? wa : wb, park[i]);
      };
      gemm64<true, false, HookP3>(c, x, xb, a0, a1, [&](auto s) {
        if constexpr (s == 33) bar();
        if constexpr (s >= 36 && s < 52) unpark(IC(s - 36));
        if constexpr (s >= 52 && s < 60) dma<0>(lds0 + 8 * SLOT + w * 8192 + (s - 52) * 1024, kvv, wkv + (s - 52) * 32 * 64);
      });
      STAMP(9);
      store_x(c, r, a0, a1, xr, act + 2 * STG + cb * 4096, stt + 256 * 1024 + (cb >> 1) * 512 + (cb & 1) * 8);
      bar();
      sfor<16, 32>(unpark);
      waitv<0>();
      set_flag(fact + 2 * 8192 + cb * 8, tag);
    }
    STAMP(10);
    REFRESH();
    u32x16 qq;
    char* kvf = ws + WS_KVF + l * 320 * KB;
    bf16x8 k0, k1;
    {
      char* fkv = flags + FL_KV + l * 2048;
      const int du = (cb >> 2) - w, ty = cb & 3;
      LAS char* reg = smem + 8 * SLOT;
      bf16x8 fu[16], cu, cs;
      { LAS const char* p = reg + w * 8192 + (4 * h + q4) * 32 + p4 * 8;
        float t0 = 0.f, t1 = 0.f;
#pragma unroll
        for (int i = 0; i < 16; ++i) {
          fu[i] = cat8(trread(p + i * 512), trread(p + i * 512 + 256));
          const u32x4 f = __builtin_bit_cast(u32x4, fu[i]);
          t0 = dot2(f[0], 0x3f803f80u, t0); t1 = dot2(f[1], 0x3f803f80u, t1); t0 = dot2(f[2], 0x3f803f80u, t0); t1 = dot2(f[3], 0x3f803f80u, t1);
        }
        cu = opq_v(cs_frag(swap_add(t0 + t1))); cs = opq_v(cs_frag(csb)); }
      poll<32>(fact + 2 * 8192 - w * 128, (unsigned)((lane & 15) * 8 + (blk ? cb >> 2 : w) * 128), tag);
      STAMP(11);
      const char* xb = act + 2 * STG; const char* stp = stt + 256 * 1024;
      bf16x8 x[64], xu[16], su[4], sp[4];
      sfor<0, 16>([&](auto i) { ldx1<true, (i % 8) * 1024 - 4096>(xu[i], c.xv, xb + (ptrdiff_t)du * 65536 + w * 16384 + (i / 8) * 8192 + 4096); });
      sfor<0, 4>([&](auto i) { ldx1<false, i * 512>(su[i], (unsigned)(h * 2048 + r * 16), stp + du * 4096); });
      f32x16 au, av;
      sfor<0, 16>([&](auto i) {
        waitx<true, 15 - i + 4 + (i < XL ? i : XL)>(xu[i]);
        mm<false, true, i == 0>(au, fu[i], xu[i]);
        if constexpr (i < XL) ld_x<i>(x, c, xb);
        mm<true, true, i == 0>(av, fu[i], xu[i]);
      });
      lds_bar();
      STAMP(12);
      f32x16 a0, a1;
      u64 fa;
      float mu, ru;
      gemm64<true, true, HookP4>(c, x, xb, a0, a1, [&](auto s) {
        if constexpr (s == 0) { pin4(su); ln_stats(su, mu, ru); }
        if constexpr (s == 1) { const bf16x8 mf = mean_frag(mu, h == 0); mm<false, false, false, true>(au, cu, mf); mm<true, false, false>(av, cu, mf); }
        if constexpr (s == 4) {
          asm volatile("" : "+v"(au), "+v"(av));
          if (ty >= 2) au = av;
#pragma unroll
          for (int gq = 0; gq < 4; ++gq) *(LAS f32x4*)(reg + w * 8192 + gq * 1024 + lane * 16) = f32x4{au[4 * gq], au[4 * gq + 1], au[4 * gq + 2], au[4 * gq + 3]};
        }
        if constexpr (s == 6) {
          lds_bar();
          if (w == 0) {
            char* dst = kvf + (tt + du) * 4096;
            auto frag = [&](auto fs, f32x4& lo, f32x4& hi) {
              lo = f32x4{}; hi = f32x4{};
#pragma unroll
              for (int j = 0; j < 4; ++j) { lo += *(LAS const f32x4*)(reg + j * 8192 + fs * 2048 + lane * 16); hi += *(LAS const f32x4*)(reg + j * 8192 + fs * 2048 + 1024 + lane * 16); }
            };
            f32x4 lo, hi;
            if (ty < 2) {
              frag(IC(0), lo, hi); lo *= ru; hi *= ru;
              st4_1<0>(u32x4{cvtpk(lo[0], lo[1]), cvtpk(lo[2], lo[3]), cvtpk(hi[0], hi[1]), cvtpk(hi[2], hi[3])}, c.xv, dst + ty * 1024);
            } else {
              const unsigned vo = (unsigned)(((ty & 1) * 16 + (r & 15) + 32 * h) * 16);
              sfor<0, 2>([&](auto fs) {
                frag(fs, lo, hi);
#pragma unroll
                for (int e = 0; e < 4; ++e) { lo[e] *= lane_get(ru, 16 * fs + 4 * h + e); hi[e] *= lane_get(ru, 16 * fs + 8 + 4 * h + e); }
                st4_1<2048 + fs * 1024>(unperm(u32x4{cvtpk(lo[0], lo[1]), cvtpk(lo[2], lo[3]), cvtpk(hi[0], hi[1]), cvtpk(hi[2], hi[3])}), vo, dst);
              });
            }
          }
        }
        if constexpr (s >= 8 && s < 12) ldx1<false, (s - 8) * 512>(sp[s - 8], (unsigned)(h * 2048 + r * 16), stp);
        if constexpr (s == 7 + XL) { if (w == 0) set_flag(fkv + ((tt + du) * 4 + ty) * 8, tag); }
        if constexpr (s == 12 + XL) { pin4(sp); ln_stats(sp, mean, rstd); }
        if constexpr (s == 14 + XL) { mm<false, false, false, true>(a0, cs, mean_frag(mean, h == 0)); mm<false, false, false, true>(a1, cs, mean_frag(mean, h == 1)); }
        if constexpr (s == 52) ld8_1(fa, lane * 8, fkv + w * 512);
      });
      waitx<false, 0>(fa);
      for (int spin = 0; spin < (1 << 15) && !__all(fa == tag); ++spin) { __builtin_amdgcn_s_sleep(8); ld8_1(fa, lane * 8, fkv + w * 512); waitx<false, 0>(fa); }
      bar();
      ldx1<false, 0>(k0, c.xv, kvf); ldx1<false, 1024>(k1, c.xv, kvf);
      sfor<0, 6>([&](auto i) { dma_c<(i % 2) * 1024>(lds0 + 8 * SLOT + w * 2048 + (i / 2) * 8192, c.xv, kvf + 2048 + w * 2048 + (i / 2) * 8192); });
      STAMP(13);
      const float sc = rstd * QSCALE;
#pragma unroll
      for (int i = 0; i < 8; ++i) { qq[i] = cvtpk(a0[2 * i] * sc, a0[2 * i + 1] * sc); qq[8 + i] = cvtpk(a1[2 * i] * sc, a1[2 * i + 1] * sc); }
      asm volatile("s_waitcnt vmcnt(6)" : "+v"(k0), "+v"(k1), "+v"(qq));
    }
    STAMP(14);
    REFRESH();
    {
      const bf16x8 qa0 = __builtin_bit_cast(bf16x8, __builtin_shufflevector(qq, qq, 0, 1, 2, 3)), qa1 = __builtin_bit_cast(bf16x8, __builtin_shufflevector(qq, qq, 4, 5, 6, 7));
      const bf16x8 qb0 = __builtin_bit_cast(bf16x8, __builtin_shufflevector(qq, qq, 8, 9, 10, 11)), qb1 = __builtin_bit_cast(bf16x8, __builtin_shufflevector(qq, qq, 12, 13, 14, 15));
      float mA = 0.f, mB = 0.f;
      const char* ksrc = kvf + 2048 + w * 2048;
      const unsigned ring = lds0 + 8 * SLOT + w * 2048;
      for (int attempt = 0; attempt < 2; ++attempt) {
        f32x16 sa0, sb;
        if (attempt == 1) {
          mA = -INFINITY; mB = -INFINITY;
          for (int t = 0; t < 64; ++t) {
            ldx1<false, 0>(k0, c.xv, kvf + t * 4096); ldx1<false, 1024>(k1, c.xv, kvf + t * 4096);
            asm volatile("s_waitcnt vmcnt(0)" : "+v"(k0), "+v"(k1));
            mm<false, false, true>(sa0, k0, qa0); mm<false, false, false>(sa0, k1, qa1); mm<false, false, true>(sb, k0, qb0); mm<false, false, false>(sb, k1, qb1);
            mdone(sa0, sb);
            mA = fmaxf(mA, max16(sa0)); mB = fmaxf(mB, max16(sb));
          }
          mA = swap_max(mA); mB = swap_max(mB);
          ldx1<false, 0>(k0, c.xv, kvf); ldx1<false, 1024>(k1, c.xv, kvf);
          sfor<0, 6>([&](auto i) { dma_c<(i % 2) * 1024>(ring + (i / 2) * 8192, c.xv, ksrc + (i / 2) * 8192); });
          asm volatile("s_waitcnt vmcnt(6)" : "+v"(k0), "+v"(k1));
        }
        mm<false, false, true, true>(sa0, k0, qa0); mm<false, false, false>(sa0, k1, qa1); mm<false, false, true>(sb, k0, qb0); mm<false, false, false>(sb, k1, qb1);
        mdone(sa0, sb);
        if (attempt == 0) { mA = swap_max(max16(sa0)); mB = swap_max(max16(sb)); }
        f32x16 nma, nmb, oa = {}, ob = {}, sb1 = {}, t0, t1;
#pragma unroll
        for (int e = 0; e < 16; ++e) { nma[e] = -mA; nmb[e] = -mB; sa0[e] -= mA; }
        const unsigned o2 = opq_v(0x3f803f80u), s0 = ((lane >> 2) & 1) ? 0u : o2, s1 = o2 - s0;
        u32x16 kvr = {}, misc;
        { const u32x4 a = __builtin_bit_cast(u32x4, k0), b = __builtin_bit_cast(u32x4, k1);
#pragma unroll
          for (int e = 0; e < 4; ++e) { kvr[e] = a[e]; kvr[4 + e] = b[e]; } }
        misc = u32x16{s0, s0, s1, s1, 0u, 0u, 0u, 0u, 0u, 0u, 0u, 0u, 0xddddddddu, lds0 + 8 * SLOT + c.xv, 0u, 0u};
        const u32x4 offs = {c.xv, c.xv + 8192, c.xv + 16384, c.xv + 24576};
        const char* ks = ksrc + 3 * 8192; const char* wsr = wt(5, 0);
        unsigned wm0 = lds0 + w * 4096, cnt = 8;
        asm volatile(
            "s_nop 1\n\t"
            "1:\n\t"
            STEP_(0, 24576, 240, 240, 0) STEP_(8192, 0, 241, 240, 1024) STEP_(16384, 8192, 242, 240, 2048) STEP_(24576, 16384, 243, 240, 3072)
            "s_add_u32 s40, s40, 0x8000\n\ts_addc_u32 s41, s41, 0\n\ts_add_u32 s42, s42, 0x4000\n\ts_addc_u32 s43, s43, 0\n\t"
            "s_add_u32 s45, s45, 0x4000\n\ts_sub_u32 %[cnt], %[cnt], 1\n\ts_cmp_lg_u32 %[cnt], 0\n\ts_cbranch_scc1 1b\n\t"
            PV0_(80, 176) PV1_(80, 176) L0_(232, 176) L1_(232, 176)
            "s_nop 15\n\t"
            : "+{v[64:79]}"(oa), "+{v[80:95]}"(ob), "+{v[128:143]}"(sa0), "=&{v[144:159]}"(t0), "=&{v[160:175]}"(t1), "+{v[176:191]}"(sb1),
              "+{v[208:223]}"(kvr), "+{v[224:239]}"(misc), "+{s[40:41]}"(ks), "+{s[42:43]}"(wsr), "+{s45}"(wm0), [cnt] "+s"(cnt)
            : "{v[96:111]}"(nma), "{v[112:127]}"(nmb), "{v[192:207]}"(qq), "{v[240:243]}"(offs), "{s44}"(ring)
            : "scc", "memory");
        const float la = __uint_as_float(misc[4]), lb = __uint_as_float(misc[8]);
        const float ia = __builtin_amdgcn_rcpf(la), ib = __builtin_amdgcn_rcpf(lb);
#pragma unroll
        for (int e = 0; e < 16; ++e) { oa[e] *= ia; ob[e] *= ib; }
        char* dst = act + 3 * STG + cb * 4096;
        st4_1<0>(__builtin_bit_cast(u32x4, pack8<0>(oa)), c.xv, dst); st4_1<1024>(__builtin_bit_cast(u32x4, pack8<1>(oa)), c.xv, dst);
        st4_1<2048>(__builtin_bit_cast(u32x4, pack8<0>(ob)), c.xv, dst); st4_1<3072>(__builtin_bit_cast(u32x4, pack8<1>(ob)), c.xv, dst);
        float chk = 0.f;
#pragma unroll
        for (int e = 0; e < 16; ++e) chk = __builtin_fmaf(oa[e], 0.f, __builtin_fmaf(ob[e], 0.f, chk));
        const bool bad = !(la < 1.2676506e30f) || !(lb < 1.2676506e30f) || !(chk == 0.f);
        wait_bar<4>();
        LAS volatile int* vote = (LAS volatile int*)(smem + 8 * SLOT);
        if (lane == 0) vote[w] = 0;
        if (attempt == 0 && bad) vote[w] = 1;
        lds_bar();
        const int redo = vote[0] | vote[1] | vote[2] | vote[3];
        if (!__builtin_amdgcn_readfirstlane(redo)) break;
        lds_bar();
      }
      waitv<0>();
      set_flag(fact + 3 * 8192 + cb * 8, tag);
    }
    STAMP(15);
    REFRESH();
    {
      f32x16 a0, a1; res_to_acc(xr, a0, a1);
      poll<16>(fact + 3 * 8192, lane * 8, tag);
      STAMP(16);
      bf16x8 x[64];
      const char* xb = act + 3 * STG;
      sfor<0, XL>([&](auto s) { ld_x<s>(x, c, xb); });
      bar();
      gemm64<false, false, HookP6>(c, x, xb, a0, a1, [&](auto s) {
        if constexpr (s == 33) bar();
        if constexpr (s >= 52) pc_kv(IC((s - 52) / 8), IC((s - 52) % 8));
      });
      STAMP(17);
      if (l == 0) {
        store_x(c, r, a0, a1, xr, act + 4 * STG + cb * 4096, stt + 512 * 1024 + (cb >> 1) * 512 + (cb & 1) * 8);
        bar();
        sfor<12, 24>([&](auto i) { pc_kv(IC(i / 8), IC(i % 8)); });
        waitv<12>();
        set_flag(fact + 4 * 8192 + cb * 8, tag);
        sfor<24, 32>([&](auto i) { pc_kv(IC(i / 8), IC(i % 8)); });
      } else {
        char* dst = out + (size_t)(tt * 32 + r) * 2048 + cb * 128 + h * 16;
        *(u32x4*)(dst) = unperm(__builtin_bit_cast(u32x4, pack8<0>(a0))); *(u32x4*)(dst + 32) = unperm(__builtin_bit_cast(u32x4, pack8<1>(a0)));
        *(u32x4*)(dst + 64) = unperm(__builtin_bit_cast(u32x4, pack8<0>(a1))); *(u32x4*)(dst + 96) = unperm(__builtin_bit_cast(u32x4, pack8<1>(a1)));
        if (tt == 0 && cb == 0) set_flag(ws + WS_EPOCH, tag);
      }
    }
    STAMP(18);
  }
}

extern "C" void kernel_launch(void* const* d_in, const int* in_sizes, int n_in, void* d_out, int out_size, void* d_ws, size_t ws_size, hipStream_t stream) {
  k_main<<<256, 256, 0, stream>>>((const char*)d_in[0], (const char*)d_in[1], (const char*)d_in[2], (const char*)d_in[3], (const char*)d_in[5], (const char*)d_in[6],
                                  (const char*)d_in[7], (const char*)d_in[8], (char*)d_ws, (char*)d_out);
}
```

```cpp
#include <hip/hip_runtime.h>
#include <hip/hip_bf16.h>
#include <stdint.h>
#include <type_traits>

typedef __attribute__((ext_vector_type(8))) short bf16x8;
typedef __attribute__((ext_vector_type(4))) short s16x4;
typedef __attribute__((ext_vector_type(2))) __bf16 bf2_t;
typedef __attribute__((ext_vector_type(16))) float f32x16;
typedef __attribute__((ext_vector_type(4))) float f32x4;
typedef __attribute__((ext_vector_type(2))) float f32x2;
typedef __attribute__((ext_vector_type(4))) unsigned u32x4;
typedef __attribute__((ext_vector_type(2))) unsigned u32x2;
#define LAS __attribute__((address_space(3)))
#define DI __device__ __forceinline__
#define MFMA32(a, b, c) __builtin_amdgcn_mfma_f32_32x32x16_bf16((a), (b), (c), 0, 0, 0)
#define MFMA16(a, b, c) __builtin_amdgcn_mfma_f32_16x16x32_bf16((a), (b), (c), 0, 0, 0)
#define SBAR() __builtin_amdgcn_sched_barrier(0)

constexpr int S_ = 2048, D_ = 1024;
constexpr float LOG2E = 1.4426950408889634f;
constexpr float QSCALE = 0.17677669529663687f * LOG2E;

constexpr size_t MB = 1u << 20, KB = 1024;
constexpr size_t STG = 256 * KB;
constexpr size_t WS_COL = 32 * MB;
constexpr size_t C_WT = 0;
constexpr size_t C_KVP = 768 * KB;
constexpr size_t C_ZP = 1024 * KB;
constexpr size_t C_CS = 1040 * KB;
constexpr size_t WS_ST = 64 * MB;
constexpr size_t WS_KVF = WS_ST + 1024 * KB;
constexpr size_t WS_FLAG = WS_ST + 1664 * KB;
constexpr size_t WS_EPOCH = WS_ST + 1920 * KB;
constexpr int FL_ACT = 0, FL_KVP = 8 * 64 * 128, FL_KV = FL_KVP + 2 * 64 * 128, FL_W = FL_KV + 2 * 2048;
typedef unsigned long long u64;

template <int I, int N, class F> DI void sfor(F&& f) { if constexpr (I < N) { f(std::integral_constant<int, I>{}); sfor<I + 1, N>(f); } }

DI unsigned cvtpk(float lo, float hi) { f32x2 v = {lo, hi}; return __builtin_bit_cast(unsigned, __builtin_convertvector(v, bf2_t)); }
DI float bflo(unsigned u) { return __uint_as_float(u << 16); }
DI float bfhi(unsigned u) { return __uint_as_float(u & 0xffff0000u); }
DI float rbf(float x) { return bflo(cvtpk(x, x)); }
DI float swap_add(float v) { auto r = __builtin_amdgcn_permlane32_swap(__float_as_uint(v), __float_as_uint(v), false, false); return __uint_as_float(r[0]) + __uint_as_float(r[1]); }
DI float swap_max(float v) { auto r = __builtin_amdgcn_permlane32_swap(__float_as_uint(v), __float_as_uint(v), false, false); return fmaxf(__uint_as_float(r[0]), __uint_as_float(r[1])); }
DI float swap_min(float v) { auto r = __builtin_amdgcn_permlane32_swap(__float_as_uint(v), __float_as_uint(v), false, false); return fminf(__uint_as_float(r[0]), __uint_as_float(r[1])); }
template <int S> DI bf16x8 pack8(const f32x16& v) {
  u32x4 p = {cvtpk(v[8 * S], v[8 * S + 1]), cvtpk(v[8 * S + 2], v[8 * S + 3]), cvtpk(v[8 * S + 4], v[8 * S + 5]), cvtpk(v[8 * S + 6], v[8 * S + 7])};
  return __builtin_bit_cast(bf16x8, p);
}
DI u32x4 unperm(u32x4 v) {
  auto r0 = __builtin_amdgcn_permlane32_swap(v[0], v[2], false, false);
  auto r1 = __builtin_amdgcn_permlane32_swap(v[1], v[3], false, false);
  return u32x4{r0[0], r1[0], r0[1], r1[1]};
}
DI float lane_get(float v, int src) { return __uint_as_float(__builtin_amdgcn_ds_bpermute(src << 2, __float_as_uint(v))); }
DI s16x4 trread(LAS const char* p) { return __builtin_amdgcn_ds_read_tr16_b64_v4i16((LAS s16x4*)p); }
DI bf16x8 cat8(s16x4 a, s16x4 b) { return __builtin_shufflevector(a, b, 0, 1, 2, 3, 4, 5, 6, 7); }
DI float dot2(unsigned a, unsigned b, float c) { return __builtin_amdgcn_fdot2_f32_bf16(__builtin_bit_cast(bf2_t, a), __builtin_bit_cast(bf2_t, b), c, false); }

template <int OFF> DI void dma(unsigned lds, unsigned v, const char* sb) {
  asm volatile("s_mov_b32 m0, %0\n\ts_nop 0\n\tglobal_load_lds_dwordx4 %1, %2 offset:%3" ::"s"(lds), "v"(v), "s"(sb), "i"(OFF) : "memory");
}
template <int OFF> DI void dma_c(unsigned lds, unsigned v, const char* sb) {
  asm volatile("s_mov_b32 m0, %0\n\ts_nop 0\n\tglobal_load_lds_dwordx4 %1, %2 offset:%3 sc1" ::"s"(lds), "v"(v), "s"(sb), "i"(OFF) : "memory");
}
constexpr int cap63(int n) { return n > 63 ? 63 : n; }
template <int N> DI void wait_bar() { asm volatile("s_waitcnt vmcnt(%0)\n\ts_barrier" ::"i"(cap63(N)) : "memory"); }
DI void lds_bar() { asm volatile("s_waitcnt lgkmcnt(0)\n\ts_barrier" ::: "memory"); }
DI void all_bar() { asm volatile("s_waitcnt vmcnt(0) lgkmcnt(0)\n\ts_barrier" ::: "memory"); }
DI void bar() { asm volatile("s_barrier" ::: "memory"); }
template <bool AG, int OFF> DI void ldx1(bf16x8& d, unsigned voff, const char* sb) {
  if constexpr (AG) asm volatile("global_load_dwordx4 %0, %1, %2 offset:%3 sc1" : "=a"(d) : "v"(voff), "s"(sb), "i"(OFF));
  else asm volatile("global_load_dwordx4 %0, %1, %2 offset:%3 sc1" : "=v"(d) : "v"(voff), "s"(sb), "i"(OFF));
}
template <int OFF> DI void ld2_1(f32x2& d, unsigned voff, const char* sb) { asm volatile("global_load_dwordx2 %0, %1, %2 offset:%3 sc1" : "=v"(d) : "v"(voff), "s"(sb), "i"(OFF)); }
DI void ld1_1(unsigned& d, unsigned voff, const void* sb) { asm volatile("global_load_dword %0, %1, %2 sc1" : "=v"(d) : "v"(voff), "s"(sb) : "memory"); }
template <int OFF> DI void st4_1(u32x4 d, unsigned voff, char* sb) { asm volatile("global_store_dwordx4 %0, %1, %2 offset:%3 sc1\n\ts_nop 1" ::"v"(voff), "v"(d), "s"(sb), "i"(OFF) : "memory"); }
DI void st2_1(f32x2 d, unsigned voff, char* sb) { asm volatile("global_store_dwordx2 %0, %1, %2 sc1" ::"v"(voff), "v"(d), "s"(sb) : "memory"); }
DI void st1_1(float d, unsigned voff, char* sb) { asm volatile("global_store_dword %0, %1, %2 sc1" ::"v"(voff), "v"(d), "s"(sb) : "memory"); }
template <int N> DI void waitv() { asm volatile("s_waitcnt vmcnt(%0)" ::"i"(cap63(N)) : "memory"); }
template <bool AG, int N, class T> DI void waitx(T& d) {
  if constexpr (AG) asm volatile("s_waitcnt vmcnt(%1)" : "+a"(d) : "i"(cap63(N))); else asm volatile("s_waitcnt vmcnt(%1)" : "+v"(d) : "i"(cap63(N)));
}
template <bool SW, bool BA, bool Z, bool NOP = false> DI void mm(f32x16& acc, const bf16x8& a, const bf16x8& b) {
#define MM_(OPS, C, OUT, BC) do { if constexpr (NOP) asm volatile("s_nop 1\n\tv_mfma_f32_32x32x16_bf16 %0, " OPS ", " C : OUT(acc) : "v"(a), BC(b)); \
                                  else asm volatile("v_mfma_f32_32x32x16_bf16 %0, " OPS ", " C : OUT(acc) : "v"(a), BC(b)); } while (0)
  if constexpr (Z) {
    if constexpr (SW) { if constexpr (BA) MM_("%2, %1", "0", "=&v", "a"); else MM_("%2, %1", "0", "=&v", "v"); }
    else { if constexpr (BA) MM_("%1, %2", "0", "=&v", "a"); else MM_("%1, %2", "0", "=&v", "v"); }
  } else {
    if constexpr (SW) { if constexpr (BA) MM_("%2, %1", "%0", "+v", "a"); else MM_("%2, %1", "%0", "+v", "v"); }
    else { if constexpr (BA) MM_("%1, %2", "%0", "+v", "a"); else MM_("%1, %2", "%0", "+v", "v"); }
  }
#undef MM_
}
DI void mdone(f32x16& a) { asm volatile("s_nop 11" : "+v"(a)); }
DI void mdone(f32x16& a, f32x16& b) { asm volatile("s_nop 11" : "+v"(a), "+v"(b)); }
DI void mdone(f32x16& a, f32x16& b, f32x16& c, f32x16& d) { asm volatile("s_nop 11" : "+v"(a), "+v"(b), "+v"(c), "+v"(d)); }
template <class T> DI T opq_v(T v) { asm volatile("" : "+v"(v)); return v; }
template <int OFF> DI void ldx0(bf16x8& d, unsigned voff, const char* sb) { asm volatile("global_load_dwordx4 %0, %1, %2 offset:%3" : "=v"(d) : "v"(voff), "s"(sb), "i"(OFF)); }
template <int OFF> DI void ds_write_a(unsigned addr, const bf16x8& v) { asm volatile("ds_write_b128 %0, %1 offset:%2" ::"v"(addr), "a"(v), "i"(OFF) : "memory"); }
template <int N, class T> DI void wait2(T& a, T& b) { asm volatile("s_waitcnt vmcnt(%2)" : "+v"(a), "+v"(b) : "i"(cap63(N))); }
DI void set_flag(char* flag, u64 tag) {
  unsigned z = 0;
  asm volatile("s_mov_b64 exec, 1\n\tglobal_atomic_swap_x2 %0, %1, %2 sc1\n\ts_mov_b64 exec, -1" ::"v"(z), "v"(tag), "s"(flag) : "memory");
}
DI void ld8_1(u64& d, unsigned voff, const char* sb) { asm volatile("global_load_dwordx2 %0, %1, %2 sc1" : "=v"(d) : "v"(voff), "s"(sb) : "memory"); }
template <int NL> DI void rd_flag(u64& v, unsigned voff, const char* sb) {
  const u64 zero = 0;
  if constexpr (NL == 64) asm volatile("global_atomic_or_x2 %0, %1, %3, %2 sc0 sc1" : "+v"(v) : "v"(voff), "s"(sb), "v"(zero) : "memory");
  else asm volatile("s_mov_b64 exec, %4\n\tglobal_atomic_or_x2 %0, %1, %3, %2 sc0 sc1\n\ts_mov_b64 exec, -1" : "+v"(v) : "v"(voff), "s"(sb), "v"(zero), "s"((1ull << (NL & 63)) - 1) : "memory");
}
template <int NL> DI void poll(const char* base, unsigned voff, u64 tag) {
  for (int spin = 0; spin < (1 << 15); ++spin) {
    u64 v = tag;
    rd_flag<NL>(v, voff, base);
    waitx<false, 0>(v);
    if (__all(v == tag)) break;
  }
  asm volatile("" ::: "memory");
}

template <int RBX> DI void block_map(int& rb, int& cb) {
  const int x = blockIdx.x & 7, i = blockIdx.x >> 3;
  constexpr int CBX = 32 / RBX, GC = 16 / CBX;
  rb = RBX * (x / GC) + i / CBX; cb = CBX * (x % GC) + i % CBX;
}

DI f32x4 sum4(const bf16x8 (&p)[4]) {
  return (__builtin_bit_cast(f32x4, p[0]) + __builtin_bit_cast(f32x4, p[1])) + (__builtin_bit_cast(f32x4, p[2]) + __builtin_bit_cast(f32x4, p[3]));
}
DI void pin4(bf16x8 (&p)[4]) { asm volatile("" : "+v"(p[0]), "+v"(p[1]), "+v"(p[2]), "+v"(p[3])); }
DI void ln_stats(const bf16x8 (&p)[4], float& mean, float& rstd) {
  const f32x4 t = sum4(p);
  const float m = swap_add(t[0] + t[2]) * (1.f / D_);
  const float var = fmaxf(swap_add(t[1] + t[3]) * (1.f / D_) - m * m, 0.f);
  mean = rbf(m);
  rstd = __builtin_amdgcn_rcpf(rbf(__builtin_amdgcn_sqrtf(rbf(rbf(var) + 1e-5f))));
}
DI bf16x8 cs_frag(float c) {
  const float hi = rbf(c), mid = rbf(c - hi), lo = (c - hi) - mid;
  return __builtin_bit_cast(bf16x8, u32x4{cvtpk(hi, mid), cvtpk(lo, 0.f), 0u, 0u});
}
DI float cs_sum(const bf16x8 (&p)[4]) { const f32x4 t = sum4(p); return (t[0] + t[1]) + (t[2] + t[3]); }
DI bf16x8 mean_frag(float mean, bool on) { const float m = on ? -mean : 0.f; return __builtin_bit_cast(bf16x8, u32x4{cvtpk(m, m), cvtpk(m, 0.f), 0u, 0u}); }

#define STAMP(i)

constexpr int SLOT = 16384, XL = 12;
struct WinPtr {
  unsigned b[3];
  DI void init(unsigned a) { sfor<0, 3>([&](auto i) { b[i] = a + i * 65536u; asm volatile("" : "+v"(b[i])); }); }
  template <int OFF> DI bf16x8 frag() const { return *(LAS const bf16x8*)((LAS const char*)(uintptr_t)b[OFF >> 16] + (OFF & 65535)); }
};
struct Ctx { unsigned xv; WinPtr p; };
#define IC(n) std::integral_constant<int, (n)>{}
constexpr int slot_a(int c) { constexpr int m[8] = {6, 0, 1, 2, 3, 7, 4, 5}; return m[c]; }
constexpr int slot_q(int c) { constexpr int m[8] = {8, 0, 1, 2, 3, 7, 4, 5}; return m[c]; }

template <int S> DI void ld_x(bf16x8 (&x)[64], const Ctx& c, const char* xb) { ldx1<false, (S % 8) * 1024 - 4096>(x[S], c.xv, xb + (S / 8) * 8192 + 4096); }
template <class H> constexpr int g64_wait(int s) {
  int n = 0;
  const int t0 = s - XL;
  if (t0 < 0) n += XL - 1 - s; else n += H::n(t0);
  for (int t = t0 < 0 ? 0 : t0 + 1; t < s; ++t) n += (t + XL < 64 ? 1 : 0) + H::n(t);
  return n;
}
template <bool MAPA, bool ZERO, class H, class FX>
DI void gemm64(const Ctx& c, bf16x8 (&x)[64], const char* xb, f32x16& acc0, f32x16& acc1, FX&& hook) {
  bf16x8 f0[3], f1[3];
  auto off = [](int s) constexpr { return (MAPA ? slot_a(s / 8) : s / 8) * SLOT + (s % 8) * 2048; };
  auto rd0 = [&](auto s) { f0[s % 3] = c.p.template frag<off(s)>(); };
  auto rd1 = [&](auto s) { f1[s % 3] = c.p.template frag<off(s) + 1024>(); };
  rd0(IC(0)); rd1(IC(0)); rd0(IC(1)); rd1(IC(1));
  sfor<0, 64>([&](auto s) {
    waitx<false, g64_wait<H>(s)>(x[s]);
    SBAR();
    mm<false, false, ZERO && s == 0>(acc0, f0[s % 3], x[s]);
    SBAR();
    if constexpr (s + 2 < 64) rd0(IC(s + 2));
    if constexpr (s + XL < 64) ld_x<s + XL>(x, c, xb);
    SBAR();
    mm<false, false, ZERO && s == 0>(acc1, f1[s % 3], x[s]);
    SBAR();
    if constexpr (s + 2 < 64) rd1(IC(s + 2));
    hook(s);
    SBAR();
  });
  mdone(acc0, acc1);
}
struct HookP6 { static constexpr int n(int s) { return s >= 52 ? 1 : 0; } };
struct HookP3 { static constexpr int n(int s) { return s >= 52 && s < 60 ? 1 : 0; } };
struct HookP4 { static constexpr int n(int s) { return s == 52 || (s >= 8 && s < 12) ? 1 : 0; } };

DI void res_to_acc(const u32x4 (&xr)[4], f32x16& a0, f32x16& a1) {
#pragma unroll
  for (int e = 0; e < 4; ++e) {
    a0[2 * e] = bflo(xr[0][e]); a0[2 * e + 1] = bfhi(xr[0][e]); a0[8 + 2 * e] = bflo(xr[1][e]); a0[9 + 2 * e] = bfhi(xr[1][e]);
    a1[2 * e] = bflo(xr[2][e]); a1[2 * e + 1] = bfhi(xr[2][e]); a1[8 + 2 * e] = bflo(xr[3][e]); a1[9 + 2 * e] = bfhi(xr[3][e]);
  }
  asm volatile("" : "+v"(a0), "+v"(a1));
}
DI void store_stats(int r, const u32x4 (&xr)[4], char* stp) {
  float s1 = 0.f, s2 = 0.f, s3 = 0.f, s4 = 0.f;
#pragma unroll
  for (int i = 0; i < 4; ++i)
#pragma unroll
    for (int e = 0; e < 4; e += 2) {
      s1 = dot2(xr[i][e], 0x3f803f80u, s1); s2 = dot2(xr[i][e], xr[i][e], s2);
      s3 = dot2(xr[i][e + 1], 0x3f803f80u, s3); s4 = dot2(xr[i][e + 1], xr[i][e + 1], s4);
    }
  st2_1(f32x2{swap_add(s1 + s3), swap_add(s2 + s4)}, (unsigned)(r * 16), stp);
}
DI void store_x(const Ctx& c, int r, const f32x16& a0, const f32x16& a1, u32x4 (&xr)[4], char* dst, char* stp) {
  xr[0] = __builtin_bit_cast(u32x4, pack8<0>(a0)); st4_1<0>(xr[0], c.xv, dst);
  xr[1] = __builtin_bit_cast(u32x4, pack8<1>(a0)); st4_1<1024>(xr[1], c.xv, dst);
  xr[2] = __builtin_bit_cast(u32x4, pack8<0>(a1)); st4_1<2048>(xr[2], c.xv, dst);
  xr[3] = __builtin_bit_cast(u32x4, pack8<1>(a1)); st4_1<3072>(xr[3], c.xv, dst);
  store_stats(r, xr, stp);
}

template <class FX> DI void hedge_exp(const f32x16& p, float sc, f32x16& ep, f32x16& en, FX&& hook) {
  float mx = fmaxf(p[0], p[1]), mn = fminf(p[0], p[1]);
#pragma unroll
  for (int i = 2; i < 16; i += 2) { mx = fmaxf(fmaxf(mx, p[i]), p[i + 1]); mn = fminf(fminf(mn, p[i]), p[i + 1]); }
  mx = swap_max(mx) * sc; mn = swap_min(mn) * sc;
  sfor<0, 16>([&](auto i) {
    SBAR();
    hook(i);
    constexpr int e = i;
    float a = __builtin_amdgcn_exp2f(__builtin_fmaf(p[e], sc, -mx)), b = __builtin_amdgcn_exp2f(__builtin_fmaf(p[e], -sc, mn));
    asm volatile("" : "+v"(a), "+v"(b));
    ep[e] = a; en[e] = b;
  });
  SBAR();
}

constexpr int a_xt(int s) {
  int t = s < 0 ? XL : (s < 15 ? XL + (3 * (s + 1)) / 2 : 34 + (2 * (s - 14)) / 3);
  if (s >= 15 && t < s + 10) t = s + 10;
  return t > 64 ? 64 : t;
}
struct SchedKV {
  int xw[64], ww[8], fw, sw, pw;
  constexpr SchedKV(bool first) : xw{}, ww{}, fw(0), sw(0), pw(0) {
    int pos = 0, xpos[64] = {}, wpos[8] = {}, fpos = 0, spos = 0, ppos = 0;
    for (int s = 0; s < XL; ++s) xpos[s] = ++pos;
    if (first) ++pos;
    for (int s = 0; s < 64; ++s) {
      if ((s + 1) % 8 == 0 && (s + 1) / 8 < 8) ww[(s + 1) / 8] = pos - wpos[(s + 1) / 8];
      if (s == 60) sw = pos - spos;
      xw[s] = pos - xpos[s];
      for (int t = a_xt(s - 1); t < a_xt(s); ++t) xpos[t] = ++pos;
      if ((s < 15 && first) || (s >= 15 && s <= 46)) wpos[(s + 1) / 8 + 2] = ++pos;
      if (s >= 48 && s <= 52) { ++pos; if (s == 51) spos = pos; if (s == 52) ppos = pos; }
      if (s == 56) fpos = ++pos;
    }
    pw = pos - ppos; fw = pos - fpos;
  }
};
constexpr int QP = 30, QC = 42;
constexpr int q_nd(int s) { return s < 6 ? 0 : (s < 14 ? 1 : (s < 42 ? ((s - 14) % 8 < 4 ? 1 : 0) : 0)); }
struct SchedQ {
  int ww[8], pw, lw[33];
  constexpr SchedQ() : ww{}, pw(0), lw{} {
    int pos = 3, stpos = 3, q6 = 0, q7 = 0, ppos = 0, lpos[34] = {};
    for (int s = 0; s < 64; ++s) {
      if ((s + 2) % 8 == 0 && (s + 2) / 8 < 8) {
        const int g = (s + 2) / 8;
        ww[g] = g == 2 ? pos - stpos : (g == 6 ? pos - q6 : (g == 7 ? pos - q7 : 63));
        if (g == 2) ++pos;
      }
      if (s == QC) pw = pos - ppos;
      pos += q_nd(s); if (s == 9) q6 = pos; if (s == 13) q7 = pos;
      if (s == QP) ppos = ++pos;
      if (s >= QC && s < QC + 17) { lpos[2 * (s - QC)] = ++pos; lpos[2 * (s - QC) + 1] = ++pos; }
    }
    for (int i = 0; i < 32; ++i) { lw[i] = pos - lpos[i]; pos += i & 1; }
    lw[32] = pos - lpos[33];
  }
};

#define R_OA "v[64:79]"
#define R_OB "v[80:95]"
#define S0_(n, q, m) "v_mfma_f32_32x32x16_bf16 v[" #n ":" #n "+15], v[208:211], v[" #q ":" #q "+3], v[" #m ":" #m "+15]\n\t"
#define S1_(n, q) "v_mfma_f32_32x32x16_bf16 v[" #n ":" #n "+15], v[212:215], v[" #q "+4:" #q "+7], v[" #n ":" #n "+15]\n\t"
#define PV0_(o, p) "v_smfmac_f32_32x32x32_bf16 v[" #o ":" #o "+15], v[216:219], v[" #p ":" #p "+7], v236\n\t"
#define PV1_(o, p) "v_smfmac_f32_32x32x32_bf16 v[" #o ":" #o "+15], v[220:223], v[" #p "+8:" #p "+15], v236\n\t"
#define L0_(l, p) "v_smfmac_f32_16x16x64_bf16 v[" #l ":" #l "+3], v[224:227], v[" #p ":" #p "+7], v236\n\t"
#define L1_(l, p) "v_smfmac_f32_16x16x64_bf16 v[" #l ":" #l "+3], v[224:227], v[" #p "+8:" #p "+15], v236\n\t"
#define E_(e, i) "v_exp_f32 v[" #e "+" #i "], v[" #e "+" #i "]\n\t"
#define RD_(d, off) "ds_read_b128 v[" #d ":" #d "+3], v237 offset:" #off "\n\t"
#define HALF_(e, n, q, m, p, o, l, X0, X1, X2, X3, X4, Y2) \
  S0_(n, q, m) X0 E_(e, 0) E_(e, 1) E_(e, 2) S1_(n, q) X1 E_(e, 3) E_(e, 4) E_(e, 5) Y2 PV0_(o, p) X2 E_(e, 6) E_(e, 7) E_(e, 8) \
  PV1_(o, p) X3 E_(e, 9) E_(e, 10) E_(e, 11) L0_(l, p) X4 E_(e, 12) E_(e, 13) L1_(l, p) E_(e, 14) E_(e, 15)
#define LG2_ "s_waitcnt lgkmcnt(2)\n\t"
#define LG0_ "s_waitcnt lgkmcnt(0)\n\t"
#define STEP_(b, nb, kv, wv, wo) \
  "s_waitcnt vmcnt(4)\n\ts_barrier\n\t" \
  HALF_(128, 160, 200, 112, 176, 80, 232, RD_(208, b + 2048), RD_(212, b + 3072), RD_(216, b), RD_(220, b + 1024), "", "") LG2_ \
  HALF_(160, 144, 192, 96, 128, 64, 228, "s_add_u32 m0, s44, " #nb "\n\t", "global_load_lds_dwordx4 v" #kv ", s[40:41] sc1\n\t", "", \
        "global_load_lds_dwordx4 v" #kv ", s[40:41] offset:1024 sc1\n\t", "", LG0_) \
  HALF_(144, 176, 200, 112, 160, 80, 232, RD_(208, b + 6144), RD_(212, b + 7168), RD_(216, b + 4096), RD_(220, b + 5120), "", "") LG2_ \
  HALF_(176, 128, 192, 96, 144, 64, 228, "s_mov_b32 m0, s45\n\t", "global_load_lds_dwordx4 v" #wv ", s[42:43] offset:" #wo " sc1\n\t", "", "", "", LG0_)
DI float max16(const f32x16& s) {
  float mx = s[0];
#pragma unroll
  for (int e = 1; e < 16; ++e) mx = fmaxf(mx, s[e]);
  return mx;
}
typedef __attribute__((ext_vector_type(8))) unsigned u32x8;
typedef __attribute__((ext_vector_type(16))) unsigned u32x16;

__global__ __launch_bounds__(256, 1) void k_main(const char* xin, const char* wqkv, const char* wphi, const char* woA, const char* wq, const char* wk, const char* wv,
                                                 const char* woB, char* ws, char* out) {
  __shared__ __attribute__((aligned(16))) char smem_[163840];
  LAS char* const smem = (LAS char*)smem_;
  const unsigned lds0 = (unsigned)(uintptr_t)smem;
  Ctx c;
  int lane = threadIdx.x & 63, w = __builtin_amdgcn_readfirstlane(threadIdx.x >> 6), r = lane & 31, h = lane >> 5, rb, cb;
  block_map<4>(rb, cb);
  int tt = rb * 4 + w, blk = (lane >> 4) & 1, q4 = (lane & 15) >> 2, p4 = lane & 3;
  c.xv = lane * 16; c.p.init(lds0 + c.xv);
  char* flags = ws + WS_FLAG;
  char* col = ws + WS_COL + (size_t)cb * 2 * MB;
  u64 tag;
  u32x4 xr[4];
  float csk, csv;
  auto wt = [&](int m, int ch) { return col + C_WT + m * 131072 + ch * SLOT + w * 4096; };
  auto pc_w = [&](int m, int slot, int ch, auto i) { dma_c<i * 1024>(lds0 + slot * SLOT + w * 4096, c.xv, wt(m, ch)); };
  auto pc_kv = [&](auto g, auto i) { pc_w(1 + i / 4, 2 * (g % 4) + i / 4, g, IC(i % 4)); };
  auto pc_q = [&](auto ch, auto i) { pc_w(0, slot_q(ch), ch, i); };
  auto ld_cs = [&](bf16x8& d, int m, auto i) { ldx1<false, i * 1024>(d, c.xv, col + C_CS + m * 4096); };
  auto raw_in = [&](auto j, int slot) {
    const int hi = rb >> 3, pitch = (j == 0 || (j == 1 && !hi)) ? 6144 : 2048, row = lane >> 3;
    const char* src = (j == 0 ? wqkv + 2048 + hi * 2048 : (j == 1 ? (hi ? woA : wqkv) : (hi ? woB : wq))) + cb * 128 + (size_t)(rb & 7) * 128 * pitch;
    const unsigned v0 = (unsigned)((32 * w + row) * pitch + ((lane & 7) ^ (((row >> 1) & 1) << 2)) * 16);
    sfor<0, 4>([&](auto i) { dma<i * 1024>(lds0 + slot * SLOT + w * 4096, v0 + i * (8 * pitch - 1024), src); });
  };
  auto raw_out = [&](auto j, int slot) {
    const int hi = rb >> 3, ch = rb & 7, m = j == 0 ? 1 + hi : (j == 1 ? 3 * hi : 4 + hi);
    LAS const char* p = smem + slot * SLOT + (w >> 1) * 8192 + (4 * h + q4) * 128 + (((((q4 >> 1) ^ w) & 1) << 2) + 2 * blk + (p4 >> 1)) * 16 + 8 * (p4 & 1);
    char* dst = col + C_WT + m * 131072 + ch * SLOT + (w >> 1) * 8192 + (w & 1) * 1024 + 4096;
    float cs = 0.f;
    sfor<0, 4>([&](auto s) {
      const u32x4 f = __builtin_bit_cast(u32x4, cat8(trread(p + s * 2048), trread(p + s * 2048 + 1024)));
      st4_1<s * 2048 - 4096>(f, c.xv, dst);
#pragma unroll
      for (int e = 0; e < 4; ++e) cs = dot2(f[e], 0x3f803f80u, cs);
    });
    st1_1(swap_add(cs), (unsigned)(r * 16), col + C_CS + m * 4096 + (ch >> 1) * 1024 + (w & 1) * 512 + ((ch & 1) * 2 + (w >> 1)) * 4);
  };
  {
    [[maybe_unused]] const int l = 0;
    STAMP(30);
    raw_in(IC(0), 7);
    u64 ep; ld8_1(ep, 0u, ws + WS_EPOCH);
    unsigned tch;
    ld1_1(tch, (unsigned)(h ? WS_COL + cb * 2 * MB + 1536 * KB : rb * 2 * MB + 7 * STG + w * 65536 + cb * 4096), ws);
    bf16x8 xt[4];
    sfor<0, 4>([&](auto i) { ldx0<i * 32>(xt[i], (unsigned)(r * 2048 + h * 16), xin + (size_t)tt * 65536 + cb * 128); });
    wait_bar<6>();
    raw_out(IC(0), 7);
    asm volatile("s_waitcnt vmcnt(5)" : "+v"(tch), "+v"(ep), "+v"(xt[0]), "+v"(xt[1]), "+v"(xt[2]), "+v"(xt[3]));
    { char* dst = ws + (size_t)rb * 2 * MB + w * 65536 + cb * 4096;
      sfor<0, 4>([&](auto i) { xr[i] = unperm(__builtin_bit_cast(u32x4, xt[i])); st4_1<i * 1024>(xr[i], c.xv, dst); });
      store_stats(r, xr, ws + WS_ST + tt * 4096 + (cb >> 1) * 512 + (cb & 1) * 8); }
    { u64 z = ((u64)__builtin_amdgcn_readfirstlane((unsigned)(ep >> 32)) << 32 | __builtin_amdgcn_readfirstlane((unsigned)ep)) + 0x9E3779B97F4A7C15ull;
      z = (z ^ (z >> 30)) * 0xBF58476D1CE4E5B9ull; z = (z ^ (z >> 27)) * 0x94D049BB133111EBull; tag = z ^ (z >> 31); }
    waitv<0>();
    set_flag(flags + FL_ACT + tt * 128 + cb * 8, tag);
    bar();
    if (w == 0) set_flag(flags + FL_W + cb * 128 + rb * 8, tag);
    poll<32>(flags, (unsigned)((lane & 15) * 8 + (blk ? FL_W + cb * 128 : FL_ACT + tt * 128)), tag);
    bf16x8 ck[4], cv[4];
    sfor<0, 4>([&](auto i) { ld_cs(ck[i], 1, i); }); sfor<0, 4>([&](auto i) { ld_cs(cv[i], 2, i); });
    sfor<0, 16>([&](auto i) { pc_kv(IC(i / 8), IC(i % 8)); });
    waitv<16>(); pin4(ck); pin4(cv);
    csk = cs_sum(ck); csv = cs_sum(cv);
    STAMP(31);
  }
  char *act, *stt, *fact;
#define REFRESH() do { \
    asm volatile("" : "+s"(w), "+s"(cb), "+s"(rb), "+s"(ws), "+s"(tag)); \
    asm volatile("" : "+v"(lane), "+v"(csk), "+v"(csv)); \
    r = lane & 31; h = lane >> 5; blk = (lane >> 4) & 1; q4 = (lane & 15) >> 2; p4 = lane & 3; tt = rb * 4 + w; \
    flags = ws + WS_FLAG; col = ws + WS_COL + (size_t)cb * 2 * MB; c.xv = lane * 16; c.p.init(lds0 + c.xv); \
    act = ws + (size_t)rb * 2 * MB + l * 4 * STG + w * 65536;     \
    fact = flags + FL_ACT + (l * 4 * 64 + tt) * 128;                  \
    stt = ws + WS_ST + (size_t)l * 512 * 1024 + tt * 4096;            \
  } while (0)
  for (int l = 0; l < 2; ++l) {
    REFRESH();
    float mean, rstd, csb;
    bf16x8 park[32];
    STAMP(0);
    {
      if (l > 0) poll<16>(fact, lane * 8, tag);
      STAMP(1);
      constexpr int NA = 60;
      bf16x8 x[64];
      f32x16 ak0, ak1, av0, av1;
      auto issue_x = [&](auto s) { ldx1<(s < NA), (s % 8) * 1024 - 4096>(x[s], c.xv, act + (s / 8) * 8192 + 4096); };
      {
        constexpr SchedKV SC(false), S1(true);
        bf16x8 sp[4], f[4][2], ck, cv;
        u64 f1;
        sfor<0, XL>(issue_x);
        wait_bar<XL>();
        if (l == 0) pc_kv(IC(2), IC(0));
        auto rd = [&](auto s, auto t) { f[t][s % 2] = c.p.template frag<(2 * ((s / 8) % 4) + t / 2) * SLOT + (s % 8) * 2048 + (t % 2) * 1024>(); };
        sfor<0, 4>([&](auto t) { rd(IC(0), t); });
        sfor<0, 64>([&](auto s) {
          if constexpr ((s + 1) % 8 == 0 && (s + 1) / 8 >= 2 && (s + 1) / 8 < 8) wait_bar<((s + 1) / 8 < 4 ? S1.ww[(s + 1) / 8] : SC.ww[(s + 1) / 8])>();
          if constexpr (s == 47) { if (l == 0 && w == 0) set_flag(flags + FL_W + 2048 + cb * 128 + rb * 8, tag); }
          if constexpr (s == 60) { waitv<SC.sw>(); pin4(sp); }
          waitx<(s < NA), SC.xw[s]>(x[s]);
          SBAR();
          mm<false, (s < NA), s == 0>(ak0, f[0][s % 2], x[s]);
          SBAR();
          if constexpr (s + 1 < 64) rd(IC(s + 1), IC(0));
          if constexpr (a_xt(s - 1) < a_xt(s)) issue_x(IC(a_xt(s - 1)));
          if constexpr (s == 60) ln_stats(sp, mean, rstd);
          SBAR();
          mm<false, (s < NA), s == 0>(ak1, f[1][s % 2], x[s]);
          SBAR();
          if constexpr (s + 1 < 64) rd(IC(s + 1), IC(1));
          if constexpr (a_xt(s - 1) + 1 < a_xt(s)) issue_x(IC(a_xt(s - 1) + 1));
          if constexpr (s == 58) ck = opq_v(cs_frag(csk));
          SBAR();
          mm<true, (s < NA), s == 0>(av0, f[2][s % 2], x[s]);
          SBAR();
          if constexpr (s + 1 < 64) rd(IC(s + 1), IC(2));
          if constexpr (s < 15) { if (l == 0) pc_kv(IC((s + 1) / 8 + 2), IC((s + 1) % 8)); }
          if constexpr (s >= 15 && s <= 46) pc_kv(IC((s + 1) / 8 + 2), IC((s + 1) % 8));
          if constexpr (s == 58) cv = opq_v(cs_frag(csv));
          SBAR();
          mm<true, (s < NA), s == 0>(av1, f[3][s % 2], x[s]);
          SBAR();
          if constexpr (s + 1 < 64) rd(IC(s + 1), IC(3));
          if constexpr (s == 1) { if (l == 0) raw_in(IC(1), 8); }
          if constexpr (s == 4) { if (l == 0) raw_in(IC(2), 9); }
          if constexpr (s == 24) { if (l == 0) raw_out(IC(1), 8); }
          if constexpr (s == 26) { if (l == 0) raw_out(IC(2), 9); }
          if constexpr (s == 56) { f1 = tag; rd_flag<16>(f1, lane * 8, flags + FL_W + 2048 + cb * 128); }
          if constexpr (s >= 48 && s < 52) ldx1<false, (s - 48) * 512>(sp[s - 48], (unsigned)(h * 2048 + r * 16), stt);
          if constexpr (s == 52) dma<0>(lds0 + 9 * SLOT + w * 1024, c.xv, wphi + cb * 4096 + w * 1024);
          if constexpr (s == 62) {
            const bf16x8 m0 = mean_frag(mean, h == 0), m1 = mean_frag(mean, h == 1);
            mm<false, false, false, true>(ak0, ck, m0); mm<false, false, false, true>(ak1, ck, m1);
            mm<true, false, false>(av0, cv, m0); mm<true, false, false>(av1, cv, m1);
          }
          SBAR();
        });
        mdone(ak0, ak1, av0, av1);
        waitx<false, SC.fw>(f1);
        if (!__all(f1 == tag)) poll<16>(flags + FL_W + 2048 + cb * 128, lane * 8, tag);
        bar();
      }
      STAMP(2);
      REFRESH();
      LAS char* pk_img = smem + 4 * SLOT;
      LAS char* v_img = smem + 6 * SLOT;
      char* kvp = col + C_KVP + l * 131072 + w * 32768;
      char* zp = col + C_ZP + l * 8192 + w * 2048;
      char* fkvp = flags + FL_KVP + (l * 64 + cb * 4 + w) * 128;
      const float sce = rstd * LOG2E;
      auto phi_mm = [&](const f32x16& t0, const f32x16& t1, f32x16& p0, f32x16& p1) {
        LAS const char* wp = smem + 9 * SLOT + (4 * h + q4) * 64 + blk * 32 + p4 * 8;
        const bf16x8 w00 = cat8(trread(wp), trread(wp + 512)), w01 = cat8(trread(wp + 1024), trread(wp + 1536));
        const bf16x8 w10 = cat8(trread(wp + 2048), trread(wp + 2560)), w11 = cat8(trread(wp + 3072), trread(wp + 3584));
        mm<false, false, true, true>(p0, w00, pack8<0>(t0)); mm<false, false, false, true>(p0, w01, pack8<1>(t0));
        mm<false, false, true, true>(p1, w10, pack8<0>(t1)); mm<false, false, false, true>(p1, w11, pack8<1>(t1));
      };
      bf16x8 cq[4], cw[4];
      {
        auto side = [&](auto i) {
          SBAR();
          if constexpr (i < 24) pc_q(IC(i / 4), IC(i % 4)); else if constexpr (i < 28) ld_cs(cq[i - 24], 0, IC(i - 24)); else ld_cs(cw[i - 28], 4, IC(i - 28));
          SBAR();
        };
        f32x16 p[2];
        side(IC(0)); side(IC(1));
        phi_mm(ak0, ak1, p[0], p[1]);
        side(IC(2)); side(IC(3));
        {
          LAS char* d = v_img + w * 2048 + lane * 16;
          float rs[16];
#pragma unroll
          for (int i = 0; i < 16; ++i) rs[i] = lane_get(rstd, (i & 3) + 8 * (i >> 2) + 4 * h);
          side(IC(4));
#pragma unroll
          for (int i = 0; i < 16; ++i) av0[i] *= rs[i];
          *(LAS bf16x8*)d = pack8<0>(av0); *(LAS bf16x8*)(d + 1024) = pack8<1>(av0);
          side(IC(5));
#pragma unroll
          for (int i = 0; i < 16; ++i) av1[i] *= rs[i];
          *(LAS bf16x8*)(d + 8192) = pack8<0>(av1); *(LAS bf16x8*)(d + 9216) = pack8<1>(av1);
        }
        asm volatile("" : "+v"(p[0]), "+v"(p[1]));
        sfor<0, 2>([&](auto hd) {
          f32x16 ep, en;
          hedge_exp(p[hd], sce, ep, en, [&](auto i) { if constexpr (i % 2 == 0) side(IC(6 + hd * 13 + i / 2)); });
          float sp = 0.f, sn = 0.f;
#pragma unroll
          for (int i = 0; i < 16; ++i) { sp += ep[i]; sn += en[i]; }
          side(IC(14 + hd * 13));
          sp = __builtin_amdgcn_rcpf(swap_add(sp)); sn = __builtin_amdgcn_rcpf(swap_add(sn));
          LAS char* row = pk_img + hd * 16384 + (w * 32 + r) * 128 + h * 8;
          sfor<0, 4>([&](auto g) {
            constexpr int gq = g;
            *(LAS u32x2*)(row + ((gq ^ (r & 7)) << 4)) = u32x2{cvtpk(ep[4 * gq] * sp, ep[4 * gq + 1] * sp), cvtpk(ep[4 * gq + 2] * sp, ep[4 * gq + 3] * sp)};
            *(LAS u32x2*)(row + (((4 + gq) ^ (r & 7)) << 4)) = u32x2{cvtpk(en[4 * gq] * sn, en[4 * gq + 1] * sn), cvtpk(en[4 * gq + 2] * sn, en[4 * gq + 3] * sn)};
            side(IC(15 + hd * 13 + gq));
          });
        });
      }
      asm volatile("s_waitcnt vmcnt(8) lgkmcnt(0)\n\ts_barrier" ::: "memory");
      {
        const int hd = w >> 1, fh = w & 1;
        f32x16 kv, z;
        const unsigned one2 = opq_v(0x3f803f80u);
        const bf16x8 ones = __builtin_bit_cast(bf16x8, u32x4{one2, one2, one2, one2});
        const int ch = fh * 4 + 2 * blk + (p4 >> 1), r0 = 4 * h + q4;
        LAS const char* pa = pk_img + hd * 16384 + r0 * 128 + ((ch ^ r0) << 4) + 8 * (p4 & 1);
        LAS const char* pb = v_img + hd * 8192 + lane * 16;
        bf16x8 a[8], b[8];
#pragma unroll
        for (int s = 0; s < 8; ++s) { a[s] = cat8(trread(pa + s * 2048), trread(pa + s * 2048 + 1024)); b[s] = *(LAS const bf16x8*)(pb + s * 1024); }
        SBAR();
        sfor<0, 8>([&](auto s) { mm<false, false, s == 0>(kv, a[s], b[s]); });
        sfor<0, 8>([&](auto s) {
          mm<false, false, s == 0, s == 0>(z, ones, a[s]);
          if constexpr (s == 1) { mdone(kv); st4_1<0>(__builtin_bit_cast(u32x4, pack8<0>(kv)), c.xv, kvp + rb * 2048); }
          if constexpr (s == 2) st4_1<1024>(__builtin_bit_cast(u32x4, pack8<1>(kv)), c.xv, kvp + rb * 2048);
          if constexpr (s == 3) { waitv<2>(); pin4(cq); pin4(cw); cq[0] = cs_frag(cs_sum(cq)); pin4(cq); }
          if constexpr (s == 5) { csb = opq_v(cs_sum(cw)); }
          SBAR();
        });
        mdone(z);
        st1_1(z[0], (unsigned)(r * 4), zp + rb * 128);
      }
      STAMP(3);
      f32x16 aq0, aq1;
      bf16x8 pp[32], zq[2];
      constexpr SchedQ SQ;
      auto ld_park = [&](auto i) { ldx1<true, (i % 4) * 1024>(park[i], c.xv, wt(4, i / 4)); };
      {
        bf16x8 f0[3], f1[3];
        u64 fv;
        auto off = [](int s) constexpr { return slot_q(s / 8) * SLOT + (s % 8) * 2048; };
        auto rd0 = [&](auto s) { f0[s % 3] = c.p.template frag<off(s)>(); };
        auto rd1 = [&](auto s) { f1[s % 3] = c.p.template frag<off(s) + 1024>(); };
        auto ldp = [&](auto i) {
          if constexpr (i < 32) ldx1<true, (i % 8) * 1024 - 4096>(pp[i], c.xv, kvp + (i / 8) * 8192 + 4096); else ldx1<false, (i - 32) * 1024>(zq[i - 32], c.xv, zp);
        };
        rd0(IC(0)); rd1(IC(0)); rd0(IC(1)); rd1(IC(1));
        sfor<0, 64>([&](auto s) {
          if constexpr ((s + 2) % 8 == 0 && (s + 2) / 8 < 8) {
            wait_bar<SQ.ww[(s + 2) / 8]>();
            if constexpr ((s + 2) / 8 == 2) set_flag(fkvp + rb * 8, tag);
          }
          if constexpr (s == QC) { waitx<false, SQ.pw>(fv); if (!__all(fv == tag)) poll<16>(fkvp, lane * 8, tag); }
          SBAR();
          mm<false, (s < NA), s == 0>(aq0, f0[s % 3], x[s]);
          SBAR();
          if constexpr (s + 2 < 64) rd0(IC(s + 2));
          if constexpr (q_nd(s)) {
            if constexpr (s < 14) pc_q(IC(6 + (s - 6) / 4), IC((s - 6) % 4)); else pc_w(3, slot_a((s - 14) / 8), (s - 14) / 8, IC((s - 14) % 8));
          }
          if constexpr (s == QP) { fv = tag; rd_flag<16>(fv, lane * 8, fkvp); }
          if constexpr (s >= QC && s < QC + 17) ldp(IC(2 * (s - QC)));
          SBAR();
          mm<false, (s < NA), s == 0>(aq1, f1[s % 3], x[s]);
          SBAR();
          if constexpr (s + 2 < 64) rd1(IC(s + 2));
          if constexpr (s >= QC && s < QC + 17) ldp(IC(2 * (s - QC) + 1));
          if constexpr (s == 2) { mm<false, false, false, true>(aq0, cq[0], mean_frag(mean, h == 0)); mm<false, false, false, true>(aq1, cq[0], mean_frag(mean, h == 1)); }
          SBAR();
        });
        mdone(aq0, aq1);
        bar();
      }
      STAMP(4);
      REFRESH();
      bf16x8 pq[2][4];
      f32x16 kv;
      {
        f32x16 p[2];
        phi_mm(aq0, aq1, p[0], p[1]);
        u32x4 s0, s1;
#pragma unroll
        for (int e = 0; e < 4; ++e) {
          const int kr = 8 * (e >> 1) + 4 * h + 2 * (e & 1), rr = r & 15;
          const unsigned v = rr == kr ? 0x3f80u : (rr == kr + 1 ? 0x3f800000u : 0u);
          s0[e] = r < 16 ? v : 0u; s1[e] = r < 16 ? 0u : v;
        }
        const bf16x8 sel0 = opq_v(__builtin_bit_cast(bf16x8, s0)), sel1 = opq_v(__builtin_bit_cast(bf16x8, s1));
        mdone(p[0], p[1]);
        sfor<0, 2>([&](auto hd) {
          f32x16 ep, en;
          hedge_exp(p[hd], sce, ep, en, [&](auto e) {
            constexpr int i = hd * 16 + e;
            waitx<true, SQ.lw[i]>(pp[i]);
            mm<false, true, i == 0>(kv, (i & 1) ? sel1 : sel0, pp[i]);
            if constexpr (i & 1) pc_w(3, slot_a(4 + i / 8), 4 + i / 8, IC((i / 2) % 4));
          });
          pq[hd][0] = pack8<0>(ep); pq[hd][1] = pack8<1>(ep); pq[hd][2] = pack8<0>(en); pq[hd][3] = pack8<1>(en);
          pin4(pq[hd]);
        });
      }
      STAMP(5);
      {
        asm volatile("s_waitcnt vmcnt(%2)" : "+v"(zq[0]), "+v"(zq[1]) : "i"(SQ.lw[32]));
        LAS char* sh = smem + 9 * SLOT + 4096;
        ld_park(IC(0)); ld_park(IC(1));
        mdone(kv);
        *(LAS bf16x8*)(sh + w * 2048 + lane * 16) = pack8<0>(kv); *(LAS bf16x8*)(sh + w * 2048 + 1024 + lane * 16) = pack8<1>(kv);
        f32x4 zs = __builtin_bit_cast(f32x4, zq[0]) + __builtin_bit_cast(f32x4, zq[1]);
#pragma unroll
        for (int e = 0; e < 4; ++e) zs[e] += lane_get(zs[e], lane ^ 8);
        ld_park(IC(2)); ld_park(IC(3));
#pragma unroll
        for (int e = 0; e < 4; ++e) zs[e] += lane_get(zs[e], lane ^ 16);
        ld_park(IC(4)); ld_park(IC(5));
#pragma unroll
        for (int e = 0; e < 4; ++e) zs[e] = swap_add(zs[e]);
        if (lane < 8) *(LAS u32x2*)(sh + 8192 + w * 64 + (lane & 1) * 32 + (lane >> 2) * 16 + ((lane >> 1) & 1) * 8) = u32x2{cvtpk(zs[0], zs[1]), cvtpk(zs[2], zs[3])};
        else if (lane < 24) *(LAS unsigned*)(sh + 8192 + 256 + w * 64 + (lane - 8) * 4) = opq_v(0x3f803f80u);
        ld_park(IC(6)); ld_park(IC(7));
        lds_bar();
        STAMP(6);
        char* dst = act + STG + cb * 4096;
        LAS const char* zb = sh + 8192 + (r & 1) * 256 + h * 32;
        bf16x8 ka[8], za[8];
#pragma unroll
        for (int k = 0; k < 8; ++k) { za[k] = *(LAS const bf16x8*)(zb + (k >> 1) * 64 + (k & 1) * 16); ka[k] = *(LAS const bf16x8*)(sh + k * 1024 + lane * 16); }
        f32x16 nm[4], dn[2];
        float ai[2], bi[2];
        auto coef = [&](auto hd) {
          const float a = __builtin_amdgcn_rcpf(dn[0][1]), b = __builtin_amdgcn_rcpf(dn[1][1]);
          const float inv = __builtin_amdgcn_rcpf(dn[0][0] * a + dn[1][0] * b + 1e-6f);
          ai[hd] = opq_v(a * inv); bi[hd] = opq_v(b * inv);
        };
        auto fin = [&](auto hd) {
          f32x16 o;
#pragma unroll
          for (int i = 0; i < 16; ++i) o[i] = nm[2 * hd][i] * ai[hd] + nm[2 * hd + 1][i] * bi[hd];
          st4_1<hd * 2048>(__builtin_bit_cast(u32x4, pack8<0>(o)), c.xv, dst);
          st4_1<hd * 2048 + 1024>(__builtin_bit_cast(u32x4, pack8<1>(o)), c.xv, dst);
        };
        ld_park(IC(8)); ld_park(IC(9));
        sfor<0, 4>([&](auto k) { mm<false, false, k % 2 == 0>(dn[k / 2], za[k], pq[0][k]); if constexpr (k % 2) ld_park(IC(10 + k / 2)); });
        sfor<0, 4>([&](auto k) { mm<false, false, k % 2 == 0>(nm[k / 2], ka[k], pq[0][k]); if constexpr (k % 2) ld_park(IC(12 + k / 2)); });
        mdone(dn[0], dn[1]); coef(IC(0));
        sfor<0, 4>([&](auto k) { mm<false, false, k % 2 == 0>(dn[k / 2], za[4 + k], pq[1][k]); if constexpr (k % 2) ld_park(IC(14 + k / 2)); });
        sfor<0, 4>([&](auto k) { mm<false, false, k % 2 == 0>(nm[2 + k / 2], ka[4 + k], pq[1][k]); if constexpr (k % 2) ld_park(IC(16 + k / 2)); });
        asm volatile("" : "+v"(nm[0]), "+v"(nm[1]));
        fin(IC(0));
        mdone(dn[0], dn[1], nm[2], nm[3]); coef(IC(1));
        fin(IC(1));
        sfor<18, 28>(ld_park);
        waitv<10>();
        set_flag(fact + 8192 + cb * 8, tag);
        sfor<28, 32>(ld_park);
      }
    }
    STAMP(7);
    REFRESH();
    {
      f32x16 a0, a1; res_to_acc(xr, a0, a1);
      poll<16>(fact + 8192, lane * 8, tag);
      sfor<0, 32>([&](auto i) { waitx<true, 63>(park[i]); });
      STAMP(8);
      bf16x8 x[64];
      const char* xb = act + STG;
      sfor<0, XL>([&](auto s) { ld_x<s>(x, c, xb); });
      bar();
      const char* wkv = ((cb & 2) ? wv : wk) + w * 256 * 64;
      const unsigned kvv = (unsigned)((lane >> 1) * 64 + (cb & 1) * 32 + (lane & 1) * 16);
      const unsigned wa = lds0 + w * 4096 + c.xv, wb = wa + 65536;
      auto unpark = [&](auto i) {
        constexpr int off = slot_a(i / 4) * SLOT + (i % 4) * 1024;
        ds_write_a<(off & 65535)>(off < 65536 ? wa : wb, park[i]);
      };
      gemm64<true, false, HookP3>(c, x, xb, a0, a1, [&](auto s) {
        if constexpr (s == 33) bar();
        if constexpr (s >= 36 && s < 52) unpark(IC(s - 36));
        if constexpr (s >= 52 && s < 60) dma<0>(lds0 + 8 * SLOT + w * 8192 + (s - 52) * 1024, kvv, wkv + (s - 52) * 32 * 64);
      });
      STAMP(9);
      store_x(c, r, a0, a1, xr, act + 2 * STG + cb * 4096, stt + 256 * 1024 + (cb >> 1) * 512 + (cb & 1) * 8);
      bar();
      sfor<16, 32>(unpark);
      waitv<0>();
      set_flag(fact + 2 * 8192 + cb * 8, tag);
    }
    STAMP(10);
    REFRESH();
    u32x16 qq;
    char* kvf = ws + WS_KVF + l * 320 * KB;
    bf16x8 k0, k1;
    {
      char* fkv = flags + FL_KV + l * 2048;
      const int du = (cb >> 2) - w, ty = cb & 3;
      LAS char* reg = smem + 8 * SLOT;
      bf16x8 fu[16], cu, cs;
      { LAS const char* p = reg + w * 8192 + (4 * h + q4) * 32 + p4 * 8;
        float t0 = 0.f, t1 = 0.f;
#pragma unroll
        for (int i = 0; i < 16; ++i) {
          fu[i] = cat8(trread(p + i * 512), trread(p + i * 512 + 256));
          const u32x4 f = __builtin_bit_cast(u32x4, fu[i]);
          t0 = dot2(f[0], 0x3f803f80u, t0); t1 = dot2(f[1], 0x3f803f80u, t1); t0 = dot2(f[2], 0x3f803f80u, t0); t1 = dot2(f[3], 0x3f803f80u, t1);
        }
        cu = opq_v(cs_frag(swap_add(t0 + t1))); cs = opq_v(cs_frag(csb)); }
      poll<32>(fact + 2 * 8192 - w * 128, (unsigned)((lane & 15) * 8 + (blk ? cb >> 2 : w) * 128), tag);
      STAMP(11);
      const char* xb = act + 2 * STG; const char* stp = stt + 256 * 1024;
      bf16x8 x[64], xu[16], su[4], sp[4];
      sfor<0, 16>([&](auto i) { ldx1<true, (i % 8) * 1024 - 4096>(xu[i], c.xv, xb + (ptrdiff_t)du * 65536 + w * 16384 + (i / 8) * 8192 + 4096); });
      sfor<0, 4>([&](auto i) { ldx1<false, i * 512>(su[i], (unsigned)(h * 2048 + r * 16), stp + du * 4096); });
      f32x16 au, av;
      sfor<0, 16>([&](auto i) {
        waitx<true, 15 - i + 4 + (i < XL ? i : XL)>(xu[i]);
        mm<false, true, i == 0>(au, fu[i], xu[i]);
        if constexpr (i < XL) ld_x<i>(x, c, xb);
        mm<true, true, i == 0>(av, fu[i], xu[i]);
      });
      lds_bar();
      STAMP(12);
      f32x16 a0, a1;
      u64 fa;
      float mu, ru;
      gemm64<true, true, HookP4>(c, x, xb, a0, a1, [&](auto s) {
        if constexpr (s == 0) { pin4(su); ln_stats(su, mu, ru); }
        if constexpr (s == 1) { const bf16x8 mf = mean_frag(mu, h == 0); mm<false, false, false, true>(au, cu, mf); mm<true, false, false>(av, cu, mf); }
        if constexpr (s == 4) {
          asm volatile("" : "+v"(au), "+v"(av));
          if (ty >= 2) au = av;
#pragma unroll
          for (int gq = 0; gq < 4; ++gq) *(LAS f32x4*)(reg + w * 8192 + gq * 1024 + lane * 16) = f32x4{au[4 * gq], au[4 * gq + 1], au[4 * gq + 2], au[4 * gq + 3]};
        }
        if constexpr (s == 6) {
          lds_bar();
          if (w == 0) {
            char* dst = kvf + (tt + du) * 4096;
            auto frag = [&](auto fs, f32x4& lo, f32x4& hi) {
              lo = f32x4{}; hi = f32x4{};
#pragma unroll
              for (int j = 0; j < 4; ++j) { lo += *(LAS const f32x4*)(reg + j * 8192 + fs * 2048 + lane * 16); hi += *(LAS const f32x4*)(reg + j * 8192 + fs * 2048 + 1024 + lane * 16); }
            };
            f32x4 lo, hi;
            if (ty < 2) {
              frag(IC(0), lo, hi); lo *= ru; hi *= ru;
              st4_1<0>(u32x4{cvtpk(lo[0], lo[1]), cvtpk(lo[2], lo[3]), cvtpk(hi[0], hi[1]), cvtpk(hi[2], hi[3])}, c.xv, dst + ty * 1024);
            } else {
              const unsigned vo = (unsigned)(((ty & 1) * 16 + (r & 15) + 32 * h) * 16);
              sfor<0, 2>([&](auto fs) {
                frag(fs, lo, hi);
#pragma unroll
                for (int e = 0; e < 4; ++e) { lo[e] *= lane_get(ru, 16 * fs + 4 * h + e); hi[e] *= lane_get(ru, 16 * fs + 8 + 4 * h + e); }
                st4_1<2048 + fs * 1024>(unperm(u32x4{cvtpk(lo[0], lo[1]), cvtpk(lo[2], lo[3]), cvtpk(hi[0], hi[1]), cvtpk(hi[2], hi[3])}), vo, dst);
              });
            }
          }
        }
        if constexpr (s >= 8 && s < 12) ldx1<false, (s - 8) * 512>(sp[s - 8], (unsigned)(h * 2048 + r * 16), stp);
        if constexpr (s == 7 + XL) { if (w == 0) set_flag(fkv + ((tt + du) * 4 + ty) * 8, tag); }
        if constexpr (s == 12 + XL) { pin4(sp); ln_stats(sp, mean, rstd); }
        if constexpr (s == 14 + XL) { mm<false, false, false, true>(a0, cs, mean_frag(mean, h == 0)); mm<false, false, false, true>(a1, cs, mean_frag(mean, h == 1)); }
        if constexpr (s == 52) ld8_1(fa, lane * 8, fkv + w * 512);
      });
      waitx<false, 0>(fa);
      for (int spin = 0; spin < (1 << 15) && !__all(fa == tag); ++spin) { __builtin_amdgcn_s_sleep(8); ld8_1(fa, lane * 8, fkv + w * 512); waitx<false, 0>(fa); }
      bar();
      ldx1<false, 0>(k0, c.xv, kvf); ldx1<false, 1024>(k1, c.xv, kvf);
      sfor<0, 6>([&](auto i) { dma_c<(i % 2) * 1024>(lds0 + 8 * SLOT + w * 2048 + (i / 2) * 8192, c.xv, kvf + 2048 + w * 2048 + (i / 2) * 8192); });
      STAMP(13);
      const float sc = rstd * QSCALE;
#pragma unroll
      for (int i = 0; i < 8; ++i) { qq[i] = cvtpk(a0[2 * i] * sc, a0[2 * i + 1] * sc); qq[8 + i] = cvtpk(a1[2 * i] * sc, a1[2 * i + 1] * sc); }
      asm volatile("s_waitcnt vmcnt(6)" : "+v"(k0), "+v"(k1), "+v"(qq));
    }
    STAMP(14);
    REFRESH();
    {
      const bf16x8 qa0 = __builtin_bit_cast(bf16x8, __builtin_shufflevector(qq, qq, 0, 1, 2, 3)), qa1 = __builtin_bit_cast(bf16x8, __builtin_shufflevector(qq, qq, 4, 5, 6, 7));
      const bf16x8 qb0 = __builtin_bit_cast(bf16x8, __builtin_shufflevector(qq, qq, 8, 9, 10, 11)), qb1 = __builtin_bit_cast(bf16x8, __builtin_shufflevector(qq, qq, 12, 13, 14, 15));
      float mA = 0.f, mB = 0.f;
      const char* ksrc = kvf + 2048 + w * 2048;
      const unsigned ring = lds0 + 8 * SLOT + w * 2048;
      for (int attempt = 0; attempt < 2; ++attempt) {
        f32x16 sa0, sb;
        if (attempt == 1) {
          mA = -INFINITY; mB = -INFINITY;
          for (int t = 0; t < 64; ++t) {
            ldx1<false, 0>(k0, c.xv, kvf + t * 4096); ldx1<false, 1024>(k1, c.xv, kvf + t * 4096);
            asm volatile("s_waitcnt vmcnt(0)" : "+v"(k0), "+v"(k1));
            mm<false, false, true>(sa0, k0, qa0); mm<false, false, false>(sa0, k1, qa1); mm<false, false, true>(sb, k0, qb0); mm<false, false, false>(sb, k1, qb1);
            mdone(sa0, sb);
            mA = fmaxf(mA, max16(sa0)); mB = fmaxf(mB, max16(sb));
          }
          mA = swap_max(mA); mB = swap_max(mB);
          ldx1<false, 0>(k0, c.xv, kvf); ldx1<false, 1024>(k1, c.xv, kvf);
          sfor<0, 6>([&](auto i) { dma_c<(i % 2) * 1024>(ring + (i / 2) * 8192, c.xv, ksrc + (i / 2) * 8192); });
          asm volatile("s_waitcnt vmcnt(6)" : "+v"(k0), "+v"(k1));
        }
        mm<false, false, true, true>(sa0, k0, qa0); mm<false, false, false>(sa0, k1, qa1); mm<false, false, true>(sb, k0, qb0); mm<false, false, false>(sb, k1, qb1);
        mdone(sa0, sb);
        if (attempt == 0) { mA = swap_max(max16(sa0)); mB = swap_max(max16(sb)); }
        f32x16 nma, nmb, oa = {}, ob = {}, sb1 = {}, t0, t1;
#pragma unroll
        for (int e = 0; e < 16; ++e) { nma[e] = -mA; nmb[e] = -mB; sa0[e] -= mA; }
        const unsigned o2 = opq_v(0x3f803f80u), s0 = ((lane >> 2) & 1) ? 0u : o2, s1 = o2 - s0;
        u32x16 kvr = {}, misc;
        { const u32x4 a = __builtin_bit_cast(u32x4, k0), b = __builtin_bit_cast(u32x4, k1);
#pragma unroll
          for (int e = 0; e < 4; ++e) { kvr[e] = a[e]; kvr[4 + e] = b[e]; } }
        misc = u32x16{s0, s0, s1, s1, 0u, 0u, 0u, 0u, 0u, 0u, 0u, 0u, 0xddddddddu, lds0 + 8 * SLOT + c.xv, 0u, 0u};
        const u32x4 offs = {c.xv, c.xv + 8192, c.xv + 16384, c.xv + 24576};
        const char* ks = ksrc + 3 * 8192; const char* wsr = wt(5, 0);
        unsigned wm0 = lds0 + w * 4096, cnt = 8;
        asm volatile(
            "s_nop 1\n\t"
            "1:\n\t"
            STEP_(0, 24576, 240, 240, 0) STEP_(8192, 0, 241, 240, 1024) STEP_(16384, 8192, 242, 240, 2048) STEP_(24576, 16384, 243, 240, 3072)
            "s_add_u32 s40, s40, 0x8000\n\ts_addc_u32 s41, s41, 0\n\ts_add_u32 s42, s42, 0x4000\n\ts_addc_u32 s43, s43, 0\n\t"
            "s_add_u32 s45, s45, 0x4000\n\ts_sub_u32 %[cnt], %[cnt], 1\n\ts_cmp_lg_u32 %[cnt], 0\n\ts_cbranch_scc1 1b\n\t"
            PV0_(80, 176) PV1_(80, 176) L0_(232, 176) L1_(232, 176)
            "s_nop 15\n\t"
            : "+{v[64:79]}"(oa), "+{v[80:95]}"(ob), "+{v[128:143]}"(sa0), "=&{v[144:159]}"(t0), "=&{v[160:175]}"(t1), "+{v[176:191]}"(sb1),
              "+{v[208:223]}"(kvr), "+{v[224:239]}"(misc), "+{s[40:41]}"(ks), "+{s[42:43]}"(wsr), "+{s45}"(wm0), [cnt] "+s"(cnt)
            : "{v[96:111]}"(nma), "{v[112:127]}"(nmb), "{v[192:207]}"(qq), "{v[240:243]}"(offs), "{s44}"(ring)
            : "scc", "memory");
        const float la = __uint_as_float(misc[4]), lb = __uint_as_float(misc[8]);
        const float ia = __builtin_amdgcn_rcpf(la), ib = __builtin_amdgcn_rcpf(lb);
#pragma unroll
        for (int e = 0; e < 16; ++e) { oa[e] *= ia; ob[e] *= ib; }
        char* dst = act + 3 * STG + cb * 4096;
        st4_1<0>(__builtin_bit_cast(u32x4, pack8<0>(oa)), c.xv, dst); st4_1<1024>(__builtin_bit_cast(u32x4, pack8<1>(oa)), c.xv, dst);
        st4_1<2048>(__builtin_bit_cast(u32x4, pack8<0>(ob)), c.xv, dst); st4_1<3072>(__builtin_bit_cast(u32x4, pack8<1>(ob)), c.xv, dst);
        float chk = 0.f;
#pragma unroll
        for (int e = 0; e < 16; ++e) chk = __builtin_fmaf(oa[e], 0.f, __builtin_fmaf(ob[e], 0.f, chk));
        const bool bad = !(la < 1.2676506e30f) || !(lb < 1.2676506e30f) || !(chk == 0.f);
        wait_bar<4>();
        LAS volatile int* vote = (LAS volatile int*)(smem + 8 * SLOT);
        if (lane == 0) vote[w] = 0;
        if (attempt == 0 && bad) vote[w] = 1;
        lds_bar();
        const int redo = vote[0] | vote[1] | vote[2] | vote[3];
        if (!__builtin_amdgcn_readfirstlane(redo)) break;
        lds_bar();
      }
      waitv<0>();
      set_flag(fact + 3 * 8192 + cb * 8, tag);
    }
    STAMP(15);
    REFRESH();
    {
      f32x16 a0, a1; res_to_acc(xr, a0, a1);
      poll<16>(fact + 3 * 8192, lane * 8, tag);
      STAMP(16);
      bf16x8 x[64];
      const char* xb = act + 3 * STG;
      sfor<0, XL>([&](auto s) { ld_x<s>(x, c, xb); });
      bar();
      gemm64<false, false, HookP6>(c, x, xb, a0, a1, [&](auto s) {
        if constexpr (s == 33) bar();
        if constexpr (s >= 52) pc_kv(IC((s - 52) / 8), IC((s - 52) % 8));
      });
      STAMP(17);
      if (l == 0) {
        store_x(c, r, a0, a1, xr, act + 4 * STG + cb * 4096, stt + 512 * 1024 + (cb >> 1) * 512 + (cb & 1) * 8);
        bar();
        sfor<12, 24>([&](auto i) { pc_kv(IC(i / 8), IC(i % 8)); });
        waitv<12>();
        set_flag(fact + 4 * 8192 + cb * 8, tag);
        sfor<24, 32>([&](auto i) { pc_kv(IC(i / 8), IC(i % 8)); });
      } else {
        char* dst = out + (size_t)(tt * 32 + r) * 2048 + cb * 128 + h * 16;
        *(u32x4*)(dst) = unperm(__builtin_bit_cast(u32x4, pack8<0>(a0))); *(u32x4*)(dst + 32) = unperm(__builtin_bit_cast(u32x4, pack8<1>(a0)));
        *(u32x4*)(dst + 64) = unperm(__builtin_bit_cast(u32x4, pack8<0>(a1))); *(u32x4*)(dst + 96) = unperm(__builtin_bit_cast(u32x4, pack8<1>(a1)));
        if (tt == 0 && cb == 0) set_flag(ws + WS_EPOCH, tag);
      }
    }
    STAMP(18);
  }
}

extern "C" void kernel_launch(void* const* d_in, const int* in_sizes, int n_in, void* d_out, int out_size, void* d_ws, size_t ws_size, hipStream_t stream) {
  k_main<<<256, 256, 0, stream>>>((const char*)d_in[0], (const char*)d_in[1], (const char*)d_in[2], (const char*)d_in[3], (const char*)d_in[5], (const char*)d_in[6],
                                  (const char*)d_in[7], (const char*)d_in[8], (char*)d_ws + ((ws_size - 68 * MB) & ~(2 * MB - 1)), (char*)d_out);
}
```

```cpp
#include <hip/hip_runtime.h>
#include <hip/hip_bf16.h>
#include <stdint.h>
#include <type_traits>

typedef __attribute__((ext_vector_type(8))) short bf16x8;
typedef __attribute__((ext_vector_type(4))) short s16x4;
typedef __attribute__((ext_vector_type(2))) __bf16 bf2_t;
typedef __attribute__((ext_vector_type(16))) float f32x16;
typedef __attribute__((ext_vector_type(4))) float f32x4;
typedef __attribute__((ext_vector_type(2))) float f32x2;
typedef __attribute__((ext_vector_type(4))) unsigned u32x4;
typedef __attribute__((ext_vector_type(2))) unsigned u32x2;
#define LAS __attribute__((address_space(3)))
#define DI __device__ __forceinline__
#define MFMA32(a, b, c) __builtin_amdgcn_mfma_f32_32x32x16_bf16((a), (b), (c), 0, 0, 0)
#define MFMA16(a, b, c) __builtin_amdgcn_mfma_f32_16x16x32_bf16((a), (b), (c), 0, 0, 0)
#define SBAR() __builtin_amdgcn_sched_barrier(0)

constexpr int S_ = 2048, D_ = 1024;
constexpr float LOG2E = 1.4426950408889634f;
constexpr float QSCALE = 0.17677669529663687f * LOG2E;

constexpr size_t MB = 1u << 20, KB = 1024;
constexpr size_t STG = 256 * KB;
constexpr size_t WS_COL = 32 * MB;
constexpr size_t C_WT = 0;
constexpr size_t C_KVP = 768 * KB;
constexpr size_t C_ZP = 1024 * KB;
constexpr size_t C_CS = 1040 * KB;
constexpr size_t WS_ST = 64 * MB;
constexpr size_t WS_KVF = WS_ST + 1024 * KB;
constexpr size_t WS_FLAG = WS_ST + 1664 * KB;
constexpr size_t WS_EPOCH = WS_ST + 1920 * KB;
constexpr int FL_ACT = 0, FL_KVP = 8 * 64 * 128, FL_KV = FL_KVP + 2 * 64 * 128, FL_W = FL_KV + 2 * 2048;
typedef unsigned long long u64;

template <int I, int N, class F> DI void sfor(F&& f) { if constexpr (I < N) { f(std::integral_constant<int, I>{}); sfor<I + 1, N>(f); } }

DI unsigned cvtpk(float lo, float hi) { f32x2 v = {lo, hi}; return __builtin_bit_cast(unsigned, __builtin_convertvector(v, bf2_t)); }
DI float bflo(unsigned u) { return __uint_as_float(u << 16); }
DI float bfhi(unsigned u) { return __uint_as_float(u & 0xffff0000u); }
DI float rbf(float x) { return bflo(cvtpk(x, x)); }
DI float swap_add(float v) { auto r = __builtin_amdgcn_permlane32_swap(__float_as_uint(v), __float_as_uint(v), false, false); return __uint_as_float(r[0]) + __uint_as_float(r[1]); }
DI float swap_max(float v) { auto r = __builtin_amdgcn_permlane32_swap(__float_as_uint(v), __float_as_uint(v), false, false); return fmaxf(__uint_as_float(r[0]), __uint_as_float(r[1])); }
DI float swap_min(float v) { auto r = __builtin_amdgcn_permlane32_swap(__float_as_uint(v), __float_as_uint(v), false, false); return fminf(__uint_as_float(r[0]), __uint_as_float(r[1])); }
template <int S> DI bf16x8 pack8(const f32x16& v) {
  u32x4 p = {cvtpk(v[8 * S], v[8 * S + 1]), cvtpk(v[8 * S + 2], v[8 * S + 3]), cvtpk(v[8 * S + 4], v[8 * S + 5]), cvtpk(v[8 * S + 6], v[8 * S + 7])};
  return __builtin_bit_cast(bf16x8, p);
}
DI u32x4 unperm(u32x4 v) {
  auto r0 = __builtin_amdgcn_permlane32_swap(v[0], v[2], false, false);
  auto r1 = __builtin_amdgcn_permlane32_swap(v[1], v[3], false, false);
  return u32x4{r0[0], r1[0], r0[1], r1[1]};
}
DI float lane_get(float v, int src) { return __uint_as_float(__builtin_amdgcn_ds_bpermute(src << 2, __float_as_uint(v))); }
DI s16x4 trread(LAS const char* p) { return __builtin_amdgcn_ds_read_tr16_b64_v4i16((LAS s16x4*)p); }
DI bf16x8 cat8(s16x4 a, s16x4 b) { return __builtin_shufflevector(a, b, 0, 1, 2, 3, 4, 5, 6, 7); }
DI float dot2(unsigned a, unsigned b, float c) { return __builtin_amdgcn_fdot2_f32_bf16(__builtin_bit_cast(bf2_t, a), __builtin_bit_cast(bf2_t, b), c, false); }

template <int OFF> DI void dma(unsigned lds, unsigned v, const char* sb) {
  asm volatile("s_mov_b32 m0, %0\n\ts_nop 0\n\tglobal_load_lds_dwordx4 %1, %2 offset:%3" ::"s"(lds), "v"(v), "s"(sb), "i"(OFF) : "memory");
}
template <int OFF> DI void dma_c(unsigned lds, unsigned v, const char* sb) {
  asm volatile("s_mov_b32 m0, %0\n\ts_nop 0\n\tglobal_load_lds_dwordx4 %1, %2 offset:%3 sc1" ::"s"(lds), "v"(v), "s"(sb), "i"(OFF) : "memory");
}
constexpr int cap63(int n) { return n > 63 ? 63 : n; }
template <int N> DI void wait_bar() { asm volatile("s_waitcnt vmcnt(%0)\n\ts_barrier" ::"i"(cap63(N)) : "memory"); }
DI void lds_bar() { asm volatile("s_waitcnt lgkmcnt(0)\n\ts_barrier" ::: "memory"); }
DI void all_bar() { asm volatile("s_waitcnt vmcnt(0) lgkmcnt(0)\n\ts_barrier" ::: "memory"); }
DI void bar() { asm volatile("s_barrier" ::: "memory"); }
template <bool AG, int OFF> DI void ldx1(bf16x8& d, unsigned voff, const char* sb) {
  if constexpr (AG) asm volatile("global_load_dwordx4 %0, %1, %2 offset:%3 sc1" : "=a"(d) : "v"(voff), "s"(sb), "i"(OFF));
  else asm volatile("global_load_dwordx4 %0, %1, %2 offset:%3 sc1" : "=v"(d) : "v"(voff), "s"(sb), "i"(OFF));
}
template <int OFF> DI void ld2_1(f32x2& d, unsigned voff, const char* sb) { asm volatile("global_load_dwordx2 %0, %1, %2 offset:%3 sc1" : "=v"(d) : "v"(voff), "s"(sb), "i"(OFF)); }
DI void ld1_1(unsigned& d, unsigned voff, const void* sb) { asm volatile("global_load_dword %0, %1, %2 sc1" : "=v"(d) : "v"(voff), "s"(sb) : "memory"); }
template <int OFF> DI void st4_1(u32x4 d, unsigned voff, char* sb) { asm volatile("global_store_dwordx4 %0, %1, %2 offset:%3 sc1\n\ts_nop 1" ::"v"(voff), "v"(d), "s"(sb), "i"(OFF) : "memory"); }
DI void st2_1(f32x2 d, unsigned voff, char* sb) { asm volatile("global_store_dwordx2 %0, %1, %2 sc1" ::"v"(voff), "v"(d), "s"(sb) : "memory"); }
DI void st1_1(float d, unsigned voff, char* sb) { asm volatile("global_store_dword %0, %1, %2 sc1" ::"v"(voff), "v"(d), "s"(sb) : "memory"); }
template <int N> DI void waitv() { asm volatile("s_waitcnt vmcnt(%0)" ::"i"(cap63(N)) : "memory"); }
template <bool AG, int N, class T> DI void waitx(T& d) {
  if constexpr (AG) asm volatile("s_waitcnt vmcnt(%1)" : "+a"(d) : "i"(cap63(N))); else asm volatile("s_waitcnt vmcnt(%1)" : "+v"(d) : "i"(cap63(N)));
}
template <bool SW, bool BA, bool Z, bool NOP = false> DI void mm(f32x16& acc, const bf16x8& a, const bf16x8& b) {
#define MM_(OPS, C, OUT, BC) do { if constexpr (NOP) asm volatile("s_nop 1\n\tv_mfma_f32_32x32x16_bf16 %0, " OPS ", " C : OUT(acc) : "v"(a), BC(b)); \
                                  else asm volatile("v_mfma_f32_32x32x16_bf16 %0, " OPS ", " C : OUT(acc) : "v"(a), BC(b)); } while (0)
  if constexpr (Z) {
    if constexpr (SW) { if constexpr (BA) MM_("%2, %1", "0", "=&v", "a"); else MM_("%2, %1", "0", "=&v", "v"); }
    else { if constexpr (BA) MM_("%1, %2", "0", "=&v", "a"); else MM_("%1, %2", "0", "=&v", "v"); }
  } else {
    if constexpr (SW) { if constexpr (BA) MM_("%2, %1", "%0", "+v", "a"); else MM_("%2, %1", "%0", "+v", "v"); }
    else { if constexpr (BA) MM_("%1, %2", "%0", "+v", "a"); else MM_("%1, %2", "%0", "+v", "v"); }
  }
#undef MM_
}
DI void mdone(f32x16& a) { asm volatile("s_nop 11" : "+v"(a)); }
DI void mdone(f32x16& a, f32x16& b) { asm volatile("s_nop 11" : "+v"(a), "+v"(b)); }
DI void mdone(f32x16& a, f32x16& b, f32x16& c, f32x16& d) { asm volatile("s_nop 11" : "+v"(a), "+v"(b), "+v"(c), "+v"(d)); }
template <class T> DI T opq_v(T v) { asm volatile("" : "+v"(v)); return v; }
template <int OFF> DI void ldx0(bf16x8& d, unsigned voff, const char* sb) { asm volatile("global_load_dwordx4 %0, %1, %2 offset:%3" : "=v"(d) : "v"(voff), "s"(sb), "i"(OFF)); }
template <int OFF> DI void ds_write_a(unsigned addr, const bf16x8& v) { asm volatile("ds_write_b128 %0, %1 offset:%2" ::"v"(addr), "a"(v), "i"(OFF) : "memory"); }
template <int N, class T> DI void wait2(T& a, T& b) { asm volatile("s_waitcnt vmcnt(%2)" : "+v"(a), "+v"(b) : "i"(cap63(N))); }
DI void set_flag(char* flag, u64 tag) {
  unsigned z = 0;
  asm volatile("s_mov_b64 exec, 1\n\tglobal_atomic_swap_x2 %0, %1, %2 sc1\n\ts_mov_b64 exec, -1" ::"v"(z), "v"(tag), "s"(flag) : "memory");
}
DI void ld8_1(u64& d, unsigned voff, const char* sb) { asm volatile("global_load_dwordx2 %0, %1, %2 sc1" : "=v"(d) : "v"(voff), "s"(sb) : "memory"); }
template <int NL> DI void rd_flag(u64& v, unsigned voff, const char* sb) {
  const u64 zero = 0;
  if constexpr (NL == 64) asm volatile("global_atomic_or_x2 %0, %1, %3, %2 sc0 sc1" : "+v"(v) : "v"(voff), "s"(sb), "v"(zero) : "memory");
  else asm volatile("s_mov_b64 exec, %4\n\tglobal_atomic_or_x2 %0, %1, %3, %2 sc0 sc1\n\ts_mov_b64 exec, -1" : "+v"(v) : "v"(voff), "s"(sb), "v"(zero), "s"((1ull << (NL & 63)) - 1) : "memory");
}
template <int NL> DI void poll(const char* base, unsigned voff, u64 tag) {
  for (int spin = 0; spin < (1 << 15); ++spin) {
    u64 v = tag;
    rd_flag<NL>(v, voff, base);
    waitx<false, 0>(v);
    if (__all(v == tag)) break;
  }
  asm volatile("" ::: "memory");
}

template <int RBX> DI void block_map(int& rb, int& cb) {
  const int x = blockIdx.x & 7, i = blockIdx.x >> 3;
  constexpr int CBX = 32 / RBX, GC = 16 / CBX;
  rb = RBX * (x / GC) + i / CBX; cb = CBX * (x % GC) + i % CBX;
}

DI f32x4 sum4(const bf16x8 (&p)[4]) {
  return (__builtin_bit_cast(f32x4, p[0]) + __builtin_bit_cast(f32x4, p[1])) + (__builtin_bit_cast(f32x4, p[2]) + __builtin_bit_cast(f32x4, p[3]));
}
DI void pin4(bf16x8 (&p)[4]) { asm volatile("" : "+v"(p[0]), "+v"(p[1]), "+v"(p[2]), "+v"(p[3])); }
DI f32x4 red16(const bf16x8 (&p)[4]) {
  f32x4 t = sum4(p);
#pragma unroll
  for (int e = 0; e < 4; ++e) {
    auto q = __builtin_amdgcn_permlane16_swap(__float_as_uint(t[e]), __float_as_uint(t[e]), false, false);
    t[e] = swap_add(__uint_as_float(q[0]) + __uint_as_float(q[1]));
  }
  return t;
}
DI void ln_stats(const bf16x8 (&p)[4], int lane, float& mean, float& rstd) {
  const f32x4 t = red16(p);
  const bool odd = lane >> 4 & 1;
  const float m = (odd ? t[2] : t[0]) * (1.f / D_);
  const float var = fmaxf((odd ? t[3] : t[1]) * (1.f / D_) - m * m, 0.f);
  const int src = ((lane & 31) >> 1) + ((lane & 1) << 4);
  mean = lane_get(rbf(m), src);
  rstd = lane_get(__builtin_amdgcn_rcpf(rbf(__builtin_amdgcn_sqrtf(rbf(rbf(var) + 1e-5f)))), src);
}
DI bf16x8 cs_frag(float c) {
  const float hi = rbf(c), mid = rbf(c - hi), lo = (c - hi) - mid;
  return __builtin_bit_cast(bf16x8, u32x4{cvtpk(hi, mid), cvtpk(lo, 0.f), 0u, 0u});
}
DI float cs_sum(const bf16x8 (&p)[4], int lane) {
  const f32x4 t = red16(p);
  const int g = lane >> 4;
  return lane_get(g & 2 ? (g & 1 ? t[3] : t[2]) : (g & 1 ? t[1] : t[0]), (lane >> 2) + ((lane & 3) << 4));
}
DI bf16x8 mean_frag(float mean, bool on) { const float m = on ? -mean : 0.f; return __builtin_bit_cast(bf16x8, u32x4{cvtpk(m, m), cvtpk(m, 0.f), 0u, 0u}); }

#define STAMP(i)

constexpr int SLOT = 16384, XL = 12;
struct WinPtr {
  unsigned b[3];
  DI void init(unsigned a) { sfor<0, 3>([&](auto i) { b[i] = a + i * 65536u; asm volatile("" : "+v"(b[i])); }); }
  template <int OFF> DI bf16x8 frag() const { return *(LAS const bf16x8*)((LAS const char*)(uintptr_t)b[OFF >> 16] + (OFF & 65535)); }
};
struct Ctx { unsigned xv; WinPtr p; };
#define IC(n) std::integral_constant<int, (n)>{}
constexpr int slot_a(int c) { constexpr int m[8] = {6, 0, 1, 2, 3, 7, 4, 5}; return m[c]; }
constexpr int slot_q(int c) { constexpr int m[8] = {8, 0, 1, 2, 3, 7, 4, 5}; return m[c]; }

template <int S> DI void ld_x(bf16x8 (&x)[64], const Ctx& c, const char* xb) { ldx1<false, (S % 8) * 1024 - 4096>(x[S], c.xv, xb + (S / 8) * 8192 + 4096); }
template <class H> constexpr int g64_wait(int s) {
  int n = 0;
  const int t0 = s - XL;
  if (t0 < 0) n += XL - 1 - s; else n += H::n(t0);
  for (int t = t0 < 0 ? 0 : t0 + 1; t < s; ++t) n += (t + XL < 64 ? 1 : 0) + H::n(t);
  return n;
}
template <bool MAPA, bool ZERO, class H, class FX>
DI void gemm64(const Ctx& c, bf16x8 (&x)[64], const char* xb, f32x16& acc0, f32x16& acc1, FX&& hook) {
  bf16x8 f0[3], f1[3];
  auto off = [](int s) constexpr { return (MAPA ? slot_a(s / 8) : s / 8) * SLOT + (s % 8) * 2048; };
  auto rd0 = [&](auto s) { f0[s % 3] = c.p.template frag<off(s)>(); };
  auto rd1 = [&](auto s) { f1[s % 3] = c.p.template frag<off(s) + 1024>(); };
  rd0(IC(0)); rd1(IC(0)); rd0(IC(1)); rd1(IC(1));
  sfor<0, 64>([&](auto s) {
    waitx<false, g64_wait<H>(s)>(x[s]);
    SBAR();
    mm<false, false, ZERO && s == 0>(acc0, f0[s % 3], x[s]);
    SBAR();
    if constexpr (s + 2 < 64) rd0(IC(s + 2));
    if constexpr (s + XL < 64) ld_x<s + XL>(x, c, xb);
    SBAR();
    mm<false, false, ZERO && s == 0>(acc1, f1[s % 3], x[s]);
    SBAR();
    if constexpr (s + 2 < 64) rd1(IC(s + 2));
    hook(s);
    SBAR();
  });
  mdone(acc0, acc1);
}
struct HookP6 { static constexpr int n(int s) { return s >= 52 ? 1 : 0; } };
struct HookP3 { static constexpr int n(int s) { return s >= 52 && s < 60 ? 1 : 0; } };
struct HookP4 { static constexpr int n(int s) { return s == 52 || s == 6 || (s >= 8 && s < 12) ? 1 : 0; } };

DI void res_to_acc(const u32x4 (&xr)[4], f32x16& a0, f32x16& a1) {
#pragma unroll
  for (int e = 0; e < 4; ++e) {
    a0[2 * e] = bflo(xr[0][e]); a0[2 * e + 1] = bfhi(xr[0][e]); a0[8 + 2 * e] = bflo(xr[1][e]); a0[9 + 2 * e] = bfhi(xr[1][e]);
    a1[2 * e] = bflo(xr[2][e]); a1[2 * e + 1] = bfhi(xr[2][e]); a1[8 + 2 * e] = bflo(xr[3][e]); a1[9 + 2 * e] = bfhi(xr[3][e]);
  }
  asm volatile("" : "+v"(a0), "+v"(a1));
}
DI void store_stats(int r, const u32x4 (&xr)[4], char* stp) {
  float s1 = 0.f, s2 = 0.f, s3 = 0.f, s4 = 0.f;
#pragma unroll
  for (int i = 0; i < 4; ++i)
#pragma unroll
    for (int e = 0; e < 4; e += 2) {
      s1 = dot2(xr[i][e], 0x3f803f80u, s1); s2 = dot2(xr[i][e], xr[i][e], s2);
      s3 = dot2(xr[i][e + 1], 0x3f803f80u, s3); s4 = dot2(xr[i][e + 1], xr[i][e + 1], s4);
    }
  st2_1(f32x2{swap_add(s1 + s3), swap_add(s2 + s4)}, (unsigned)(r * 8), stp);
}
DI void store_x(const Ctx& c, int r, const f32x16& a0, const f32x16& a1, u32x4 (&xr)[4], char* dst, char* stp) {
  xr[0] = __builtin_bit_cast(u32x4, pack8<0>(a0)); st4_1<0>(xr[0], c.xv, dst);
  xr[1] = __builtin_bit_cast(u32x4, pack8<1>(a0)); st4_1<1024>(xr[1], c.xv, dst);
  xr[2] = __builtin_bit_cast(u32x4, pack8<0>(a1)); st4_1<2048>(xr[2], c.xv, dst);
  xr[3] = __builtin_bit_cast(u32x4, pack8<1>(a1)); st4_1<3072>(xr[3], c.xv, dst);
  store_stats(r, xr, stp);
}

template <class FX> DI void hedge_exp(const f32x16& p, float sc, f32x16& ep, f32x16& en, FX&& hook) {
  float mx = fmaxf(p[0], p[1]), mn = fminf(p[0], p[1]);
#pragma unroll
  for (int i = 2; i < 16; i += 2) { mx = fmaxf(fmaxf(mx, p[i]), p[i + 1]); mn = fminf(fminf(mn, p[i]), p[i + 1]); }
  mx = swap_max(mx) * sc; mn = swap_min(mn) * sc;
  sfor<0, 16>([&](auto i) {
    SBAR();
    hook(i);
    constexpr int e = i;
    float a = __builtin_amdgcn_exp2f(__builtin_fmaf(p[e], sc, -mx)), b = __builtin_amdgcn_exp2f(__builtin_fmaf(p[e], -sc, mn));
    asm volatile("" : "+v"(a), "+v"(b));
    ep[e] = a; en[e] = b;
  });
  SBAR();
}

constexpr int a_xt(int s) {
  int t = s < 0 ? XL : (s < 15 ? XL + (3 * (s + 1)) / 2 : 34 + (2 * (s - 14)) / 3);
  if (s >= 15 && t < s + 10) t = s + 10;
  return t > 64 ? 64 : t;
}
struct SchedKV {
  int xw[64], ww[8], fw, sw, pw;
  constexpr SchedKV(bool first) : xw{}, ww{}, fw(0), sw(0), pw(0) {
    int pos = 0, xpos[64] = {}, wpos[8] = {}, fpos = 0, spos = 0, ppos = 0;
    for (int s = 0; s < XL; ++s) xpos[s] = ++pos;
    if (first) ++pos;
    for (int s = 0; s < 64; ++s) {
      if ((s + 1) % 8 == 0 && (s + 1) / 8 < 8) ww[(s + 1) / 8] = pos - wpos[(s + 1) / 8];
      if (s == 60) sw = pos - spos;
      xw[s] = pos - xpos[s];
      for (int t = a_xt(s - 1); t < a_xt(s); ++t) xpos[t] = ++pos;
      if ((s < 15 && first) || (s >= 15 && s <= 46)) wpos[(s + 1) / 8 + 2] = ++pos;
      if (s >= 48 && s <= 52) { ++pos; if (s == 51) spos = pos; if (s == 52) ppos = pos; }
      if (s == 56) fpos = ++pos;
    }
    pw = pos - ppos; fw = pos - fpos;
  }
};
constexpr int QP = 30, QC = 42;
constexpr int q_nd(int s) { return s < 6 ? 0 : (s < 14 ? 1 : (s < 42 ? ((s - 14) % 8 < 4 ? 1 : 0) : 0)); }
struct SchedQ {
  int ww[8], pw, lw[33];
  constexpr SchedQ() : ww{}, pw(0), lw{} {
    int pos = 3, stpos = 3, q6 = 0, q7 = 0, ppos = 0, lpos[34] = {};
    for (int s = 0; s < 64; ++s) {
      if ((s + 2) % 8 == 0 && (s + 2) / 8 < 8) {
        const int g = (s + 2) / 8;
        ww[g] = g == 2 ? pos - stpos : (g == 6 ? pos - q6 : (g == 7 ? pos - q7 : 63));
        if (g == 2) ++pos;
      }
      if (s == QC) pw = pos - ppos;
      pos += q_nd(s); if (s == 9) q6 = pos; if (s == 13) q7 = pos;
      if (s == QP) ppos = ++pos;
      if (s >= QC && s < QC + 17) { lpos[2 * (s - QC)] = ++pos; lpos[2 * (s - QC) + 1] = ++pos; }
    }
    for (int i = 0; i < 32; ++i) { lw[i] = pos - lpos[i]; pos += i & 1; }
    lw[32] = pos - lpos[33];
  }
};

#define R_OA "v[64:79]"
#define R_OB "v[80:95]"
#define S0_(n, q, m) "v_mfma_f32_32x32x16_bf16 v[" #n ":" #n "+15], v[208:211], v[" #q ":" #q "+3], v[" #m ":" #m "+15]\n\t"
#define S1_(n, q) "v_mfma_f32_32x32x16_bf16 v[" #n ":" #n "+15], v[212:215], v[" #q "+4:" #q "+7], v[" #n ":" #n "+15]\n\t"
#define PV0_(o, p) "v_smfmac_f32_32x32x32_bf16 v[" #o ":" #o "+15], v[216:219], v[" #p ":" #p "+7], v236\n\t"
#define PV1_(o, p) "v_smfmac_f32_32x32x32_bf16 v[" #o ":" #o "+15], v[220:223], v[" #p "+8:" #p "+15], v236\n\t"
#define L0_(l, p) "v_smfmac_f32_16x16x64_bf16 v[" #l ":" #l "+3], v[224:227], v[" #p ":" #p "+7], v236\n\t"
#define L1_(l, p) "v_smfmac_f32_16x16x64_bf16 v[" #l ":" #l "+3], v[224:227], v[" #p "+8:" #p "+15], v236\n\t"
#define E_(e, i) "v_exp_f32 v[" #e "+" #i "], v[" #e "+" #i "]\n\t"
#define RD_(d, off) "ds_read_b128 v[" #d ":" #d "+3], v237 offset:" #off "\n\t"
#define HALF_(e, n, q, m, p, o, l, X0, X1, X2, X3, X4, X5) \
  S0_(n, q, m) X0 E_(e, 0) E_(e, 1) E_(e, 2) S1_(n, q) X1 E_(e, 3) E_(e, 4) E_(e, 5) PV0_(o, p) X2 E_(e, 6) E_(e, 7) E_(e, 8) \
  PV1_(o, p) X3 E_(e, 9) E_(e, 10) E_(e, 11) L0_(l, p) X4 E_(e, 12) E_(e, 13) L1_(l, p) X5 E_(e, 14) E_(e, 15)
#define LG2_ "s_waitcnt lgkmcnt(2)\n\t"
#define LG0_ "s_waitcnt lgkmcnt(0)\n\t"
#define STEP_(b, nb, kv, wo, Y0, Y1) \
  HALF_(128, 160, 200, 112, 176, 80, 232, "", "", RD_(208, b + 2048), RD_(212, b + 3072), RD_(216, b), RD_(220, b + 1024) LG2_) \
  HALF_(160, 144, 192, 96, 128, 64, 228, "", LG0_, "s_add_u32 m0, s44, " #nb "\n\t", "", "global_load_lds_dwordx4 v" #kv ", s[40:41] sc1\n\t", \
        "global_load_lds_dwordx4 v" #kv ", s[40:41] offset:1024 sc1\n\t") \
  HALF_(144, 176, 200, 112, 160, 80, 232, Y0, "", RD_(208, b + 6144), RD_(212, b + 7168), RD_(216, b + 4096), RD_(220, b + 5120) LG2_) \
  HALF_(176, 128, 192, 96, 144, 64, 228, "s_mov_b32 m0, s45\n\t", LG0_, "global_load_lds_dwordx4 v240, s[42:43] offset:" #wo " sc1\n\t", Y1, \
        "s_waitcnt vmcnt(4)\n\t", "s_barrier\n\t")
DI float max16(const f32x16& s) {
  float mx = s[0];
#pragma unroll
  for (int e = 1; e < 16; ++e) mx = fmaxf(mx, s[e]);
  return mx;
}
typedef __attribute__((ext_vector_type(8))) unsigned u32x8;
typedef __attribute__((ext_vector_type(16))) unsigned u32x16;

__global__ __launch_bounds__(256, 1) void k_main(const char* xin, const char* wqkv, const char* wphi, const char* woA, const char* wq, const char* wk, const char* wv,
                                                 const char* woB, char* ws, char* out) {
  __shared__ __attribute__((aligned(16))) char smem_[163840];
  LAS char* const smem = (LAS char*)smem_;
  const unsigned lds0 = (unsigned)(uintptr_t)smem;
  Ctx c;
  int lane = threadIdx.x & 63, w = __builtin_amdgcn_readfirstlane(threadIdx.x >> 6), r = lane & 31, h = lane >> 5, rb, cb;
  block_map<4>(rb, cb);
  int tt = rb * 4 + w, blk = (lane >> 4) & 1, q4 = (lane & 15) >> 2, p4 = lane & 3;
  c.xv = lane * 16; c.p.init(lds0 + c.xv);
  char* flags = ws + WS_FLAG;
  char* col = ws + WS_COL + (size_t)cb * 2 * MB;
  u64 tag;
  u32x4 xr[4];
  float csk, csv;
  auto wt = [&](int m, int ch) { return col + C_WT + m * 131072 + ch * SLOT + w * 4096; };
  auto pc_w = [&](int m, int slot, int ch, auto i) { dma_c<i * 1024>(lds0 + slot * SLOT + w * 4096, c.xv, wt(m, ch)); };
  auto pc_kv = [&](auto g, auto i) { pc_w(1 + i / 4, 2 * (g % 4) + i / 4, g, IC(i % 4)); };
  auto pc_q = [&](auto ch, auto i) { pc_w(0, slot_q(ch), ch, i); };
  auto ld_cs = [&](bf16x8& d, int m, auto i) { ldx1<false, i * 1024>(d, c.xv, col + C_CS + m * 4096); };
  auto raw_in = [&](auto j, int slot) {
    const int hi = rb >> 3, pitch = (j == 0 || (j == 1 && !hi)) ? 6144 : 2048, row = lane >> 3;
    const char* src = (j == 0 ? wqkv + 2048 + hi * 2048 : (j == 1 ? (hi ? woA : wqkv) : (hi ? woB : wq))) + cb * 128 + (size_t)(rb & 7) * 128 * pitch;
    const unsigned v0 = (unsigned)((32 * w + row) * pitch + ((lane & 7) ^ (((row >> 1) & 1) << 2)) * 16);
    sfor<0, 4>([&](auto i) { dma<i * 1024>(lds0 + slot * SLOT + w * 4096, v0 + i * (8 * pitch - 1024), src); });
  };
  auto raw_out = [&](auto j, int slot) {
    const int hi = rb >> 3, ch = rb & 7, m = j == 0 ? 1 + hi : (j == 1 ? 3 * hi : 4 + hi);
    LAS const char* p = smem + slot * SLOT + (w >> 1) * 8192 + (4 * h + q4) * 128 + (((((q4 >> 1) ^ w) & 1) << 2) + 2 * blk + (p4 >> 1)) * 16 + 8 * (p4 & 1);
    char* dst = col + C_WT + m * 131072 + ch * SLOT + (w >> 1) * 8192 + (w & 1) * 1024 + 4096;
    float cs = 0.f;
    sfor<0, 4>([&](auto s) {
      const u32x4 f = __builtin_bit_cast(u32x4, cat8(trread(p + s * 2048), trread(p + s * 2048 + 1024)));
      st4_1<s * 2048 - 4096>(f, c.xv, dst);
#pragma unroll
      for (int e = 0; e < 4; ++e) cs = dot2(f[e], 0x3f803f80u, cs);
    });
    st1_1(swap_add(cs), (unsigned)(r * 4), col + C_CS + m * 4096 + (ch * 2 + (w >> 1)) * 256 + (w & 1) * 128);
  };
  {
    [[maybe_unused]] const int l = 0;
    STAMP(30);
    bf16x8 cf;
    { const char* pc; asm volatile("s_getpc_b64 %0" : "=s"(pc));
      ldx0<0>(cf, c.xv, pc + (((blockIdx.x >> 3) * 4 + w) % 46) * 1024); }
    raw_in(IC(0), 7);
    u64 ep; ld8_1(ep, 0u, ws + WS_EPOCH);
    bf16x8 xt[4];
    sfor<0, 4>([&](auto i) { ldx0<i * 32>(xt[i], (unsigned)(r * 2048 + h * 16), xin + (size_t)tt * 65536 + cb * 128); });
    { const unsigned z = 0, vo = (unsigned)(lane ? FL_W + cb * 128 : FL_ACT + tt * 128);
      asm volatile("s_mov_b64 exec, 3\n\tglobal_atomic_or %0, %1, %2 sc1\n\ts_mov_b64 exec, -1" ::"v"(vo), "v"(z), "s"(flags) : "memory"); }
    wait_bar<6>();
    raw_out(IC(0), 7);
    asm volatile("s_waitcnt vmcnt(6)" : "+v"(cf), "+v"(ep), "+v"(xt[0]), "+v"(xt[1]), "+v"(xt[2]), "+v"(xt[3]));
    { char* dst = ws + (size_t)rb * 2 * MB + w * 65536 + cb * 4096;
      sfor<0, 4>([&](auto i) { xr[i] = unperm(__builtin_bit_cast(u32x4, xt[i])); st4_1<i * 1024>(xr[i], c.xv, dst); });
      store_stats(r, xr, ws + WS_ST + tt * 4096 + cb * 256); }
    { u64 z = ((u64)__builtin_amdgcn_readfirstlane((unsigned)(ep >> 32)) << 32 | __builtin_amdgcn_readfirstlane((unsigned)ep)) + 0x9E3779B97F4A7C15ull;
      z = (z ^ (z >> 30)) * 0xBF58476D1CE4E5B9ull; z = (z ^ (z >> 27)) * 0x94D049BB133111EBull; tag = z ^ (z >> 31); }
    waitv<0>();
    set_flag(flags + FL_ACT + tt * 128 + cb * 8, tag);
    bar();
    if (w == 0) set_flag(flags + FL_W + cb * 128 + rb * 8, tag);
    raw_in(IC(1), 8); raw_in(IC(2), 9);
    unsigned tch;
    {
      const int gw = blockIdx.x * 4 + w; const unsigned z = 0;
      const unsigned vo = (unsigned)(lane < 5 ? WS_KVF - WS_ST + (gw * 5 + lane) * 128 : WS_FLAG - WS_ST + gw * 128);
      asm volatile("s_mov_b64 exec, 63\n\tglobal_atomic_or %0, %1, %2 sc1\n\ts_mov_b64 exec, -1" ::"v"(vo), "v"(z), "s"(ws + WS_ST) : "memory");
      const char* sm = (gw & 512) ? ((gw & 256) ? wk : wv) : wphi;
      asm volatile("global_load_dword %0, %1, %2" : "=v"(tch) : "v"((unsigned)((gw & 255) * 256 + lane * 4)), "s"(sm));
    }
    poll<32>(flags, (unsigned)((lane & 15) * 8 + (blk ? FL_W + cb * 128 : FL_ACT + tt * 128)), tag);
    waitx<false, 0>(tch);
    bf16x8 ck[4], cv[4];
    sfor<0, 4>([&](auto i) { ld_cs(ck[i], 1, i); }); sfor<0, 4>([&](auto i) { ld_cs(cv[i], 2, i); });
    sfor<0, 16>([&](auto i) { pc_kv(IC(i / 8), IC(i % 8)); });
    waitv<16>(); pin4(ck); pin4(cv);
    csk = cs_sum(ck, lane); csv = cs_sum(cv, lane);
    STAMP(31);
  }
  char *act, *stt, *fact;
#define REFRESH() do { \
    asm volatile("" : "+s"(w), "+s"(cb), "+s"(rb), "+s"(ws), "+s"(tag)); \
    asm volatile("" : "+v"(lane), "+v"(csk), "+v"(csv)); \
    r = lane & 31; h = lane >> 5; blk = (lane >> 4) & 1; q4 = (lane & 15) >> 2; p4 = lane & 3; tt = rb * 4 + w; \
    flags = ws + WS_FLAG; col = ws + WS_COL + (size_t)cb * 2 * MB; c.xv = lane * 16; c.p.init(lds0 + c.xv); \
    act = ws + (size_t)rb * 2 * MB + l * 4 * STG + w * 65536;     \
    fact = flags + FL_ACT + (l * 4 * 64 + tt) * 128;                  \
    stt = ws + WS_ST + (size_t)l * 512 * 1024 + tt * 4096;            \
  } while (0)
  for (int l = 0; l < 2; ++l) {
    REFRESH();
    float mean, rstd, csb;
    bf16x8 park[32];
    STAMP(0);
    {
      if (l > 0) poll<16>(fact, lane * 8, tag);
      STAMP(1);
      constexpr int NA = 60;
      bf16x8 x[64];
      f32x16 ak0, ak1, av0, av1;
      auto issue_x = [&](auto s) { ldx1<(s < NA), (s % 8) * 1024 - 4096>(x[s], c.xv, act + (s / 8) * 8192 + 4096); };
      {
        constexpr SchedKV SC(false), S1(true);
        bf16x8 sp[4], f[4][2], ck, cv;
        u64 f1;
        sfor<0, XL>(issue_x);
        wait_bar<XL>();
        if (l == 0) pc_kv(IC(2), IC(0));
        auto rd = [&](auto s, auto t) { f[t][s % 2] = c.p.template frag<(2 * ((s / 8) % 4) + t / 2) * SLOT + (s % 8) * 2048 + (t % 2) * 1024>(); };
        sfor<0, 4>([&](auto t) { rd(IC(0), t); });
        sfor<0, 64>([&](auto s) {
          if constexpr ((s + 1) % 8 == 0 && (s + 1) / 8 >= 2 && (s + 1) / 8 < 8) wait_bar<((s + 1) / 8 < 4 ? S1.ww[(s + 1) / 8] : SC.ww[(s + 1) / 8])>();
          if constexpr (s == 47) { if (l == 0 && w == 0) set_flag(flags + FL_W + 2048 + cb * 128 + rb * 8, tag); }
          if constexpr (s == 60) { waitv<SC.sw>(); pin4(sp); }
          waitx<(s < NA), SC.xw[s]>(x[s]);
          SBAR();
          mm<false, (s < NA), s == 0>(ak0, f[0][s % 2], x[s]);
          SBAR();
          if constexpr (s + 1 < 64) rd(IC(s + 1), IC(0));
          if constexpr (a_xt(s - 1) < a_xt(s)) issue_x(IC(a_xt(s - 1)));
          if constexpr (s == 60) ln_stats(sp, lane, mean, rstd);
          SBAR();
          mm<false, (s < NA), s == 0>(ak1, f[1][s % 2], x[s]);
          SBAR();
          if constexpr (s + 1 < 64) rd(IC(s + 1), IC(1));
          if constexpr (a_xt(s - 1) + 1 < a_xt(s)) issue_x(IC(a_xt(s - 1) + 1));
          if constexpr (s == 58) ck = opq_v(cs_frag(csk));
          SBAR();
          mm<true, (s < NA), s == 0>(av0, f[2][s % 2], x[s]);
          SBAR();
          if constexpr (s + 1 < 64) rd(IC(s + 1), IC(2));
          if constexpr (s < 15) { if (l == 0) pc_kv(IC((s + 1) / 8 + 2), IC((s + 1) % 8)); }
          if constexpr (s >= 15 && s <= 46) pc_kv(IC((s + 1) / 8 + 2), IC((s + 1) % 8));
          if constexpr (s == 58) cv = opq_v(cs_frag(csv));
          SBAR();
          mm<true, (s < NA), s == 0>(av1, f[3][s % 2], x[s]);
          SBAR();
          if constexpr (s + 1 < 64) rd(IC(s + 1), IC(3));
          if constexpr (s == 18) { if (l == 0) raw_out(IC(1), 8); }
          if constexpr (s == 26) { if (l == 0) raw_out(IC(2), 9); }
          if constexpr (s == 56) { f1 = tag; rd_flag<16>(f1, lane * 8, flags + FL_W + 2048 + cb * 128); }
          if constexpr (s >= 48 && s < 52) ldx1<false, (s - 48) * 1024>(sp[s - 48], c.xv, stt);
          if constexpr (s == 52) dma<0>(lds0 + 9 * SLOT + w * 1024, c.xv, wphi + cb * 4096 + w * 1024);
          if constexpr (s == 62) {
            const bf16x8 m0 = mean_frag(mean, h == 0), m1 = mean_frag(mean, h == 1);
            mm<false, false, false, true>(ak0, ck, m0); mm<false, false, false, true>(ak1, ck, m1);
            mm<true, false, false>(av0, cv, m0); mm<true, false, false>(av1, cv, m1);
          }
          SBAR();
        });
        mdone(ak0, ak1, av0, av1);
        waitx<false, SC.fw>(f1);
        if (!__all(f1 == tag)) poll<16>(flags + FL_W + 2048 + cb * 128, lane * 8, tag);
        bar();
      }
      STAMP(2);
      REFRESH();
      LAS char* pk_img = smem + 4 * SLOT;
      LAS char* v_img = smem + 6 * SLOT;
      char* kvp = col + C_KVP + l * 131072 + w * 32768;
      char* zp = col + C_ZP + l * 8192 + w * 2048;
      char* fkvp = flags + FL_KVP + (l * 64 + cb * 4 + w) * 128;
      const float sce = rstd * LOG2E;
      auto phi_mm = [&](const f32x16& t0, const f32x16& t1, f32x16& p0, f32x16& p1) {
        LAS const char* wp = smem + 9 * SLOT + (4 * h + q4) * 64 + blk * 32 + p4 * 8;
        const bf16x8 w00 = cat8(trread(wp), trread(wp + 512)), w01 = cat8(trread(wp + 1024), trread(wp + 1536));
        const bf16x8 w10 = cat8(trread(wp + 2048), trread(wp + 2560)), w11 = cat8(trread(wp + 3072), trread(wp + 3584));
        mm<false, false, true, true>(p0, w00, pack8<0>(t0)); mm<false, false, false, true>(p0, w01, pack8<1>(t0));
        mm<false, false, true, true>(p1, w10, pack8<0>(t1)); mm<false, false, false, true>(p1, w11, pack8<1>(t1));
      };
      bf16x8 cq[4], cw[4];
      {
        auto side = [&](auto i) {
          SBAR();
          if constexpr (i < 24) pc_q(IC(i / 4), IC(i % 4)); else if constexpr (i < 28) ld_cs(cq[i - 24], 0, IC(i - 24)); else ld_cs(cw[i - 28], 4, IC(i - 28));
          SBAR();
        };
        f32x16 p[2];
        side(IC(0)); side(IC(1));
        phi_mm(ak0, ak1, p[0], p[1]);
        side(IC(2)); side(IC(3));
        {
          LAS char* d = v_img + w * 2048 + lane * 16;
          float rs[16];
#pragma unroll
          for (int i = 0; i < 16; ++i) rs[i] = lane_get(rstd, (i & 3) + 8 * (i >> 2) + 4 * h);
          side(IC(4));
#pragma unroll
          for (int i = 0; i < 16; ++i) av0[i] *= rs[i];
          *(LAS bf16x8*)d = pack8<0>(av0); *(LAS bf16x8*)(d + 1024) = pack8<1>(av0);
          side(IC(5));
#pragma unroll
          for (int i = 0; i < 16; ++i) av1[i] *= rs[i];
          *(LAS bf16x8*)(d + 8192) = pack8<0>(av1); *(LAS bf16x8*)(d + 9216) = pack8<1>(av1);
        }
        asm volatile("" : "+v"(p[0]), "+v"(p[1]));
        sfor<0, 2>([&](auto hd) {
          f32x16 ep, en;
          hedge_exp(p[hd], sce, ep, en, [&](auto i) { if constexpr (i % 2 == 0) side(IC(6 + hd * 13 + i / 2)); });
          float sp = 0.f, sn = 0.f;
#pragma unroll
          for (int i = 0; i < 16; ++i) { sp += ep[i]; sn += en[i]; }
          side(IC(14 + hd * 13));
          sp = __builtin_amdgcn_rcpf(swap_add(sp)); sn = __builtin_amdgcn_rcpf(swap_add(sn));
          LAS char* row = pk_img + hd * 16384 + (w * 32 + r) * 128 + h * 8;
          sfor<0, 4>([&](auto g) {
            constexpr int gq = g;
            *(LAS u32x2*)(row + ((gq ^ (r & 7)) << 4)) = u32x2{cvtpk(ep[4 * gq] * sp, ep[4 * gq + 1] * sp), cvtpk(ep[4 * gq + 2] * sp, ep[4 * gq + 3] * sp)};
            *(LAS u32x2*)(row + (((4 + gq) ^ (r & 7)) << 4)) = u32x2{cvtpk(en[4 * gq] * sn, en[4 * gq + 1] * sn), cvtpk(en[4 * gq + 2] * sn, en[4 * gq + 3] * sn)};
            side(IC(15 + hd * 13 + gq));
          });
        });
      }
      asm volatile("s_waitcnt vmcnt(8) lgkmcnt(0)\n\ts_barrier" ::: "memory");
      {
        const int hd = w >> 1, fh = w & 1;
        f32x16 kv, z;
        const unsigned one2 = opq_v(0x3f803f80u);
        const bf16x8 ones = __builtin_bit_cast(bf16x8, u32x4{one2, one2, one2, one2});
        const int ch = fh * 4 + 2 * blk + (p4 >> 1), r0 = 4 * h + q4;
        LAS const char* pa = pk_img + hd * 16384 + r0 * 128 + ((ch ^ r0) << 4) + 8 * (p4 & 1);
        LAS const char* pb = v_img + hd * 8192 + lane * 16;
        bf16x8 a[8], b[8];
#pragma unroll
        for (int s = 0; s < 8; ++s) { a[s] = cat8(trread(pa + s * 2048), trread(pa + s * 2048 + 1024)); b[s] = *(LAS const bf16x8*)(pb + s * 1024); }
        SBAR();
        sfor<0, 8>([&](auto s) { mm<false, false, s == 0>(kv, a[s], b[s]); });
        sfor<0, 8>([&](auto s) {
          mm<false, false, s == 0, s == 0>(z, ones, a[s]);
          if constexpr (s == 1) { mdone(kv); st4_1<0>(__builtin_bit_cast(u32x4, pack8<0>(kv)), c.xv, kvp + rb * 2048); }
          if constexpr (s == 2) st4_1<1024>(__builtin_bit_cast(u32x4, pack8<1>(kv)), c.xv, kvp + rb * 2048);
          if constexpr (s == 3) { waitv<2>(); pin4(cq); pin4(cw); cq[0] = cs_frag(cs_sum(cq, lane)); pin4(cq); }
          if constexpr (s == 5) { csb = opq_v(cs_sum(cw, lane)); }
          SBAR();
        });
        mdone(z);
        st1_1(z[0], (unsigned)(r * 4), zp + rb * 128);
      }
      STAMP(3);
      f32x16 aq0, aq1;
      bf16x8 pp[32], zq[2];
      constexpr SchedQ SQ;
      auto ld_park = [&](auto i) { ldx1<true, (i % 4) * 1024>(park[i], c.xv, wt(4, i / 4)); };
      {
        bf16x8 f0[3], f1[3];
        u64 fv;
        auto off = [](int s) constexpr { return slot_q(s / 8) * SLOT + (s % 8) * 2048; };
        auto rd0 = [&](auto s) { f0[s % 3] = c.p.template frag<off(s)>(); };
        auto rd1 = [&](auto s) { f1[s % 3] = c.p.template frag<off(s) + 1024>(); };
        auto ldp = [&](auto i) {
          if constexpr (i < 32) ldx1<true, (i % 8) * 1024 - 4096>(pp[i], c.xv, kvp + (i / 8) * 8192 + 4096); else ldx1<false, (i - 32) * 1024>(zq[i - 32], c.xv, zp);
        };
        rd0(IC(0)); rd1(IC(0)); rd0(IC(1)); rd1(IC(1));
        sfor<0, 64>([&](auto s) {
          if constexpr ((s + 2) % 8 == 0 && (s + 2) / 8 < 8) {
            wait_bar<SQ.ww[(s + 2) / 8]>();
            if constexpr ((s + 2) / 8 == 2) set_flag(fkvp + rb * 8, tag);
          }
          if constexpr (s == QC) { waitx<false, SQ.pw>(fv); if (!__all(fv == tag)) poll<16>(fkvp, lane * 8, tag); }
          SBAR();
          mm<false, (s < NA), s == 0>(aq0, f0[s % 3], x[s]);
          SBAR();
          if constexpr (s + 2 < 64) rd0(IC(s + 2));
          if constexpr (q_nd(s)) {
            if constexpr (s < 14) pc_q(IC(6 + (s - 6) / 4), IC((s - 6) % 4)); else pc_w(3, slot_a((s - 14) / 8), (s - 14) / 8, IC((s - 14) % 8));
          }
          if constexpr (s == QP) { fv = tag; rd_flag<16>(fv, lane * 8, fkvp); }
          if constexpr (s >= QC && s < QC + 17) ldp(IC(2 * (s - QC)));
          SBAR();
          mm<false, (s < NA), s == 0>(aq1, f1[s % 3], x[s]);
          SBAR();
          if constexpr (s + 2 < 64) rd1(IC(s + 2));
          if constexpr (s >= QC && s < QC + 17) ldp(IC(2 * (s - QC) + 1));
          if constexpr (s == 2) { mm<false, false, false, true>(aq0, cq[0], mean_frag(mean, h == 0)); mm<false, false, false, true>(aq1, cq[0], mean_frag(mean, h == 1)); }
          SBAR();
        });
        mdone(aq0, aq1);
        bar();
      }
      STAMP(4);
      REFRESH();
      bf16x8 pq[2][4];
      f32x16 kv;
      {
        f32x16 p[2];
        phi_mm(aq0, aq1, p[0], p[1]);
        u32x4 s0, s1;
#pragma unroll
        for (int e = 0; e < 4; ++e) {
          const int kr = 8 * (e >> 1) + 4 * h + 2 * (e & 1), rr = r & 15;
          const unsigned v = rr == kr ? 0x3f80u : (rr == kr + 1 ? 0x3f800000u : 0u);
          s0[e] = r < 16 ? v : 0u; s1[e] = r < 16 ? 0u : v;
        }
        const bf16x8 sel0 = opq_v(__builtin_bit_cast(bf16x8, s0)), sel1 = opq_v(__builtin_bit_cast(bf16x8, s1));
        mdone(p[0], p[1]);
        sfor<0, 2>([&](auto hd) {
          f32x16 ep, en;
          hedge_exp(p[hd], sce, ep, en, [&](auto e) {
            constexpr int i = hd * 16 + e;
            waitx<true, SQ.lw[i]>(pp[i]);
            mm<false, true, i == 0>(kv, (i & 1) ? sel1 : sel0, pp[i]);
            if constexpr (i & 1) pc_w(3, slot_a(4 + i / 8), 4 + i / 8, IC((i / 2) % 4));
          });
          pq[hd][0] = pack8<0>(ep); pq[hd][1] = pack8<1>(ep); pq[hd][2] = pack8<0>(en); pq[hd][3] = pack8<1>(en);
          pin4(pq[hd]);
        });
      }
      STAMP(5);
      {
        asm volatile("s_waitcnt vmcnt(%2)" : "+v"(zq[0]), "+v"(zq[1]) : "i"(SQ.lw[32]));
        LAS char* sh = smem + 9 * SLOT + 4096;
        ld_park(IC(0)); ld_park(IC(1));
        mdone(kv);
        *(LAS bf16x8*)(sh + w * 2048 + lane * 16) = pack8<0>(kv); *(LAS bf16x8*)(sh + w * 2048 + 1024 + lane * 16) = pack8<1>(kv);
        f32x4 zs = __builtin_bit_cast(f32x4, zq[0]) + __builtin_bit_cast(f32x4, zq[1]);
#pragma unroll
        for (int e = 0; e < 4; ++e) zs[e] += lane_get(zs[e], lane ^ 8);
        ld_park(IC(2)); ld_park(IC(3));
#pragma unroll
        for (int e = 0; e < 4; ++e) zs[e] += lane_get(zs[e], lane ^ 16);
        ld_park(IC(4)); ld_park(IC(5));
#pragma unroll
        for (int e = 0; e < 4; ++e) zs[e] = swap_add(zs[e]);
        if (lane < 8) *(LAS u32x2*)(sh + 8192 + w * 64 + (lane & 1) * 32 + (lane >> 2) * 16 + ((lane >> 1) & 1) * 8) = u32x2{cvtpk(zs[0], zs[1]), cvtpk(zs[2], zs[3])};
        else if (lane < 24) *(LAS unsigned*)(sh + 8192 + 256 + w * 64 + (lane - 8) * 4) = opq_v(0x3f803f80u);
        ld_park(IC(6)); ld_park(IC(7));
        lds_bar();
        STAMP(6);
        char* dst = act + STG + cb * 4096;
        LAS const char* zb = sh + 8192 + (r & 1) * 256 + h * 32;
        bf16x8 ka[8], za[8];
#pragma unroll
        for (int k = 0; k < 8; ++k) { za[k] = *(LAS const bf16x8*)(zb + (k >> 1) * 64 + (k & 1) * 16); ka[k] = *(LAS const bf16x8*)(sh + k * 1024 + lane * 16); }
        f32x16 nm[4], dn[2];
        float ai[2], bi[2];
        auto coef = [&](auto hd) {
          const float a = __builtin_amdgcn_rcpf(dn[0][1]), b = __builtin_amdgcn_rcpf(dn[1][1]);
          const float inv = __builtin_amdgcn_rcpf(dn[0][0] * a + dn[1][0] * b + 1e-6f);
          ai[hd] = opq_v(a * inv); bi[hd] = opq_v(b * inv);
        };
        auto fin = [&](auto hd) {
          f32x16 o;
#pragma unroll
          for (int i = 0; i < 16; ++i) o[i] = nm[2 * hd][i] * ai[hd] + nm[2 * hd + 1][i] * bi[hd];
          st4_1<hd * 2048>(__builtin_bit_cast(u32x4, pack8<0>(o)), c.xv, dst);
          st4_1<hd * 2048 + 1024>(__builtin_bit_cast(u32x4, pack8<1>(o)), c.xv, dst);
        };
        ld_park(IC(8)); ld_park(IC(9));
        sfor<0, 4>([&](auto k) { mm<false, false, k % 2 == 0>(dn[k / 2], za[k], pq[0][k]); if constexpr (k % 2) ld_park(IC(10 + k / 2)); });
        sfor<0, 4>([&](auto k) { mm<false, false, k % 2 == 0>(nm[k / 2], ka[k], pq[0][k]); if constexpr (k % 2) ld_park(IC(12 + k / 2)); });
        mdone(dn[0], dn[1]); coef(IC(0));
        sfor<0, 4>([&](auto k) { mm<false, false, k % 2 == 0>(dn[k / 2], za[4 + k], pq[1][k]); if constexpr (k % 2) ld_park(IC(14 + k / 2)); });
        sfor<0, 4>([&](auto k) { mm<false, false, k % 2 == 0>(nm[2 + k / 2], ka[4 + k], pq[1][k]); if constexpr (k % 2) ld_park(IC(16 + k / 2)); });
        asm volatile("" : "+v"(nm[0]), "+v"(nm[1]));
        fin(IC(0));
        mdone(dn[0], dn[1], nm[2], nm[3]); coef(IC(1));
        fin(IC(1));
        sfor<18, 28>(ld_park);
        waitv<10>();
        set_flag(fact + 8192 + cb * 8, tag);
        sfor<28, 32>(ld_park);
      }
    }
    STAMP(7);
    REFRESH();
    {
      f32x16 a0, a1; res_to_acc(xr, a0, a1);
      poll<16>(fact + 8192, lane * 8, tag);
      sfor<0, 32>([&](auto i) { waitx<true, 63>(park[i]); });
      STAMP(8);
      bf16x8 x[64];
      const char* xb = act + STG;
      sfor<0, XL>([&](auto s) { ld_x<s>(x, c, xb); });
      bar();
      const char* wkv = ((cb & 2) ? wv : wk) + w * 256 * 64;
      const unsigned kvv = (unsigned)((lane >> 1) * 64 + (cb & 1) * 32 + (lane & 1) * 16);
      const unsigned wa = lds0 + w * 4096 + c.xv, wb = wa + 65536;
      auto unpark = [&](auto i) {
        constexpr int off = slot_a(i / 4) * SLOT + (i % 4) * 1024;
        ds_write_a<(off & 65535)>(off < 65536 ? wa : wb, park[i]);
      };
      gemm64<true, false, HookP3>(c, x, xb, a0, a1, [&](auto s) {
        if constexpr (s == 33) bar();
        if constexpr (s >= 36 && s < 52) unpark(IC(s - 36));
        if constexpr (s >= 52 && s < 60) dma<0>(lds0 + 8 * SLOT + w * 8192 + (s - 52) * 1024, kvv, wkv + (s - 52) * 32 * 64);
      });
      STAMP(9);
      store_x(c, r, a0, a1, xr, act + 2 * STG + cb * 4096, stt + 256 * 1024 + cb * 256);
      bar();
      sfor<16, 32>(unpark);
      waitv<0>();
      set_flag(fact + 2 * 8192 + cb * 8, tag);
    }
    STAMP(10);
    REFRESH();
    u32x16 qq;
    char* kvf = ws + WS_KVF + l * 320 * KB;
    bf16x8 k0, k1;
    {
      char* fkv = flags + FL_KV + l * 2048;
      const int du = (cb >> 2) - w, ty = cb & 3;
      LAS char* reg = smem + 8 * SLOT;
      bf16x8 fu[16], cu, cs;
      { LAS const char* p = reg + w * 8192 + (4 * h + q4) * 32 + p4 * 8;
        float t0 = 0.f, t1 = 0.f;
#pragma unroll
        for (int i = 0; i < 16; ++i) {
          fu[i] = cat8(trread(p + i * 512), trread(p + i * 512 + 256));
          const u32x4 f = __builtin_bit_cast(u32x4, fu[i]);
          t0 = dot2(f[0], 0x3f803f80u, t0); t1 = dot2(f[1], 0x3f803f80u, t1); t0 = dot2(f[2], 0x3f803f80u, t0); t1 = dot2(f[3], 0x3f803f80u, t1);
        }
        cu = opq_v(cs_frag(swap_add(t0 + t1))); cs = opq_v(cs_frag(csb)); }
      poll<32>(fact + 2 * 8192 - w * 128, (unsigned)((lane & 15) * 8 + (blk ? cb >> 2 : w) * 128), tag);
      STAMP(11);
      const char* xb = act + 2 * STG; const char* stp = stt + 256 * 1024;
      bf16x8 x[64], xu[16], su[4], sp[4];
      sfor<0, 16>([&](auto i) { ldx1<true, (i % 8) * 1024 - 4096>(xu[i], c.xv, xb + (ptrdiff_t)du * 65536 + w * 16384 + (i / 8) * 8192 + 4096); });
      sfor<0, 4>([&](auto i) { ldx1<false, i * 1024>(su[i], c.xv, stp + du * 4096); });
      f32x16 au, av;
      sfor<0, 16>([&](auto i) {
        waitx<true, 15 - i + 4 + (i < XL ? i : XL)>(xu[i]);
        mm<false, true, i == 0>(au, fu[i], xu[i]);
        if constexpr (i < XL) ld_x<i>(x, c, xb);
        mm<true, true, i == 0>(av, fu[i], xu[i]);
      });
      lds_bar();
      STAMP(12);
      f32x16 a0, a1;
      u64 fa;
      float mu, ru;
      gemm64<true, true, HookP4>(c, x, xb, a0, a1, [&](auto s) {
        if constexpr (s == 0) { pin4(su); ln_stats(su, lane, mu, ru); }
        if constexpr (s == 1) { const bf16x8 mf = mean_frag(mu, h == 0); mm<false, false, false, true>(au, cu, mf); mm<true, false, false>(av, cu, mf); }
        if constexpr (s == 4) {
          asm volatile("" : "+v"(au), "+v"(av));
          if (ty >= 2) au = av;
#pragma unroll
          for (int gq = 0; gq < 4; ++gq) *(LAS f32x4*)(reg + w * 8192 + gq * 1024 + lane * 16) = f32x4{au[4 * gq], au[4 * gq + 1], au[4 * gq + 2], au[4 * gq + 3]};
        }
        if constexpr (s == 6) {
          lds_bar();
          f32x4 t = {};
#pragma unroll
          for (int j = 0; j < 4; ++j) t += *(LAS const f32x4*)(reg + j * 8192 + (ty < 2 ? w & 1 : w) * 1024 + lane * 16);
#pragma unroll
          for (int e = 0; e < 4; ++e) t[e] *= lane_get(ru, ty < 2 ? r : 8 * w + 4 * h + e);
          const unsigned vo = ty < 2 ? (unsigned)(ty * 1024 + lane * 16 + (w & 1) * 8)
                                     : (unsigned)(2048 + (w >> 1) * 1024 + ((ty & 1) * 16 + (r & 15) + 32 * (w & 1)) * 16 + h * 8);
          st2_1(__builtin_bit_cast(f32x2, u32x2{cvtpk(t[0], t[1]), cvtpk(t[2], t[3])}), vo, kvf + (tt + du) * 4096);
        }
        if constexpr (s >= 8 && s < 12) ldx1<false, (s - 8) * 1024>(sp[s - 8], c.xv, stp);
        if constexpr (s == 7 + XL) { bar(); if (w == 0) set_flag(fkv + ((tt + du) * 4 + ty) * 8, tag); }
        if constexpr (s == 12 + XL) { pin4(sp); ln_stats(sp, lane, mean, rstd); }
        if constexpr (s == 14 + XL) { mm<false, false, false, true>(a0, cs, mean_frag(mean, h == 0)); mm<false, false, false, true>(a1, cs, mean_frag(mean, h == 1)); }
        if constexpr (s == 52) ld8_1(fa, lane * 8, fkv + w * 512);
      });
      waitx<false, 0>(fa);
      for (int spin = 0; spin < (1 << 15) && !__all(fa == tag); ++spin) { __builtin_amdgcn_s_sleep(8); ld8_1(fa, lane * 8, fkv + w * 512); waitx<false, 0>(fa); }
      bar();
      ldx1<false, 0>(k0, c.xv, kvf); ldx1<false, 1024>(k1, c.xv, kvf);
      sfor<0, 6>([&](auto i) { dma_c<(i % 2) * 1024>(lds0 + 8 * SLOT + w * 2048 + (i / 2) * 8192, c.xv, kvf + 2048 + w * 2048 + (i / 2) * 8192); });
      STAMP(13);
      const float sc = rstd * QSCALE;
#pragma unroll
      for (int i = 0; i < 8; ++i) { qq[i] = cvtpk(a0[2 * i] * sc, a0[2 * i + 1] * sc); qq[8 + i] = cvtpk(a1[2 * i] * sc, a1[2 * i + 1] * sc); }
      asm volatile("s_waitcnt vmcnt(6)" : "+v"(k0), "+v"(k1), "+v"(qq));
    }
    STAMP(14);
    REFRESH();
    {
      const bf16x8 qa0 = __builtin_bit_cast(bf16x8, __builtin_shufflevector(qq, qq, 0, 1, 2, 3)), qa1 = __builtin_bit_cast(bf16x8, __builtin_shufflevector(qq, qq, 4, 5, 6, 7));
      const bf16x8 qb0 = __builtin_bit_cast(bf16x8, __builtin_shufflevector(qq, qq, 8, 9, 10, 11)), qb1 = __builtin_bit_cast(bf16x8, __builtin_shufflevector(qq, qq, 12, 13, 14, 15));
      float mA = 0.f, mB = 0.f;
      const char* ksrc = kvf + 2048 + w * 2048;
      const unsigned ring = lds0 + 8 * SLOT + w * 2048;
      for (int attempt = 0; attempt < 2; ++attempt) {
        f32x16 sa0, sb;
        if (attempt == 1) {
          mA = -INFINITY; mB = -INFINITY;
          for (int t = 0; t < 64; ++t) {
            ldx1<false, 0>(k0, c.xv, kvf + t * 4096); ldx1<false, 1024>(k1, c.xv, kvf + t * 4096);
            asm volatile("s_waitcnt vmcnt(0)" : "+v"(k0), "+v"(k1));
            mm<false, false, true>(sa0, k0, qa0); mm<false, false, false>(sa0, k1, qa1); mm<false, false, true>(sb, k0, qb0); mm<false, false, false>(sb, k1, qb1);
            mdone(sa0, sb);
            mA = fmaxf(mA, max16(sa0)); mB = fmaxf(mB, max16(sb));
          }
          mA = swap_max(mA); mB = swap_max(mB);
          ldx1<false, 0>(k0, c.xv, kvf); ldx1<false, 1024>(k1, c.xv, kvf);
          sfor<0, 6>([&](auto i) { dma_c<(i % 2) * 1024>(ring + (i / 2) * 8192, c.xv, ksrc + (i / 2) * 8192); });
          asm volatile("s_waitcnt vmcnt(6)" : "+v"(k0), "+v"(k1));
        }
        mm<false, false, true, true>(sa0, k0, qa0); mm<false, false, false>(sa0, k1, qa1); mm<false, false, true>(sb, k0, qb0); mm<false, false, false>(sb, k1, qb1);
        mdone(sa0, sb);
        if (attempt == 0) { mA = swap_max(max16(sa0)); mB = swap_max(max16(sb)); }
        f32x16 nma, nmb, oa = {}, ob = {}, sb1 = {}, t0, t1;
#pragma unroll
        for (int e = 0; e < 16; ++e) { nma[e] = -mA; nmb[e] = -mB; sa0[e] -= mA; }
        const unsigned o2 = opq_v(0x3f803f80u), s0 = ((lane >> 2) & 1) ? 0u : o2, s1 = o2 - s0;
        u32x16 kvr = {}, misc;
        { const u32x4 a = __builtin_bit_cast(u32x4, k0), b = __builtin_bit_cast(u32x4, k1);
#pragma unroll
          for (int e = 0; e < 4; ++e) { kvr[e] = a[e]; kvr[4 + e] = b[e]; } }
        misc = u32x16{s0, s0, s1, s1, 0u, 0u, 0u, 0u, 0u, 0u, 0u, 0u, 0xddddddddu, lds0 + 8 * SLOT + c.xv, 0u, 0u};
        const u32x4 offs = {c.xv, c.xv + 8192, c.xv + 16384, c.xv + 24576};
        const char* ks = ksrc + 3 * 8192; const char* wsr = wt(5, 0);
        unsigned wm0 = lds0 + w * 4096, cnt = 8;
        asm volatile(
            "s_waitcnt vmcnt(4)\n\ts_barrier\n\t"
            "1:\n\t"
            STEP_(0, 24576, 240, 0, "s_sub_u32 %[cnt], %[cnt], 1\n\t", "") STEP_(8192, 0, 241, 1024, "", "") STEP_(16384, 8192, 242, 2048, "", "")
            STEP_(24576, 16384, 243, 3072, "s_add_u32 s40, s40, 0x8000\n\ts_addc_u32 s41, s41, 0\n\t", "s_add_u32 s42, s42, 0x4000\n\ts_addc_u32 s43, s43, 0\n\t")
            "s_add_u32 s45, s45, 0x4000\n\ts_cmp_lg_u32 %[cnt], 0\n\ts_cbranch_scc1 1b\n\t"
            PV0_(80, 176) PV1_(80, 176) L0_(232, 176) L1_(232, 176)
            "s_nop 15\n\t"
            : "+{v[64:79]}"(oa), "+{v[80:95]}"(ob), "+{v[128:143]}"(sa0), "=&{v[144:159]}"(t0), "=&{v[160:175]}"(t1), "+{v[176:191]}"(sb1),
              "+{v[208:223]}"(kvr), "+{v[224:239]}"(misc), "+{s[40:41]}"(ks), "+{s[42:43]}"(wsr), "+{s45}"(wm0), [cnt] "+s"(cnt)
            : "{v[96:111]}"(nma), "{v[112:127]}"(nmb), "{v[192:207]}"(qq), "{v[240:243]}"(offs), "{s44}"(ring)
            : "scc", "memory");
        const float la = __uint_as_float(misc[4]), lb = __uint_as_float(misc[8]);
        const float ia = __builtin_amdgcn_rcpf(la), ib = __builtin_amdgcn_rcpf(lb);
#pragma unroll
        for (int e = 0; e < 16; ++e) { oa[e] *= ia; ob[e] *= ib; }
        char* dst = act + 3 * STG + cb * 4096;
        st4_1<0>(__builtin_bit_cast(u32x4, pack8<0>(oa)), c.xv, dst); st4_1<1024>(__builtin_bit_cast(u32x4, pack8<1>(oa)), c.xv, dst);
        st4_1<2048>(__builtin_bit_cast(u32x4, pack8<0>(ob)), c.xv, dst); st4_1<3072>(__builtin_bit_cast(u32x4, pack8<1>(ob)), c.xv, dst);
        float chk = 0.f;
#pragma unroll
        for (int e = 0; e < 16; ++e) chk = __builtin_fmaf(oa[e], 0.f, __builtin_fmaf(ob[e], 0.f, chk));
        const bool bad = !(la < 1.2676506e30f) || !(lb < 1.2676506e30f) || !(chk == 0.f);
        wait_bar<4>();
        LAS volatile int* vote = (LAS volatile int*)(smem + 8 * SLOT);
        if (lane == 0) vote[w] = 0;
        if (attempt == 0 && bad) vote[w] = 1;
        lds_bar();
        const int redo = vote[0] | vote[1] | vote[2] | vote[3];
        if (!__builtin_amdgcn_readfirstlane(redo)) break;
        lds_bar();
      }
      waitv<0>();
      set_flag(fact + 3 * 8192 + cb * 8, tag);
    }
    STAMP(15);
    REFRESH();
    {
      f32x16 a0, a1; res_to_acc(xr, a0, a1);
      poll<16>(fact + 3 * 8192, lane * 8, tag);
      STAMP(16);
      bf16x8 x[64];
      const char* xb = act + 3 * STG;
      sfor<0, XL>([&](auto s) { ld_x<s>(x, c, xb); });
      bar();
      gemm64<false, false, HookP6>(c, x, xb, a0, a1, [&](auto s) {
        if constexpr (s == 33) bar();
        if constexpr (s >= 52) pc_kv(IC((s - 52) / 8), IC((s - 52) % 8));
      });
      STAMP(17);
      if (l == 0) {
        store_x(c, r, a0, a1, xr, act + 4 * STG + cb * 4096, stt + 512 * 1024 + cb * 256);
        bar();
        sfor<12, 24>([&](auto i) { pc_kv(IC(i / 8), IC(i % 8)); });
        waitv<12>();
        set_flag(fact + 4 * 8192 + cb * 8, tag);
        sfor<24, 32>([&](auto i) { pc_kv(IC(i / 8), IC(i % 8)); });
      } else {
        char* dst = out + (size_t)(tt * 32 + r) * 2048 + cb * 128 + h * 16;
        *(u32x4*)(dst) = unperm(__builtin_bit_cast(u32x4, pack8<0>(a0))); *(u32x4*)(dst + 32) = unperm(__builtin_bit_cast(u32x4, pack8<1>(a0)));
        *(u32x4*)(dst + 64) = unperm(__builtin_bit_cast(u32x4, pack8<0>(a1))); *(u32x4*)(dst + 96) = unperm(__builtin_bit_cast(u32x4, pack8<1>(a1)));
        if (tt == 0 && cb == 0) set_flag(ws + WS_EPOCH, tag);
      }
    }
    STAMP(18);
  }
}

extern "C" void kernel_launch(void* const* d_in, const int* in_sizes, int n_in, void* d_out, int out_size, void* d_ws, size_t ws_size, hipStream_t stream) {
  k_main<<<256, 256, 0, stream>>>((const char*)d_in[0], (const char*)d_in[1], (const char*)d_in[2], (const char*)d_in[3], (const char*)d_in[5], (const char*)d_in[6],
                                  (const char*)d_in[7], (const char*)d_in[8], (char*)d_ws, (char*)d_out);
}
```

```cpp
#include <hip/hip_runtime.h>
#include <hip/hip_bf16.h>
#include <stdint.h>
#include <type_traits>

typedef __attribute__((ext_vector_type(8))) short bf16x8;
typedef __attribute__((ext_vector_type(4))) short s16x4;
typedef __attribute__((ext_vector_type(2))) __bf16 bf2_t;
typedef __attribute__((ext_vector_type(16))) float f32x16;
typedef __attribute__((ext_vector_type(4))) float f32x4;
typedef __attribute__((ext_vector_type(2))) float f32x2;
typedef __attribute__((ext_vector_type(4))) unsigned u32x4;
typedef __attribute__((ext_vector_type(2))) unsigned u32x2;
#define LAS __attribute__((address_space(3)))
#define DI __device__ __forceinline__
#define MFMA32(a, b, c) __builtin_amdgcn_mfma_f32_32x32x16_bf16((a), (b), (c), 0, 0, 0)
#define MFMA16(a, b, c) __builtin_amdgcn_mfma_f32_16x16x32_bf16((a), (b), (c), 0, 0, 0)
#define SBAR() __builtin_amdgcn_sched_barrier(0)

constexpr int S_ = 2048, D_ = 1024;
constexpr float LOG2E = 1.4426950408889634f;
constexpr float QSCALE = 0.17677669529663687f * LOG2E;

constexpr size_t MB = 1u << 20, KB = 1024;
constexpr size_t STG = 256 * KB;
constexpr size_t WS_COL = 32 * MB;
constexpr size_t C_WT = 0;
constexpr size_t C_KVP = 768 * KB;
constexpr size_t C_ZP = 1024 * KB;
constexpr size_t C_CS = 1040 * KB;
constexpr size_t WS_ST = 64 * MB;
constexpr size_t WS_KVF = WS_ST + 1024 * KB;
constexpr size_t WS_FLAG = WS_ST + 1664 * KB;
constexpr size_t WS_EPOCH = WS_ST + 1920 * KB;
constexpr int FL_ACT = 0, FL_KVP = 8 * 64 * 128, FL_KV = FL_KVP + 2 * 64 * 128, FL_W = FL_KV + 2 * 2048;
typedef unsigned long long u64;

template <int I, int N, class F> DI void sfor(F&& f) { if constexpr (I < N) { f(std::integral_constant<int, I>{}); sfor<I + 1, N>(f); } }

DI unsigned cvtpk(float lo, float hi) { f32x2 v = {lo, hi}; return __builtin_bit_cast(unsigned, __builtin_convertvector(v, bf2_t)); }
DI float bflo(unsigned u) { return __uint_as_float(u << 16); }
DI float bfhi(unsigned u) { return __uint_as_float(u & 0xffff0000u); }
DI float rbf(float x) { return bflo(cvtpk(x, x)); }
DI float swap_add(float v) { auto r = __builtin_amdgcn_permlane32_swap(__float_as_uint(v), __float_as_uint(v), false, false); return __uint_as_float(r[0]) + __uint_as_float(r[1]); }
DI float swap_max(float v) { auto r = __builtin_amdgcn_permlane32_swap(__float_as_uint(v), __float_as_uint(v), false, false); return fmaxf(__uint_as_float(r[0]), __uint_as_float(r[1])); }
DI float swap_min(float v) { auto r = __builtin_amdgcn_permlane32_swap(__float_as_uint(v), __float_as_uint(v), false, false); return fminf(__uint_as_float(r[0]), __uint_as_float(r[1])); }
template <int S> DI bf16x8 pack8(const f32x16& v) {
  u32x4 p = {cvtpk(v[8 * S], v[8 * S + 1]), cvtpk(v[8 * S + 2], v[8 * S + 3]), cvtpk(v[8 * S + 4], v[8 * S + 5]), cvtpk(v[8 * S + 6], v[8 * S + 7])};
  return __builtin_bit_cast(bf16x8, p);
}
DI u32x4 unperm(u32x4 v) {
  auto r0 = __builtin_amdgcn_permlane32_swap(v[0], v[2], false, false);
  auto r1 = __builtin_amdgcn_permlane32_swap(v[1], v[3], false, false);
  return u32x4{r0[0], r1[0], r0[1], r1[1]};
}
DI float lane_get(float v, int src) { return __uint_as_float(__builtin_amdgcn_ds_bpermute(src << 2, __float_as_uint(v))); }
DI s16x4 trread(LAS const char* p) { return __builtin_amdgcn_ds_read_tr16_b64_v4i16((LAS s16x4*)p); }
DI bf16x8 cat8(s16x4 a, s16x4 b) { return __builtin_shufflevector(a, b, 0, 1, 2, 3, 4, 5, 6, 7); }
DI float dot2(unsigned a, unsigned b, float c) { return __builtin_amdgcn_fdot2_f32_bf16(__builtin_bit_cast(bf2_t, a), __builtin_bit_cast(bf2_t, b), c, false); }

template <int OFF> DI void dma(unsigned lds, unsigned v, const char* sb) {
  asm volatile("s_mov_b32 m0, %0\n\ts_nop 0\n\tglobal_load_lds_dwordx4 %1, %2 offset:%3" ::"s"(lds), "v"(v), "s"(sb), "i"(OFF) : "memory");
}
template <int OFF> DI void dma_c(unsigned lds, unsigned v, const char* sb) {
  asm volatile("s_mov_b32 m0, %0\n\ts_nop 0\n\tglobal_load_lds_dwordx4 %1, %2 offset:%3 sc1" ::"s"(lds), "v"(v), "s"(sb), "i"(OFF) : "memory");
}
constexpr int cap63(int n) { return n > 63 ? 63 : n; }
template <int N> DI void wait_bar() { asm volatile("s_waitcnt vmcnt(%0)\n\ts_barrier" ::"i"(cap63(N)) : "memory"); }
DI void lds_bar() { asm volatile("s_waitcnt lgkmcnt(0)\n\ts_barrier" ::: "memory"); }
DI void all_bar() { asm volatile("s_waitcnt vmcnt(0) lgkmcnt(0)\n\ts_barrier" ::: "memory"); }
DI void bar() { asm volatile("s_barrier" ::: "memory"); }
template <bool AG, int OFF> DI void ldx1(bf16x8& d, unsigned voff, const char* sb) {
  if constexpr (AG) asm volatile("global_load_dwordx4 %0, %1, %2 offset:%3 sc1" : "=a"(d) : "v"(voff), "s"(sb), "i"(OFF));
  else asm volatile("global_load_dwordx4 %0, %1, %2 offset:%3 sc1" : "=v"(d) : "v"(voff), "s"(sb), "i"(OFF));
}
template <int OFF> DI void ld2_1(f32x2& d, unsigned voff, const char* sb) { asm volatile("global_load_dwordx2 %0, %1, %2 offset:%3 sc1" : "=v"(d) : "v"(voff), "s"(sb), "i"(OFF)); }
DI void ld1_1(unsigned& d, unsigned voff, const void* sb) { asm volatile("global_load_dword %0, %1, %2 sc1" : "=v"(d) : "v"(voff), "s"(sb) : "memory"); }
template <int OFF> DI void st4_1(u32x4 d, unsigned voff, char* sb) { asm volatile("global_store_dwordx4 %0, %1, %2 offset:%3 sc1\n\ts_nop 1" ::"v"(voff), "v"(d), "s"(sb), "i"(OFF) : "memory"); }
DI void st2_1(f32x2 d, unsigned voff, char* sb) { asm volatile("global_store_dwordx2 %0, %1, %2 sc1" ::"v"(voff), "v"(d), "s"(sb) : "memory"); }
DI void st1_1(float d, unsigned voff, char* sb) { asm volatile("global_store_dword %0, %1, %2 sc1" ::"v"(voff), "v"(d), "s"(sb) : "memory"); }
template <int N> DI void waitv() { asm volatile("s_waitcnt vmcnt(%0)" ::"i"(cap63(N)) : "memory"); }
template <bool AG, int N, class T> DI void waitx(T& d) {
  if constexpr (AG) asm volatile("s_waitcnt vmcnt(%1)" : "+a"(d) : "i"(cap63(N))); else asm volatile("s_waitcnt vmcnt(%1)" : "+v"(d) : "i"(cap63(N)));
}
template <bool SW, bool BA, bool Z, bool NOP = false> DI void mm(f32x16& acc, const bf16x8& a, const bf16x8& b) {
#define MM_(OPS, C, OUT, BC) do { if constexpr (NOP) asm volatile("s_nop 1\n\tv_mfma_f32_32x32x16_bf16 %0, " OPS ", " C : OUT(acc) : "v"(a), BC(b)); \
                                  else asm volatile("v_mfma_f32_32x32x16_bf16 %0, " OPS ", " C : OUT(acc) : "v"(a), BC(b)); } while (0)
  if constexpr (Z) {
    if constexpr (SW) { if constexpr (BA) MM_("%2, %1", "0", "=&v", "a"); else MM_("%2, %1", "0", "=&v", "v"); }
    else { if constexpr (BA) MM_("%1, %2", "0", "=&v", "a"); else MM_("%1, %2", "0", "=&v", "v"); }
  } else {
    if constexpr (SW) { if constexpr (BA) MM_("%2, %1", "%0", "+v", "a"); else MM_("%2, %1", "%0", "+v", "v"); }
    else { if constexpr (BA) MM_("%1, %2", "%0", "+v", "a"); else MM_("%1, %2", "%0", "+v", "v"); }
  }
#undef MM_
}
DI void mdone(f32x16& a) { asm volatile("s_nop 11" : "+v"(a)); }
DI void mdone(f32x16& a, f32x16& b) { asm volatile("s_nop 11" : "+v"(a), "+v"(b)); }
DI void mdone(f32x16& a, f32x16& b, f32x16& c, f32x16& d) { asm volatile("s_nop 11" : "+v"(a), "+v"(b), "+v"(c), "+v"(d)); }
template <class T> DI T opq_v(T v) { asm volatile("" : "+v"(v)); return v; }
template <int OFF> DI void ldx0(bf16x8& d, unsigned voff, const char* sb) { asm volatile("global_load_dwordx4 %0, %1, %2 offset:%3" : "=v"(d) : "v"(voff), "s"(sb), "i"(OFF)); }
template <int OFF> DI void ds_write_a(unsigned addr, const bf16x8& v) { asm volatile("ds_write_b128 %0, %1 offset:%2" ::"v"(addr), "a"(v), "i"(OFF) : "memory"); }
template <int N, class T> DI void wait2(T& a, T& b) { asm volatile("s_waitcnt vmcnt(%2)" : "+v"(a), "+v"(b) : "i"(cap63(N))); }
DI void set_flag(char* flag, u64 tag) {
  unsigned z = 0;
  asm volatile("s_mov_b64 exec, 1\n\tglobal_atomic_swap_x2 %0, %1, %2 sc1\n\ts_mov_b64 exec, -1" ::"v"(z), "v"(tag), "s"(flag) : "memory");
}
DI void ld8_1(u64& d, unsigned voff, const char* sb) { asm volatile("global_load_dwordx2 %0, %1, %2 sc1" : "=v"(d) : "v"(voff), "s"(sb) : "memory"); }
template <int NL> DI void rd_flag(u64& v, unsigned voff, const char* sb) {
  const u64 zero = 0;
  if constexpr (NL == 64) asm volatile("global_atomic_or_x2 %0, %1, %3, %2 sc0 sc1" : "+v"(v) : "v"(voff), "s"(sb), "v"(zero) : "memory");
  else asm volatile("s_mov_b64 exec, %4\n\tglobal_atomic_or_x2 %0, %1, %3, %2 sc0 sc1\n\ts_mov_b64 exec, -1" : "+v"(v) : "v"(voff), "s"(sb), "v"(zero), "s"((1ull << (NL & 63)) - 1) : "memory");
}
template <int NL> DI void poll(const char* base, unsigned voff, u64 tag) {
  for (int spin = 0; spin < (1 << 15); ++spin) {
    u64 v = tag;
    rd_flag<NL>(v, voff, base);
    waitx<false, 0>(v);
    if (__all(v == tag)) break;
  }
  asm volatile("" ::: "memory");
}

template <int RBX> DI void block_map(int& rb, int& cb) {
  const int x = blockIdx.x & 7, i = blockIdx.x >> 3;
  constexpr int CBX = 32 / RBX, GC = 16 / CBX;
  rb = RBX * (x / GC) + i / CBX; cb = CBX * (x % GC) + i % CBX;
}

DI float a_fma(float a, float b, float c) { float d; asm volatile("v_fma_f32 %0, %1, %2, %3" : "=v"(d) : "v"(a), "v"(b), "v"(c)); return d; }
DI float a_fms(float a, float b, float c) { float d; asm volatile("v_fma_f32 %0, %1, -%2, %3" : "=v"(d) : "v"(a), "v"(b), "v"(c)); return d; }
DI void a_exp(float& x) { asm volatile("v_exp_f32 %0, %0" : "+v"(x)); }
DI void a_add(float& s, float x) { asm volatile("v_add_f32 %0, %0, %1" : "+v"(s) : "v"(x)); }
DI void a_mul(float& x, float s) { asm volatile("v_mul_f32 %0, %0, %1" : "+v"(x) : "v"(s)); }
DI float a_max3(float a, float b, float c) { float d; asm volatile("v_max3_f32 %0, %1, %2, %3" : "=v"(d) : "v"(a), "v"(b), "v"(c)); return d; }
DI float a_min3(float a, float b, float c) { float d; asm volatile("v_min3_f32 %0, %1, %2, %3" : "=v"(d) : "v"(a), "v"(b), "v"(c)); return d; }

DI void pin2(f32x16& a, f32x16& b) { asm volatile("" : "+v"(a), "+v"(b)); }
DI bf16x8 cs_frag(float c) {
  const float hi = rbf(c), mid = rbf(c - hi), lo = (c - hi) - mid;
  return __builtin_bit_cast(bf16x8, u32x4{cvtpk(hi, mid), cvtpk(lo, 0.f), 0u, 0u});
}
template <bool CS> struct Red {
  bf16x8 p[4];
  float t[4], a, b, sd;
  template <int K> DI void run(int lane) {
    if constexpr (K == 0) asm volatile("" : "+v"(p[0]), "+v"(p[1]), "+v"(p[2]), "+v"(p[3]));
    if constexpr (K < 3) {
      const f32x4 u = __builtin_bit_cast(f32x4, p[K + 1]), v = __builtin_bit_cast(f32x4, p[0]);
#pragma unroll
      for (int e = 0; e < 4; ++e) { if (K == 0) t[e] = v[e]; a_add(t[e], u[e]); }
    }
    if constexpr (K == 3) {
      auto x = __builtin_amdgcn_permlane16_swap(__float_as_uint(t[0]), __float_as_uint(t[1]), false, false);
      auto y = __builtin_amdgcn_permlane16_swap(__float_as_uint(t[2]), __float_as_uint(t[3]), false, false);
      a = opq_v(__uint_as_float(x[0]) + __uint_as_float(x[1])); b = opq_v(__uint_as_float(y[0]) + __uint_as_float(y[1]));
    }
    if constexpr (K == 4) { auto x = __builtin_amdgcn_permlane32_swap(__float_as_uint(a), __float_as_uint(b), false, false); a = opq_v(__uint_as_float(x[0]) + __uint_as_float(x[1])); }
    if constexpr (K == 5) {
      if constexpr (CS) a = lane_get(a, (lane >> 2) + ((lane & 3) << 4));
      else { const int src = ((lane & 31) >> 1) + ((lane & 1) << 5); b = lane_get(a, src + 16); a = lane_get(a, src); }
    }
    if constexpr (CS) {
      if constexpr (K == 8) { t[0] = opq_v(rbf(a)); t[1] = opq_v(a - t[0]); }
      if constexpr (K == 9) { const float mid = rbf(t[1]); p[0] = opq_v(__builtin_bit_cast(bf16x8, u32x4{cvtpk(t[0], mid), cvtpk(t[1] - mid, 0.f), 0u, 0u})); }
    } else {
      if constexpr (K == 8) { const float m = a * (1.f / D_); b = opq_v(fmaxf(b * (1.f / D_) - m * m, 0.f)); a = opq_v(rbf(m)); }
      if constexpr (K == 9) b = opq_v(rbf(rbf(b) + 1e-5f));
      if constexpr (K == 10) { sd = opq_v(rbf(__builtin_amdgcn_sqrtf(b))); b = opq_v(__builtin_amdgcn_rcpf(sd)); }
    }
  }
};
DI void pin4(bf16x8 (&p)[4]) { asm volatile("" : "+v"(p[0]), "+v"(p[1]), "+v"(p[2]), "+v"(p[3])); }
DI bf16x8 mean_frag(float mean, bool on) { const float m = on ? -mean : 0.f; return __builtin_bit_cast(bf16x8, u32x4{cvtpk(m, m), cvtpk(m, 0.f), 0u, 0u}); }

#define STAMP(i)

constexpr int SLOT = 16384, XL = 12;
struct WinPtr {
  unsigned b[3];
  DI void init(unsigned a) { sfor<0, 3>([&](auto i) { b[i] = a + i * 65536u; asm volatile("" : "+v"(b[i])); }); }
  template <int OFF> DI bf16x8 frag() const { return *(LAS const bf16x8*)((LAS const char*)(uintptr_t)b[OFF >> 16] + (OFF & 65535)); }
};
struct Ctx { unsigned xv; WinPtr p; };
#define IC(n) std::integral_constant<int, (n)>{}

template <int S> DI void ld_x(bf16x8 (&x)[64], const Ctx& c, const char* xb) { ldx1<false, (S % 8) * 1024 - 4096>(x[S], c.xv, xb + (S / 8) * 8192 + 4096); }
template <class H> constexpr int g64_wait(int s) {
  int n = 0;
  const int t0 = s - XL;
  if (t0 < 0) n += XL - 1 - s; else n += H::n(t0);
  for (int t = t0 < 0 ? 0 : t0 + 1; t < s; ++t) n += (t + XL < 64 ? 1 : 0) + H::n(t);
  return n;
}
constexpr int a_slot(int g);
template <bool MAPA, bool ZERO, class H, class FX, class FA>
DI void gemm64(const Ctx& c, bf16x8 (&x)[64], const char* xb, f32x16& acc0, f32x16& acc1, FX&& hook, FA&& aux) {
  bf16x8 f0[3], f1[3];
  auto off = [](int s) constexpr { return (MAPA ? a_slot(24 + s / 8) : s / 8) * SLOT + (s % 8) * 2048; };
  auto rd0 = [&](auto s) { f0[s % 3] = c.p.template frag<off(s)>(); };
  auto rd1 = [&](auto s) { f1[s % 3] = c.p.template frag<off(s) + 1024>(); };
  rd0(IC(0)); rd1(IC(0)); rd0(IC(1)); rd1(IC(1));
  sfor<0, 64>([&](auto s) {
    waitx<false, g64_wait<H>(s)>(x[s]);
    SBAR();
    mm<false, false, ZERO && s == 0>(acc0, f0[s % 3], x[s]);
    SBAR();
    if constexpr (s + 2 < 64) rd0(IC(s + 2));
    if constexpr (s + XL < 64) ld_x<s + XL>(x, c, xb);
    aux(IC(2 * s));
    SBAR();
    mm<false, false, ZERO && s == 0>(acc1, f1[s % 3], x[s]);
    SBAR();
    if constexpr (s + 2 < 64) rd1(IC(s + 2));
    hook(s);
    aux(IC(2 * s + 1));
    SBAR();
  });
  mdone(acc0, acc1);
}
struct HookP6 { static constexpr int n(int s) { return s >= 52 ? 1 : 0; } };
struct HookP3 { static constexpr int n(int s) { return s >= 52 ? 1 : 0; } };
struct HookP4 { static constexpr int n(int s) { return s == 52 || s == 3 || (s >= 8 && s < 12) ? 1 : 0; } };

DI void res_to_acc(const u32x4 (&xr)[4], f32x16& a0, f32x16& a1) {
#pragma unroll
  for (int e = 0; e < 4; ++e) {
    a0[2 * e] = bflo(xr[0][e]); a0[2 * e + 1] = bfhi(xr[0][e]); a0[8 + 2 * e] = bflo(xr[1][e]); a0[9 + 2 * e] = bfhi(xr[1][e]);
    a1[2 * e] = bflo(xr[2][e]); a1[2 * e + 1] = bfhi(xr[2][e]); a1[8 + 2 * e] = bflo(xr[3][e]); a1[9 + 2 * e] = bfhi(xr[3][e]);
  }
  asm volatile("" : "+v"(a0), "+v"(a1));
}
DI void store_stats(int r, const u32x4 (&xr)[4], char* stp) {
  float s1 = 0.f, s2 = 0.f, s3 = 0.f, s4 = 0.f;
#pragma unroll
  for (int i = 0; i < 4; ++i)
#pragma unroll
    for (int e = 0; e < 4; e += 2) {
      s1 = dot2(xr[i][e], 0x3f803f80u, s1); s2 = dot2(xr[i][e], xr[i][e], s2);
      s3 = dot2(xr[i][e + 1], 0x3f803f80u, s3); s4 = dot2(xr[i][e + 1], xr[i][e + 1], s4);
    }
  st2_1(f32x2{swap_add(s1 + s3), swap_add(s2 + s4)}, (unsigned)(r * 8), stp);
}
DI void store_x(const Ctx& c, int r, const f32x16& a0, const f32x16& a1, u32x4 (&xr)[4], char* dst, char* stp) {
  xr[0] = __builtin_bit_cast(u32x4, pack8<0>(a0)); st4_1<0>(xr[0], c.xv, dst);
  xr[1] = __builtin_bit_cast(u32x4, pack8<1>(a0)); st4_1<1024>(xr[1], c.xv, dst);
  xr[2] = __builtin_bit_cast(u32x4, pack8<0>(a1)); st4_1<2048>(xr[2], c.xv, dst);
  xr[3] = __builtin_bit_cast(u32x4, pack8<1>(a1)); st4_1<3072>(xr[3], c.xv, dst);
  store_stats(r, xr, stp);
}

template <class FX> DI void hedge_exp(const f32x16& p, float sc, f32x16& ep, f32x16& en, FX&& hook) {
  float mx = fmaxf(p[0], p[1]), mn = fminf(p[0], p[1]);
#pragma unroll
  for (int i = 2; i < 16; i += 2) { mx = fmaxf(fmaxf(mx, p[i]), p[i + 1]); mn = fminf(fminf(mn, p[i]), p[i + 1]); }
  mx = swap_max(mx) * sc; mn = swap_min(mn) * sc;
  sfor<0, 16>([&](auto i) {
    SBAR();
    hook(i);
    constexpr int e = i;
    float a = __builtin_amdgcn_exp2f(__builtin_fmaf(p[e], sc, -mx)), b = __builtin_amdgcn_exp2f(__builtin_fmaf(p[e], -sc, mn));
    asm volatile("" : "+v"(a), "+v"(b));
    ep[e] = a; en[e] = b;
  });
  SBAR();
}

constexpr int a_slot(int g) { constexpr int m[32] = {0, 1, 2, 3, 4, 0, 1, 2, 5, 6, 7, 5, 3, 4, 6, 7, 8, 5, 3, 4, 6, 7, 0, 1, 2, 5, 3, 4, 6, 7, 0, 1}; return m[g]; }
constexpr int a_need(int g) { return g < 8 ? 8 * g - 2 : (g < 16 ? 16 * (g - 8) - 2 : 128 + 8 * (g - 16) - 2); }
constexpr bool a_bar(int s) { return (s + 2) % 8 == 0 && s >= 14 && s < 190 && (s < 64 || s >= 126 || (s + 2) % 16 == 0); }
constexpr int a_pre(int n) { return n < 20 ? n : n + 12; }
constexpr int QP = 128 + 24, QC = 128 + 36;
constexpr int a_piece(int s) {
  constexpr int t[][2] = {{14, 5}, {22, 6}, {26, 11}, {30, 7}, {52, 12}, {56, 13}, {60, 14}, {64, 15}, {78, 16}, {82, 17}, {94, 18}, {110, 19},
                          {128, 20}, {132, 21}, {136, 22}, {140, 23}, {144, 24}, {150, 25}, {158, 26}};
  for (auto& e : t) if (s >= e[0] && s < e[0] + 4) return e[1] * 4 + s - e[0];
  return -1;
}
constexpr int a_first(int s) { constexpr int g[5] = {2, 9, 3, 4, 10}; return s < 20 ? g[s / 4] * 4 + s % 4 : -1; }
constexpr int a_park(int s) { return s == 148 || s == 149 ? s - 148 : (s >= 154 && s < 158 ? s - 152 : (s == 162 || s == 163 ? s - 156 : -1)); }
constexpr bool a_side(int s) { return (s >= 2 && s < 14) || s == 40 || (s >= 86 && s < 94) || a_park(s) >= 0; }
struct SchedA {
  int xw[64], bw[192], fw, cw, pw, zw, lw[32];
  constexpr SchedA(bool first) : xw{}, bw{}, fw(0), cw(0), pw(0), zw(0), lw{} {
    int pos = 0, xpos[64] = {}, wpos[32] = {}, fpos = 0, cpos = 0, stpos = 0, ppos = 0, lpos[34] = {};
    for (int s = 0; s < XL; ++s) xpos[s] = ++pos;
    for (int s = 0; s < 192; ++s) {
      if (s == 128) { cw = pos + 2 - cpos; pos += 3; stpos = pos; }
      if (a_bar(s)) {
        bw[s] = 63;
        for (int g = 0; g < 24; ++g) if (a_need(g) == s && wpos[g] && pos - wpos[g] < bw[s]) bw[s] = pos - wpos[g];
        if (s == 142) { if (pos - stpos < bw[s]) bw[s] = pos - stpos; ++pos; }
      }
      if (s == 78) fw = pos - fpos;
      if (s == QC) pw = pos - ppos;
      if (s < 64) xw[s] = pos - xpos[s];
      if (s + XL < 64) xpos[s + XL] = ++pos;
      if (s == 62) fpos = ++pos;
      if (s == QP) ppos = ++pos;
      if (s >= QC && s < QC + 17) lpos[2 * (s - QC)] = ++pos;
      if (a_piece(s) >= 0) wpos[a_piece(s) / 4] = ++pos;
      if (a_side(s)) { ++pos; if (s < 128) cpos = pos; }
      if (s >= QC && s < QC + 17) lpos[2 * (s - QC) + 1] = ++pos;
      if (first && a_first(s) >= 0) wpos[a_first(s) / 4] = ++pos;
      if (s == 175) zw = pos - lpos[1];
      if (s >= 176) lw[s - 176] = pos - lpos[2 + s - 176];
    }
    for (int i = 16; i < 32; ++i) lw[i] = pos - lpos[2 + i];
  }
};
constexpr int cmin(int a, int b) { return a < b ? a : b; }

#define R_OA "v[64:79]"
#define R_OB "v[80:95]"
#define S0_(n, q, m) "v_mfma_f32_32x32x16_bf16 v[" #n ":" #n "+15], v[208:211], v[" #q ":" #q "+3], v[" #m ":" #m "+15]\n\t"
#define S1_(n, q) "v_mfma_f32_32x32x16_bf16 v[" #n ":" #n "+15], v[212:215], v[" #q "+4:" #q "+7], v[" #n ":" #n "+15]\n\t"
#define PV0_(o, p) "v_smfmac_f32_32x32x32_bf16 v[" #o ":" #o "+15], v[216:219], v[" #p ":" #p "+7], v236\n\t"
#define PV1_(o, p) "v_smfmac_f32_32x32x32_bf16 v[" #o ":" #o "+15], v[220:223], v[" #p "+8:" #p "+15], v236\n\t"
#define L0_(l, p) "v_smfmac_f32_16x16x64_bf16 v[" #l ":" #l "+3], v[224:227], v[" #p ":" #p "+7], v236\n\t"
#define L1_(l, p) "v_smfmac_f32_16x16x64_bf16 v[" #l ":" #l "+3], v[224:227], v[" #p "+8:" #p "+15], v236\n\t"
#define E_(e, i) "v_exp_f32 v[" #e "+" #i "], v[" #e "+" #i "]\n\t"
#define RD_(d, off) "ds_read_b128 v[" #d ":" #d "+3], v237 offset:" #off "\n\t"
#define HALF_(e, n, q, m, p, o, l, X0, X1, X2, X3, X4, X5) \
  S0_(n, q, m) X0 E_(e, 0) E_(e, 1) E_(e, 2) S1_(n, q) X1 E_(e, 3) E_(e, 4) E_(e, 5) PV0_(o, p) X2 E_(e, 6) E_(e, 7) E_(e, 8) \
  PV1_(o, p) X3 E_(e, 9) E_(e, 10) E_(e, 11) L0_(l, p) X4 E_(e, 12) E_(e, 13) L1_(l, p) X5 E_(e, 14) E_(e, 15)
#define LG2_ "s_waitcnt lgkmcnt(2)\n\t"
#define LG0_ "s_waitcnt lgkmcnt(0)\n\t"
#define STEP_(b, nb, kv, wo, Y0, Y1) \
  HALF_(128, 160, 200, 112, 176, 80, 232, "", "", RD_(208, b + 2048), RD_(212, b + 3072), RD_(216, b), RD_(220, b + 1024) LG2_) \
  HALF_(160, 144, 192, 96, 128, 64, 228, "", LG0_, "s_add_u32 m0, s44, " #nb "\n\t", "", "global_load_lds_dwordx4 v" #kv ", s[40:41] sc1\n\t", \
        "global_load_lds_dwordx4 v" #kv ", s[40:41] offset:1024 sc1\n\t") \
  HALF_(144, 176, 200, 112, 160, 80, 232, Y0, "", RD_(208, b + 6144), RD_(212, b + 7168), RD_(216, b + 4096), RD_(220, b + 5120) LG2_) \
  HALF_(176, 128, 192, 96, 144, 64, 228, "s_mov_b32 m0, s45\n\t", LG0_, "global_load_lds_dwordx4 v240, s[42:43] offset:" #wo " sc1\n\t", Y1, \
        "s_waitcnt vmcnt(4)\n\t", "s_barrier\n\t")
DI float max16(const f32x16& s) {
  float mx = s[0];
#pragma unroll
  for (int e = 1; e < 16; ++e) mx = fmaxf(mx, s[e]);
  return mx;
}
typedef __attribute__((ext_vector_type(8))) unsigned u32x8;
typedef __attribute__((ext_vector_type(16))) unsigned u32x16;

__global__ __launch_bounds__(256, 1) void k_main(const char* xin, const char* wqkv, const char* wphi, const char* woA, const char* wq, const char* wk, const char* wv,
                                                 const char* woB, char* ws, char* out) {
  __shared__ __attribute__((aligned(16))) char smem_[163840];
  LAS char* const smem = (LAS char*)smem_;
  const unsigned lds0 = (unsigned)(uintptr_t)smem;
  Ctx c;
  int lane = threadIdx.x & 63, w = __builtin_amdgcn_readfirstlane(threadIdx.x >> 6), r = lane & 31, h = lane >> 5, rb, cb;
  block_map<4>(rb, cb);
  int tt = rb * 4 + w, blk = (lane >> 4) & 1, q4 = (lane & 15) >> 2, p4 = lane & 3;
  c.xv = lane * 16; c.p.init(lds0 + c.xv);
  char* flags = ws + WS_FLAG;
  char* col = ws + WS_COL + (size_t)cb * 2 * MB;
  u64 tag;
  u32x4 xr[4];
  auto wt = [&](int m, int ch) { return col + C_WT + m * 131072 + ch * SLOT + w * 4096; };
  auto pc_w = [&](int m, int slot, int ch, auto i) { dma_c<i * 1024>(lds0 + slot * SLOT + w * 4096, c.xv, wt(m, ch)); };
  auto pc_a = [&](auto n) { constexpr int g = n / 4; pc_w(g < 8 ? 1 : (g < 16 ? 2 : (g < 24 ? 0 : 3)), a_slot(g), g % 8, IC(n % 4)); };
  auto ld_cs = [&](bf16x8& d, int m, auto i) { ldx1<false, i * 1024>(d, c.xv, col + C_CS + m * 4096); };
  auto raw_in = [&](auto j, int slot) {
    const int hi = rb >> 3, pitch = (j == 0 || (j == 1 && !hi)) ? 6144 : 2048, row = lane >> 3;
    const char* src = (j == 0 ? wqkv + 2048 + hi * 2048 : (j == 1 ? (hi ? woA : wqkv) : (hi ? woB : wq))) + cb * 128 + (size_t)(rb & 7) * 128 * pitch;
    const unsigned v0 = (unsigned)((32 * w + row) * pitch + ((lane & 7) ^ (((row >> 1) & 1) << 2)) * 16);
    sfor<0, 4>([&](auto i) { dma<i * 1024>(lds0 + slot * SLOT + w * 4096, v0 + i * (8 * pitch - 1024), src); });
  };
  auto raw_out = [&](auto j, int slot) {
    const int hi = rb >> 3, ch = rb & 7, m = j == 0 ? 1 + hi : (j == 1 ? 3 * hi : 4 + hi);
    LAS const char* p = smem + slot * SLOT + (w >> 1) * 8192 + (4 * h + q4) * 128 + (((((q4 >> 1) ^ w) & 1) << 2) + 2 * blk + (p4 >> 1)) * 16 + 8 * (p4 & 1);
    const bool isv = m == 2;
    char* dst = col + C_WT + m * 131072 + (isv ? (w & 1) * 65536 + ch * 8192 + (w >> 1) * 4096 : ch * SLOT + (w >> 1) * 8192 + (w & 1) * 1024);
    float cs = 0.f;
    sfor<0, 4>([&](auto s) {
      const u32x4 f = __builtin_bit_cast(u32x4, cat8(trread(p + s * 2048), trread(p + s * 2048 + 1024)));
      st4_1<0>(f, c.xv, dst + s * (isv ? 1024 : 2048));
#pragma unroll
      for (int e = 0; e < 4; ++e) cs = dot2(f[e], 0x3f803f80u, cs);
    });
    st1_1(swap_add(cs), (unsigned)(r * 4), col + C_CS + m * 4096 + (ch * 2 + (w >> 1)) * 256 + (w & 1) * 128);
  };
  {
    [[maybe_unused]] const int l = 0;
    STAMP(30);
    bf16x8 cf;
    { const char* pc; asm volatile("s_getpc_b64 %0" : "=s"(pc));
      ldx0<0>(cf, c.xv, pc + (((blockIdx.x >> 3) * 4 + w) % 46) * 1024); }
    raw_in(IC(0), 7);
    u64 ep; ld8_1(ep, 0u, ws + WS_EPOCH);
    bf16x8 xt[4];
    sfor<0, 4>([&](auto i) { ldx0<i * 32>(xt[i], (unsigned)(r * 2048 + h * 16), xin + (size_t)tt * 65536 + cb * 128); });
    { const unsigned z = 0, vo = (unsigned)(lane ? FL_W + cb * 128 : FL_ACT + tt * 128);
      asm volatile("s_mov_b64 exec, 3\n\tglobal_atomic_or %0, %1, %2 sc1\n\ts_mov_b64 exec, -1" ::"v"(vo), "v"(z), "s"(flags) : "memory"); }
    wait_bar<6>();
    raw_out(IC(0), 7);
    asm volatile("s_waitcnt vmcnt(6)" : "+v"(cf), "+v"(ep), "+v"(xt[0]), "+v"(xt[1]), "+v"(xt[2]), "+v"(xt[3]));
    { char* dst = ws + (size_t)rb * 2 * MB + w * 65536 + cb * 4096;
      sfor<0, 4>([&](auto i) { xr[i] = unperm(__builtin_bit_cast(u32x4, xt[i])); st4_1<i * 1024>(xr[i], c.xv, dst); });
      store_stats(r, xr, ws + WS_ST + tt * 4096 + cb * 256); }
    { u64 z = ((u64)__builtin_amdgcn_readfirstlane((unsigned)(ep >> 32)) << 32 | __builtin_amdgcn_readfirstlane((unsigned)ep)) + 0x9E3779B97F4A7C15ull;
      z = (z ^ (z >> 30)) * 0xBF58476D1CE4E5B9ull; z = (z ^ (z >> 27)) * 0x94D049BB133111EBull; tag = z ^ (z >> 31); }
    waitv<0>();
    set_flag(flags + FL_ACT + tt * 128 + cb * 8, tag);
    bar();
    if (w == 0) set_flag(flags + FL_W + cb * 128 + rb * 8, tag);
    raw_in(IC(1), 8); raw_in(IC(2), 9);
    unsigned tch;
    {
      const int gw = blockIdx.x * 4 + w; const unsigned z = 0;
      const unsigned vo = (unsigned)(lane < 5 ? WS_KVF - WS_ST + (gw * 5 + lane) * 128 : WS_FLAG - WS_ST + gw * 128);
      asm volatile("s_mov_b64 exec, 63\n\tglobal_atomic_or %0, %1, %2 sc1\n\ts_mov_b64 exec, -1" ::"v"(vo), "v"(z), "s"(ws + WS_ST) : "memory");
      const char* sm = (gw & 512) ? ((gw & 256) ? wk : wv) : wphi;
      asm volatile("global_load_dword %0, %1, %2" : "=v"(tch) : "v"((unsigned)((gw & 255) * 256 + lane * 4)), "s"(sm));
    }
    poll<32>(flags, (unsigned)((lane & 15) * 8 + (blk ? FL_W + cb * 128 : FL_ACT + tt * 128)), tag);
    waitx<false, 0>(tch);
    sfor<0, 4>(pc_a); sfor<32, 36>(pc_a); sfor<4, 8>(pc_a);
    STAMP(31);
  }
  char *act, *stt, *fact;
#define REFRESH() do { \
    asm volatile("" : "+s"(w), "+s"(cb), "+s"(rb), "+s"(ws), "+s"(tag)); \
    asm volatile("" : "+v"(lane)); \
    r = lane & 31; h = lane >> 5; blk = (lane >> 4) & 1; q4 = (lane & 15) >> 2; p4 = lane & 3; tt = rb * 4 + w; \
    flags = ws + WS_FLAG; col = ws + WS_COL + (size_t)cb * 2 * MB; c.xv = lane * 16; c.p.init(lds0 + c.xv); \
    act = ws + (size_t)rb * 2 * MB + l * 4 * STG + w * 65536;     \
    fact = flags + FL_ACT + (l * 4 * 64 + tt) * 128;                  \
    stt = ws + WS_ST + (size_t)l * 512 * 1024 + tt * 4096;            \
  } while (0)
  for (int l = 0; l < 2; ++l) {
    REFRESH();
    float mean, rstd;
    Red<true> cw;
    bf16x8 park[24];
    STAMP(0);
    {
      if (l > 0) poll<16>(fact, lane * 8, tag);
      STAMP(1);
      constexpr int NA = 60;
      constexpr SchedA SQ(false), S1(true);
      bf16x8 x[64], f0[3], f1[3], f2[3], wf[4], kp[4], pp[32], zq[2], sel[2], mf0, mf1;
      Red<false> st; Red<true> ck, cv, cq;
      f32x16 acc[6], p[2], kvs;
      u64 fv;
      float mx[2], mn[2], e[16], se, sce;
      unsigned arow;
      LAS char* pk_img = smem;
      LAS char* v_img = smem + 2 * SLOT;
      LAS char* sh = smem + 9 * SLOT + 4096;
      char* kvp = col + C_KVP + l * 131072 + w * 32768;
      char* zp = col + C_ZP + l * 8192 + w * 2048;
      char* fkvp = flags + FL_KVP + (l * 64 + cb * 4 + w) * 128;
      char* fwl = flags + FL_W + 2048 + cb * 128;
      auto issue_x = [&](auto s) { ldx1<(s < NA), (s % 8) * 1024 - 4096>(x[s], c.xv, act + (s / 8) * 8192 + 4096); };
      auto ld_park = [&](auto i) { ldx1<true, (i % 4) * 1024>(park[i], c.xv, wt(4, 2 + i / 4)); };
      auto ldp = [&](auto i) {
        if constexpr (i >= 2) ldx1<true, ((i - 2) % 8) * 1024 - 4096>(pp[i - 2], c.xv, kvp + ((i - 2) / 8) * 8192 + 4096); else ldx1<false, i * 1024>(zq[i], c.xv, zp);
      };
      auto rd_wf = [&](auto k) { LAS const char* wp = smem + 9 * SLOT + (4 * h + q4) * 64 + blk * 32 + p4 * 8 + k * 1024; wf[k] = cat8(trread(wp), trread(wp + 512)); };
      auto rd01 = [&](auto s, auto t) {
        if constexpr (s < 64 || (s >= 128 && s < 192)) { const bf16x8 f = c.p.template frag<a_slot(s / 8) * SLOT + (s % 8) * 2048 + t * 1024>(); if constexpr (t == 0) f0[s % 3] = f; else f1[s % 3] = f; }
      };
      auto rd2 = [&](auto s) { if constexpr (s < 128) f2[s % 3] = c.p.template frag<a_slot(8 + s / 16) * SLOT + (s % 16) * 1024>(); };
      auto v_out = [&](auto t, auto hf) {
        *(LAS bf16x8*)(v_img + t * 8192 + w * 2048 + hf * 1024 + lane * 16) = pack8<hf>(acc[2 + t]);
      };
      auto sum_mm = [&](auto i) { waitx<true, SQ.lw[i]>(pp[i]); mm<false, true, i == 0>(kvs, sel[i & 1], pp[i]); };
      auto kmap = [&](auto gi) {
        constexpr int g = gi;
        if constexpr (g == 4) { pin2(acc[0], acc[1]); }
        if constexpr (g >= 6 && g < 10) mm<false, false, g % 2 == 0>(p[(g - 6) / 2], wf[g - 6], kp[g - 6]);
        if constexpr (g >= 4 && g < 8) kp[g - 4] = opq_v(pack8<g % 2>(acc[(g - 4) / 2]));
        if constexpr (g == 10) mm<true, false, false>(acc[3], cv.p[0], mf1);
        if constexpr (g >= 12 && g < 24) {
          constexpr int hd = (g - 12) / 6, t = (g - 12) % 6;
          const f32x16& q = p[hd];
          if constexpr (t == 0) { mx[hd] = a_max3(q[0], q[1], q[2]); mn[hd] = a_min3(q[0], q[1], q[2]); mx[hd] = a_max3(mx[hd], q[3], q[4]); mn[hd] = a_min3(mn[hd], q[3], q[4]); }
          if constexpr (t >= 1 && t < 4) {
            mx[hd] = a_max3(mx[hd], q[4 * t + 1], q[4 * t + 2]); mn[hd] = a_min3(mn[hd], q[4 * t + 1], q[4 * t + 2]);
            mx[hd] = a_max3(mx[hd], q[4 * t + 3], q[(4 * t + 4) & 15]); mn[hd] = a_min3(mn[hd], q[4 * t + 3], q[(4 * t + 4) & 15]);
          }
          if constexpr (t == 4) mx[hd] = opq_v(swap_max(mx[hd]) * -sce);
          if constexpr (t == 5) mn[hd] = opq_v(swap_min(mn[hd]) * sce);
        }
        if constexpr (g >= 24) {
          constexpr int grp = (g - 24) / 26, j = (g - 24) % 26, hd = grp >> 1, sg = grp & 1;
          if constexpr (j >= 1 && j <= 16) a_exp(e[j - 1]);
          if constexpr (j < 16) e[j] = sg ? a_fms(p[hd][j], sce, mn[hd]) : a_fma(p[hd][j], sce, mx[hd]);
          if constexpr (j == 2) se = e[0];
          if constexpr (j > 2 && j <= 17) a_add(se, e[j - 2]);
          if constexpr (j == 17) { se = opq_v(__builtin_amdgcn_rcpf(swap_add(se)) * rstd); if constexpr (grp < 2) v_out(IC(0), IC(grp)); }
          if constexpr (j >= 18) {
            constexpr int gq = (j - 18) / 2;
            if constexpr (j % 2 == 0) { a_mul(e[4 * gq], se); a_mul(e[4 * gq + 1], se); a_mul(e[4 * gq + 2], se); a_mul(e[4 * gq + 3], se); }
            else *(LAS u32x2*)((LAS char*)(uintptr_t)(arow ^ ((sg * 4 + gq) << 4)) + hd * 16384) = u32x2{cvtpk(e[4 * gq], e[4 * gq + 1]), cvtpk(e[4 * gq + 2], e[4 * gq + 3])};
          }
        }
      };
      auto aux = [&](auto s, auto i) {
        constexpr int g1 = 3 * s + i, g3 = 2 * (s - 128) + i;
        if constexpr (s < 64) {
          if constexpr (g1 >= 60 && g1 < 71) st.template run<g1 - 60>(lane);
          if constexpr (g1 >= 72 && g1 < 82) ck.template run<g1 - 72>(lane);
          if constexpr (g1 >= 84 && g1 < 94) cv.template run<g1 - 84>(lane);
          if constexpr (g1 == 71) { mean = st.a; rstd = st.b; sce = opq_v(rstd * LOG2E); }
          if constexpr (g1 == 95) mf0 = opq_v(mean_frag(mean, h == 0));
          if constexpr (g1 == 96) mf1 = opq_v(mean_frag(mean, h == 1));
          if constexpr (g1 == 99) mm<false, false, false>(acc[0], ck.p[0], mf0);
          if constexpr (g1 == 100) mm<false, false, false>(acc[1], ck.p[0], mf1);
          if constexpr (g1 == 101) mm<true, false, false>(acc[2], cv.p[0], mf0);
          if constexpr (g1 == 130) *(LAS short*)(sh + 8704 + w * 64 + (r >> 4) * 32 + ((r >> 2) & 1) * 16 + ((r >> 3) & 1) * 8 + (r & 3) * 2) = (short)(__float_as_uint(st.sd) >> 16);
          if constexpr (g1 >= 168 && g1 < 172) rd_wf(IC(g1 - 168));
          if constexpr (g1 == 174) arow = opq_v((unsigned)(uintptr_t)pk_img + (w * 32 + r) * 128 + h * 8 + ((r & 7) << 4));
        } else if constexpr (s >= 128) {
          if constexpr (g3 < 10) cq.template run<g3>(lane);
          if constexpr (g3 == 12) mm<false, false, false>(acc[4], cq.p[0], mf0);
          if constexpr (g3 == 13) mm<false, false, false>(acc[5], cq.p[0], mf1);
          if constexpr (g3 >= 16 && g3 < 26) cw.template run<g3 - 16>(lane);
          if constexpr (g3 >= 32 && g3 < 40) {
            constexpr int n = (g3 - 32) % 4, t = (g3 - 32) / 4;
            const int kr = 16 * t + 8 * (n >> 1) + 4 * h + 2 * (n & 1);
            u32x4 sv = __builtin_bit_cast(u32x4, sel[t]); sv[n] = opq_v(r == kr ? 0x3f80u : (r == kr + 1 ? 0x3f800000u : 0u)); sel[t] = __builtin_bit_cast(bf16x8, sv);
          }
          if constexpr (g3 == 42) *(LAS unsigned*)(sh + 8192 + 256 + lane * 4) = opq_v(0x3f803f80u);
          if constexpr (g3 == 95) {
            wait2<SQ.zw>(zq[0], zq[1]);
#pragma unroll
            for (int n = 0; n < 4; ++n) { e[n] = __builtin_bit_cast(f32x4, zq[0])[n]; a_add(e[n], __builtin_bit_cast(f32x4, zq[1])[n]); }
          }
          if constexpr (g3 == 96) {
            auto a = __builtin_amdgcn_permlane16_swap(__float_as_uint(e[0]), __float_as_uint(e[1]), false, false);
            auto b = __builtin_amdgcn_permlane16_swap(__float_as_uint(e[2]), __float_as_uint(e[3]), false, false);
            e[0] = opq_v(__uint_as_float(a[0]) + __uint_as_float(a[1])); e[1] = opq_v(__uint_as_float(b[0]) + __uint_as_float(b[1]));
          }
          if constexpr (g3 == 98) { auto a = __builtin_amdgcn_permlane32_swap(__float_as_uint(e[0]), __float_as_uint(e[1]), false, false); e[0] = opq_v(__uint_as_float(a[0]) + __uint_as_float(a[1])); }
          if constexpr (g3 == 100) e[1] = lane_get(e[0], lane ^ 8);
          if constexpr (g3 == 106)
            *(LAS short*)(sh + 8192 + w * 64 + (lane & 1) * 32 + ((lane >> 2) & 1) * 16 + ((lane >> 1) & 1) * 8 + (lane >> 4) * 2) = (short)cvtpk(e[0] + e[1], 0.f);
          if constexpr (g3 >= 116 && g3 < 124 && g3 % 2 == 0) rd_wf(IC((g3 - 116) / 2));
        }
      };
      auto step = [&](auto s) {
        constexpr int ps = s / 64;
        if constexpr (a_bar(s)) wait_bar<cmin(S1.bw[s], SQ.bw[s])>();
        if constexpr (s == 46) { if (l == 0 && w == 0) set_flag(fwl + rb * 8, tag); }
        if constexpr (s == 78) { waitx<false, SQ.fw>(fv); if (!__all(fv == tag)) poll<16>(fwl, lane * 8, tag); }
        if constexpr (s == 142) set_flag(fkvp + rb * 8, tag);
        if constexpr (s == QC) { waitx<false, SQ.pw>(fv); if (!__all(fv == tag)) poll<16>(fkvp, lane * 8, tag); }
        if constexpr (s < 64) waitx<(s < NA), SQ.xw[s % 64]>(x[s % 64]);
        SBAR();
        if constexpr (ps != 1) mm<false, (s % 64 < NA), s % 64 == 0>(acc[2 * ps], f0[s % 3], x[s % 64]);
        else mm<true, (s % 64 < NA), s % 64 == 0>(acc[3], f2[s % 3], x[s % 64]);
        SBAR();
        rd01(IC(s + 2), IC(0));
        if constexpr (ps == 1) rd2(IC(s + 2));
        if constexpr (s + XL < 64) issue_x(IC(s + XL));
        if constexpr (s == 62) { fv = tag; rd_flag<16>(fv, lane * 8, fwl); }
        if constexpr (s == QP) { fv = tag; rd_flag<16>(fv, lane * 8, fkvp); }
        if constexpr (s >= QC && s < QC + 17) ldp(IC(2 * (s - QC)));
        aux(s, IC(0));
        SBAR();
        if constexpr (ps != 1) mm<false, (s % 64 < NA), s % 64 == 0>(acc[2 * ps + 1], f1[s % 3], x[s % 64]);
        SBAR();
        rd01(IC(s + 2), IC(1));
        if constexpr (a_piece(s) >= 0) pc_a(IC(a_piece(s)));
        if constexpr (s >= 2 && s < 6) ldx1<false, (s - 2) * 1024>(st.p[s - 2], c.xv, stt);
        if constexpr (s >= 6 && s < 10) ld_cs(ck.p[s - 6], 1, IC(s - 6));
        if constexpr (s >= 10 && s < 14) ld_cs(cv.p[s - 10], 2, IC(s - 10));
        if constexpr (s == 40) dma<0>(lds0 + 9 * SLOT + w * 1024, c.xv, wphi + cb * 4096 + w * 1024);
        if constexpr (s >= 86 && s < 90) ld_cs(cq.p[s - 86], 0, IC(s - 86));
        if constexpr (s >= 90 && s < 94) ld_cs(cw.p[s - 90], 4, IC(s - 90));
        if constexpr (a_park(s) >= 0) ld_park(IC(a_park(s)));
        if constexpr (s >= QC && s < QC + 17) ldp(IC(2 * (s - QC) + 1));
        aux(s, IC(1));
        if constexpr (ps == 0) {
          SBAR();
          mm<true, (s < NA), s == 0>(acc[2], f2[s % 3], x[s]);
          SBAR();
          rd2(IC(s + 2));
          if constexpr (a_first(s) >= 0) { if (l == 0) pc_a(IC(a_first(s))); }
          aux(s, IC(2));
        }
        if constexpr (s == 26) { if (l == 0) raw_out(IC(1), 8); }
        if constexpr (s == 30) { if (l == 0) raw_out(IC(2), 9); }
        if constexpr (ps == 1) { kmap(IC(2 * (s - 64))); kmap(IC(2 * (s - 64) + 1)); }
        if constexpr (s >= 176) sum_mm(IC(s - 176));
        SBAR();
      };
      sfor<0, XL>(issue_x);
      wait_bar<XL>();
      sfor<0, 2>([&](auto s) { rd01(s, IC(0)); rd01(s, IC(1)); rd2(s); });
      sfor<0, 64>(step);
      STAMP(2);
      sfor<64, 128>(step);
      STAMP(19);
      mdone(acc[3]);
      v_out(IC(1), IC(0)); v_out(IC(1), IC(1));
      lds_bar();
      {
        const int hd = w >> 1, fh = w & 1;
        f32x16 kv, z;
        const int ch = fh * 4 + 2 * blk + (p4 >> 1), r0 = 4 * h + q4;
        LAS const char* pa = pk_img + hd * 16384 + r0 * 128 + ((ch ^ r0) << 4) + 8 * (p4 & 1);
        LAS const char* pb = v_img + hd * 8192 + lane * 16;
        bf16x8 a[8], b[8], d[8];
#pragma unroll
        for (int s = 0; s < 8; ++s) { a[s] = cat8(trread(pa + s * 2048), trread(pa + s * 2048 + 1024)); b[s] = *(LAS const bf16x8*)(pb + s * 1024); }
#pragma unroll
        for (int s = 0; s < 8; ++s) d[s] = *(LAS const bf16x8*)(sh + 8704 + s * 32 + h * 16);
        SBAR();
        sfor<0, 8>([&](auto s) { mm<false, false, s == 0>(kv, a[s], b[s]); });
        sfor<0, 8>([&](auto s) {
          mm<false, false, s == 0>(z, d[s], a[s]);
          if constexpr (s == 1) { mdone(kv); st4_1<0>(__builtin_bit_cast(u32x4, pack8<0>(kv)), c.xv, kvp + rb * 2048); }
          if constexpr (s == 2) { st4_1<1024>(__builtin_bit_cast(u32x4, pack8<1>(kv)), c.xv, kvp + rb * 2048); waitv<SQ.cw>(); }
          SBAR();
        });
        mdone(z);
        st1_1(z[0], (unsigned)(r * 4), zp + rb * 128);
      }
      STAMP(3);
      sfor<128, 192>(step);
      mdone(acc[4], acc[5]);
      STAMP(4);
      {
        bf16x8 pq[2][4], ka[8], za[8];
        f32x16 ep, en, nm[4], dn[2];
        float ai[2], bi[2];
        char* dst = act + STG + cb * 4096;
        sfor<0, 4>([&](auto k) { mm<false, false, k % 2 == 0, true>(p[k / 2], wf[k], pack8<k % 2>(acc[4 + k / 2])); });
        mdone(p[0], p[1]);
        hedge_exp(p[0], sce, ep, en, [&](auto i) { sum_mm(IC(16 + i)); });
        pq[0][0] = pack8<0>(ep); pq[0][1] = pack8<1>(ep); pq[0][2] = pack8<0>(en); pq[0][3] = pack8<1>(en);
        pin4(pq[0]);
        asm volatile("" : "+v"(kvs));
        *(LAS bf16x8*)(sh + w * 2048 + lane * 16) = pack8<0>(kvs); *(LAS bf16x8*)(sh + w * 2048 + 1024 + lane * 16) = pack8<1>(kvs);
        lds_bar();
        STAMP(5);
        LAS const char* zb = sh + 8192 + (r & 1) * 256 + h * 32;
#pragma unroll
        for (int k = 0; k < 8; ++k) { za[k] = *(LAS const bf16x8*)(zb + (k >> 1) * 64 + (k & 1) * 16); ka[k] = *(LAS const bf16x8*)(sh + k * 1024 + lane * 16); }
        auto coef = [&](auto hd) {
          const float a = __builtin_amdgcn_rcpf(dn[0][1]), b = __builtin_amdgcn_rcpf(dn[1][1]);
          const float inv = __builtin_amdgcn_rcpf(dn[0][0] * a + dn[1][0] * b + 1e-6f);
          ai[hd] = opq_v(a * inv); bi[hd] = opq_v(b * inv);
        };
        auto fin = [&](auto hd) {
          f32x16 o;
#pragma unroll
          for (int i = 0; i < 16; ++i) o[i] = nm[2 * hd][i] * ai[hd] + nm[2 * hd + 1][i] * bi[hd];
          st4_1<hd * 2048>(__builtin_bit_cast(u32x4, pack8<0>(o)), c.xv, dst);
          st4_1<hd * 2048 + 1024>(__builtin_bit_cast(u32x4, pack8<1>(o)), c.xv, dst);
        };
        hedge_exp(p[1], sce, ep, en, [&](auto i) {
          if constexpr (i < 4) mm<false, false, i % 2 == 0>(dn[i / 2], za[i], pq[0][i]);
          else if constexpr (i < 8) mm<false, false, i % 2 == 0>(nm[i / 2 - 2], ka[i - 4], pq[0][i - 4]);
          if constexpr (i % 2) pc_a(IC(108 + i / 2));
        });
        pq[1][0] = pack8<0>(ep); pq[1][1] = pack8<1>(ep); pq[1][2] = pack8<0>(en); pq[1][3] = pack8<1>(en);
        pin4(pq[1]); pin2(dn[0], dn[1]); pin2(nm[0], nm[1]);
        coef(IC(0));
        STAMP(6);
        sfor<0, 4>([&](auto k) { mm<false, false, k % 2 == 0, k == 0>(dn[k / 2], za[4 + k], pq[1][k]); if constexpr (k % 2) pc_a(IC(116 + k / 2)); });
        sfor<0, 4>([&](auto k) { mm<false, false, k % 2 == 0>(nm[2 + k / 2], ka[4 + k], pq[1][k]); if constexpr (k % 2) pc_a(IC(118 + k / 2)); });
        fin(IC(0));
        pin2(dn[0], dn[1]); pin2(nm[2], nm[3]);
        coef(IC(1));
        fin(IC(1));
        sfor<120, 124>(pc_a);
        waitv<4>();
        set_flag(fact + 8192 + cb * 8, tag);
        sfor<124, 128>(pc_a); sfor<8, 24>(ld_park);
      }
    }
    STAMP(7);
    REFRESH();
    {
      f32x16 a0, a1; res_to_acc(xr, a0, a1);
      poll<16>(fact + 8192, lane * 8, tag);
      sfor<0, 24>([&](auto i) { waitx<true, 63>(park[i]); });
      STAMP(8);
      bf16x8 x[64];
      const char* xb = act + STG;
      sfor<0, XL>([&](auto s) { ld_x<s>(x, c, xb); });
      bar();
      const char* wkv = ((cb & 2) ? wv : wk) + w * 256 * 64;
      const unsigned kvv = (unsigned)((lane >> 1) * 64 + (cb & 1) * 32 + (lane & 1) * 16);
      const unsigned wa = lds0 + w * 4096 + c.xv, wb = wa + 65536;
      auto unpark = [&](auto i) {
        constexpr int off = a_slot(26 + i / 4) * SLOT + (i % 4) * 1024;
        ds_write_a<(off & 65535)>(off < 65536 ? wa : wb, park[i]);
      };
      gemm64<true, false, HookP3>(c, x, xb, a0, a1, [&](auto s) {
        if constexpr (s == 33) bar();
        if constexpr (s >= 36 && s < 44) unpark(IC(s - 36));
        if constexpr (s >= 52 && s < 60) dma<0>(lds0 + 8 * SLOT + w * 8192 + (s - 52) * 1024, kvv, wkv + (s - 52) * 32 * 64);
        if constexpr (s >= 60) pc_w(4, a_slot(24), 0, IC(s - 60));
      }, [](auto) {});
      STAMP(9);
      store_x(c, r, a0, a1, xr, act + 2 * STG + cb * 4096, stt + 256 * 1024 + cb * 256);
      bar();
      sfor<8, 24>(unpark);
      waitv<0>();
      set_flag(fact + 2 * 8192 + cb * 8, tag);
      sfor<4, 8>([&](auto i) { pc_w(4, a_slot(24 + i / 4), i / 4, IC(i % 4)); });
    }
    STAMP(10);
    REFRESH();
    u32x16 qq;
    char* kvf = ws + WS_KVF + l * 320 * KB;
    bf16x8 k0, k1;
    {
      char* fkv = flags + FL_KV + l * 2048;
      const int du = (cb >> 2) - w, ty = cb & 3;
      LAS char* reg = smem + 8 * SLOT;
      bf16x8 fu[16], cu;
      { LAS const char* p = reg + w * 8192 + (4 * h + q4) * 32 + p4 * 8;
        float t0 = 0.f, t1 = 0.f;
#pragma unroll
        for (int i = 0; i < 16; ++i) {
          fu[i] = cat8(trread(p + i * 512), trread(p + i * 512 + 256));
          const u32x4 f = __builtin_bit_cast(u32x4, fu[i]);
          t0 = dot2(f[0], 0x3f803f80u, t0); t1 = dot2(f[1], 0x3f803f80u, t1); t0 = dot2(f[2], 0x3f803f80u, t0); t1 = dot2(f[3], 0x3f803f80u, t1);
        }
        cu = opq_v(cs_frag(swap_add(t0 + t1))); }
      poll<32>(fact + 2 * 8192 - w * 128, (unsigned)((lane & 15) * 8 + (blk ? cb >> 2 : w) * 128), tag);
      STAMP(11);
      const char* xb = act + 2 * STG; const char* stp = stt + 256 * 1024;
      bf16x8 x[64], xu[16], mf0, mf1;
      Red<false> su, sp;
      sfor<0, 4>([&](auto i) { ldx1<false, i * 1024>(su.p[i], c.xv, stp + du * 4096); });
      sfor<0, 16>([&](auto i) { ldx1<true, (i % 8) * 1024 - 4096>(xu[i], c.xv, xb + (ptrdiff_t)du * 65536 + w * 16384 + (i / 8) * 8192 + 4096); });
      f32x16 au, av;
      sfor<0, 16>([&](auto i) {
        waitx<true, 15 - i + (i < XL ? i : XL)>(xu[i]);
        SBAR();
        mm<false, true, i == 0>(au, fu[i], xu[i]);
        if constexpr (i < XL) ld_x<i>(x, c, xb);
        mm<true, true, i == 0>(av, fu[i], xu[i]);
        if constexpr (i < 11) su.template run<i>(lane);
        if constexpr (i == 12) mf0 = opq_v(mean_frag(su.a, h == 0));
        if constexpr (i == 14) { mm<false, false, false>(au, cu, mf0); mm<true, false, false>(av, cu, mf0); }
        SBAR();
      });
      lds_bar();
      STAMP(12);
      f32x16 a0, a1;
      u64 fa;
      const float ru = su.b;
      gemm64<true, true, HookP4>(c, x, xb, a0, a1, [&](auto s) {
        if constexpr (s == 1) {
          pin2(au, av);
          if (ty >= 2) au = av;
#pragma unroll
          for (int gq = 0; gq < 4; ++gq) *(LAS f32x4*)(reg + w * 8192 + gq * 1024 + lane * 16) = f32x4{au[4 * gq], au[4 * gq + 1], au[4 * gq + 2], au[4 * gq + 3]};
        }
        if constexpr (s == 3) {
          lds_bar();
          f32x4 t = {};
#pragma unroll
          for (int j = 0; j < 4; ++j) t += *(LAS const f32x4*)(reg + j * 8192 + (ty < 2 ? w & 1 : w) * 1024 + lane * 16);
#pragma unroll
          for (int e = 0; e < 4; ++e) t[e] *= lane_get(ru, ty < 2 ? r : 8 * w + 4 * h + e);
          const unsigned vo = ty < 2 ? (unsigned)(ty * 1024 + lane * 16 + (w & 1) * 8)
                                     : (unsigned)(2048 + (w >> 1) * 1024 + ((ty & 1) * 16 + (r & 15) + 32 * (w & 1)) * 16 + h * 8);
          st2_1(__builtin_bit_cast(f32x2, u32x2{cvtpk(t[0], t[1]), cvtpk(t[2], t[3])}), vo, kvf + (tt + du) * 4096);
        }
        if constexpr (s >= 8 && s < 12) ldx1<false, (s - 8) * 1024>(sp.p[s - 8], c.xv, stp);
        if constexpr (s == 4 + XL) { bar(); if (w == 0) set_flag(fkv + ((tt + du) * 4 + ty) * 8, tag); }
        if constexpr (s == 52) ld8_1(fa, lane * 8, fkv + w * 512);
      }, [&](auto g) {
        if constexpr (g >= 48 && g < 59) sp.template run<g - 48>(lane);
        if constexpr (g == 60) mf0 = opq_v(mean_frag(sp.a, h == 0));
        if constexpr (g == 61) mf1 = opq_v(mean_frag(sp.a, h == 1));
        if constexpr (g == 64) mm<false, false, false>(a0, cw.p[0], mf0);
        if constexpr (g == 65) mm<false, false, false>(a1, cw.p[0], mf1);
      });
      rstd = sp.b;
      waitx<false, 0>(fa);
      for (int spin = 0; spin < (1 << 15) && !__all(fa == tag); ++spin) { __builtin_amdgcn_s_sleep(8); ld8_1(fa, lane * 8, fkv + w * 512); waitx<false, 0>(fa); }
      bar();
      ldx1<false, 0>(k0, c.xv, kvf); ldx1<false, 1024>(k1, c.xv, kvf);
      sfor<0, 6>([&](auto i) { dma_c<(i % 2) * 1024>(lds0 + 8 * SLOT + w * 2048 + (i / 2) * 8192, c.xv, kvf + 2048 + w * 2048 + (i / 2) * 8192); });
      STAMP(13);
      const float sc = rstd * QSCALE;
#pragma unroll
      for (int i = 0; i < 8; ++i) { qq[i] = cvtpk(a0[2 * i] * sc, a0[2 * i + 1] * sc); qq[8 + i] = cvtpk(a1[2 * i] * sc, a1[2 * i + 1] * sc); }
      asm volatile("s_waitcnt vmcnt(6)" : "+v"(k0), "+v"(k1), "+v"(qq));
    }
    STAMP(14);
    REFRESH();
    {
      const bf16x8 qa0 = __builtin_bit_cast(bf16x8, __builtin_shufflevector(qq, qq, 0, 1, 2, 3)), qa1 = __builtin_bit_cast(bf16x8, __builtin_shufflevector(qq, qq, 4, 5, 6, 7));
      const bf16x8 qb0 = __builtin_bit_cast(bf16x8, __builtin_shufflevector(qq, qq, 8, 9, 10, 11)), qb1 = __builtin_bit_cast(bf16x8, __builtin_shufflevector(qq, qq, 12, 13, 14, 15));
      float mA = 0.f, mB = 0.f;
      const char* ksrc = kvf + 2048 + w * 2048;
      const unsigned ring = lds0 + 8 * SLOT + w * 2048;
      for (int attempt = 0; attempt < 2; ++attempt) {
        f32x16 sa0, sb;
        if (attempt == 1) {
          mA = -INFINITY; mB = -INFINITY;
          for (int t = 0; t < 64; ++t) {
            ldx1<false, 0>(k0, c.xv, kvf + t * 4096); ldx1<false, 1024>(k1, c.xv, kvf + t * 4096);
            asm volatile("s_waitcnt vmcnt(0)" : "+v"(k0), "+v"(k1));
            mm<false, false, true>(sa0, k0, qa0); mm<false, false, false>(sa0, k1, qa1); mm<false, false, true>(sb, k0, qb0); mm<false, false, false>(sb, k1, qb1);
            mdone(sa0, sb);
            mA = fmaxf(mA, max16(sa0)); mB = fmaxf(mB, max16(sb));
          }
          mA = swap_max(mA); mB = swap_max(mB);
          ldx1<false, 0>(k0, c.xv, kvf); ldx1<false, 1024>(k1, c.xv, kvf);
          sfor<0, 6>([&](auto i) { dma_c<(i % 2) * 1024>(ring + (i / 2) * 8192, c.xv, ksrc + (i / 2) * 8192); });
          asm volatile("s_waitcnt vmcnt(6)" : "+v"(k0), "+v"(k1));
        }
        mm<false, false, true, true>(sa0, k0, qa0); mm<false, false, false>(sa0, k1, qa1); mm<false, false, true>(sb, k0, qb0); mm<false, false, false>(sb, k1, qb1);
        mdone(sa0, sb);
        if (attempt == 0) { mA = swap_max(max16(sa0)); mB = swap_max(max16(sb)); }
        f32x16 nma, nmb, oa = {}, ob = {}, sb1 = {}, t0, t1;
#pragma unroll
        for (int e = 0; e < 16; ++e) { nma[e] = -mA; nmb[e] = -mB; sa0[e] -= mA; }
        const unsigned o2 = opq_v(0x3f803f80u), s0 = ((lane >> 2) & 1) ? 0u : o2, s1 = o2 - s0;
        u32x16 kvr = {}, misc;
        { const u32x4 a = __builtin_bit_cast(u32x4, k0), b = __builtin_bit_cast(u32x4, k1);
#pragma unroll
          for (int e = 0; e < 4; ++e) { kvr[e] = a[e]; kvr[4 + e] = b[e]; } }
        misc = u32x16{s0, s0, s1, s1, 0u, 0u, 0u, 0u, 0u, 0u, 0u, 0u, 0xddddddddu, lds0 + 8 * SLOT + c.xv, 0u, 0u};
        const u32x4 offs = {c.xv, c.xv + 8192, c.xv + 16384, c.xv + 24576};
        const char* ks = ksrc + 3 * 8192; const char* wsr = wt(5, 0);
        unsigned wm0 = lds0 + w * 4096, cnt = 8;
        asm volatile(
            "s_waitcnt vmcnt(4)\n\ts_barrier\n\t"
            "1:\n\t"
            STEP_(0, 24576, 240, 0, "s_sub_u32 %[cnt], %[cnt], 1\n\t", "") STEP_(8192, 0, 241, 1024, "", "") STEP_(16384, 8192, 242, 2048, "", "")
            STEP_(24576, 16384, 243, 3072, "s_add_u32 s40, s40, 0x8000\n\ts_addc_u32 s41, s41, 0\n\t", "s_add_u32 s42, s42, 0x4000\n\ts_addc_u32 s43, s43, 0\n\t")
            "s_add_u32 s45, s45, 0x4000\n\ts_cmp_lg_u32 %[cnt], 0\n\ts_cbranch_scc1 1b\n\t"
            PV0_(80, 176) PV1_(80, 176) L0_(232, 176) L1_(232, 176)
            "s_nop 15\n\t"
            : "+{v[64:79]}"(oa), "+{v[80:95]}"(ob), "+{v[128:143]}"(sa0), "=&{v[144:159]}"(t0), "=&{v[160:175]}"(t1), "+{v[176:191]}"(sb1),
              "+{v[208:223]}"(kvr), "+{v[224:239]}"(misc), "+{s[40:41]}"(ks), "+{s[42:43]}"(wsr), "+{s45}"(wm0), [cnt] "+s"(cnt)
            : "{v[96:111]}"(nma), "{v[112:127]}"(nmb), "{v[192:207]}"(qq), "{v[240:243]}"(offs), "{s44}"(ring)
            : "scc", "memory");
        const float la = __uint_as_float(misc[4]), lb = __uint_as_float(misc[8]);
        const float ia = __builtin_amdgcn_rcpf(la), ib = __builtin_amdgcn_rcpf(lb);
#pragma unroll
        for (int e = 0; e < 16; ++e) { oa[e] *= ia; ob[e] *= ib; }
        char* dst = act + 3 * STG + cb * 4096;
        st4_1<0>(__builtin_bit_cast(u32x4, pack8<0>(oa)), c.xv, dst); st4_1<1024>(__builtin_bit_cast(u32x4, pack8<1>(oa)), c.xv, dst);
        st4_1<2048>(__builtin_bit_cast(u32x4, pack8<0>(ob)), c.xv, dst); st4_1<3072>(__builtin_bit_cast(u32x4, pack8<1>(ob)), c.xv, dst);
        float chk = 0.f;
#pragma unroll
        for (int e = 0; e < 16; ++e) chk = __builtin_fmaf(oa[e], 0.f, __builtin_fmaf(ob[e], 0.f, chk));
        const bool bad = !(la < 1.2676506e30f) || !(lb < 1.2676506e30f) || !(chk == 0.f);
        wait_bar<4>();
        LAS volatile int* vote = (LAS volatile int*)(smem + 8 * SLOT);
        if (lane == 0) vote[w] = 0;
        if (attempt == 0 && bad) vote[w] = 1;
        lds_bar();
        const int redo = vote[0] | vote[1] | vote[2] | vote[3];
        if (!__builtin_amdgcn_readfirstlane(redo)) break;
        lds_bar();
      }
      waitv<0>();
      set_flag(fact + 3 * 8192 + cb * 8, tag);
    }
    STAMP(15);
    REFRESH();
    {
      f32x16 a0, a1; res_to_acc(xr, a0, a1);
      poll<16>(fact + 3 * 8192, lane * 8, tag);
      STAMP(16);
      bf16x8 x[64];
      const char* xb = act + 3 * STG;
      sfor<0, XL>([&](auto s) { ld_x<s>(x, c, xb); });
      bar();
      gemm64<false, false, HookP6>(c, x, xb, a0, a1, [&](auto s) {
        if constexpr (s == 33) bar();
        if constexpr (s >= 52) pc_a(IC(a_pre(s - 52)));
      }, [](auto) {});
      STAMP(17);
      if (l == 0) {
        store_x(c, r, a0, a1, xr, act + 4 * STG + cb * 4096, stt + 512 * 1024 + cb * 256);
        bar();
        sfor<12, 16>([&](auto n) { pc_a(IC(a_pre(n))); });
        waitv<4>();
        set_flag(fact + 4 * 8192 + cb * 8, tag);
        sfor<16, 32>([&](auto n) { pc_a(IC(a_pre(n))); });
      } else {
        char* dst = out + (size_t)(tt * 32 + r) * 2048 + cb * 128 + h * 16;
        *(u32x4*)(dst) = unperm(__builtin_bit_cast(u32x4, pack8<0>(a0))); *(u32x4*)(dst + 32) = unperm(__builtin_bit_cast(u32x4, pack8<1>(a0)));
        *(u32x4*)(dst + 64) = unperm(__builtin_bit_cast(u32x4, pack8<0>(a1))); *(u32x4*)(dst + 96) = unperm(__builtin_bit_cast(u32x4, pack8<1>(a1)));
        if (tt == 0 && cb == 0) set_flag(ws + WS_EPOCH, tag);
      }
    }
    STAMP(18);
  }
}

extern "C" void kernel_launch(void* const* d_in, const int* in_sizes, int n_in, void* d_out, int out_size, void* d_ws, size_t ws_size, hipStream_t stream) {
  k_main<<<256, 256, 0, stream>>>((const char*)d_in[0], (const char*)d_in[1], (const char*)d_in[2], (const char*)d_in[3], (const char*)d_in[5], (const char*)d_in[6],
                                  (const char*)d_in[7], (const char*)d_in[8], (char*)d_ws, (char*)d_out);
}
```

```cpp
#include <hip/hip_runtime.h>
#include <hip/hip_bf16.h>
#include <stdint.h>
#include <type_traits>

typedef __attribute__((ext_vector_type(8))) short bf16x8;
typedef __attribute__((ext_vector_type(4))) short s16x4;
typedef __attribute__((ext_vector_type(2))) __bf16 bf2_t;
typedef __attribute__((ext_vector_type(16))) float f32x16;
typedef __attribute__((ext_vector_type(4))) float f32x4;
typedef __attribute__((ext_vector_type(2))) float f32x2;
typedef __attribute__((ext_vector_type(4))) unsigned u32x4;
typedef __attribute__((ext_vector_type(2))) unsigned u32x2;
#define LAS __attribute__((address_space(3)))
#define DI __device__ __forceinline__
#define MFMA32(a, b, c) __builtin_amdgcn_mfma_f32_32x32x16_bf16((a), (b), (c), 0, 0, 0)
#define MFMA16(a, b, c) __builtin_amdgcn_mfma_f32_16x16x32_bf16((a), (b), (c), 0, 0, 0)
#define SBAR() __builtin_amdgcn_sched_barrier(0)

constexpr int S_ = 2048, D_ = 1024;
constexpr float LOG2E = 1.4426950408889634f;
constexpr float QSCALE = 0.17677669529663687f * LOG2E;

constexpr size_t MB = 1u << 20, KB = 1024;
constexpr size_t STG = 256 * KB;
constexpr size_t WS_COL = 32 * MB;
constexpr size_t C_WT = 0;
constexpr size_t C_KVP = 768 * KB;
constexpr size_t C_ZP = 1024 * KB;
constexpr size_t C_CS = 1040 * KB;
constexpr size_t WS_ST = 64 * MB;
constexpr size_t WS_KVF = WS_ST + 1024 * KB;
constexpr size_t WS_FLAG = WS_ST + 1664 * KB;
constexpr size_t WS_EPOCH = WS_ST + 1920 * KB;
constexpr int FL_ACT = 0, FL_KVP = 8 * 64 * 128, FL_KV = FL_KVP + 2 * 64 * 128, FL_W = FL_KV + 2 * 2048;
typedef unsigned long long u64;

template <int I, int N, class F> DI void sfor(F&& f) { if constexpr (I < N) { f(std::integral_constant<int, I>{}); sfor<I + 1, N>(f); } }

DI unsigned cvtpk(float lo, float hi) { f32x2 v = {lo, hi}; return __builtin_bit_cast(unsigned, __builtin_convertvector(v, bf2_t)); }
DI float bflo(unsigned u) { return __uint_as_float(u << 16); }
DI float bfhi(unsigned u) { return __uint_as_float(u & 0xffff0000u); }
DI float rbf(float x) { return bflo(cvtpk(x, x)); }
DI float swap_add(float v) { auto r = __builtin_amdgcn_permlane32_swap(__float_as_uint(v), __float_as_uint(v), false, false); return __uint_as_float(r[0]) + __uint_as_float(r[1]); }
DI float swap_max(float v) { auto r = __builtin_amdgcn_permlane32_swap(__float_as_uint(v), __float_as_uint(v), false, false); return fmaxf(__uint_as_float(r[0]), __uint_as_float(r[1])); }
DI float swap_min(float v) { auto r = __builtin_amdgcn_permlane32_swap(__float_as_uint(v), __float_as_uint(v), false, false); return fminf(__uint_as_float(r[0]), __uint_as_float(r[1])); }
template <int S> DI bf16x8 pack8(const f32x16& v) {
  u32x4 p = {cvtpk(v[8 * S], v[8 * S + 1]), cvtpk(v[8 * S + 2], v[8 * S + 3]), cvtpk(v[8 * S + 4], v[8 * S + 5]), cvtpk(v[8 * S + 6], v[8 * S + 7])};
  return __builtin_bit_cast(bf16x8, p);
}
DI u32x4 unperm(u32x4 v) {
  auto r0 = __builtin_amdgcn_permlane32_swap(v[0], v[2], false, false);
  auto r1 = __builtin_amdgcn_permlane32_swap(v[1], v[3], false, false);
  return u32x4{r0[0], r1[0], r0[1], r1[1]};
}
DI float lane_get(float v, int src) { return __uint_as_float(__builtin_amdgcn_ds_bpermute(src << 2, __float_as_uint(v))); }
DI s16x4 trread(LAS const char* p) { return __builtin_amdgcn_ds_read_tr16_b64_v4i16((LAS s16x4*)p); }
DI bf16x8 cat8(s16x4 a, s16x4 b) { return __builtin_shufflevector(a, b, 0, 1, 2, 3, 4, 5, 6, 7); }
DI float dot2(unsigned a, unsigned b, float c) { return __builtin_amdgcn_fdot2_f32_bf16(__builtin_bit_cast(bf2_t, a), __builtin_bit_cast(bf2_t, b), c, false); }

template <int OFF> DI void dma(unsigned lds, unsigned v, const char* sb) {
  asm volatile("s_mov_b32 m0, %0\n\ts_nop 0\n\tglobal_load_lds_dwordx4 %1, %2 offset:%3" ::"s"(lds), "v"(v), "s"(sb), "i"(OFF) : "memory");
}
template <int OFF> DI void dma_c(unsigned lds, unsigned v, const char* sb) {
  asm volatile("s_mov_b32 m0, %0\n\ts_nop 0\n\tglobal_load_lds_dwordx4 %1, %2 offset:%3 sc1" ::"s"(lds), "v"(v), "s"(sb), "i"(OFF) : "memory");
}
template <int OFF> DI void dma_n(unsigned v, const char* sb) {
  asm volatile("global_load_lds_dwordx4 %0, %1 offset:%2 sc1" ::"v"(v), "s"(sb), "i"(OFF) : "memory");
}
constexpr int cap63(int n) { return n > 63 ? 63 : n; }
template <int N> DI void wait_bar() { asm volatile("s_waitcnt vmcnt(%0)\n\ts_barrier" ::"i"(cap63(N)) : "memory"); }
DI void lds_bar() { asm volatile("s_waitcnt lgkmcnt(0)\n\ts_barrier" ::: "memory"); }
DI void all_bar() { asm volatile("s_waitcnt vmcnt(0) lgkmcnt(0)\n\ts_barrier" ::: "memory"); }
DI void bar() { asm volatile("s_barrier" ::: "memory"); }
template <bool AG, int OFF> DI void ldx1(bf16x8& d, unsigned voff, const char* sb) {
  if constexpr (AG) asm volatile("global_load_dwordx4 %0, %1, %2 offset:%3 sc1" : "=a"(d) : "v"(voff), "s"(sb), "i"(OFF));
  else asm volatile("global_load_dwordx4 %0, %1, %2 offset:%3 sc1" : "=v"(d) : "v"(voff), "s"(sb), "i"(OFF));
}
template <int OFF> DI void ld2_1(f32x2& d, unsigned voff, const char* sb) { asm volatile("global_load_dwordx2 %0, %1, %2 offset:%3 sc1" : "=v"(d) : "v"(voff), "s"(sb), "i"(OFF)); }
DI void ld1_1(unsigned& d, unsigned voff, const void* sb) { asm volatile("global_load_dword %0, %1, %2 sc1" : "=v"(d) : "v"(voff), "s"(sb) : "memory"); }
template <int OFF> DI void st4_1(u32x4 d, unsigned voff, char* sb) { asm volatile("global_store_dwordx4 %0, %1, %2 offset:%3 sc1\n\ts_nop 1" ::"v"(voff), "v"(d), "s"(sb), "i"(OFF) : "memory"); }
DI void st2_1(f32x2 d, unsigned voff, char* sb) { asm volatile("global_store_dwordx2 %0, %1, %2 sc1" ::"v"(voff), "v"(d), "s"(sb) : "memory"); }
DI void st1_1(float d, unsigned voff, char* sb) { asm volatile("global_store_dword %0, %1, %2 sc1" ::"v"(voff), "v"(d), "s"(sb) : "memory"); }
template <int N> DI void waitv() { asm volatile("s_waitcnt vmcnt(%0)" ::"i"(cap63(N)) : "memory"); }
template <bool AG, int N, class T> DI void waitx(T& d) {
  if constexpr (AG) asm volatile("s_waitcnt vmcnt(%1)" : "+a"(d) : "i"(cap63(N))); else asm volatile("s_waitcnt vmcnt(%1)" : "+v"(d) : "i"(cap63(N)));
}
template <bool SW, bool BA, bool Z, bool NOP = false> DI void mm(f32x16& acc, const bf16x8& a, const bf16x8& b) {
#define MM_(OPS, C, OUT, BC) do { if constexpr (NOP) asm volatile("s_nop 1\n\tv_mfma_f32_32x32x16_bf16 %0, " OPS ", " C : OUT(acc) : "v"(a), BC(b)); \
                                  else asm volatile("v_mfma_f32_32x32x16_bf16 %0, " OPS ", " C : OUT(acc) : "v"(a), BC(b)); } while (0)
  if constexpr (Z) {
    if constexpr (SW) { if constexpr (BA) MM_("%2, %1", "0", "=&v", "a"); else MM_("%2, %1", "0", "=&v", "v"); }
    else { if constexpr (BA) MM_("%1, %2", "0", "=&v", "a"); else MM_("%1, %2", "0", "=&v", "v"); }
  } else {
    if constexpr (SW) { if constexpr (BA) MM_("%2, %1", "%0", "+v", "a"); else MM_("%2, %1", "%0", "+v", "v"); }
    else { if constexpr (BA) MM_("%1, %2", "%0", "+v", "a"); else MM_("%1, %2", "%0", "+v", "v"); }
  }
#undef MM_
}
DI void mdone(f32x16& a) { asm volatile("s_nop 11" : "+v"(a)); }
DI void mdone(f32x16& a, f32x16& b) { asm volatile("s_nop 11" : "+v"(a), "+v"(b)); }
DI void mdone(f32x16& a, f32x16& b, f32x16& c, f32x16& d) { asm volatile("s_nop 11" : "+v"(a), "+v"(b), "+v"(c), "+v"(d)); }
template <class T> DI T opq_v(T v) { asm volatile("" : "+v"(v)); return v; }
template <int OFF> DI void ldx0(bf16x8& d, unsigned voff, const char* sb) { asm volatile("global_load_dwordx4 %0, %1, %2 offset:%3" : "=v"(d) : "v"(voff), "s"(sb), "i"(OFF)); }
template <int OFF> DI void ldx1m(bf16x8& d, unsigned voff, const char* sb, unsigned long long m) {
  asm volatile("s_mov_b64 exec, %4\n\tglobal_load_dwordx4 %0, %1, %2 offset:%3 sc1\n\ts_mov_b64 exec, -1" : "=v"(d) : "v"(voff), "s"(sb), "i"(OFF), "s"(m));
}
template <bool AG> DI void ds_read2(bf16x8& d, unsigned addr) {
  if constexpr (AG) asm volatile("ds_read2_b64 %0, %1 offset1:2" : "=a"(d) : "v"(addr) : "memory"); else asm volatile("ds_read2_b64 %0, %1 offset1:2" : "=v"(d) : "v"(addr) : "memory");
}
template <bool AG> DI void waitl(bf16x8& d) { if constexpr (AG) asm volatile("s_waitcnt lgkmcnt(0)" : "+a"(d)::"memory"); else asm volatile("s_waitcnt lgkmcnt(0)" : "+v"(d)::"memory"); }
template <int OFF> DI void ds_write_a(unsigned addr, const bf16x8& v) { asm volatile("ds_write_b128 %0, %1 offset:%2" ::"v"(addr), "a"(v), "i"(OFF) : "memory"); }
template <int N, class T> DI void wait2(T& a, T& b) { asm volatile("s_waitcnt vmcnt(%2)" : "+v"(a), "+v"(b) : "i"(cap63(N))); }
DI void set_flag(char* flag, u64 tag) {
  unsigned z = 0;
  asm volatile("s_mov_b64 exec, 1\n\tglobal_atomic_swap_x2 %0, %1, %2 sc1\n\ts_mov_b64 exec, -1" ::"v"(z), "v"(tag), "s"(flag) : "memory");
}
DI void ld8_1(u64& d, unsigned voff, const char* sb) { asm volatile("global_load_dwordx2 %0, %1, %2 sc1" : "=v"(d) : "v"(voff), "s"(sb) : "memory"); }
template <int NL> DI void rd_flag(u64& v, unsigned voff, const char* sb) {
  const u64 zero = 0;
  if constexpr (NL == 64) asm volatile("global_atomic_or_x2 %0, %1, %3, %2 sc0 sc1" : "+v"(v) : "v"(voff), "s"(sb), "v"(zero) : "memory");
  else asm volatile("s_mov_b64 exec, %4\n\tglobal_atomic_or_x2 %0, %1, %3, %2 sc0 sc1\n\ts_mov_b64 exec, -1" : "+v"(v) : "v"(voff), "s"(sb), "v"(zero), "s"((1ull << (NL & 63)) - 1) : "memory");
}
template <int NL> DI void poll(const char* base, unsigned voff, u64 tag) {
  for (int spin = 0; spin < (1 << 15); ++spin) {
    u64 v = tag;
    rd_flag<NL>(v, voff, base);
    waitx<false, 0>(v);
    if (__all(v == tag)) break;
  }
  asm volatile("" ::: "memory");
}

template <int RBX> DI void block_map(int& rb, int& cb) {
  const int x = blockIdx.x & 7, i = blockIdx.x >> 3;
  constexpr int CBX = 32 / RBX, GC = 16 / CBX;
  rb = RBX * (x / GC) + i / CBX; cb = CBX * (x % GC) + i % CBX;
}

DI float a_fma(float a, float b, float c) { float d; asm volatile("v_fma_f32 %0, %1, %2, %3" : "=v"(d) : "v"(a), "v"(b), "v"(c)); return d; }
DI float a_fms(float a, float b, float c) { float d; asm volatile("v_fma_f32 %0, %1, -%2, %3" : "=v"(d) : "v"(a), "v"(b), "v"(c)); return d; }
DI void a_exp(float& x) { asm volatile("v_exp_f32 %0, %0" : "+v"(x)); }
DI void a_add(float& s, float x) { asm volatile("v_add_f32 %0, %0, %1" : "+v"(s) : "v"(x)); }
DI void a_mul(float& x, float s) { asm volatile("v_mul_f32 %0, %0, %1" : "+v"(x) : "v"(s)); }
DI float a_max3(float a, float b, float c) { float d; asm volatile("v_max3_f32 %0, %1, %2, %3" : "=v"(d) : "v"(a), "v"(b), "v"(c)); return d; }
DI float a_min3(float a, float b, float c) { float d; asm volatile("v_min3_f32 %0, %1, %2, %3" : "=v"(d) : "v"(a), "v"(b), "v"(c)); return d; }

DI void pin2(f32x16& a, f32x16& b) { asm volatile("" : "+v"(a), "+v"(b)); }
DI bf16x8 cs_frag(float c) {
  const float hi = rbf(c), mid = rbf(c - hi), lo = (c - hi) - mid;
  return __builtin_bit_cast(bf16x8, u32x4{cvtpk(hi, mid), cvtpk(lo, 0.f), 0u, 0u});
}
template <bool CS> struct Red {
  bf16x8 p[4];
  float t[4], a, b, sd;
  template <int K> DI void run(int lane) {
    if constexpr (K == 0) asm volatile("" : "+v"(p[0]), "+v"(p[1]), "+v"(p[2]), "+v"(p[3]));
    if constexpr (K < 3) {
      const f32x4 u = __builtin_bit_cast(f32x4, p[K + 1]), v = __builtin_bit_cast(f32x4, p[0]);
#pragma unroll
      for (int e = 0; e < 4; ++e) { if (K == 0) t[e] = v[e]; a_add(t[e], u[e]); }
    }
    if constexpr (K == 3) {
      auto x = __builtin_amdgcn_permlane16_swap(__float_as_uint(t[0]), __float_as_uint(t[1]), false, false);
      auto y = __builtin_amdgcn_permlane16_swap(__float_as_uint(t[2]), __float_as_uint(t[3]), false, false);
      a = opq_v(__uint_as_float(x[0]) + __uint_as_float(x[1])); b = opq_v(__uint_as_float(y[0]) + __uint_as_float(y[1]));
    }
    if constexpr (K == 4) { auto x = __builtin_amdgcn_permlane32_swap(__float_as_uint(a), __float_as_uint(b), false, false); a = opq_v(__uint_as_float(x[0]) + __uint_as_float(x[1])); }
    if constexpr (K == 5) {
      if constexpr (CS) a = lane_get(a, (lane >> 2) + ((lane & 3) << 4));
      else { const int src = ((lane & 31) >> 1) + ((lane & 1) << 5); b = lane_get(a, src + 16); a = lane_get(a, src); }
    }
    if constexpr (CS) {
      if constexpr (K == 8) { t[0] = opq_v(rbf(a)); t[1] = opq_v(a - t[0]); }
      if constexpr (K == 9) { const float mid = rbf(t[1]); p[0] = opq_v(__builtin_bit_cast(bf16x8, u32x4{cvtpk(t[0], mid), cvtpk(t[1] - mid, 0.f), 0u, 0u})); }
    } else {
      if constexpr (K == 8) { const float m = a * (1.f / D_); b = opq_v(fmaxf(b * (1.f / D_) - m * m, 0.f)); a = opq_v(rbf(m)); }
      if constexpr (K == 9) b = opq_v(rbf(rbf(b) + 1e-5f));
      if constexpr (K == 10) { sd = opq_v(rbf(__builtin_amdgcn_sqrtf(b))); b = opq_v(__builtin_amdgcn_rcpf(sd)); }
    }
  }
};
DI void pin4(bf16x8 (&p)[4]) { asm volatile("" : "+v"(p[0]), "+v"(p[1]), "+v"(p[2]), "+v"(p[3])); }
DI bf16x8 mean_frag(float mean, bool on) { const float m = on ? -mean : 0.f; return __builtin_bit_cast(bf16x8, u32x4{cvtpk(m, m), cvtpk(m, 0.f), 0u, 0u}); }

#define STAMP(i)

constexpr int CODE_KB = 50;
constexpr int SLOT = 16384, XL = 12;
struct WinPtr {
  unsigned b[3];
  DI void init(unsigned a) { sfor<0, 3>([&](auto i) { b[i] = a + i * 65536u; asm volatile("" : "+v"(b[i])); }); }
  template <int OFF> DI bf16x8 frag() const { return *(LAS const bf16x8*)((LAS const char*)(uintptr_t)b[OFF >> 16] + (OFF & 65535)); }
};
struct Ctx { unsigned xv; WinPtr p; };
#define IC(n) std::integral_constant<int, (n)>{}

template <int S> DI void ld_x(bf16x8 (&x)[64], const Ctx& c, const char* xb) { ldx1<false, (S % 8) * 1024 - 4096>(x[S], c.xv, xb + (S / 8) * 8192 + 4096); }
template <class H> constexpr int g64_wait(int s) {
  int n = 0;
  const int t0 = s - XL;
  if (t0 < 0) n += XL - 1 - s; else n += H::n(t0);
  for (int t = t0 < 0 ? 0 : t0 + 1; t < s; ++t) n += (t + XL < 64 ? 1 : 0) + H::n(t);
  return n;
}
constexpr int a_slot(int g);
template <bool MAPA, bool ZERO, class H, class FX, class FA>
DI void gemm64(const Ctx& c, bf16x8 (&x)[64], const char* xb, f32x16& acc0, f32x16& acc1, FX&& hook, FA&& aux) {
  bf16x8 f0[3], f1[3];
  auto off = [](int s) constexpr { return (MAPA ? a_slot(24 + s / 8) : s / 8) * SLOT + (s % 8) * 2048; };
  auto rd0 = [&](auto s) { f0[s % 3] = c.p.template frag<off(s)>(); };
  auto rd1 = [&](auto s) { f1[s % 3] = c.p.template frag<off(s) + 1024>(); };
  rd0(IC(0)); rd1(IC(0)); rd0(IC(1)); rd1(IC(1));
  sfor<0, 64>([&](auto s) {
    waitx<false, g64_wait<H>(s)>(x[s]);
    SBAR();
    mm<false, false, ZERO && s == 0>(acc0, f0[s % 3], x[s]);
    SBAR();
    if constexpr (s + 2 < 64) rd0(IC(s + 2));
    if constexpr (s + XL < 64) ld_x<s + XL>(x, c, xb);
    aux(IC(2 * s));
    SBAR();
    mm<false, false, ZERO && s == 0>(acc1, f1[s % 3], x[s]);
    SBAR();
    if constexpr (s + 2 < 64) rd1(IC(s + 2));
    hook(s);
    aux(IC(2 * s + 1));
    SBAR();
  });
  mdone(acc0, acc1);
}
struct HookP6 { static constexpr int n(int s) { return s >= 52 ? 1 : 0; } };
struct HookP3 { static constexpr int n(int s) { return s >= 52 ? 1 : 0; } };
struct HookP4 { static constexpr int n(int s) { return s == 52 || s == 6 || (s >= 8 && s < 12) ? 1 : 0; } };

DI void res_to_acc(const u32x4 (&xr)[4], f32x16& a0, f32x16& a1) {
#pragma unroll
  for (int e = 0; e < 4; ++e) {
    a0[2 * e] = bflo(xr[0][e]); a0[2 * e + 1] = bfhi(xr[0][e]); a0[8 + 2 * e] = bflo(xr[1][e]); a0[9 + 2 * e] = bfhi(xr[1][e]);
    a1[2 * e] = bflo(xr[2][e]); a1[2 * e + 1] = bfhi(xr[2][e]); a1[8 + 2 * e] = bflo(xr[3][e]); a1[9 + 2 * e] = bfhi(xr[3][e]);
  }
  asm volatile("" : "+v"(a0), "+v"(a1));
}
DI void store_stats(int r, const u32x4 (&xr)[4], char* stp) {
  float s1 = 0.f, s2 = 0.f, s3 = 0.f, s4 = 0.f;
#pragma unroll
  for (int i = 0; i < 4; ++i)
#pragma unroll
    for (int e = 0; e < 4; e += 2) {
      s1 = dot2(xr[i][e], 0x3f803f80u, s1); s2 = dot2(xr[i][e], xr[i][e], s2);
      s3 = dot2(xr[i][e + 1], 0x3f803f80u, s3); s4 = dot2(xr[i][e + 1], xr[i][e + 1], s4);
    }
  st2_1(f32x2{swap_add(s1 + s3), swap_add(s2 + s4)}, (unsigned)(r * 8), stp);
}
DI void store_x(const Ctx& c, int r, const f32x16& a0, const f32x16& a1, u32x4 (&xr)[4], char* dst, char* stp) {
  xr[0] = __builtin_bit_cast(u32x4, pack8<0>(a0)); st4_1<0>(xr[0], c.xv, dst);
  xr[1] = __builtin_bit_cast(u32x4, pack8<1>(a0)); st4_1<1024>(xr[1], c.xv, dst);
  xr[2] = __builtin_bit_cast(u32x4, pack8<0>(a1)); st4_1<2048>(xr[2], c.xv, dst);
  xr[3] = __builtin_bit_cast(u32x4, pack8<1>(a1)); st4_1<3072>(xr[3], c.xv, dst);
  store_stats(r, xr, stp);
}

template <class FX> DI void hedge_exp(const f32x16& p, float sc, f32x16& ep, f32x16& en, FX&& hook) {
  float mx = fmaxf(p[0], p[1]), mn = fminf(p[0], p[1]);
#pragma unroll
  for (int i = 2; i < 16; i += 2) { mx = fmaxf(fmaxf(mx, p[i]), p[i + 1]); mn = fminf(fminf(mn, p[i]), p[i + 1]); }
  mx = swap_max(mx) * sc; mn = swap_min(mn) * sc;
  sfor<0, 16>([&](auto i) {
    SBAR();
    hook(i);
    constexpr int e = i;
    float a = __builtin_amdgcn_exp2f(__builtin_fmaf(p[e], sc, -mx)), b = __builtin_amdgcn_exp2f(__builtin_fmaf(p[e], -sc, mn));
    asm volatile("" : "+v"(a), "+v"(b));
    ep[e] = a; en[e] = b;
  });
  SBAR();
}

constexpr int a_slot(int g) { constexpr int m[32] = {0, 1, 2, 3, 4, 0, 1, 2, 5, 6, 7, 5, 3, 4, 6, 7, 8, 5, 3, 4, 6, 7, 0, 1, 2, 5, 3, 4, 6, 7, 0, 1}; return m[g]; }
constexpr int a_need(int g) { return g < 8 ? 8 * g - 2 : (g < 16 ? 16 * (g - 8) - (g > 12 ? 10 : 2) : 128 + 8 * (g - 16) - 2); }
constexpr bool a_bar(int s) { return (s + 2) % 8 == 0 && s >= 14 && s < 190; }
constexpr int a_pre(int n) { return n < 20 ? n : n + 12; }
constexpr int QP = 128 + 24, QC = 128 + 36;
constexpr int a_piece(int s) {
  constexpr int t[][3] = {{20, 5, 1}, {24, 6, 1}, {28, 11, 1}, {34, 7, 1}, {46, 12, 1}, {54, 13, 1}, {64, 14, 2}, {72, 15, 2}, {80, 16, 2}, {88, 17, 2}, {96, 18, 2},
                          {112, 19, 2}, {128, 20, 1}, {132, 21, 1}, {136, 22, 1}, {140, 23, 1}, {144, 24, 1}, {150, 25, 1}, {158, 26, 1}};
  for (auto& e : t) if (s >= e[0] && s < e[0] + 4 * e[2] && (s - e[0]) % e[2] == 0) return e[1] * 4 + (s - e[0]) / e[2];
  return -1;
}
constexpr int a_first(int s) { constexpr int g[8] = {0, 8, 1, 2, 9, 3, 4, 10}; return s < 12 ? g[(s + 20) / 4] * 4 + (s + 20) % 4 : -1; }
constexpr int a_park(int s) { return s == 148 || s == 149 ? s - 148 : (s >= 154 && s < 158 ? s - 152 : (s == 162 || s == 163 ? s - 156 : -1)); }
constexpr bool a_side(int s, bool first) { return (s >= 2 && s < (first ? 14 : 6)) || s == 40 || (s >= 104 && s < 112 && s % 2 == 0) || (s >= 120 && s < 128 && s % 2 == 0) || a_park(s) >= 0; }
struct SchedA {
  int xw[64], bw[192], sw[64], fw, cw, pw, zw, lw[32];
  constexpr SchedA(bool first) : xw{}, bw{}, sw{}, fw(0), cw(0), pw(0), zw(0), lw{} {
    int pos = 0, xpos[64] = {}, wpos[32] = {}, spos[192] = {}, fpos = 0, stpos = 0, ppos = 0, lpos[34] = {};
    if (first) { for (int s = -20; s < 0; ++s) wpos[a_first(s) / 4] = ++pos; pos += 2; }
    else for (int s = 0; s < XL; ++s) xpos[s] = ++pos;
    for (int s = 0; s < 192; ++s) {
      if (s == 128) { cw = pos + 2 - spos[126]; pos += 3; stpos = pos; }
      if (a_bar(s) || s == 6) {
        bw[s] = 63;
        for (int g = 0; g < 24; ++g) if (a_need(g) == s && wpos[g] && pos - wpos[g] < bw[s]) bw[s] = pos - wpos[g];
        if (s == 54 && pos - spos[40] < bw[s]) bw[s] = pos - spos[40];
        if (s == 142) { if (pos - stpos < bw[s]) bw[s] = pos - stpos; ++pos; }
      }
      if (s == 20 || s == 24 || s == 28) sw[s] = pos - spos[s - 15];
      if (s == 78) fw = pos - fpos;
      if (s == QC) pw = pos - ppos;
      if (s < 64) xw[s] = pos - xpos[s];
      if (!first && s + XL < 64) xpos[s + XL] = ++pos;
      if (s == 65) fpos = ++pos;
      if (s == QP) ppos = ++pos;
      if (s >= QC && s < QC + 17) lpos[2 * (s - QC)] = ++pos;
      if (a_piece(s) >= 0) wpos[a_piece(s) / 4] = ++pos;
      if (a_side(s, first)) spos[s] = ++pos;
      if (s >= QC && s < QC + 17) lpos[2 * (s - QC) + 1] = ++pos;
      if (first && a_first(s) >= 0) wpos[a_first(s) / 4] = ++pos;
      if (s == 175) zw = pos - lpos[1];
      if (s >= 176) lw[s - 176] = pos - lpos[2 + s - 176];
    }
    for (int i = 16; i < 32; ++i) lw[i] = pos - lpos[2 + i];
  }
};
constexpr int cmin(int a, int b) { return a < b ? a : b; }

#define R_OA "v[64:79]"
#define R_OB "v[80:95]"
#define S0_(n, q, m) "v_mfma_f32_32x32x16_bf16 v[" #n ":" #n "+15], v[208:211], v[" #q ":" #q "+3], v[" #m ":" #m "+15]\n\t"
#define S1_(n, q) "v_mfma_f32_32x32x16_bf16 v[" #n ":" #n "+15], v[212:215], v[" #q "+4:" #q "+7], v[" #n ":" #n "+15]\n\t"
#define PV0_(o, p) "v_smfmac_f32_32x32x32_bf16 v[" #o ":" #o "+15], v[216:219], v[" #p ":" #p "+7], v236\n\t"
#define PV1_(o, p) "v_smfmac_f32_32x32x32_bf16 v[" #o ":" #o "+15], v[220:223], v[" #p "+8:" #p "+15], v236\n\t"
#define L0_(l, p) "v_smfmac_f32_16x16x64_bf16 v[" #l ":" #l "+3], v[224:227], v[" #p ":" #p "+7], v236\n\t"
#define L1_(l, p) "v_smfmac_f32_16x16x64_bf16 v[" #l ":" #l "+3], v[224:227], v[" #p "+8:" #p "+15], v236\n\t"
#define E_(e, i) "v_exp_f32 v[" #e "+" #i "], v[" #e "+" #i "]\n\t"
#define RD_(d, off) "ds_read_b128 v[" #d ":" #d "+3], v237 offset:" #off "\n\t"
#define HALF_(e, n, q, m, p, o, l, X0, X1, X2, X3, X4, X5) \
  S0_(n, q, m) X0 E_(e, 0) E_(e, 1) E_(e, 2) S1_(n, q) X1 E_(e, 3) E_(e, 4) E_(e, 5) PV0_(o, p) X2 E_(e, 6) E_(e, 7) E_(e, 8) \
  PV1_(o, p) X3 E_(e, 9) E_(e, 10) E_(e, 11) L0_(l, p) X4 E_(e, 12) E_(e, 13) L1_(l, p) X5 E_(e, 14) E_(e, 15)
#define LG2_ "s_waitcnt lgkmcnt(2)\n\t"
#define LG0_ "s_waitcnt lgkmcnt(0)\n\t"
#define STEP_(b, nb, kv, wo, Y0, Y1) \
  HALF_(128, 160, 200, 112, 176, 80, 232, "", "", RD_(208, b + 2048), RD_(212, b + 3072), RD_(216, b), RD_(220, b + 1024) LG2_) \
  HALF_(160, 144, 192, 96, 128, 64, 228, "", LG0_, "s_add_u32 m0, s44, " #nb "\n\t", "", "global_load_lds_dwordx4 v" #kv ", s[40:41] sc1\n\t", \
        "global_load_lds_dwordx4 v" #kv ", s[40:41] offset:1024 sc1\n\t") \
  HALF_(144, 176, 200, 112, 160, 80, 232, Y0, "", RD_(208, b + 6144), RD_(212, b + 7168), RD_(216, b + 4096), RD_(220, b + 5120) LG2_) \
  HALF_(176, 128, 192, 96, 144, 64, 228, "s_mov_b32 m0, s45\n\t", LG0_, "global_load_lds_dwordx4 v240, s[42:43] offset:" #wo " sc1\n\t", Y1, \
        "s_waitcnt vmcnt(4)\n\t", "s_barrier\n\t")
DI float max16(const f32x16& s) {
  float mx = s[0];
#pragma unroll
  for (int e = 1; e < 16; ++e) mx = fmaxf(mx, s[e]);
  return mx;
}
typedef __attribute__((ext_vector_type(8))) unsigned u32x8;
typedef __attribute__((ext_vector_type(16))) unsigned u32x16;

__global__ __launch_bounds__(256, 1) void k_main(const char* xin, const char* wqkv, const char* wphi, const char* woA, const char* wq, const char* wk, const char* wv,
                                                 const char* woB, char* ws, char* out) {
  __shared__ __attribute__((aligned(16))) char smem_[163840];
  LAS char* const smem = (LAS char*)smem_;
  const unsigned lds0 = (unsigned)(uintptr_t)smem;
  Ctx c;
  int lane = threadIdx.x & 63, w = __builtin_amdgcn_readfirstlane(threadIdx.x >> 6), r = lane & 31, h = lane >> 5, rb, cb;
  block_map<4>(rb, cb);
  int tt = rb * 4 + w, blk = (lane >> 4) & 1, q4 = (lane & 15) >> 2, p4 = lane & 3;
  c.xv = lane * 16; c.p.init(lds0 + c.xv);
  char* flags = ws + WS_FLAG;
  char* col = ws + WS_COL + (size_t)cb * 2 * MB;
  const int ut = rb * 4 + (cb >> 2), ty = cb & 3;
  char* act = ws + (size_t)rb * 2 * MB + w * 65536;
  char* acto = act + cb * 4096;
  char* fact = flags + FL_ACT + tt * 128;
  char* facto = fact + cb * 8;
  char* stt = ws + WS_ST + tt * 4096;
  char* stto = stt + cb * 256;
  char* kvp = col + C_KVP + w * 32768;
  char* zp = col + C_ZP + w * 2048;
  char* fkvp = flags + FL_KVP + (cb * 4 + w) * 128;
  char* fwl = flags + FL_W + 2048 + cb * 128;
  char* kvf = ws + WS_KVF;
  char* fkv = flags + FL_KV;
  const char* xu_p = ws + (size_t)rb * 2 * MB + 2 * STG + (cb >> 2) * 65536 + w * 16384 + 4096;
  const char* su_p = ws + WS_ST + 256 * KB + ut * 4096;
  const char* wkv = ((cb & 2) ? wv : wk) + w * 256 * 64;
  const char* wph = wphi + cb * 4096 + w * 1024;
  u64 tag = 0;
  u32x4 xr[4] = {};
  char* colw = col + w * 4096;
  unsigned ldsw = lds0 + w * 4096;
  auto wt = [&](int m, int ch) { return colw + C_WT + m * 131072 + ch * SLOT; };
  auto pc_w = [&](int m, int slot, int ch, auto i) { if constexpr (i == 0) dma_c<0>(ldsw + slot * SLOT, c.xv, wt(m, ch)); else dma_n<i * 1024>(c.xv, wt(m, ch)); };
  auto pc_a = [&](auto n) { constexpr int g = n / 4; pc_w(g < 8 ? 1 : (g < 16 ? 2 : (g < 24 ? 0 : 3)), a_slot(g), g % 8, IC(n % 4)); };
  u64 csm = ~0ull;
  float csv[4] = {};
  auto ld_cs = [&](bf16x8& d, int m, auto i) { ldx1m<i * 1024>(d, c.xv, col + C_CS + m * 4096, csm); };
  auto raw_in = [&](auto j, int slot) {
    const int hi = rb >> 3, pitch = (j == 0 || (j == 1 && !hi)) ? 6144 : 2048, row = lane >> 3;
    const char* src = (j == 0 ? wqkv + 2048 + hi * 2048 : (j == 1 ? (hi ? woA : wqkv) : (hi ? woB : wq))) + cb * 128 + (size_t)(rb & 7) * 128 * pitch;
    const unsigned v0 = (unsigned)((32 * w + row) * pitch + ((lane & 7) ^ (((row >> 1) & 1) << 2)) * 16);
    sfor<0, 4>([&](auto i) { dma<i * 1024>(lds0 + slot * SLOT + w * 4096, v0 + i * (8 * pitch - 1024), src); });
  };
  auto raw_out = [&](auto j, int slot) {
    const int hi = rb >> 3, ch = rb & 7, m = j == 0 ? 1 + hi : (j == 1 ? 3 * hi : 4 + hi);
    LAS const char* p = smem + slot * SLOT + (w >> 1) * 8192 + (4 * h + q4) * 128 + (((((q4 >> 1) ^ w) & 1) << 2) + 2 * blk + (p4 >> 1)) * 16 + 8 * (p4 & 1);
    const bool isv = m == 2;
    char* dst = col + C_WT + m * 131072 + (isv ? (w & 1) * 65536 + ch * 8192 + (w >> 1) * 4096 : ch * SLOT + (w >> 1) * 8192 + (w & 1) * 1024);
    float cs = 0.f;
    sfor<0, 4>([&](auto s) {
      const u32x4 f = __builtin_bit_cast(u32x4, cat8(trread(p + s * 2048), trread(p + s * 2048 + 1024)));
      st4_1<0>(f, c.xv, dst + s * (isv ? 1024 : 2048));
#pragma unroll
      for (int e = 0; e < 4; ++e) cs = dot2(f[e], 0x3f803f80u, cs);
    });
    st1_1(swap_add(cs), (unsigned)(r * 4), col + C_CS + m * 4096 + (ch * 2 + (w >> 1)) * 256 + (w & 1) * 128);
  };
  const int w0 = w; const unsigned ldsw0 = ldsw;
#define REFRESH() do { \
    w = w0; ldsw = ldsw0; \
    asm volatile("" : "+s"(w), "+s"(tag), "+s"(act), "+s"(acto), "+s"(fact), "+s"(facto), "+s"(stt), "+s"(stto), "+s"(col), "+s"(colw), "+s"(ldsw)); \
    asm volatile("" : "+v"(lane)); \
    r = lane & 31; h = lane >> 5; blk = (lane >> 4) & 1; q4 = (lane & 15) >> 2; p4 = lane & 3; c.xv = lane * 16; c.p.init(lds0 + c.xv); \
  } while (0)
  int l = 0;
  float mean, rstd;
  Red<true> cw;
  bf16x8 park[24];
      constexpr int NA = 60;
      constexpr SchedA SQ(false), S1(true);
      bf16x8 x[64], f0[3], f1[3], f2[5], wf[4], kp[4], pp[32], zq[2], sel[2], mf0, mf1;
      Red<false> st; Red<true> ck, cv, cq;
      f32x16 acc[6], p[2], kvs;
      u64 fv;
      float mx[2], mn[2], e[16], se, sce;
      unsigned arow;
      LAS char* pk_img = smem;
      LAS char* v_img = smem + 2 * SLOT;
      LAS char* sh = smem + 9 * SLOT + 4096;
      auto issue_x = [&](auto s) { ldx1<(s < NA), (s % 8) * 1024 - 4096>(x[s], c.xv, act + (s / 8) * 8192 + 4096); };
      auto ld_park = [&](auto i) { ldx1<true, (i % 4) * 1024>(park[i], c.xv, wt(4, 2 + i / 4)); };
      auto ldp = [&](auto i) {
        if constexpr (i >= 2) ldx1<true, ((i - 2) % 8) * 1024 - 4096>(pp[i - 2], c.xv, kvp + ((i - 2) / 8) * 8192 + 4096); else ldx1<false, i * 1024>(zq[i], c.xv, zp);
      };
      auto rd_wf = [&](auto k) { LAS const char* wp = smem + 9 * SLOT + (4 * h + q4) * 64 + blk * 32 + p4 * 8 + k * 1024; wf[k] = cat8(trread(wp), trread(wp + 512)); };
      auto rd01 = [&](auto s, auto t) {
        if constexpr (s < 64 || (s >= 128 && s < 192)) { const bf16x8 f = c.p.template frag<a_slot(s / 8) * SLOT + (s % 8) * 2048 + t * 1024>(); if constexpr (t == 0) f0[s % 3] = f; else f1[s % 3] = f; }
      };
      auto rd2 = [&](auto s) { if constexpr (s < 128) f2[s % 5] = c.p.template frag<a_slot(8 + s / 16) * SLOT + (s % 16) * 1024>(); };
      auto v_out = [&](auto t, auto hf) {
        *(LAS bf16x8*)(v_img + t * 8192 + w * 2048 + hf * 1024 + lane * 16) = pack8<hf>(acc[2 + t]);
      };
      auto sum_mm = [&](auto i) { waitx<true, SQ.lw[i]>(pp[i]); mm<false, true, i == 0>(kvs, sel[i & 1], pp[i]); };
      auto kmap = [&](auto gi) {
        constexpr int g = gi;
        if constexpr (g == 4) { pin2(acc[0], acc[1]); }
        if constexpr (g >= 6 && g < 10) mm<false, false, g % 2 == 0>(p[(g - 6) / 2], wf[g - 6], kp[g - 6]);
        if constexpr (g >= 4 && g < 8) kp[g - 4] = opq_v(pack8<g % 2>(acc[(g - 4) / 2]));
        if constexpr (g == 10) mm<true, false, false>(acc[3], cv.p[0], mf1);
        if constexpr (g >= 12 && g < 24) {
          constexpr int hd = (g - 12) / 6, t = (g - 12) % 6;
          const f32x16& q = p[hd];
          if constexpr (t == 0) { mx[hd] = a_max3(q[0], q[1], q[2]); mn[hd] = a_min3(q[0], q[1], q[2]); mx[hd] = a_max3(mx[hd], q[3], q[4]); mn[hd] = a_min3(mn[hd], q[3], q[4]); }
          if constexpr (t >= 1 && t < 4) {
            mx[hd] = a_max3(mx[hd], q[4 * t + 1], q[4 * t + 2]); mn[hd] = a_min3(mn[hd], q[4 * t + 1], q[4 * t + 2]);
            mx[hd] = a_max3(mx[hd], q[4 * t + 3], q[(4 * t + 4) & 15]); mn[hd] = a_min3(mn[hd], q[4 * t + 3], q[(4 * t + 4) & 15]);
          }
          if constexpr (t == 4) mx[hd] = opq_v(swap_max(mx[hd]) * -sce);
          if constexpr (t == 5) mn[hd] = opq_v(swap_min(mn[hd]) * sce);
        }
        if constexpr (g >= 24) {
          constexpr int grp = (g - 24) / 26, j = (g - 24) % 26, hd = grp >> 1, sg = grp & 1;
          if constexpr (j >= 1 && j <= 16) a_exp(e[j - 1]);
          if constexpr (j < 16) e[j] = sg ? a_fms(p[hd][j], sce, mn[hd]) : a_fma(p[hd][j], sce, mx[hd]);
          if constexpr (j == 2) se = e[0];
          if constexpr (j > 2 && j <= 17) a_add(se, e[j - 2]);
          if constexpr (j == 17) { se = opq_v(__builtin_amdgcn_rcpf(swap_add(se)) * rstd); if constexpr (grp < 2) v_out(IC(0), IC(grp)); }
          if constexpr (j >= 18) {
            constexpr int gq = (j - 18) / 2;
            if constexpr (j % 2 == 0) { a_mul(e[4 * gq], se); a_mul(e[4 * gq + 1], se); a_mul(e[4 * gq + 2], se); a_mul(e[4 * gq + 3], se); }
            else *(LAS u32x2*)((LAS char*)(uintptr_t)(arow ^ ((sg * 4 + gq) << 4)) + hd * 16384) = u32x2{cvtpk(e[4 * gq], e[4 * gq + 1]), cvtpk(e[4 * gq + 2], e[4 * gq + 3])};
          }
        }
      };
      auto keep = [&](Red<true>& q, int n) { csv[n] = l == 0 ? q.a : csv[n]; q.a = csv[n]; };
      auto aux = [&](auto s, auto i, auto fl) {
        constexpr int g1 = 3 * s + i, g3 = 2 * (s - 128) + i;
        if constexpr (s < 64) {
          if constexpr (g1 >= 60 && g1 < 71) st.template run<g1 - 60>(lane);
          if constexpr (g1 == 80) { if constexpr (fl) csv[0] = ck.a; else ck.a = csv[0]; }
          if constexpr (g1 == 92) { if constexpr (fl) csv[1] = cv.a; else cv.a = csv[1]; }
          if constexpr (g1 >= (fl ? 72 : 80) && g1 < 82) ck.template run<g1 - 72>(lane);
          if constexpr (g1 >= (fl ? 84 : 92) && g1 < 94) cv.template run<g1 - 84>(lane);
          if constexpr (g1 == 71) { mean = st.a; rstd = st.b; sce = opq_v(rstd * LOG2E); }
          if constexpr (g1 == 95) mf0 = opq_v(mean_frag(mean, h == 0));
          if constexpr (g1 == 96) mf1 = opq_v(mean_frag(mean, h == 1));
          if constexpr (g1 == 99) mm<false, false, false>(acc[0], ck.p[0], mf0);
          if constexpr (g1 == 100) mm<false, false, false>(acc[1], ck.p[0], mf1);
          if constexpr (g1 == 101) mm<true, false, false>(acc[2], cv.p[0], mf0);
          if constexpr (g1 == 130) *(LAS short*)(sh + 8704 + w * 64 + (r >> 4) * 32 + ((r >> 2) & 1) * 16 + ((r >> 3) & 1) * 8 + (r & 3) * 2) = (short)(__float_as_uint(st.sd) >> 16);
          if constexpr (g1 >= 168 && g1 < 172) rd_wf(IC(g1 - 168));
          if constexpr (g1 == 174) arow = opq_v((unsigned)(uintptr_t)pk_img + (w * 32 + r) * 128 + h * 8 + ((r & 7) << 4));
        } else if constexpr (s >= 128) {
          if constexpr (g3 == 8) keep(cq, 2);
          if constexpr (g3 == 24) keep(cw, 3);
          if constexpr (g3 < 10) cq.template run<g3>(lane);
          if constexpr (g3 == 12) mm<false, false, false>(acc[4], cq.p[0], mf0);
          if constexpr (g3 == 13) mm<false, false, false>(acc[5], cq.p[0], mf1);
          if constexpr (g3 >= 16 && g3 < 26) cw.template run<g3 - 16>(lane);
          if constexpr (g3 >= 32 && g3 < 40) {
            constexpr int n = (g3 - 32) % 4, t = (g3 - 32) / 4;
            const int kr = 16 * t + 8 * (n >> 1) + 4 * h + 2 * (n & 1);
            u32x4 sv = __builtin_bit_cast(u32x4, sel[t]); sv[n] = opq_v(r == kr ? 0x3f80u : (r == kr + 1 ? 0x3f800000u : 0u)); sel[t] = __builtin_bit_cast(bf16x8, sv);
          }
          if constexpr (g3 == 40) { sel[0] = opq_v(sel[0]); sel[1] = opq_v(sel[1]); }
          if constexpr (g3 == 42) *(LAS unsigned*)(sh + 8192 + 256 + lane * 4) = opq_v(0x3f803f80u);
          if constexpr (g3 == 95) {
            wait2<SQ.zw>(zq[0], zq[1]);
#pragma unroll
            for (int n = 0; n < 4; ++n) { e[n] = __builtin_bit_cast(f32x4, zq[0])[n]; a_add(e[n], __builtin_bit_cast(f32x4, zq[1])[n]); }
          }
          if constexpr (g3 == 96) {
            auto a = __builtin_amdgcn_permlane16_swap(__float_as_uint(e[0]), __float_as_uint(e[1]), false, false);
            auto b = __builtin_amdgcn_permlane16_swap(__float_as_uint(e[2]), __float_as_uint(e[3]), false, false);
            e[0] = opq_v(__uint_as_float(a[0]) + __uint_as_float(a[1])); e[1] = opq_v(__uint_as_float(b[0]) + __uint_as_float(b[1]));
          }
          if constexpr (g3 == 98) { auto a = __builtin_amdgcn_permlane32_swap(__float_as_uint(e[0]), __float_as_uint(e[1]), false, false); e[0] = opq_v(__uint_as_float(a[0]) + __uint_as_float(a[1])); }
          if constexpr (g3 == 100) e[1] = lane_get(e[0], lane ^ 8);
          if constexpr (g3 == 106)
            *(LAS short*)(sh + 8192 + w * 64 + (lane & 1) * 32 + ((lane >> 2) & 1) * 16 + ((lane >> 1) & 1) * 8 + (lane >> 4) * 2) = (short)cvtpk(e[0] + e[1], 0.f);
          if constexpr (g3 >= 116 && g3 < 124 && g3 % 2 == 0) rd_wf(IC((g3 - 116) / 2));
        }
      };
      auto step = [&](auto s, auto fl) {
        constexpr int ps = s / 64;
        constexpr bool first = fl;
        if constexpr (a_bar(s) || (first && s == 6)) wait_bar<(ps ? cmin(S1.bw[s], SQ.bw[s]) : (first ? S1.bw[s] : SQ.bw[s]))>();
        if constexpr (s == 20 || (first && (s == 24 || s == 28))) waitv<(first ? S1.sw[s % 64] : SQ.sw[s % 64])>();
        if constexpr (s == 78) { waitx<false, SQ.fw>(fv); if (!__all(fv == tag)) poll<16>(fwl, lane * 8, tag); }
        if constexpr (s == 142) set_flag(fkvp + rb * 8, tag);
        if constexpr (s == QC) { waitx<false, SQ.pw>(fv); if (!__all(fv == tag)) poll<16>(fkvp, lane * 8, tag); }
        if constexpr (s < 64 && !first) waitx<(s < NA), SQ.xw[s % 64]>(x[s % 64]);
        SBAR();
        if constexpr (ps != 1) mm<false, (s % 64 < NA), s % 64 == 0>(acc[2 * ps], f0[s % 3], x[s % 64]);
        else mm<true, (s % 64 < NA), s % 64 == 0>(acc[3], f2[s % 5], x[s % 64]);
        SBAR();
        rd01(IC(s + 2), IC(0));
        if constexpr (ps == 1) rd2(IC(s + 4));
        if constexpr (s + XL < 64 && !first) issue_x(IC(s + XL));
        if constexpr (s == 65) ld8_1(fv, (lane & 15) * 8, fwl);
        if constexpr (s == QP) { fv = tag; rd_flag<16>(fv, lane * 8, fkvp); }
        if constexpr (s >= QC && s < QC + 17) ldp(IC(2 * (s - QC)));
        aux(s, IC(0), fl);
        SBAR();
        if constexpr (ps != 1) mm<false, (s % 64 < NA), s % 64 == 0>(acc[2 * ps + 1], f1[s % 3], x[s % 64]);
        SBAR();
        rd01(IC(s + 2), IC(1));
        if constexpr (a_piece(s) >= 0) pc_a(IC(a_piece(s)));
        if constexpr (s >= 2 && s < 6) ldx1<false, (s - 2) * 1024>(st.p[s - 2], c.xv, stt);
        if constexpr (first && s >= 6 && s < 10) ld_cs(ck.p[s - 6], 1, IC(s - 6));
        if constexpr (first && s >= 10 && s < 14) ld_cs(cv.p[s - 10], 2, IC(s - 10));
        if constexpr (s == 40) dma<0>(lds0 + 9 * SLOT + w * 1024, c.xv, wph);
        if constexpr (s >= 104 && s < 112 && s % 2 == 0) ld_cs(cq.p[(s - 104) / 2], 0, IC((s - 104) / 2));
        if constexpr (s >= 120 && s < 128 && s % 2 == 0) ld_cs(cw.p[(s - 120) / 2], 4, IC((s - 120) / 2));
        if constexpr (a_park(s) >= 0) ld_park(IC(a_park(s)));
        if constexpr (s >= QC && s < QC + 17) ldp(IC(2 * (s - QC) + 1));
        aux(s, IC(1), fl);
        if constexpr (ps == 0) {
          SBAR();
          mm<true, (s < NA), s == 0>(acc[2], f2[s % 5], x[s]);
          SBAR();
          rd2(IC(s + 2));
          if constexpr (s >= 62) rd2(IC(s + 4));
          if constexpr (first && a_first(s) >= 0) pc_a(IC(a_first(s)));
          aux(s, IC(2), fl);
        }
        if constexpr (ps == 1) { kmap(IC(2 * (s - 64))); kmap(IC(2 * (s - 64) + 1)); }
        if constexpr (s >= 176) sum_mm(IC(s - 176));
        SBAR();
      };
      {
        STAMP(30);
        bf16x8 cf;
        { const char* pc; asm volatile("s_getpc_b64 %0" : "=s"(pc));
          ldx0<0>(cf, c.xv, pc + (((blockIdx.x >> 3) * 4 + w) % CODE_KB) * 1024); }
        raw_in(IC(0), 7);
        u64 ep; ld8_1(ep, 0u, ws + WS_EPOCH);
        bf16x8 xt[4];
        const char* xw = xin + (size_t)tt * 65536;
        sfor<0, 4>([&](auto i) { ldx0<i * 32>(xt[i], (unsigned)(r * 2048 + h * 16), xw + cb * 128); });
        { const unsigned z = 0, vo = (unsigned)(lane ? FL_W + cb * 128 : FL_ACT + tt * 128);
          asm volatile("s_mov_b64 exec, 3\n\tglobal_atomic_or %0, %1, %2 sc1\n\ts_mov_b64 exec, -1" ::"v"(vo), "v"(z), "s"(flags) : "memory"); }
        wait_bar<6>();
        raw_out(IC(0), 7);
        asm volatile("s_waitcnt vmcnt(6)" : "+v"(cf), "+v"(ep), "+v"(xt[0]), "+v"(xt[1]), "+v"(xt[2]), "+v"(xt[3]));
        sfor<0, 4>([&](auto i) { xr[i] = unperm(__builtin_bit_cast(u32x4, xt[i])); });
        store_stats(r, xr, stto);
        raw_in(IC(1), 8); raw_in(IC(2), 9);
        { u64 z = ((u64)__builtin_amdgcn_readfirstlane((unsigned)(ep >> 32)) << 32 | __builtin_amdgcn_readfirstlane((unsigned)ep)) + 0x9E3779B97F4A7C15ull;
          z = (z ^ (z >> 30)) * 0xBF58476D1CE4E5B9ull; z = (z ^ (z >> 27)) * 0x94D049BB133111EBull; tag = z ^ (z >> 31); asm volatile("" : "+s"(tag)); }
        waitv<8>();
        set_flag(facto, tag);
        bar();
        if (w == 0) set_flag(flags + FL_W + cb * 128 + rb * 8, tag);
        const unsigned xs = (unsigned)((lane >> 3) * 2048 + ((lane & 7) ^ (((lane >> 3) & 3) << 1)) * 16), xl = lds0 + w * 28672;
        const unsigned xo = xl + r * 128 + (r >> 3) * 16 + h * 8;
        auto x_in = [&](auto g) { sfor<0, 4>([&](auto i) { dma<i * 1040>(xl + (g % 6) * 4160, xs + i * (16384 - 1040), xw + g * 128); }); };
        sfor<0, 6>(x_in);
        sfor<0, 16>([&](auto g) {
          waitv<4 * (g < 10 ? 5 : 15 - g) + (g > 2 && g < 9 ? 10 : 0)>();
          sfor<4 * g, 4 * g + 4>([&](auto s) { ds_read2<(s < NA)>(x[s], xo + (g % 6) * 4160 + (((s % 4) ^ (r & 3)) << 5)); });
          sfor<4 * g, 4 * g + 4>([&](auto s) { waitl<(s < NA)>(x[s]); });
          if constexpr (g + 6 < 16) x_in(IC(g + 6));
          if constexpr (g == 2) { bar(); raw_out(IC(1), 8); raw_out(IC(2), 9); }
        });
        unsigned tch;
        {
          const int gw = blockIdx.x * 4 + w; const unsigned z = 0;
          const unsigned vo = (unsigned)(lane < 5 ? WS_KVF - WS_ST + (gw * 5 + lane) * 128 : WS_FLAG - WS_ST + gw * 128);
          poll<32>(flags, (unsigned)((lane & 15) * 8 + (blk ? FL_W + cb * 128 : FL_ACT + tt * 128)), tag);
          bar();
          if (w == 0) set_flag(fwl + rb * 8, tag);
          sfor<-20, 0>([&](auto n) { pc_a(IC(a_first(n))); });
          asm volatile("s_mov_b64 exec, 63\n\tglobal_atomic_or %0, %1, %2 sc1\n\ts_mov_b64 exec, -1" ::"v"(vo), "v"(z), "s"(ws + WS_ST) : "memory");
          const char* sm = (gw & 512) ? ((gw & 256) ? wk : wv) : wphi;
          asm volatile("global_load_dword %0, %1, %2" : "=v"(tch) : "v"((unsigned)((gw & 255) * 256 + lane * 4)), "s"(sm));
        }
        STAMP(31); STAMP(0); STAMP(1);
        wait_bar<14>();
        sfor<0, 2>([&](auto s) { rd01(s, IC(0)); rd01(s, IC(1)); rd2(s); });
        sfor<0, 64>([&](auto s) { step(s, std::true_type{}); });
        waitx<false, 63>(tch);
      }
  asm volatile("" : "+s"(w), "+s"(tag), "+s"(act), "+s"(acto), "+s"(fact), "+s"(facto), "+s"(stt), "+s"(stto), "+s"(col), "+s"(colw), "+s"(ldsw));
  asm volatile("" : "+s"(kvp), "+s"(zp), "+s"(fkvp), "+s"(fwl), "+s"(rb), "+s"(kvf), "+s"(fkv), "+s"(xu_p), "+s"(su_p), "+s"(wkv), "+s"(csm), "+s"(l));
  for (;;) {
    {
      REFRESH();
      asm volatile("" : "+s"(kvp), "+s"(zp), "+s"(fkvp), "+s"(fwl), "+s"(rb));
      STAMP(2);
      sfor<64, 128>([&](auto s) { step(s, std::false_type{}); });
      STAMP(19);
      mdone(acc[3]);
      v_out(IC(1), IC(0)); v_out(IC(1), IC(1));
      lds_bar();
      {
        const int hd = w >> 1, fh = w & 1;
        f32x16 kv, z;
        const int ch = fh * 4 + 2 * blk + (p4 >> 1), r0 = 4 * h + q4;
        LAS const char* pa = pk_img + hd * 16384 + r0 * 128 + ((ch ^ r0) << 4) + 8 * (p4 & 1);
        LAS const char* pb = v_img + hd * 8192 + lane * 16;
        bf16x8 a[8], b[8], d[8];
#pragma unroll
        for (int s = 0; s < 8; ++s) { a[s] = cat8(trread(pa + s * 2048), trread(pa + s * 2048 + 1024)); b[s] = *(LAS const bf16x8*)(pb + s * 1024); }
#pragma unroll
        for (int s = 0; s < 8; ++s) d[s] = *(LAS const bf16x8*)(sh + 8704 + s * 32 + h * 16);
        SBAR();
        sfor<0, 8>([&](auto s) { mm<false, false, s == 0>(kv, a[s], b[s]); });
        sfor<0, 8>([&](auto s) {
          mm<false, false, s == 0>(z, d[s], a[s]);
          if constexpr (s == 1) { mdone(kv); st4_1<0>(__builtin_bit_cast(u32x4, pack8<0>(kv)), c.xv, kvp + rb * 2048); }
          if constexpr (s == 2) { st4_1<1024>(__builtin_bit_cast(u32x4, pack8<1>(kv)), c.xv, kvp + rb * 2048); waitv<SQ.cw>(); }
          SBAR();
        });
        mdone(z);
        st1_1(z[0], (unsigned)(r * 4), zp + rb * 128);
      }
      STAMP(3);
      sfor<128, 192>([&](auto s) { step(s, std::false_type{}); });
      mdone(acc[4], acc[5]);
      STAMP(4);
      {
        bf16x8 pq[2][4], ka[8], za[8];
        f32x16 ep, en, nm[4], dn[2];
        float ai[2], bi[2];
        char* dst = acto + STG;
        sfor<0, 4>([&](auto k) { mm<false, false, k % 2 == 0, true>(p[k / 2], wf[k], pack8<k % 2>(acc[4 + k / 2])); });
        mdone(p[0], p[1]);
        hedge_exp(p[0], sce, ep, en, [&](auto i) { sum_mm(IC(16 + i)); });
        pq[0][0] = pack8<0>(ep); pq[0][1] = pack8<1>(ep); pq[0][2] = pack8<0>(en); pq[0][3] = pack8<1>(en);
        pin4(pq[0]);
        asm volatile("" : "+v"(kvs));
        *(LAS bf16x8*)(sh + w * 2048 + lane * 16) = pack8<0>(kvs); *(LAS bf16x8*)(sh + w * 2048 + 1024 + lane * 16) = pack8<1>(kvs);
        lds_bar();
        STAMP(5);
        LAS const char* zb = sh + 8192 + (r & 1) * 256 + h * 32;
#pragma unroll
        for (int k = 0; k < 8; ++k) { za[k] = *(LAS const bf16x8*)(zb + (k >> 1) * 64 + (k & 1) * 16); ka[k] = *(LAS const bf16x8*)(sh + k * 1024 + lane * 16); }
        auto coef = [&](auto hd) {
          const float a = __builtin_amdgcn_rcpf(dn[0][1]), b = __builtin_amdgcn_rcpf(dn[1][1]);
          const float inv = __builtin_amdgcn_rcpf(dn[0][0] * a + dn[1][0] * b + 1e-6f);
          ai[hd] = opq_v(a * inv); bi[hd] = opq_v(b * inv);
        };
        auto fin = [&](auto hd) {
          f32x16 o;
#pragma unroll
          for (int i = 0; i < 16; ++i) o[i] = nm[2 * hd][i] * ai[hd] + nm[2 * hd + 1][i] * bi[hd];
          st4_1<hd * 2048>(__builtin_bit_cast(u32x4, pack8<0>(o)), c.xv, dst);
          st4_1<hd * 2048 + 1024>(__builtin_bit_cast(u32x4, pack8<1>(o)), c.xv, dst);
        };
        hedge_exp(p[1], sce, ep, en, [&](auto i) {
          if constexpr (i < 4) mm<false, false, i % 2 == 0>(dn[i / 2], za[i], pq[0][i]);
          else if constexpr (i < 8) mm<false, false, i % 2 == 0>(nm[i / 2 - 2], ka[i - 4], pq[0][i - 4]);
          if constexpr (i % 2) pc_a(IC(108 + i / 2));
        });
        pq[1][0] = pack8<0>(ep); pq[1][1] = pack8<1>(ep); pq[1][2] = pack8<0>(en); pq[1][3] = pack8<1>(en);
        pin4(pq[1]); pin2(dn[0], dn[1]); pin2(nm[0], nm[1]);
        coef(IC(0));
        STAMP(6);
        sfor<0, 4>([&](auto k) { mm<false, false, k % 2 == 0, k == 0>(dn[k / 2], za[4 + k], pq[1][k]); if constexpr (k % 2) pc_a(IC(116 + k / 2)); });
        sfor<0, 4>([&](auto k) { mm<false, false, k % 2 == 0>(nm[2 + k / 2], ka[4 + k], pq[1][k]); if constexpr (k % 2) pc_a(IC(118 + k / 2)); });
        fin(IC(0));
        pin2(dn[0], dn[1]); pin2(nm[2], nm[3]);
        coef(IC(1));
        fin(IC(1));
        sfor<120, 124>(pc_a);
        waitv<4>();
        set_flag(facto + 8192, tag);
        sfor<124, 128>(pc_a); sfor<8, 24>(ld_park);
      }
    }
    STAMP(7);
    REFRESH();
    LAS char* reg = smem + 8 * SLOT;
    bf16x8 fu[16], cu;
    {
      f32x16 a0, a1; res_to_acc(xr, a0, a1);
      poll<16>(fact + 8192, lane * 8, tag);
      sfor<0, 24>([&](auto i) { waitx<true, 63>(park[i]); });
      STAMP(8);
      bf16x8 x[64];
      const char* xb = act + STG;
      sfor<0, XL>([&](auto s) { ld_x<s>(x, c, xb); });
      bar();
      asm volatile("" : "+s"(wkv));
      const unsigned kvv = (unsigned)((lane >> 1) * 64 + (cb & 1) * 32 + (lane & 1) * 16);
      const unsigned wa = ldsw + c.xv, wb = wa + 65536;
      auto unpark = [&](auto i) {
        constexpr int off = a_slot(26 + i / 4) * SLOT + (i % 4) * 1024;
        ds_write_a<(off & 65535)>(off < 65536 ? wa : wb, park[i]);
      };
      gemm64<true, false, HookP3>(c, x, xb, a0, a1, [&](auto s) {
        if constexpr (s == 33) bar();
        if constexpr (s >= 36 && s < 44) unpark(IC(s - 36));
        if constexpr (s >= 52 && s < 60) dma<0>(lds0 + 8 * SLOT + w * 8192 + (s - 52) * 1024, kvv, wkv + (s - 52) * 32 * 64);
        if constexpr (s >= 60) pc_w(4, a_slot(24), 0, IC(s - 60));
      }, [](auto) {});
      STAMP(9);
      store_x(c, r, a0, a1, xr, acto + 2 * STG, stto + 256 * 1024);
      waitv<5>();
      { LAS const char* p = reg + w * 8192 + (4 * h + q4) * 32 + p4 * 8;
        float t0 = 0.f, t1 = 0.f;
#pragma unroll
        for (int i = 0; i < 16; ++i) {
          fu[i] = cat8(trread(p + i * 512), trread(p + i * 512 + 256));
          const u32x4 f = __builtin_bit_cast(u32x4, fu[i]);
          t0 = dot2(f[0], 0x3f803f80u, t0); t1 = dot2(f[1], 0x3f803f80u, t1); t0 = dot2(f[2], 0x3f803f80u, t0); t1 = dot2(f[3], 0x3f803f80u, t1);
        }
        cu = opq_v(cs_frag(swap_add(t0 + t1))); }
      waitv<0>();
      set_flag(facto + 2 * 8192, tag);
      sfor<4, 8>([&](auto i) { pc_w(4, a_slot(24 + i / 4), i / 4, IC(i % 4)); });
      bar();
      sfor<8, 24>(unpark);
      lds_bar();
    }
    STAMP(10);
    REFRESH();
    f32x16 a0, a1;
    asm volatile("" : "+s"(kvf), "+s"(fkv), "+s"(xu_p), "+s"(su_p));
    {
      poll<32>(fact + 2 * 8192 - w * 128, (unsigned)((lane & 15) * 8 + (blk ? cb >> 2 : w) * 128), tag);
      STAMP(11);
      const char* xb = act + 2 * STG; const char* stp = stt + 256 * 1024;
      bf16x8 x[64], xu[16], mf0, mf1;
      Red<false> su, sp;
      sfor<0, 4>([&](auto i) { ldx1<false, i * 1024>(su.p[i], c.xv, su_p); });
      sfor<0, 16>([&](auto i) { ldx1<true, (i % 8) * 1024 - 4096>(xu[i], c.xv, xu_p + (i / 8) * 8192); });
      f32x16 au, av;
      sfor<0, 16>([&](auto i) {
        waitx<true, 15 - i + (i < XL ? i : XL)>(xu[i]);
        SBAR();
        mm<false, true, i == 0>(au, fu[i], xu[i]);
        if constexpr (i < XL) ld_x<i>(x, c, xb);
        mm<true, true, i == 0>(av, fu[i], xu[i]);
        if constexpr (i < 11) su.template run<i>(lane);
        if constexpr (i == 12) mf0 = opq_v(mean_frag(su.a, h == 0));
        if constexpr (i == 14) { mm<false, false, false>(au, cu, mf0); mm<true, false, false>(av, cu, mf0); }
        SBAR();
      });
      STAMP(12);
      u64 fa;
      f32x4 t[4], ru;
      unsigned vo;
      gemm64<true, true, HookP4>(c, x, xb, a0, a1, [&](auto s) {
        if constexpr (s == 1) {
          pin2(au, av);
          auto put = [&](const f32x16& a) {
#pragma unroll
            for (int gq = 0; gq < 4; ++gq) *(LAS f32x4*)(reg + w * 8192 + gq * 1024 + lane * 16) = f32x4{a[4 * gq], a[4 * gq + 1], a[4 * gq + 2], a[4 * gq + 3]};
          };
          if (ty < 2) { put(au); asm volatile("" ::: "memory"); } else put(av);
        }
        if constexpr (s == 3) {
          lds_bar();
#pragma unroll
          for (int j = 0; j < 4; ++j) t[j] = *(LAS const f32x4*)(reg + j * 8192 + (ty < 2 ? w & 1 : w) * 1024 + lane * 16);
        }
        if constexpr (s == 6) st2_1(__builtin_bit_cast(f32x2, u32x2{cvtpk(t[0][0], t[0][1]), cvtpk(t[0][2], t[0][3])}), vo, kvf);
        if constexpr (s >= 8 && s < 12) ldx1<false, (s - 8) * 1024>(sp.p[s - 8], c.xv, stp);
        if constexpr (s == 7 + XL) { bar(); if (w == 0) set_flag(fkv + (ut * 4 + ty) * 8, tag); }
        if constexpr (s == 52) ld8_1(fa, lane * 8 + w * 512, fkv);
      }, [&](auto g) {
        if constexpr (g == 0) {
#pragma unroll
          for (int e = 0; e < 4; ++e) ru[e] = lane_get(su.b, ty < 2 ? r : 8 * w + 4 * h + e);
        }
        if constexpr (g == 4) vo = opq_v(ut * 4096 + (ty < 2 ? (unsigned)(ty * 1024 + lane * 16 + (w & 1) * 8)
                                                               : (unsigned)(2048 + (w >> 1) * 1024 + ((ty & 1) * 16 + (r & 15) + 32 * (w & 1)) * 16 + h * 8)));
        if constexpr (g >= 9 && g < 13) {
#pragma unroll
          for (int e = 0; e < 4; ++e) { float v = t[0][e]; if constexpr (g < 12) a_add(v, t[g - 8][e]); else a_mul(v, ru[e]); t[0][e] = v; }
        }
        if constexpr (g >= 48 && g < 59) sp.template run<g - 48>(lane);
        if constexpr (g == 60) mf0 = opq_v(mean_frag(sp.a, h == 0));
        if constexpr (g == 61) mf1 = opq_v(mean_frag(sp.a, h == 1));
        if constexpr (g == 64) mm<false, false, false>(a0, cw.p[0], mf0);
        if constexpr (g == 65) mm<false, false, false>(a1, cw.p[0], mf1);
      });
      rstd = sp.b;
      waitx<false, 0>(fa);
#pragma unroll 1
      for (int spin = 0; spin < (1 << 15) && !__all(fa == tag); ++spin) { __builtin_amdgcn_s_sleep(8); ld8_1(fa, lane * 8 + w * 512, fkv); waitx<false, 0>(fa); }
      bar();
      STAMP(13);
    }
    STAMP(14);
    {
      u32x16 qq, misc;
      misc[14] = misc[15] = __float_as_uint(rstd * QSCALE);
#pragma unroll 1
      for (int attempt = 0; attempt < 2; ++attempt) {
        if (attempt == 1) {
          const bf16x8 qa0 = __builtin_bit_cast(bf16x8, __builtin_shufflevector(qq, qq, 0, 1, 2, 3)), qa1 = __builtin_bit_cast(bf16x8, __builtin_shufflevector(qq, qq, 4, 5, 6, 7));
          const bf16x8 qb0 = __builtin_bit_cast(bf16x8, __builtin_shufflevector(qq, qq, 8, 9, 10, 11)), qb1 = __builtin_bit_cast(bf16x8, __builtin_shufflevector(qq, qq, 12, 13, 14, 15));
          float mA = -INFINITY, mB = -INFINITY;
          for (int t = 0; t < 64; ++t) {
            bf16x8 k0, k1;
            ldx1<false, 0>(k0, c.xv, kvf + t * 4096); ldx1<false, 1024>(k1, c.xv, kvf + t * 4096);
            wait2<0>(k0, k1);
            mm<false, false, true>(a0, k0, qa0); mm<false, false, false>(a0, k1, qa1); mm<false, false, true>(a1, k0, qb0); mm<false, false, false>(a1, k1, qb1);
            mdone(a0, a1);
            mA = fmaxf(mA, max16(a0)); mB = fmaxf(mB, max16(a1));
          }
          misc[14] = __float_as_uint(swap_max(mA)); misc[15] = __float_as_uint(swap_max(mB));
        }
        f32x16 oa, ob, t0, t1, t2, t3;
        u32x16 kvr; u32x4 offs;
        offs[0] = c.xv;
        const char* ks = kvf + 2048 + w * 2048; const char* wsr = wt(5, 0);
        unsigned wm0 = ldsw, cnt = 8;
        asm volatile(
            "global_load_dwordx4 v[208:211], v240, %[kv] sc1\n\tglobal_load_dwordx4 v[212:215], v240, %[kv] offset:1024 sc1\n\t"
            "s_mov_b32 m0, s44\n\tv_add_u32 v241, 0x2000, v240\n\t"
            "global_load_lds_dwordx4 v240, s[40:41] sc1\n\tglobal_load_lds_dwordx4 v240, s[40:41] offset:1024 sc1\n\t"
            "s_add_u32 m0, s44, 0x2000\n\tv_add_u32 v242, 0x4000, v240\n\t"
            "global_load_lds_dwordx4 v241, s[40:41] sc1\n\tglobal_load_lds_dwordx4 v241, s[40:41] offset:1024 sc1\n\t"
            "s_add_u32 m0, s44, 0x4000\n\tv_add_u32 v243, 0x6000, v240\n\t"
            "global_load_lds_dwordx4 v242, s[40:41] sc1\n\tglobal_load_lds_dwordx4 v242, s[40:41] offset:1024 sc1\n\t"
            "s_add_u32 s40, s40, 0x6000\n\ts_addc_u32 s41, s41, 0\n\t"
            "s_cmp_lg_u32 %[att], 0\n\ts_cbranch_scc1 2f\n\t"
            "v_pk_mul_f32 v[128:129], v[128:129], v[238:239]\n\t"
            "v_pk_mul_f32 v[130:131], v[130:131], v[238:239]\n\t"
            "v_pk_mul_f32 v[132:133], v[132:133], v[238:239]\n\t"
            "v_pk_mul_f32 v[134:135], v[134:135], v[238:239]\n\t"
            "v_pk_mul_f32 v[136:137], v[136:137], v[238:239]\n\t"
            "v_pk_mul_f32 v[138:139], v[138:139], v[238:239]\n\t"
            "v_pk_mul_f32 v[140:141], v[140:141], v[238:239]\n\t"
            "v_pk_mul_f32 v[142:143], v[142:143], v[238:239]\n\t"
            "v_pk_mul_f32 v[144:145], v[144:145], v[238:239]\n\t"
            "v_pk_mul_f32 v[146:147], v[146:147], v[238:239]\n\t"
            "v_pk_mul_f32 v[148:149], v[148:149], v[238:239]\n\t"
            "v_pk_mul_f32 v[150:151], v[150:151], v[238:239]\n\t"
            "v_pk_mul_f32 v[152:153], v[152:153], v[238:239]\n\t"
            "v_pk_mul_f32 v[154:155], v[154:155], v[238:239]\n\t"
            "v_pk_mul_f32 v[156:157], v[156:157], v[238:239]\n\t"
            "v_pk_mul_f32 v[158:159], v[158:159], v[238:239]\n\t"
            "v_cvt_pk_bf16_f32 v192, v128, v129\n\t"
            "v_cvt_pk_bf16_f32 v193, v130, v131\n\t"
            "v_cvt_pk_bf16_f32 v194, v132, v133\n\t"
            "v_cvt_pk_bf16_f32 v195, v134, v135\n\t"
            "v_cvt_pk_bf16_f32 v196, v136, v137\n\t"
            "v_cvt_pk_bf16_f32 v197, v138, v139\n\t"
            "v_cvt_pk_bf16_f32 v198, v140, v141\n\t"
            "v_cvt_pk_bf16_f32 v199, v142, v143\n\t"
            "v_cvt_pk_bf16_f32 v200, v144, v145\n\t"
            "v_cvt_pk_bf16_f32 v201, v146, v147\n\t"
            "v_cvt_pk_bf16_f32 v202, v148, v149\n\t"
            "v_cvt_pk_bf16_f32 v203, v150, v151\n\t"
            "v_cvt_pk_bf16_f32 v204, v152, v153\n\t"
            "v_cvt_pk_bf16_f32 v205, v154, v155\n\t"
            "v_cvt_pk_bf16_f32 v206, v156, v157\n\t"
            "v_cvt_pk_bf16_f32 v207, v158, v159\n\t"
            "2:\n\t"
            "v_mov_b64 v[64:65], 0\n\t"
            "v_mov_b64 v[66:67], 0\n\t"
            "v_mov_b64 v[68:69], 0\n\t"
            "v_mov_b64 v[70:71], 0\n\t"
            "v_mov_b64 v[72:73], 0\n\t"
            "v_mov_b64 v[74:75], 0\n\t"
            "v_mov_b64 v[76:77], 0\n\t"
            "v_mov_b64 v[78:79], 0\n\t"
            "v_mov_b64 v[80:81], 0\n\t"
            "v_mov_b64 v[82:83], 0\n\t"
            "v_mov_b64 v[84:85], 0\n\t"
            "v_mov_b64 v[86:87], 0\n\t"
            "v_mov_b64 v[88:89], 0\n\t"
            "v_mov_b64 v[90:91], 0\n\t"
            "v_mov_b64 v[92:93], 0\n\t"
            "v_mov_b64 v[94:95], 0\n\t"
            "v_mov_b64 v[176:177], 0\n\t"
            "v_mov_b64 v[178:179], 0\n\t"
            "v_mov_b64 v[180:181], 0\n\t"
            "v_mov_b64 v[182:183], 0\n\t"
            "v_mov_b64 v[184:185], 0\n\t"
            "v_mov_b64 v[186:187], 0\n\t"
            "v_mov_b64 v[188:189], 0\n\t"
            "v_mov_b64 v[190:191], 0\n\t"
            "v_mov_b64 v[216:217], 0\n\t"
            "v_mov_b64 v[218:219], 0\n\t"
            "v_mov_b64 v[220:221], 0\n\t"
            "v_mov_b64 v[222:223], 0\n\t"
            "v_mov_b64 v[228:229], 0\n\t"
            "v_mov_b64 v[230:231], 0\n\t"
            "v_mov_b64 v[232:233], 0\n\t"
            "v_mov_b64 v[234:235], 0\n\t"
            "v_bfe_u32 v226, v240, 6, 1\n\tv_mov_b32 v236, 0xdddddddd\n\tv_mul_u32_u24 v226, 0x3f80, v226\n\tv_add_u32 v237, %[rg], v240\n\t"
            "v_lshl_or_b32 v226, v226, 16, v226\n\tv_mov_b32 v227, v226\n\tv_xor_b32 v224, 0x3f803f80, v226\n\tv_mov_b32 v225, v224\n\t"
            "s_waitcnt vmcnt(6)\n\ts_cbranch_scc1 3f\n\t"
            "v_mfma_f32_32x32x16_bf16 v[128:143], v[208:211], v[192:195], 0\n\tv_mfma_f32_32x32x16_bf16 v[160:175], v[208:211], v[200:203], 0\n\t"
            "v_mfma_f32_32x32x16_bf16 v[128:143], v[212:215], v[196:199], v[128:143]\n\tv_mfma_f32_32x32x16_bf16 v[160:175], v[212:215], v[204:207], v[160:175]\n\t"
            "s_nop 11\n\t"
            "v_max3_f32 v238, v128, v129, v130\n\tv_max3_f32 v239, v160, v161, v162\n\t"
            "v_max3_f32 v238, v238, v131, v132\n\tv_max3_f32 v239, v239, v163, v164\n\t"
            "v_max3_f32 v238, v238, v133, v134\n\tv_max3_f32 v239, v239, v165, v166\n\t"
            "v_max3_f32 v238, v238, v135, v136\n\tv_max3_f32 v239, v239, v167, v168\n\t"
            "v_max3_f32 v238, v238, v137, v138\n\tv_max3_f32 v239, v239, v169, v170\n\t"
            "v_max3_f32 v238, v238, v139, v140\n\tv_max3_f32 v239, v239, v171, v172\n\t"
            "v_max3_f32 v238, v238, v141, v142\n\tv_max3_f32 v239, v239, v173, v174\n\t"
            "v_max3_f32 v238, v238, v143, v143\n\tv_max3_f32 v239, v239, v175, v175\n\t"
            "v_mov_b32 v96, v238\n\tv_mov_b32 v97, v239\n\ts_nop 1\n\tv_permlane32_swap_b32 v238, v96\n\tv_permlane32_swap_b32 v239, v97\n\t"
            "v_max_f32 v238, v238, v96\n\tv_max_f32 v239, v239, v97\n\t"
            "3:\n\t"
            "v_xor_b32 v96, 0x80000000, v238\n\tv_xor_b32 v112, 0x80000000, v239\n\tv_mov_b32 v97, v96\n\tv_mov_b32 v113, v112\n\t"
            "v_mov_b64 v[98:99], v[96:97]\n\tv_mov_b64 v[114:115], v[112:113]\n\t"
            "v_mov_b64 v[100:101], v[96:97]\n\tv_mov_b64 v[116:117], v[112:113]\n\t"
            "v_mov_b64 v[102:103], v[96:97]\n\tv_mov_b64 v[118:119], v[112:113]\n\t"
            "v_mov_b64 v[104:105], v[96:97]\n\tv_mov_b64 v[120:121], v[112:113]\n\t"
            "v_mov_b64 v[106:107], v[96:97]\n\tv_mov_b64 v[122:123], v[112:113]\n\t"
            "v_mov_b64 v[108:109], v[96:97]\n\tv_mov_b64 v[124:125], v[112:113]\n\t"
            "v_mov_b64 v[110:111], v[96:97]\n\tv_mov_b64 v[126:127], v[112:113]\n\t"
            "s_nop 1\n\t" S0_(128, 192, 96) S1_(128, 192)
            "s_waitcnt vmcnt(4)\n\ts_barrier\n\ts_nop 8\n\t"
            "1:\n\t"
            STEP_(0, 24576, 240, 0, "s_sub_u32 %[cnt], %[cnt], 1\n\t", "") STEP_(8192, 0, 241, 1024, "", "") STEP_(16384, 8192, 242, 2048, "", "")
            STEP_(24576, 16384, 243, 3072, "s_add_u32 s40, s40, 0x8000\n\ts_addc_u32 s41, s41, 0\n\t", "s_add_u32 s42, s42, 0x4000\n\ts_addc_u32 s43, s43, 0\n\t")
            "s_add_u32 s45, s45, 0x4000\n\ts_cmp_lg_u32 %[cnt], 0\n\ts_cbranch_scc1 1b\n\t"
            L0_(232, 176) L1_(232, 176) PV0_(80, 176) PV1_(80, 176)
            "s_nop 11\n\t"
            : "=&{v[64:79]}"(oa), "=&{v[80:95]}"(ob), "=&{v[96:111]}"(t0), "=&{v[112:127]}"(t1), "+{v[128:143]}"(a0), "+{v[144:159]}"(a1), "=&{v[160:175]}"(t2),
              "=&{v[176:191]}"(t3), "+{v[192:207]}"(qq), "=&{v[208:223]}"(kvr), "+{v[224:239]}"(misc), "+{v[240:243]}"(offs),
              "+{s[40:41]}"(ks), "+{s[42:43]}"(wsr), "+{s45}"(wm0), [cnt] "+s"(cnt)
            : "{s44}"(lds0 + 8 * SLOT + w * 2048), [kv] "s"(kvf), [att] "s"(attempt), [rg] "s"(lds0 + 8 * SLOT)
            : "scc", "memory");
        const float la = __uint_as_float(misc[4]), lb = __uint_as_float(misc[8]);
        const float ia = __builtin_amdgcn_rcpf(la), ib = __builtin_amdgcn_rcpf(lb);
#pragma unroll
        for (int e = 0; e < 16; ++e) { oa[e] *= ia; ob[e] *= ib; }
        char* dst = acto + 3 * STG;
        st4_1<0>(__builtin_bit_cast(u32x4, pack8<0>(oa)), c.xv, dst); st4_1<1024>(__builtin_bit_cast(u32x4, pack8<1>(oa)), c.xv, dst);
        st4_1<2048>(__builtin_bit_cast(u32x4, pack8<0>(ob)), c.xv, dst); st4_1<3072>(__builtin_bit_cast(u32x4, pack8<1>(ob)), c.xv, dst);
        float chk = 0.f;
#pragma unroll
        for (int e = 0; e < 16; ++e) chk = __builtin_fmaf(oa[e], 0.f, __builtin_fmaf(ob[e], 0.f, chk));
        const bool bad = !(la < 1.2676506e30f) || !(lb < 1.2676506e30f) || !(chk == 0.f);
        wait_bar<4>();
        LAS volatile int* vote = (LAS volatile int*)(smem + 8 * SLOT);
        if (lane == 0) vote[w] = 0;
        if (attempt == 0 && bad) vote[w] = 1;
        lds_bar();
        const int redo = vote[0] | vote[1] | vote[2] | vote[3];
        if (!__builtin_amdgcn_readfirstlane(redo)) break;
        lds_bar();
      }
      waitv<0>();
      set_flag(facto + 3 * 8192, tag);
    }
    STAMP(15);
    REFRESH();
    {
      f32x16 a0, a1; res_to_acc(xr, a0, a1);
      poll<16>(fact + 3 * 8192, lane * 8, tag);
      STAMP(16);
      bf16x8 x[64];
      const char* xb = act + 3 * STG;
      sfor<0, XL>([&](auto s) { ld_x<s>(x, c, xb); });
      bar();
      gemm64<false, false, HookP6>(c, x, xb, a0, a1, [&](auto s) {
        if constexpr (s == 33) bar();
        if constexpr (s >= 52) pc_a(IC(a_pre(s - 52)));
      }, [](auto) {});
      STAMP(17);
      if (l == 0) {
        store_x(c, r, a0, a1, xr, acto + 4 * STG, stto + 512 * 1024);
        bar();
        sfor<12, 16>([&](auto n) { pc_a(IC(a_pre(n))); });
        waitv<4>();
        set_flag(facto + 4 * 8192, tag);
        sfor<16, 32>([&](auto n) { pc_a(IC(a_pre(n))); });
      } else {
        char* dst = out + (size_t)(tt * 32 + r) * 2048 + cb * 128 + h * 16;
        *(u32x4*)(dst) = unperm(__builtin_bit_cast(u32x4, pack8<0>(a0))); *(u32x4*)(dst + 32) = unperm(__builtin_bit_cast(u32x4, pack8<1>(a0)));
        *(u32x4*)(dst + 64) = unperm(__builtin_bit_cast(u32x4, pack8<0>(a1))); *(u32x4*)(dst + 96) = unperm(__builtin_bit_cast(u32x4, pack8<1>(a1)));
        if (tt == 0 && cb == 0) set_flag(ws + WS_EPOCH, tag);
      }
    }
    STAMP(18);
    if (l == 1) break;
    csm = 1;
    act += 4 * STG; acto += 4 * STG; fact += 4 * 8192; facto += 4 * 8192; stt += 512 * KB; stto += 512 * KB;
    kvp += 131072; zp += 8192; fkvp += 8192; kvf += 320 * KB; fkv += 2048; xu_p += 4 * STG; su_p += 512 * KB;
    l = 1;
    REFRESH();
    STAMP(0);
        poll<16>(fact, lane * 8, tag);
        STAMP(1);
        sfor<0, XL>(issue_x);
        wait_bar<XL>();
        sfor<0, 2>([&](auto s) { rd01(s, IC(0)); rd01(s, IC(1)); rd2(s); });
        sfor<0, 64>([&](auto s) { step(s, std::false_type{}); });
  }
}

extern "C" void kernel_launch(void* const* d_in, const int* in_sizes, int n_in, void* d_out, int out_size, void* d_ws, size_t ws_size, hipStream_t stream) {
  k_main<<<256, 256, 0, stream>>>((const char*)d_in[0], (const char*)d_in[1], (const char*)d_in[2], (const char*)d_in[3], (const char*)d_in[5], (const char*)d_in[6],
                                  (const char*)d_in[7], (const char*)d_in[8], (char*)d_ws, (char*)d_out);
}
```

```cpp
#include <hip/hip_runtime.h>
#include <hip/hip_bf16.h>
#include <stdint.h>
#include <type_traits>

typedef __attribute__((ext_vector_type(8))) short bf16x8;
typedef __attribute__((ext_vector_type(4))) short s16x4;
typedef __attribute__((ext_vector_type(2))) __bf16 bf2_t;
typedef __attribute__((ext_vector_type(16))) float f32x16;
typedef __attribute__((ext_vector_type(4))) float f32x4;
typedef __attribute__((ext_vector_type(2))) float f32x2;
typedef __attribute__((ext_vector_type(4))) unsigned u32x4;
typedef __attribute__((ext_vector_type(2))) unsigned u32x2;
#define LAS __attribute__((address_space(3)))
#define DI __device__ __forceinline__
#define MFMA32(a, b, c) __builtin_amdgcn_mfma_f32_32x32x16_bf16((a), (b), (c), 0, 0, 0)
#define MFMA16(a, b, c) __builtin_amdgcn_mfma_f32_16x16x32_bf16((a), (b), (c), 0, 0, 0)
#define SBAR() __builtin_amdgcn_sched_barrier(0)

constexpr int S_ = 2048, D_ = 1024;
constexpr float LOG2E = 1.4426950408889634f;
constexpr float QSCALE = 0.17677669529663687f * LOG2E;

constexpr size_t MB = 1u << 20, KB = 1024;
constexpr size_t STG = 256 * KB;
constexpr size_t WS_COL = 32 * MB;
constexpr size_t C_WT = 0;
constexpr size_t C_KVP = 768 * KB;
constexpr size_t C_ZP = 1024 * KB;
constexpr size_t C_CS = 1040 * KB;
constexpr size_t WS_ST = 64 * MB;
constexpr size_t WS_KVF = WS_ST + 1024 * KB;
constexpr size_t WS_FLAG = WS_ST + 1664 * KB;
constexpr size_t WS_EPOCH = WS_ST + 1920 * KB;
constexpr int FL_ACT = 0, FL_KVP = 8 * 64 * 128, FL_KV = FL_KVP + 2 * 64 * 128, FL_W = FL_KV + 2 * 2048;
typedef unsigned long long u64;

template <int I, int N, class F> DI void sfor(F&& f) { if constexpr (I < N) { f(std::integral_constant<int, I>{}); sfor<I + 1, N>(f); } }

DI unsigned cvtpk(float lo, float hi) { f32x2 v = {lo, hi}; return __builtin_bit_cast(unsigned, __builtin_convertvector(v, bf2_t)); }
DI float bflo(unsigned u) { return __uint_as_float(u << 16); }
DI float bfhi(unsigned u) { return __uint_as_float(u & 0xffff0000u); }
DI float rbf(float x) { return bflo(cvtpk(x, x)); }
DI float swap_add(float v) { auto r = __builtin_amdgcn_permlane32_swap(__float_as_uint(v), __float_as_uint(v), false, false); return __uint_as_float(r[0]) + __uint_as_float(r[1]); }
DI float swap_max(float v) { auto r = __builtin_amdgcn_permlane32_swap(__float_as_uint(v), __float_as_uint(v), false, false); return fmaxf(__uint_as_float(r[0]), __uint_as_float(r[1])); }
DI float swap_min(float v) { auto r = __builtin_amdgcn_permlane32_swap(__float_as_uint(v), __float_as_uint(v), false, false); return fminf(__uint_as_float(r[0]), __uint_as_float(r[1])); }
template <int S> DI bf16x8 pack8(const f32x16& v) {
  u32x4 p = {cvtpk(v[8 * S], v[8 * S + 1]), cvtpk(v[8 * S + 2], v[8 * S + 3]), cvtpk(v[8 * S + 4], v[8 * S + 5]), cvtpk(v[8 * S + 6], v[8 * S + 7])};
  return __builtin_bit_cast(bf16x8, p);
}
DI u32x4 unperm(u32x4 v) {
  auto r0 = __builtin_amdgcn_permlane32_swap(v[0], v[2], false, false);
  auto r1 = __builtin_amdgcn_permlane32_swap(v[1], v[3], false, false);
  return u32x4{r0[0], r1[0], r0[1], r1[1]};
}
DI float lane_get(float v, int src) { return __uint_as_float(__builtin_amdgcn_ds_bpermute(src << 2, __float_as_uint(v))); }
DI s16x4 trread(LAS const char* p) { return __builtin_amdgcn_ds_read_tr16_b64_v4i16((LAS s16x4*)p); }
DI bf16x8 cat8(s16x4 a, s16x4 b) { return __builtin_shufflevector(a, b, 0, 1, 2, 3, 4, 5, 6, 7); }
DI float dot2(unsigned a, unsigned b, float c) { return __builtin_amdgcn_fdot2_f32_bf16(__builtin_bit_cast(bf2_t, a), __builtin_bit_cast(bf2_t, b), c, false); }

template <int OFF> DI void dma(unsigned lds, unsigned v, const char* sb) {
  asm volatile("s_mov_b32 m0, %0\n\ts_nop 0\n\tglobal_load_lds_dwordx4 %1, %2 offset:%3" ::"s"(lds), "v"(v), "s"(sb), "i"(OFF) : "memory");
}
template <int OFF> DI void dma_c(unsigned lds, unsigned v, const char* sb) {
  asm volatile("s_mov_b32 m0, %0\n\ts_nop 0\n\tglobal_load_lds_dwordx4 %1, %2 offset:%3 sc1" ::"s"(lds), "v"(v), "s"(sb), "i"(OFF) : "memory");
}
template <int OFF> DI void dma_n(unsigned v, const char* sb) {
  asm volatile("global_load_lds_dwordx4 %0, %1 offset:%2 sc1" ::"v"(v), "s"(sb), "i"(OFF) : "memory");
}
constexpr int cap63(int n) { return n > 63 ? 63 : n; }
template <int N> DI void wait_bar() { asm volatile("s_waitcnt vmcnt(%0)\n\ts_barrier" ::"i"(cap63(N)) : "memory"); }
DI void lds_bar() { asm volatile("s_waitcnt lgkmcnt(0)\n\ts_barrier" ::: "memory"); }
DI void all_bar() { asm volatile("s_waitcnt vmcnt(0) lgkmcnt(0)\n\ts_barrier" ::: "memory"); }
DI void bar() { asm volatile("s_barrier" ::: "memory"); }
template <bool AG, int OFF> DI void ldx1(bf16x8& d, unsigned voff, const char* sb) {
  if constexpr (AG) asm volatile("global_load_dwordx4 %0, %1, %2 offset:%3 sc1" : "=a"(d) : "v"(voff), "s"(sb), "i"(OFF));
  else asm volatile("global_load_dwordx4 %0, %1, %2 offset:%3 sc1" : "=v"(d) : "v"(voff), "s"(sb), "i"(OFF));
}
template <int OFF> DI void ld2_1(f32x2& d, unsigned voff, const char* sb) { asm volatile("global_load_dwordx2 %0, %1, %2 offset:%3 sc1" : "=v"(d) : "v"(voff), "s"(sb), "i"(OFF)); }
DI void ld1_1(unsigned& d, unsigned voff, const void* sb) { asm volatile("global_load_dword %0, %1, %2 sc1" : "=v"(d) : "v"(voff), "s"(sb) : "memory"); }
template <int OFF> DI void st4_1(u32x4 d, unsigned voff, char* sb) { asm volatile("global_store_dwordx4 %0, %1, %2 offset:%3 sc1\n\ts_nop 1" ::"v"(voff), "v"(d), "s"(sb), "i"(OFF) : "memory"); }
DI void st2_1(f32x2 d, unsigned voff, char* sb) { asm volatile("global_store_dwordx2 %0, %1, %2 sc1" ::"v"(voff), "v"(d), "s"(sb) : "memory"); }
DI void st1_1(float d, unsigned voff, char* sb) { asm volatile("global_store_dword %0, %1, %2 sc1" ::"v"(voff), "v"(d), "s"(sb) : "memory"); }
template <int N> DI void waitv() { asm volatile("s_waitcnt vmcnt(%0)" ::"i"(cap63(N)) : "memory"); }
template <bool AG, int N, class T> DI void waitx(T& d) {
  if constexpr (AG) asm volatile("s_waitcnt vmcnt(%1)" : "+a"(d) : "i"(cap63(N))); else asm volatile("s_waitcnt vmcnt(%1)" : "+v"(d) : "i"(cap63(N)));
}
template <bool SW, bool BA, bool Z, bool NOP = false> DI void mm(f32x16& acc, const bf16x8& a, const bf16x8& b) {
#define MM_(OPS, C, OUT, BC) do { if constexpr (NOP) asm volatile("s_nop 1\n\tv_mfma_f32_32x32x16_bf16 %0, " OPS ", " C : OUT(acc) : "v"(a), BC(b)); \
                                  else asm volatile("v_mfma_f32_32x32x16_bf16 %0, " OPS ", " C : OUT(acc) : "v"(a), BC(b)); } while (0)
  if constexpr (Z) {
    if constexpr (SW) { if constexpr (BA) MM_("%2, %1", "0", "=&v", "a"); else MM_("%2, %1", "0", "=&v", "v"); }
    else { if constexpr (BA) MM_("%1, %2", "0", "=&v", "a"); else MM_("%1, %2", "0", "=&v", "v"); }
  } else {
    if constexpr (SW) { if constexpr (BA) MM_("%2, %1", "%0", "+v", "a"); else MM_("%2, %1", "%0", "+v", "v"); }
    else { if constexpr (BA) MM_("%1, %2", "%0", "+v", "a"); else MM_("%1, %2", "%0", "+v", "v"); }
  }
#undef MM_
}
DI void mdone(f32x16& a) { asm volatile("s_nop 11" : "+v"(a)); }
DI void mdone(f32x16& a, f32x16& b) { asm volatile("s_nop 11" : "+v"(a), "+v"(b)); }
DI void mdone(f32x16& a, f32x16& b, f32x16& c, f32x16& d) { asm volatile("s_nop 11" : "+v"(a), "+v"(b), "+v"(c), "+v"(d)); }
template <class T> DI T opq_v(T v) { asm volatile("" : "+v"(v)); return v; }
template <int OFF> DI void ldx0(bf16x8& d, unsigned voff, const char* sb) { asm volatile("global_load_dwordx4 %0, %1, %2 offset:%3" : "=v"(d) : "v"(voff), "s"(sb), "i"(OFF)); }
template <int OFF> DI void ldx1m(bf16x8& d, unsigned voff, const char* sb, unsigned long long m) {
  asm volatile("s_mov_b64 exec, %4\n\tglobal_load_dwordx4 %0, %1, %2 offset:%3 sc1\n\ts_mov_b64 exec, -1" : "=v"(d) : "v"(voff), "s"(sb), "i"(OFF), "s"(m));
}
template <bool AG> DI void ds_read2(bf16x8& d, unsigned addr) {
  if constexpr (AG) asm volatile("ds_read2_b64 %0, %1 offset1:2" : "=a"(d) : "v"(addr) : "memory"); else asm volatile("ds_read2_b64 %0, %1 offset1:2" : "=v"(d) : "v"(addr) : "memory");
}
template <bool AG> DI void waitl(bf16x8& d) { if constexpr (AG) asm volatile("s_waitcnt lgkmcnt(0)" : "+a"(d)::"memory"); else asm volatile("s_waitcnt lgkmcnt(0)" : "+v"(d)::"memory"); }
template <int OFF> DI void ds_write_a(unsigned addr, const bf16x8& v) { asm volatile("ds_write_b128 %0, %1 offset:%2" ::"v"(addr), "a"(v), "i"(OFF) : "memory"); }
template <int N, class T> DI void wait2(T& a, T& b) { asm volatile("s_waitcnt vmcnt(%2)" : "+v"(a), "+v"(b) : "i"(cap63(N))); }
DI void set_flag(char* flag, u64 tag) {
  unsigned z = 0;
  asm volatile("s_mov_b64 exec, 1\n\tglobal_atomic_swap_x2 %0, %1, %2 sc1\n\ts_mov_b64 exec, -1" ::"v"(z), "v"(tag), "s"(flag) : "memory");
}
DI void ld8_1(u64& d, unsigned voff, const char* sb) { asm volatile("global_load_dwordx2 %0, %1, %2 sc1" : "=v"(d) : "v"(voff), "s"(sb) : "memory"); }
template <int NL> DI void rd_flag(u64& v, unsigned voff, const char* sb) {
  const u64 zero = 0;
  if constexpr (NL == 64) asm volatile("global_atomic_or_x2 %0, %1, %3, %2 sc0 sc1" : "+v"(v) : "v"(voff), "s"(sb), "v"(zero) : "memory");
  else asm volatile("s_mov_b64 exec, %4\n\tglobal_atomic_or_x2 %0, %1, %3, %2 sc0 sc1\n\ts_mov_b64 exec, -1" : "+v"(v) : "v"(voff), "s"(sb), "v"(zero), "s"((1ull << (NL & 63)) - 1) : "memory");
}
template <int NL> DI void poll(const char* base, unsigned voff, u64 tag) {
  for (int spin = 0; spin < (1 << 15); ++spin) {
    u64 v = tag;
    rd_flag<NL>(v, voff, base);
    waitx<false, 0>(v);
    if (__all(v == tag)) break;
  }
  asm volatile("" ::: "memory");
}

template <int RBX> DI void block_map(int& rb, int& cb) {
  const int x = blockIdx.x & 7, i = blockIdx.x >> 3;
  constexpr int CBX = 32 / RBX, GC = 16 / CBX;
  rb = RBX * (x / GC) + i / CBX; cb = CBX * (x % GC) + i % CBX;
}

DI float a_fma(float a, float b, float c) { float d; asm volatile("v_fma_f32 %0, %1, %2, %3" : "=v"(d) : "v"(a), "v"(b), "v"(c)); return d; }
DI float a_fms(float a, float b, float c) { float d; asm volatile("v_fma_f32 %0, %1, -%2, %3" : "=v"(d) : "v"(a), "v"(b), "v"(c)); return d; }
DI void a_exp(float& x) { asm volatile("v_exp_f32 %0, %0" : "+v"(x)); }
DI void a_add(float& s, float x) { asm volatile("v_add_f32 %0, %0, %1" : "+v"(s) : "v"(x)); }
DI void a_mul(float& x, float s) { asm volatile("v_mul_f32 %0, %0, %1" : "+v"(x) : "v"(s)); }
DI float a_max3(float a, float b, float c) { float d; asm volatile("v_max3_f32 %0, %1, %2, %3" : "=v"(d) : "v"(a), "v"(b), "v"(c)); return d; }
DI float a_min3(float a, float b, float c) { float d; asm volatile("v_min3_f32 %0, %1, %2, %3" : "=v"(d) : "v"(a), "v"(b), "v"(c)); return d; }

DI void pin2(f32x16& a, f32x16& b) { asm volatile("" : "+v"(a), "+v"(b)); }
DI bf16x8 cs_frag(float c) {
  const float hi = rbf(c), mid = rbf(c - hi), lo = (c - hi) - mid;
  return __builtin_bit_cast(bf16x8, u32x4{cvtpk(hi, mid), cvtpk(lo, 0.f), 0u, 0u});
}
template <bool CS> struct Red {
  bf16x8 p[4];
  float t[4], a, b, sd;
  template <int K> DI void run(int lane) {
    if constexpr (K == 0) asm volatile("" : "+v"(p[0]), "+v"(p[1]), "+v"(p[2]), "+v"(p[3]));
    if constexpr (K < 3) {
      const f32x4 u = __builtin_bit_cast(f32x4, p[K + 1]), v = __builtin_bit_cast(f32x4, p[0]);
#pragma unroll
      for (int e = 0; e < 4; ++e) { if (K == 0) t[e] = v[e]; a_add(t[e], u[e]); }
    }
    if constexpr (K == 3) {
      auto x = __builtin_amdgcn_permlane16_swap(__float_as_uint(t[0]), __float_as_uint(t[1]), false, false);
      auto y = __builtin_amdgcn_permlane16_swap(__float_as_uint(t[2]), __float_as_uint(t[3]), false, false);
      a = opq_v(__uint_as_float(x[0]) + __uint_as_float(x[1])); b = opq_v(__uint_as_float(y[0]) + __uint_as_float(y[1]));
    }
    if constexpr (K == 4) { auto x = __builtin_amdgcn_permlane32_swap(__float_as_uint(a), __float_as_uint(b), false, false); a = opq_v(__uint_as_float(x[0]) + __uint_as_float(x[1])); }
    if constexpr (K == 5) {
      if constexpr (CS) a = lane_get(a, (lane >> 2) + ((lane & 3) << 4));
      else { const int src = ((lane & 31) >> 1) + ((lane & 1) << 5); b = lane_get(a, src + 16); a = lane_get(a, src); }
    }
    if constexpr (CS) {
      if constexpr (K == 8) { t[0] = opq_v(rbf(a)); t[1] = opq_v(a - t[0]); }
      if constexpr (K == 9) { const float mid = rbf(t[1]); p[0] = opq_v(__builtin_bit_cast(bf16x8, u32x4{cvtpk(t[0], mid), cvtpk(t[1] - mid, 0.f), 0u, 0u})); }
    } else {
      if constexpr (K == 8) { const float m = a * (1.f / D_); b = opq_v(fmaxf(b * (1.f / D_) - m * m, 0.f)); a = opq_v(rbf(m)); }
      if constexpr (K == 9) b = opq_v(rbf(rbf(b) + 1e-5f));
      if constexpr (K == 10) { sd = opq_v(rbf(__builtin_amdgcn_sqrtf(b))); b = opq_v(__builtin_amdgcn_rcpf(sd)); }
    }
  }
};
DI void pin4(bf16x8 (&p)[4]) { asm volatile("" : "+v"(p[0]), "+v"(p[1]), "+v"(p[2]), "+v"(p[3])); }
DI bf16x8 mean_frag(float mean, bool on) { const float m = on ? -mean : 0.f; return __builtin_bit_cast(bf16x8, u32x4{cvtpk(m, m), cvtpk(m, 0.f), 0u, 0u}); }

#define STAMP(i)

constexpr int CODE_KB = 50;
constexpr int SLOT = 16384, XL = 12;
struct WinPtr {
  unsigned b[3];
  DI void init(unsigned a) { sfor<0, 3>([&](auto i) { b[i] = a + i * 65536u; asm volatile("" : "+v"(b[i])); }); }
  template <int OFF> DI bf16x8 frag() const { return *(LAS const bf16x8*)((LAS const char*)(uintptr_t)b[OFF >> 16] + (OFF & 65535)); }
};
struct Ctx { unsigned xv; WinPtr p; };
#define IC(n) std::integral_constant<int, (n)>{}

template <int S> DI void ld_x(bf16x8 (&x)[64], const Ctx& c, const char* xb) { ldx1<false, (S % 8) * 1024 - 4096>(x[S], c.xv, xb + (S / 8) * 8192 + 4096); }
template <class H> constexpr int g64_wait(int s) {
  int n = 0;
  const int t0 = s - XL;
  if (t0 < 0) n += XL - 1 - s; else n += H::n(t0);
  for (int t = t0 < 0 ? 0 : t0 + 1; t < s; ++t) n += (t + XL < 64 ? 1 : 0) + H::n(t);
  return n;
}
constexpr int a_slot(int g);
template <bool MAPA, bool ZERO, class H, class FX, class FA>
DI void gemm64(const Ctx& c, bf16x8 (&x)[64], const char* xb, f32x16& acc0, f32x16& acc1, FX&& hook, FA&& aux) {
  bf16x8 f0[3], f1[3];
  auto off = [](int s) constexpr { return (MAPA ? a_slot(24 + s / 8) : s / 8) * SLOT + (s % 8) * 2048; };
  auto rd0 = [&](auto s) { f0[s % 3] = c.p.template frag<off(s)>(); };
  auto rd1 = [&](auto s) { f1[s % 3] = c.p.template frag<off(s) + 1024>(); };
  rd0(IC(0)); rd1(IC(0)); rd0(IC(1)); rd1(IC(1));
  sfor<0, 64>([&](auto s) {
    waitx<false, g64_wait<H>(s)>(x[s]);
    SBAR();
    mm<false, false, ZERO && s == 0>(acc0, f0[s % 3], x[s]);
    SBAR();
    if constexpr (s + 2 < 64) rd0(IC(s + 2));
    if constexpr (s + XL < 64) ld_x<s + XL>(x, c, xb);
    aux(IC(2 * s));
    SBAR();
    mm<false, false, ZERO && s == 0>(acc1, f1[s % 3], x[s]);
    SBAR();
    if constexpr (s + 2 < 64) rd1(IC(s + 2));
    hook(s);
    aux(IC(2 * s + 1));
    SBAR();
  });
  mdone(acc0, acc1);
}
struct HookP6 { static constexpr int n(int s) { return s >= 52 ? 1 : 0; } };
struct HookP3 { static constexpr int n(int s) { return s >= 52 ? 1 : 0; } };
struct HookP4 { static constexpr int n(int s) { return s == 52 || s == 6 || (s >= 8 && s < 12) ? 1 : 0; } };

DI void res_to_acc(const u32x4 (&xr)[4], f32x16& a0, f32x16& a1) {
#pragma unroll
  for (int e = 0; e < 4; ++e) {
    a0[2 * e] = bflo(xr[0][e]); a0[2 * e + 1] = bfhi(xr[0][e]); a0[8 + 2 * e] = bflo(xr[1][e]); a0[9 + 2 * e] = bfhi(xr[1][e]);
    a1[2 * e] = bflo(xr[2][e]); a1[2 * e + 1] = bfhi(xr[2][e]); a1[8 + 2 * e] = bflo(xr[3][e]); a1[9 + 2 * e] = bfhi(xr[3][e]);
  }
  asm volatile("" : "+v"(a0), "+v"(a1));
}
DI void store_stats(int r, const u32x4 (&xr)[4], char* stp) {
  float s1 = 0.f, s2 = 0.f, s3 = 0.f, s4 = 0.f;
#pragma unroll
  for (int i = 0; i < 4; ++i)
#pragma unroll
    for (int e = 0; e < 4; e += 2) {
      s1 = dot2(xr[i][e], 0x3f803f80u, s1); s2 = dot2(xr[i][e], xr[i][e], s2);
      s3 = dot2(xr[i][e + 1], 0x3f803f80u, s3); s4 = dot2(xr[i][e + 1], xr[i][e + 1], s4);
    }
  st2_1(f32x2{swap_add(s1 + s3), swap_add(s2 + s4)}, (unsigned)(r * 8), stp);
}
DI void store_x(const Ctx& c, int r, const f32x16& a0, const f32x16& a1, u32x4 (&xr)[4], char* dst, char* stp) {
  xr[0] = __builtin_bit_cast(u32x4, pack8<0>(a0)); st4_1<0>(xr[0], c.xv, dst);
  xr[1] = __builtin_bit_cast(u32x4, pack8<1>(a0)); st4_1<1024>(xr[1], c.xv, dst);
  xr[2] = __builtin_bit_cast(u32x4, pack8<0>(a1)); st4_1<2048>(xr[2], c.xv, dst);
  xr[3] = __builtin_bit_cast(u32x4, pack8<1>(a1)); st4_1<3072>(xr[3], c.xv, dst);
  store_stats(r, xr, stp);
}

template <class FX> DI void hedge_exp(const f32x16& p, float sc, f32x16& ep, f32x16& en, FX&& hook) {
  float mx = fmaxf(p[0], p[1]), mn = fminf(p[0], p[1]);
#pragma unroll
  for (int i = 2; i < 16; i += 2) { mx = fmaxf(fmaxf(mx, p[i]), p[i + 1]); mn = fminf(fminf(mn, p[i]), p[i + 1]); }
  mx = swap_max(mx) * sc; mn = swap_min(mn) * sc;
  sfor<0, 16>([&](auto i) {
    SBAR();
    hook(i);
    constexpr int e = i;
    float a = __builtin_amdgcn_exp2f(__builtin_fmaf(p[e], sc, -mx)), b = __builtin_amdgcn_exp2f(__builtin_fmaf(p[e], -sc, mn));
    asm volatile("" : "+v"(a), "+v"(b));
    ep[e] = a; en[e] = b;
  });
  SBAR();
}

constexpr int a_slot(int g) { constexpr int m[32] = {0, 1, 2, 3, 4, 0, 1, 2, 5, 6, 7, 5, 3, 4, 6, 7, 8, 5, 3, 4, 6, 7, 0, 1, 2, 5, 3, 4, 6, 7, 0, 1}; return m[g]; }
constexpr int a_need(int g) { return g < 8 ? 8 * g - 2 : (g < 16 ? 16 * (g - 8) - (g > 12 ? 10 : 2) : 128 + 8 * (g - 16) - 2); }
constexpr bool a_bar(int s) { return (s + 2) % 8 == 0 && s >= 14 && s < 190; }
constexpr int a_pre(int n) { return n < 20 ? n : n + 12; }
constexpr int QP = 128 + 24, QC = 128 + 36;
constexpr int a_piece(int s) {
  constexpr int t[][3] = {{20, 5, 1}, {24, 6, 1}, {28, 11, 1}, {34, 7, 1}, {46, 12, 1}, {54, 13, 1}, {64, 14, 2}, {72, 15, 2}, {80, 16, 2}, {88, 17, 2}, {96, 18, 2},
                          {112, 19, 2}, {128, 20, 1}, {132, 21, 1}, {136, 22, 1}, {140, 23, 1}, {144, 24, 1}, {150, 25, 1}, {158, 26, 1}};
  for (auto& e : t) if (s >= e[0] && s < e[0] + 4 * e[2] && (s - e[0]) % e[2] == 0) return e[1] * 4 + (s - e[0]) / e[2];
  return -1;
}
constexpr int a_first(int s) { constexpr int g[8] = {0, 8, 1, 2, 9, 3, 4, 10}; return s < 12 ? g[(s + 20) / 4] * 4 + (s + 20) % 4 : -1; }
constexpr int a_park(int s) { return s == 148 || s == 149 ? s - 148 : (s >= 154 && s < 158 ? s - 152 : (s == 162 || s == 163 ? s - 156 : -1)); }
constexpr bool a_side(int s, bool first) { return (s >= 2 && s < (first ? 14 : 6)) || s == 40 || (s >= 104 && s < 112 && s % 2 == 0) || (s >= 120 && s < 128 && s % 2 == 0) || a_park(s) >= 0; }
struct SchedA {
  int xw[64], bw[192], sw[64], fw, cw, pw, zw, lw[32];
  constexpr SchedA(bool first) : xw{}, bw{}, sw{}, fw(0), cw(0), pw(0), zw(0), lw{} {
    int pos = 0, xpos[64] = {}, wpos[32] = {}, spos[192] = {}, fpos = 0, stpos = 0, ppos = 0, lpos[34] = {};
    if (first) { for (int s = -20; s < 0; ++s) wpos[a_first(s) / 4] = ++pos; pos += 2; }
    else for (int s = 0; s < XL; ++s) xpos[s] = ++pos;
    for (int s = 0; s < 192; ++s) {
      if (s == 128) { cw = pos + 2 - spos[126]; pos += 3; stpos = pos; }
      if (a_bar(s) || s == 6) {
        bw[s] = 63;
        for (int g = 0; g < 24; ++g) if (a_need(g) == s && wpos[g] && pos - wpos[g] < bw[s]) bw[s] = pos - wpos[g];
        if (s == 54 && pos - spos[40] < bw[s]) bw[s] = pos - spos[40];
        if (s == 142) { if (pos - stpos < bw[s]) bw[s] = pos - stpos; ++pos; }
      }
      if (s == 20 || s == 24 || s == 28) sw[s] = pos - spos[s - 15];
      if (s == 78) fw = pos - fpos;
      if (s == QC) pw = pos - ppos;
      if (s < 64) xw[s] = pos - xpos[s];
      if (!first && s + XL < 64) xpos[s + XL] = ++pos;
      if (s == 65) fpos = ++pos;
      if (s == QP) ppos = ++pos;
      if (s >= QC && s < QC + 17) lpos[2 * (s - QC)] = ++pos;
      if (a_piece(s) >= 0) wpos[a_piece(s) / 4] = ++pos;
      if (a_side(s, first)) spos[s] = ++pos;
      if (s >= QC && s < QC + 17) lpos[2 * (s - QC) + 1] = ++pos;
      if (first && a_first(s) >= 0) wpos[a_first(s) / 4] = ++pos;
      if (s == 175) zw = pos - lpos[1];
      if (s >= 176) lw[s - 176] = pos - lpos[2 + s - 176];
    }
    for (int i = 16; i < 32; ++i) lw[i] = pos - lpos[2 + i];
  }
};
constexpr int cmin(int a, int b) { return a < b ? a : b; }

#define R_OA "v[64:79]"
#define R_OB "v[80:95]"
#define S0_(n, q, m) "v_mfma_f32_32x32x16_bf16 v[" #n ":" #n "+15], v[208:211], v[" #q ":" #q "+3], v[" #m ":" #m "+15]\n\t"
#define S1_(n, q) "v_mfma_f32_32x32x16_bf16 v[" #n ":" #n "+15], v[212:215], v[" #q "+4:" #q "+7], v[" #n ":" #n "+15]\n\t"
#define PV0_(o, p) "v_smfmac_f32_32x32x32_bf16 v[" #o ":" #o "+15], v[216:219], v[" #p ":" #p "+7], v236\n\t"
#define PV1_(o, p) "v_smfmac_f32_32x32x32_bf16 v[" #o ":" #o "+15], v[220:223], v[" #p "+8:" #p "+15], v236\n\t"
#define L0_(l, p) "v_smfmac_f32_16x16x64_bf16 v[" #l ":" #l "+3], v[224:227], v[" #p ":" #p "+7], v236\n\t"
#define L1_(l, p) "v_smfmac_f32_16x16x64_bf16 v[" #l ":" #l "+3], v[224:227], v[" #p "+8:" #p "+15], v236\n\t"
#define E_(e, i) "v_exp_f32 v[" #e "+" #i "], v[" #e "+" #i "]\n\t"
#define RD_(d, off) "ds_read_b128 v[" #d ":" #d "+3], v237 offset:" #off "\n\t"
#define HALF_(e, n, q, m, p, o, l, X0, X1, X2, X3, X4, X5) \
  S0_(n, q, m) X0 E_(e, 0) E_(e, 1) E_(e, 2) S1_(n, q) X1 E_(e, 3) E_(e, 4) E_(e, 5) PV0_(o, p) X2 E_(e, 6) E_(e, 7) E_(e, 8) \
  PV1_(o, p) X3 E_(e, 9) E_(e, 10) E_(e, 11) L0_(l, p) X4 E_(e, 12) E_(e, 13) L1_(l, p) X5 E_(e, 14) E_(e, 15)
#define LG2_ "s_waitcnt lgkmcnt(2)\n\t"
#define LG0_ "s_waitcnt lgkmcnt(0)\n\t"
#define STEP_(b, nb, kv, wo, Y0, Y1) \
  HALF_(128, 160, 200, 112, 176, 80, 232, "", "", RD_(208, b + 2048), RD_(212, b + 3072), RD_(216, b), RD_(220, b + 1024) LG2_) \
  HALF_(160, 144, 192, 96, 128, 64, 228, "", LG0_, "s_add_u32 m0, s44, " #nb "\n\t", "", "global_load_lds_dwordx4 v" #kv ", s[40:41] sc1\n\t", \
        "global_load_lds_dwordx4 v" #kv ", s[40:41] offset:1024 sc1\n\t") \
  HALF_(144, 176, 200, 112, 160, 80, 232, Y0, "", RD_(208, b + 6144), RD_(212, b + 7168), RD_(216, b + 4096), RD_(220, b + 5120) LG2_) \
  HALF_(176, 128, 192, 96, 144, 64, 228, "s_mov_b32 m0, s45\n\t", LG0_, "global_load_lds_dwordx4 v240, s[42:43] offset:" #wo " sc1\n\t", Y1, \
        "s_waitcnt vmcnt(4)\n\t", "s_barrier\n\t")
DI float max16(const f32x16& s) {
  float mx = s[0];
#pragma unroll
  for (int e = 1; e < 16; ++e) mx = fmaxf(mx, s[e]);
  return mx;
}
typedef __attribute__((ext_vector_type(8))) unsigned u32x8;
typedef __attribute__((ext_vector_type(16))) unsigned u32x16;

__global__ __launch_bounds__(256, 1) void k_main(const char* xin, const char* wqkv, const char* wphi, const char* woA, const char* wq, const char* wk, const char* wv,
                                                 const char* woB, char* ws, char* out) {
  __shared__ __attribute__((aligned(16))) char smem_[163840];
  LAS char* const smem = (LAS char*)smem_;
  const unsigned lds0 = (unsigned)(uintptr_t)smem;
  Ctx c;
  int lane = threadIdx.x & 63, w = __builtin_amdgcn_readfirstlane(threadIdx.x >> 6), r = lane & 31, h = lane >> 5, rb, cb;
  block_map<4>(rb, cb);
  int tt = rb * 4 + w, blk = (lane >> 4) & 1, q4 = (lane & 15) >> 2, p4 = lane & 3;
  c.xv = lane * 16; c.p.init(lds0 + c.xv);
  char* flags = ws + WS_FLAG;
  char* col = ws + WS_COL + (size_t)cb * 2 * MB;
  const int ut = rb * 4 + (cb >> 2), ty = cb & 3;
  char* act = ws + (size_t)rb * 2 * MB + w * 65536;
  char* acto = act + cb * 4096;
  char* fact = flags + FL_ACT + tt * 128;
  char* facto = fact + cb * 8;
  char* stt = ws + WS_ST + tt * 4096;
  char* stto = stt + cb * 256;
  char* kvp = col + C_KVP + w * 32768;
  char* zp = col + C_ZP + w * 2048;
  char* fkvp = flags + FL_KVP + (cb * 4 + w) * 128;
  char* fwl = flags + FL_W + 2048 + cb * 128;
  char* kvf = ws + WS_KVF;
  char* fkv = flags + FL_KV;
  const char* xu_p = ws + (size_t)rb * 2 * MB + 2 * STG + (cb >> 2) * 65536 + w * 16384 + 4096;
  const char* su_p = ws + WS_ST + 256 * KB + ut * 4096;
  const char* wkv = ((cb & 2) ? wv : wk) + w * 256 * 64;
  const char* wph = wphi + cb * 4096 + w * 1024;
  u64 tag = 0;
  u32x4 xr[4] = {};
  char* colw = col + w * 4096;
  unsigned ldsw = lds0 + w * 4096;
  auto wt = [&](int m, int ch) { return colw + C_WT + m * 131072 + ch * SLOT; };
  auto pc_w = [&](int m, int slot, int ch, auto i) { if constexpr (i == 0) dma_c<0>(ldsw + slot * SLOT, c.xv, wt(m, ch)); else dma_n<i * 1024>(c.xv, wt(m, ch)); };
  auto pc_a = [&](auto n) { constexpr int g = n / 4; pc_w(g < 8 ? 1 : (g < 16 ? 2 : (g < 24 ? 0 : 3)), a_slot(g), g % 8, IC(n % 4)); };
  u64 csm = ~0ull;
  float csv[4] = {};
  auto ld_cs = [&](bf16x8& d, int m, auto i) { ldx1m<i * 1024>(d, c.xv, col + C_CS + m * 4096, csm); };
  auto raw_in = [&](auto j, int slot) {
    const int hi = rb >> 3, pitch = (j == 0 || (j == 1 && !hi)) ? 6144 : 2048, row = lane >> 3;
    const char* src = (j == 0 ? wqkv + 2048 + hi * 2048 : (j == 1 ? (hi ? woA : wqkv) : (hi ? woB : wq))) + cb * 128 + (size_t)(rb & 7) * 128 * pitch;
    const unsigned v0 = (unsigned)((32 * w + row) * pitch + ((lane & 7) ^ (((row >> 1) & 1) << 2)) * 16);
    sfor<0, 4>([&](auto i) { dma<i * 1024>(lds0 + slot * SLOT + w * 4096, v0 + i * (8 * pitch - 1024), src); });
  };
  auto raw_out = [&](auto j, int slot) {
    const int hi = rb >> 3, ch = rb & 7, m = j == 0 ? 1 + hi : (j == 1 ? 3 * hi : 4 + hi);
    LAS const char* p = smem + slot * SLOT + (w >> 1) * 8192 + (4 * h + q4) * 128 + (((((q4 >> 1) ^ w) & 1) << 2) + 2 * blk + (p4 >> 1)) * 16 + 8 * (p4 & 1);
    const bool isv = m == 2;
    char* dst = col + C_WT + m * 131072 + (isv ? (w & 1) * 65536 + ch * 8192 + (w >> 1) * 4096 : ch * SLOT + (w >> 1) * 8192 + (w & 1) * 1024);
    float cs = 0.f;
    sfor<0, 4>([&](auto s) {
      const u32x4 f = __builtin_bit_cast(u32x4, cat8(trread(p + s * 2048), trread(p + s * 2048 + 1024)));
      st4_1<0>(f, c.xv, dst + s * (isv ? 1024 : 2048));
#pragma unroll
      for (int e = 0; e < 4; ++e) cs = dot2(f[e], 0x3f803f80u, cs);
    });
    st1_1(swap_add(cs), (unsigned)(r * 4), col + C_CS + m * 4096 + (ch * 2 + (w >> 1)) * 256 + (w & 1) * 128);
  };
  const int w0 = w; const unsigned ldsw0 = ldsw;
#define REFRESH() do { \
    w = w0; ldsw = ldsw0; \
    asm volatile("" : "+s"(w), "+s"(tag), "+s"(act), "+s"(acto), "+s"(fact), "+s"(facto), "+s"(stt), "+s"(stto), "+s"(col), "+s"(colw), "+s"(ldsw)); \
    asm volatile("" : "+v"(lane)); \
    r = lane & 31; h = lane >> 5; blk = (lane >> 4) & 1; q4 = (lane & 15) >> 2; p4 = lane & 3; c.xv = lane * 16; c.p.init(lds0 + c.xv); \
  } while (0)
  int l = 0;
  float mean, rstd;
  Red<true> cw;
  bf16x8 park[24];
      constexpr int NA = 60;
      constexpr SchedA SQ(false), S1(true);
      bf16x8 x[64], f0[3], f1[3], f2[5], wf[4], kp[4], pp[32], zq[2], sel[2], mf0, mf1;
      Red<false> st; Red<true> ck, cv, cq;
      f32x16 acc[6], p[2], kvs;
      u64 fv;
      float mx[2], mn[2], e[16], se, sce;
      unsigned arow;
      LAS char* pk_img = smem;
      LAS char* v_img = smem + 2 * SLOT;
      LAS char* sh = smem + 9 * SLOT + 4096;
      auto issue_x = [&](auto s) { ldx1<(s < NA), (s % 8) * 1024 - 4096>(x[s], c.xv, act + (s / 8) * 8192 + 4096); };
      auto ld_park = [&](auto i) { ldx1<true, (i % 4) * 1024>(park[i], c.xv, wt(4, 2 + i / 4)); };
      auto ldp = [&](auto i) {
        if constexpr (i >= 2) ldx1<true, ((i - 2) % 8) * 1024 - 4096>(pp[i - 2], c.xv, kvp + ((i - 2) / 8) * 8192 + 4096); else ldx1<false, i * 1024>(zq[i], c.xv, zp);
      };
      auto rd_wf = [&](auto k) { LAS const char* wp = smem + 9 * SLOT + (4 * h + q4) * 64 + blk * 32 + p4 * 8 + k * 1024; wf[k] = cat8(trread(wp), trread(wp + 512)); };
      auto rd01 = [&](auto s, auto t) {
        if constexpr (s < 64 || (s >= 128 && s < 192)) { const bf16x8 f = c.p.template frag<a_slot(s / 8) * SLOT + (s % 8) * 2048 + t * 1024>(); if constexpr (t == 0) f0[s % 3] = f; else f1[s % 3] = f; }
      };
      auto rd2 = [&](auto s) { if constexpr (s < 128) f2[s % 5] = c.p.template frag<a_slot(8 + s / 16) * SLOT + (s % 16) * 1024>(); };
      auto v_out = [&](auto t, auto hf) {
        *(LAS bf16x8*)(v_img + t * 8192 + w * 2048 + hf * 1024 + lane * 16) = pack8<hf>(acc[2 + t]);
      };
      auto sum_mm = [&](auto i) { waitx<true, SQ.lw[i]>(pp[i]); mm<false, true, i == 0>(kvs, sel[i & 1], pp[i]); };
      auto kmap = [&](auto gi) {
        constexpr int g = gi;
        if constexpr (g == 4) { pin2(acc[0], acc[1]); }
        if constexpr (g >= 6 && g < 10) mm<false, false, g % 2 == 0>(p[(g - 6) / 2], wf[g - 6], kp[g - 6]);
        if constexpr (g >= 4 && g < 8) kp[g - 4] = opq_v(pack8<g % 2>(acc[(g - 4) / 2]));
        if constexpr (g == 10) mm<true, false, false>(acc[3], cv.p[0], mf1);
        if constexpr (g >= 12 && g < 24) {
          constexpr int hd = (g - 12) / 6, t = (g - 12) % 6;
          const f32x16& q = p[hd];
          if constexpr (t == 0) { mx[hd] = a_max3(q[0], q[1], q[2]); mn[hd] = a_min3(q[0], q[1], q[2]); mx[hd] = a_max3(mx[hd], q[3], q[4]); mn[hd] = a_min3(mn[hd], q[3], q[4]); }
          if constexpr (t >= 1 && t < 4) {
            mx[hd] = a_max3(mx[hd], q[4 * t + 1], q[4 * t + 2]); mn[hd] = a_min3(mn[hd], q[4 * t + 1], q[4 * t + 2]);
            mx[hd] = a_max3(mx[hd], q[4 * t + 3], q[(4 * t + 4) & 15]); mn[hd] = a_min3(mn[hd], q[4 * t + 3], q[(4 * t + 4) & 15]);
          }
          if constexpr (t == 4) mx[hd] = opq_v(swap_max(mx[hd]) * -sce);
          if constexpr (t == 5) mn[hd] = opq_v(swap_min(mn[hd]) * sce);
        }
        if constexpr (g >= 24) {
          constexpr int grp = (g - 24) / 26, j = (g - 24) % 26, hd = grp >> 1, sg = grp & 1;
          if constexpr (j >= 1 && j <= 16) a_exp(e[j - 1]);
          if constexpr (j < 16) e[j] = sg ? a_fms(p[hd][j], sce, mn[hd]) : a_fma(p[hd][j], sce, mx[hd]);
          if constexpr (j == 2) se = e[0];
          if constexpr (j > 2 && j <= 17) a_add(se, e[j - 2]);
          if constexpr (j == 17) { se = opq_v(__builtin_amdgcn_rcpf(swap_add(se)) * rstd); if constexpr (grp < 2) v_out(IC(0), IC(grp)); }
          if constexpr (j >= 18) {
            constexpr int gq = (j - 18) / 2;
            if constexpr (j % 2 == 0) { a_mul(e[4 * gq], se); a_mul(e[4 * gq + 1], se); a_mul(e[4 * gq + 2], se); a_mul(e[4 * gq + 3], se); }
            else *(LAS u32x2*)((LAS char*)(uintptr_t)(arow ^ ((sg * 4 + gq) << 4)) + hd * 16384) = u32x2{cvtpk(e[4 * gq], e[4 * gq + 1]), cvtpk(e[4 * gq + 2], e[4 * gq + 3])};
          }
        }
      };
      auto keep = [&](Red<true>& q, int n) { csv[n] = l == 0 ? q.a : csv[n]; q.a = csv[n]; };
      auto aux = [&](auto s, auto i, auto fl) {
        constexpr int g1 = 3 * s + i, g3 = 2 * (s - 128) + i;
        if constexpr (s < 64) {
          if constexpr (g1 >= 60 && g1 < 71) st.template run<g1 - 60>(lane);
          if constexpr (g1 == 80) { if constexpr (fl) csv[0] = ck.a; else ck.a = csv[0]; }
          if constexpr (g1 == 92) { if constexpr (fl) csv[1] = cv.a; else cv.a = csv[1]; }
          if constexpr (g1 >= (fl ? 72 : 80) && g1 < 82) ck.template run<g1 - 72>(lane);
          if constexpr (g1 >= (fl ? 84 : 92) && g1 < 94) cv.template run<g1 - 84>(lane);
          if constexpr (g1 == 71) { mean = st.a; rstd = st.b; sce = opq_v(rstd * LOG2E); }
          if constexpr (g1 == 95) mf0 = opq_v(mean_frag(mean, h == 0));
          if constexpr (g1 == 96) mf1 = opq_v(mean_frag(mean, h == 1));
          if constexpr (g1 == 99) mm<false, false, false>(acc[0], ck.p[0], mf0);
          if constexpr (g1 == 100) mm<false, false, false>(acc[1], ck.p[0], mf1);
          if constexpr (g1 == 101) mm<true, false, false>(acc[2], cv.p[0], mf0);
          if constexpr (g1 == 130) *(LAS short*)(sh + 8704 + w * 64 + (r >> 4) * 32 + ((r >> 2) & 1) * 16 + ((r >> 3) & 1) * 8 + (r & 3) * 2) = (short)(__float_as_uint(st.sd) >> 16);
          if constexpr (g1 >= 168 && g1 < 172) rd_wf(IC(g1 - 168));
          if constexpr (g1 == 174) arow = opq_v((unsigned)(uintptr_t)pk_img + (w * 32 + r) * 128 + h * 8 + ((r & 7) << 4));
        } else if constexpr (s >= 128) {
          if constexpr (g3 == 8) keep(cq, 2);
          if constexpr (g3 == 24) keep(cw, 3);
          if constexpr (g3 < 10) cq.template run<g3>(lane);
          if constexpr (g3 == 12) mm<false, false, false>(acc[4], cq.p[0], mf0);
          if constexpr (g3 == 13) mm<false, false, false>(acc[5], cq.p[0], mf1);
          if constexpr (g3 >= 16 && g3 < 26) cw.template run<g3 - 16>(lane);
          if constexpr (g3 >= 32 && g3 < 40) {
            constexpr int n = (g3 - 32) % 4, t = (g3 - 32) / 4;
            const int kr = 16 * t + 8 * (n >> 1) + 4 * h + 2 * (n & 1);
            u32x4 sv = __builtin_bit_cast(u32x4, sel[t]); sv[n] = opq_v(r == kr ? 0x3f80u : (r == kr + 1 ? 0x3f800000u : 0u)); sel[t] = __builtin_bit_cast(bf16x8, sv);
          }
          if constexpr (g3 == 40) { sel[0] = opq_v(sel[0]); sel[1] = opq_v(sel[1]); }
          if constexpr (g3 == 42) *(LAS unsigned*)(sh + 8192 + 256 + lane * 4) = opq_v(0x3f803f80u);
          if constexpr (g3 == 95) {
            wait2<SQ.zw>(zq[0], zq[1]);
#pragma unroll
            for (int n = 0; n < 4; ++n) { e[n] = __builtin_bit_cast(f32x4, zq[0])[n]; a_add(e[n], __builtin_bit_cast(f32x4, zq[1])[n]); }
          }
          if constexpr (g3 == 96) {
            auto a = __builtin_amdgcn_permlane16_swap(__float_as_uint(e[0]), __float_as_uint(e[1]), false, false);
            auto b = __builtin_amdgcn_permlane16_swap(__float_as_uint(e[2]), __float_as_uint(e[3]), false, false);
            e[0] = opq_v(__uint_as_float(a[0]) + __uint_as_float(a[1])); e[1] = opq_v(__uint_as_float(b[0]) + __uint_as_float(b[1]));
          }
          if constexpr (g3 == 98) { auto a = __builtin_amdgcn_permlane32_swap(__float_as_uint(e[0]), __float_as_uint(e[1]), false, false); e[0] = opq_v(__uint_as_float(a[0]) + __uint_as_float(a[1])); }
          if constexpr (g3 == 100) e[1] = lane_get(e[0], lane ^ 8);
          if constexpr (g3 == 106)
            *(LAS short*)(sh + 8192 + w * 64 + (lane & 1) * 32 + ((lane >> 2) & 1) * 16 + ((lane >> 1) & 1) * 8 + (lane >> 4) * 2) = (short)cvtpk(e[0] + e[1], 0.f);
          if constexpr (g3 >= 116 && g3 < 124 && g3 % 2 == 0) rd_wf(IC((g3 - 116) / 2));
        }
      };
      auto step = [&](auto s, auto fl) {
        constexpr int ps = s / 64;
        constexpr bool first = fl;
        if constexpr (a_bar(s) || (first && s == 6)) wait_bar<(ps ? cmin(S1.bw[s], SQ.bw[s]) : (first ? S1.bw[s] : SQ.bw[s]))>();
        if constexpr (s == 20 || (first && (s == 24 || s == 28))) waitv<(first ? S1.sw[s % 64] : SQ.sw[s % 64])>();
        if constexpr (s == 78) { waitx<false, SQ.fw>(fv); if (!__all(fv == tag)) poll<16>(fwl, lane * 8, tag); }
        if constexpr (s == 142) set_flag(fkvp + rb * 8, tag);
        if constexpr (s == QC) { waitx<false, SQ.pw>(fv); if (!__all(fv == tag)) poll<16>(fkvp, lane * 8, tag); }
        if constexpr (s < 64 && !first) waitx<(s < NA), SQ.xw[s % 64]>(x[s % 64]);
        SBAR();
        if constexpr (ps != 1) mm<false, (s % 64 < NA), s % 64 == 0>(acc[2 * ps], f0[s % 3], x[s % 64]);
        else mm<true, (s % 64 < NA), s % 64 == 0>(acc[3], f2[s % 5], x[s % 64]);
        SBAR();
        rd01(IC(s + 2), IC(0));
        if constexpr (ps == 1) rd2(IC(s + 4));
        if constexpr (s + XL < 64 && !first) issue_x(IC(s + XL));
        if constexpr (s == 65) ld8_1(fv, (lane & 15) * 8, fwl);
        if constexpr (s == QP) { fv = tag; rd_flag<16>(fv, lane * 8, fkvp); }
        if constexpr (s >= QC && s < QC + 17) ldp(IC(2 * (s - QC)));
        aux(s, IC(0), fl);
        SBAR();
        if constexpr (ps != 1) mm<false, (s % 64 < NA), s % 64 == 0>(acc[2 * ps + 1], f1[s % 3], x[s % 64]);
        SBAR();
        rd01(IC(s + 2), IC(1));
        if constexpr (a_piece(s) >= 0) pc_a(IC(a_piece(s)));
        if constexpr (s >= 2 && s < 6) ldx1<false, (s - 2) * 1024>(st.p[s - 2], c.xv, stt);
        if constexpr (first && s >= 6 && s < 10) ld_cs(ck.p[s - 6], 1, IC(s - 6));
        if constexpr (first && s >= 10 && s < 14) ld_cs(cv.p[s - 10], 2, IC(s - 10));
        if constexpr (s == 40) dma<0>(lds0 + 9 * SLOT + w * 1024, c.xv, wph);
        if constexpr (s >= 104 && s < 112 && s % 2 == 0) ld_cs(cq.p[(s - 104) / 2], 0, IC((s - 104) / 2));
        if constexpr (s >= 120 && s < 128 && s % 2 == 0) ld_cs(cw.p[(s - 120) / 2], 4, IC((s - 120) / 2));
        if constexpr (a_park(s) >= 0) ld_park(IC(a_park(s)));
        if constexpr (s >= QC && s < QC + 17) ldp(IC(2 * (s - QC) + 1));
        aux(s, IC(1), fl);
        if constexpr (ps == 0) {
          SBAR();
          mm<true, (s < NA), s == 0>(acc[2], f2[s % 5], x[s]);
          SBAR();
          rd2(IC(s + 2));
          if constexpr (s >= 62) rd2(IC(s + 4));
          if constexpr (first && a_first(s) >= 0) pc_a(IC(a_first(s)));
          aux(s, IC(2), fl);
        }
        if constexpr (ps == 1) { kmap(IC(2 * (s - 64))); kmap(IC(2 * (s - 64) + 1)); }
        if constexpr (s >= 176) sum_mm(IC(s - 176));
        SBAR();
      };
      {
        STAMP(30);
        bf16x8 cf;
        { const char* pc; asm volatile("s_getpc_b64 %0" : "=s"(pc));
          ldx0<0>(cf, c.xv, pc + (((blockIdx.x >> 3) * 4 + w) % CODE_KB) * 1024); }
        raw_in(IC(0), 7);
        u64 ep; ld8_1(ep, 0u, ws + WS_EPOCH);
        bf16x8 xt[4];
        const char* xw = xin + (size_t)tt * 65536;
        sfor<0, 4>([&](auto i) { ldx0<i * 32>(xt[i], (unsigned)(r * 2048 + h * 16), xw + cb * 128); });
        { const unsigned z = 0, vo = (unsigned)(lane ? FL_W + cb * 128 : FL_ACT + tt * 128);
          asm volatile("s_mov_b64 exec, 3\n\tglobal_atomic_or %0, %1, %2 sc1\n\ts_mov_b64 exec, -1" ::"v"(vo), "v"(z), "s"(flags) : "memory"); }
        wait_bar<6>();
        raw_out(IC(0), 7);
        asm volatile("s_waitcnt vmcnt(6)" : "+v"(cf), "+v"(ep), "+v"(xt[0]), "+v"(xt[1]), "+v"(xt[2]), "+v"(xt[3]));
        sfor<0, 4>([&](auto i) { xr[i] = unperm(__builtin_bit_cast(u32x4, xt[i])); });
        store_stats(r, xr, stto);
        raw_in(IC(1), 8); raw_in(IC(2), 9);
        { u64 z = ((u64)__builtin_amdgcn_readfirstlane((unsigned)(ep >> 32)) << 32 | __builtin_amdgcn_readfirstlane((unsigned)ep)) + 0x9E3779B97F4A7C15ull;
          z = (z ^ (z >> 30)) * 0xBF58476D1CE4E5B9ull; z = (z ^ (z >> 27)) * 0x94D049BB133111EBull; tag = z ^ (z >> 31); asm volatile("" : "+s"(tag)); }
        waitv<8>();
        set_flag(facto, tag);
        bar();
        if (w == 0) set_flag(flags + FL_W + cb * 128 + rb * 8, tag);
        const unsigned xs = (unsigned)((lane >> 3) * 2048 + ((lane & 7) ^ (((lane >> 3) & 3) << 1)) * 16), xl = lds0 + w * 28672;
        const unsigned xo = xl + r * 128 + (r >> 3) * 16 + h * 8;
        auto x_in = [&](auto g) { sfor<0, 4>([&](auto i) { dma<i * 1040>(xl + (g % 6) * 4160, xs + i * (16384 - 1040), xw + g * 128); }); };
        sfor<0, 6>(x_in);
        sfor<0, 16>([&](auto g) {
          waitv<4 * (g < 10 ? 5 : 15 - g) + (g > 2 && g < 9 ? 10 : 0)>();
          sfor<4 * g, 4 * g + 4>([&](auto s) { ds_read2<(s < NA)>(x[s], xo + (g % 6) * 4160 + (((s % 4) ^ (r & 3)) << 5)); });
          sfor<4 * g, 4 * g + 4>([&](auto s) { waitl<(s < NA)>(x[s]); });
          if constexpr (g + 6 < 16) x_in(IC(g + 6));
          if constexpr (g == 2) { bar(); raw_out(IC(1), 8); raw_out(IC(2), 9); }
        });
        unsigned tch;
        {
          const int gw = blockIdx.x * 4 + w; const unsigned z = 0;
          const unsigned vo = (unsigned)(lane < 5 ? WS_KVF - WS_ST + (gw * 5 + lane) * 128 : WS_FLAG - WS_ST + gw * 128);
          poll<32>(flags, (unsigned)((lane & 15) * 8 + (blk ? FL_W + cb * 128 : FL_ACT + tt * 128)), tag);
          bar();
          if (w == 0) set_flag(fwl + rb * 8, tag);
          sfor<-20, 0>([&](auto n) { pc_a(IC(a_first(n))); });
          asm volatile("s_mov_b64 exec, 63\n\tglobal_atomic_or %0, %1, %2 sc1\n\ts_mov_b64 exec, -1" ::"v"(vo), "v"(z), "s"(ws + WS_ST) : "memory");
          const char* sm = (gw & 512) ? ((gw & 256) ? wk : wv) : wphi;
          asm volatile("global_load_dword %0, %1, %2" : "=v"(tch) : "v"((unsigned)((gw & 255) * 256 + lane * 4)), "s"(sm));
        }
        STAMP(31); STAMP(0); STAMP(1);
        wait_bar<14>();
        sfor<0, 2>([&](auto s) { rd01(s, IC(0)); rd01(s, IC(1)); rd2(s); });
        sfor<0, 64>([&](auto s) { step(s, std::true_type{}); });
        waitx<false, 63>(tch);
      }
  asm volatile("" : "+s"(w), "+s"(tag), "+s"(act), "+s"(acto), "+s"(fact), "+s"(facto), "+s"(stt), "+s"(stto), "+s"(col), "+s"(colw), "+s"(ldsw));
  asm volatile("" : "+s"(kvp), "+s"(zp), "+s"(fkvp), "+s"(fwl), "+s"(rb), "+s"(kvf), "+s"(fkv), "+s"(xu_p), "+s"(su_p), "+s"(wkv), "+s"(csm), "+s"(l));
  for (;;) {
    {
      REFRESH();
      asm volatile("" : "+s"(kvp), "+s"(zp), "+s"(fkvp), "+s"(fwl), "+s"(rb));
      STAMP(2);
      sfor<64, 128>([&](auto s) { step(s, std::false_type{}); });
      STAMP(19);
      mdone(acc[3]);
      v_out(IC(1), IC(0)); v_out(IC(1), IC(1));
      lds_bar();
      {
        const int hd = w >> 1, fh = w & 1;
        f32x16 kv, z;
        const int ch = fh * 4 + 2 * blk + (p4 >> 1), r0 = 4 * h + q4;
        LAS const char* pa = pk_img + hd * 16384 + r0 * 128 + ((ch ^ r0) << 4) + 8 * (p4 & 1);
        LAS const char* pb = v_img + hd * 8192 + lane * 16;
        bf16x8 a[8], b[8], d[8];
#pragma unroll
        for (int s = 0; s < 8; ++s) { a[s] = cat8(trread(pa + s * 2048), trread(pa + s * 2048 + 1024)); b[s] = *(LAS const bf16x8*)(pb + s * 1024); }
#pragma unroll
        for (int s = 0; s < 8; ++s) d[s] = *(LAS const bf16x8*)(sh + 8704 + s * 32 + h * 16);
        SBAR();
        sfor<0, 8>([&](auto s) { mm<false, false, s == 0>(kv, a[s], b[s]); });
        sfor<0, 8>([&](auto s) {
          mm<false, false, s == 0>(z, d[s], a[s]);
          if constexpr (s == 1) { mdone(kv); st4_1<0>(__builtin_bit_cast(u32x4, pack8<0>(kv)), c.xv, kvp + rb * 2048); }
          if constexpr (s == 2) { st4_1<1024>(__builtin_bit_cast(u32x4, pack8<1>(kv)), c.xv, kvp + rb * 2048); waitv<SQ.cw>(); }
          SBAR();
        });
        mdone(z);
        st1_1(z[0], (unsigned)(r * 4), zp + rb * 128);
      }
      STAMP(3);
      sfor<128, 192>([&](auto s) { step(s, std::false_type{}); });
      mdone(acc[4], acc[5]);
      STAMP(4);
      {
        bf16x8 pq[2][4], ka[8], za[8];
        f32x16 ep, en, nm[4], dn[2];
        float ai[2], bi[2];
        char* dst = acto + STG;
        sfor<0, 4>([&](auto k) { mm<false, false, k % 2 == 0, true>(p[k / 2], wf[k], pack8<k % 2>(acc[4 + k / 2])); });
        mdone(p[0], p[1]);
        hedge_exp(p[0], sce, ep, en, [&](auto i) { sum_mm(IC(16 + i)); });
        pq[0][0] = pack8<0>(ep); pq[0][1] = pack8<1>(ep); pq[0][2] = pack8<0>(en); pq[0][3] = pack8<1>(en);
        pin4(pq[0]);
        asm volatile("" : "+v"(kvs));
        *(LAS bf16x8*)(sh + w * 2048 + lane * 16) = pack8<0>(kvs); *(LAS bf16x8*)(sh + w * 2048 + 1024 + lane * 16) = pack8<1>(kvs);
        lds_bar();
        STAMP(5);
        LAS const char* zb = sh + 8192 + (r & 1) * 256 + h * 32;
#pragma unroll
        for (int k = 0; k < 8; ++k) { za[k] = *(LAS const bf16x8*)(zb + (k >> 1) * 64 + (k & 1) * 16); ka[k] = *(LAS const bf16x8*)(sh + k * 1024 + lane * 16); }
        auto coef = [&](auto hd) {
          const float a = __builtin_amdgcn_rcpf(dn[0][1]), b = __builtin_amdgcn_rcpf(dn[1][1]);
          const float inv = __builtin_amdgcn_rcpf(dn[0][0] * a + dn[1][0] * b + 1e-6f);
          ai[hd] = opq_v(a * inv); bi[hd] = opq_v(b * inv);
        };
        auto fin = [&](auto hd) {
          f32x16 o;
#pragma unroll
          for (int i = 0; i < 16; ++i) o[i] = nm[2 * hd][i] * ai[hd] + nm[2 * hd + 1][i] * bi[hd];
          st4_1<hd * 2048>(__builtin_bit_cast(u32x4, pack8<0>(o)), c.xv, dst);
          st4_1<hd * 2048 + 1024>(__builtin_bit_cast(u32x4, pack8<1>(o)), c.xv, dst);
        };
        hedge_exp(p[1], sce, ep, en, [&](auto i) {
          if constexpr (i < 4) mm<false, false, i % 2 == 0>(dn[i / 2], za[i], pq[0][i]);
          else if constexpr (i < 8) mm<false, false, i % 2 == 0>(nm[i / 2 - 2], ka[i - 4], pq[0][i - 4]);
          if constexpr (i % 2) pc_a(IC(108 + i / 2));
        });
        pq[1][0] = pack8<0>(ep); pq[1][1] = pack8<1>(ep); pq[1][2] = pack8<0>(en); pq[1][3] = pack8<1>(en);
        pin4(pq[1]); pin2(dn[0], dn[1]); pin2(nm[0], nm[1]);
        coef(IC(0));
        STAMP(6);
        sfor<0, 4>([&](auto k) { mm<false, false, k % 2 == 0, k == 0>(dn[k / 2], za[4 + k], pq[1][k]); if constexpr (k % 2) pc_a(IC(116 + k / 2)); });
        sfor<0, 4>([&](auto k) { mm<false, false, k % 2 == 0>(nm[2 + k / 2], ka[4 + k], pq[1][k]); if constexpr (k % 2) pc_a(IC(118 + k / 2)); });
        fin(IC(0));
        pin2(dn[0], dn[1]); pin2(nm[2], nm[3]);
        coef(IC(1));
        fin(IC(1));
        sfor<120, 124>(pc_a);
        waitv<4>();
        set_flag(facto + 8192, tag);
        sfor<124, 128>(pc_a); sfor<8, 24>(ld_park);
      }
    }
    STAMP(7);
    REFRESH();
    LAS char* reg = smem + 8 * SLOT;
    bf16x8 fu[16], cu;
    {
      f32x16 a0, a1; res_to_acc(xr, a0, a1);
      poll<16>(fact + 8192, lane * 8, tag);
      sfor<0, 24>([&](auto i) { waitx<true, 63>(park[i]); });
      STAMP(8);
      bf16x8 x[64];
      const char* xb = act + STG;
      sfor<0, XL>([&](auto s) { ld_x<s>(x, c, xb); });
      bar();
      asm volatile("" : "+s"(wkv));
      const unsigned kvv = (unsigned)((lane >> 1) * 64 + (cb & 1) * 32 + (lane & 1) * 16);
      const unsigned wa = ldsw + c.xv, wb = wa + 65536;
      auto unpark = [&](auto i) {
        constexpr int off = a_slot(26 + i / 4) * SLOT + (i % 4) * 1024;
        ds_write_a<(off & 65535)>(off < 65536 ? wa : wb, park[i]);
      };
      gemm64<true, false, HookP3>(c, x, xb, a0, a1, [&](auto s) {
        if constexpr (s == 33) bar();
        if constexpr (s >= 36 && s < 44) unpark(IC(s - 36));
        if constexpr (s >= 52 && s < 60) dma<0>(lds0 + 8 * SLOT + w * 8192 + (s - 52) * 1024, kvv, wkv + (s - 52) * 32 * 64);
        if constexpr (s >= 60) pc_w(4, a_slot(24), 0, IC(s - 60));
      }, [](auto) {});
      STAMP(9);
      store_x(c, r, a0, a1, xr, acto + 2 * STG, stto + 256 * 1024);
      waitv<5>();
      { LAS const char* p = reg + w * 8192 + (4 * h + q4) * 32 + p4 * 8;
        float t0 = 0.f, t1 = 0.f;
#pragma unroll
        for (int i = 0; i < 16; ++i) {
          fu[i] = cat8(trread(p + i * 512), trread(p + i * 512 + 256));
          const u32x4 f = __builtin_bit_cast(u32x4, fu[i]);
          t0 = dot2(f[0], 0x3f803f80u, t0); t1 = dot2(f[1], 0x3f803f80u, t1); t0 = dot2(f[2], 0x3f803f80u, t0); t1 = dot2(f[3], 0x3f803f80u, t1);
        }
        cu = opq_v(cs_frag(swap_add(t0 + t1))); }
      waitv<0>();
      set_flag(facto + 2 * 8192, tag);
      sfor<4, 8>([&](auto i) { pc_w(4, a_slot(24 + i / 4), i / 4, IC(i % 4)); });
      bar();
      sfor<8, 24>(unpark);
      lds_bar();
    }
    STAMP(10);
    REFRESH();
    f32x16 a0, a1;
    asm volatile("" : "+s"(kvf), "+s"(fkv), "+s"(xu_p), "+s"(su_p));
    {
      poll<32>(fact + 2 * 8192 - w * 128, (unsigned)((lane & 15) * 8 + (blk ? cb >> 2 : w) * 128), tag);
      STAMP(11);
      const char* xb = act + 2 * STG; const char* stp = stt + 256 * 1024;
      bf16x8 x[64], xu[16], mf0, mf1;
      Red<false> su, sp;
      sfor<0, 4>([&](auto i) { ldx1<false, i * 1024>(su.p[i], c.xv, su_p); });
      sfor<0, 16>([&](auto i) { ldx1<true, (i % 8) * 1024 - 4096>(xu[i], c.xv, xu_p + (i / 8) * 8192); });
      f32x16 au, av;
      sfor<0, 16>([&](auto i) {
        waitx<true, 15 - i + (i < XL ? i : XL)>(xu[i]);
        SBAR();
        mm<false, true, i == 0>(au, fu[i], xu[i]);
        if constexpr (i < XL) ld_x<i>(x, c, xb);
        mm<true, true, i == 0>(av, fu[i], xu[i]);
        if constexpr (i < 11) su.template run<i>(lane);
        if constexpr (i == 12) mf0 = opq_v(mean_frag(su.a, h == 0));
        if constexpr (i == 14) { mm<false, false, false>(au, cu, mf0); mm<true, false, false>(av, cu, mf0); }
        SBAR();
      });
      STAMP(12);
      u64 fa;
      f32x4 t[4], ru;
      unsigned vo;
      gemm64<true, true, HookP4>(c, x, xb, a0, a1, [&](auto s) {
        if constexpr (s == 1) {
          pin2(au, av);
          auto put = [&](const f32x16& a) {
#pragma unroll
            for (int gq = 0; gq < 4; ++gq) *(LAS f32x4*)(reg + w * 8192 + gq * 1024 + lane * 16) = f32x4{a[4 * gq], a[4 * gq + 1], a[4 * gq + 2], a[4 * gq + 3]};
          };
          if (ty < 2) { put(au); asm volatile("" ::: "memory"); } else put(av);
        }
        if constexpr (s == 3) {
          lds_bar();
#pragma unroll
          for (int j = 0; j < 4; ++j) t[j] = *(LAS const f32x4*)(reg + j * 8192 + (ty < 2 ? w & 1 : w) * 1024 + lane * 16);
        }
        if constexpr (s == 6) st2_1(__builtin_bit_cast(f32x2, u32x2{cvtpk(t[0][0], t[0][1]), cvtpk(t[0][2], t[0][3])}), vo, kvf);
        if constexpr (s >= 8 && s < 12) ldx1<false, (s - 8) * 1024>(sp.p[s - 8], c.xv, stp);
        if constexpr (s == 7 + XL) { bar(); if (w == 0) set_flag(fkv + (ut * 4 + ty) * 8, tag); }
        if constexpr (s == 52) ld8_1(fa, lane * 8 + w * 512, fkv);
      }, [&](auto g) {
        if constexpr (g == 0) {
#pragma unroll
          for (int e = 0; e < 4; ++e) ru[e] = lane_get(su.b, ty < 2 ? r : 8 * w + 4 * h + e);
        }
        if constexpr (g == 4) vo = opq_v(ut * 4096 + (ty < 2 ? (unsigned)(ty * 1024 + lane * 16 + (w & 1) * 8)
                                                               : (unsigned)(2048 + (w >> 1) * 1024 + ((ty & 1) * 16 + (r & 15) + 32 * (w & 1)) * 16 + h * 8)));
        if constexpr (g >= 9 && g < 13) {
#pragma unroll
          for (int e = 0; e < 4; ++e) { float v = t[0][e]; if constexpr (g < 12) a_add(v, t[g - 8][e]); else a_mul(v, ru[e]); t[0][e] = v; }
        }
        if constexpr (g >= 48 && g < 59) sp.template run<g - 48>(lane);
        if constexpr (g == 60) mf0 = opq_v(mean_frag(sp.a, h == 0));
        if constexpr (g == 61) mf1 = opq_v(mean_frag(sp.a, h == 1));
        if constexpr (g == 64) mm<false, false, false>(a0, cw.p[0], mf0);
        if constexpr (g == 65) mm<false, false, false>(a1, cw.p[0], mf1);
      });
      rstd = sp.b;
      waitx<false, 0>(fa);
#pragma unroll 1
      for (int spin = 0; spin < (1 << 15) && !__all(fa == tag); ++spin) { __builtin_amdgcn_s_sleep(8); ld8_1(fa, lane * 8 + w * 512, fkv); waitx<false, 0>(fa); }
      bar();
      STAMP(13);
    }
    STAMP(14);
    {
      u32x16 qq, misc;
      misc[14] = misc[15] = __float_as_uint(rstd * QSCALE);
#pragma unroll 1
      for (int attempt = 0; attempt < 2; ++attempt) {
        if (attempt == 1) {
          const bf16x8 qa0 = __builtin_bit_cast(bf16x8, __builtin_shufflevector(qq, qq, 0, 1, 2, 3)), qa1 = __builtin_bit_cast(bf16x8, __builtin_shufflevector(qq, qq, 4, 5, 6, 7));
          const bf16x8 qb0 = __builtin_bit_cast(bf16x8, __builtin_shufflevector(qq, qq, 8, 9, 10, 11)), qb1 = __builtin_bit_cast(bf16x8, __builtin_shufflevector(qq, qq, 12, 13, 14, 15));
          float mA = -INFINITY, mB = -INFINITY;
          for (int t = 0; t < 64; ++t) {
            bf16x8 k0, k1;
            ldx1<false, 0>(k0, c.xv, kvf + t * 4096); ldx1<false, 1024>(k1, c.xv, kvf + t * 4096);
            wait2<0>(k0, k1);
            mm<false, false, true>(a0, k0, qa0); mm<false, false, false>(a0, k1, qa1); mm<false, false, true>(a1, k0, qb0); mm<false, false, false>(a1, k1, qb1);
            mdone(a0, a1);
            mA = fmaxf(mA, max16(a0)); mB = fmaxf(mB, max16(a1));
          }
          misc[14] = __float_as_uint(swap_max(mA)); misc[15] = __float_as_uint(swap_max(mB));
        }
        f32x16 oa, ob, t0, t1, t2, t3;
        u32x16 kvr; u32x4 offs;
        offs[0] = c.xv;
        const char* ks = kvf + 2048 + w * 2048; const char* wsr = wt(5, 0);
        unsigned wm0 = ldsw, cnt = 8;
        asm volatile(
            "global_load_dwordx4 v[208:211], v240, %[kv] sc1\n\tglobal_load_dwordx4 v[212:215], v240, %[kv] offset:1024 sc1\n\t"
            "s_mov_b32 m0, s44\n\tv_add_u32 v241, 0x2000, v240\n\t"
            "global_load_lds_dwordx4 v240, s[40:41] sc1\n\tglobal_load_lds_dwordx4 v240, s[40:41] offset:1024 sc1\n\t"
            "s_add_u32 m0, s44, 0x2000\n\tv_add_u32 v242, 0x4000, v240\n\t"
            "global_load_lds_dwordx4 v241, s[40:41] sc1\n\tglobal_load_lds_dwordx4 v241, s[40:41] offset:1024 sc1\n\t"
            "s_add_u32 m0, s44, 0x4000\n\tv_add_u32 v243, 0x6000, v240\n\t"
            "global_load_lds_dwordx4 v242, s[40:41] sc1\n\tglobal_load_lds_dwordx4 v242, s[40:41] offset:1024 sc1\n\t"
            "s_add_u32 s40, s40, 0x6000\n\ts_addc_u32 s41, s41, 0\n\t"
            "s_cmp_lg_u32 %[att], 0\n\ts_cbranch_scc1 2f\n\t"
            "v_pk_mul_f32 v[128:129], v[128:129], v[238:239]\n\t"
            "v_pk_mul_f32 v[130:131], v[130:131], v[238:239]\n\t"
            "v_pk_mul_f32 v[132:133], v[132:133], v[238:239]\n\t"
            "v_pk_mul_f32 v[134:135], v[134:135], v[238:239]\n\t"
            "v_pk_mul_f32 v[136:137], v[136:137], v[238:239]\n\t"
            "v_pk_mul_f32 v[138:139], v[138:139], v[238:239]\n\t"
            "v_pk_mul_f32 v[140:141], v[140:141], v[238:239]\n\t"
            "v_pk_mul_f32 v[142:143], v[142:143], v[238:239]\n\t"
            "v_pk_mul_f32 v[144:145], v[144:145], v[238:239]\n\t"
            "v_pk_mul_f32 v[146:147], v[146:147], v[238:239]\n\t"
            "v_pk_mul_f32 v[148:149], v[148:149], v[238:239]\n\t"
            "v_pk_mul_f32 v[150:151], v[150:151], v[238:239]\n\t"
            "v_pk_mul_f32 v[152:153], v[152:153], v[238:239]\n\t"
            "v_pk_mul_f32 v[154:155], v[154:155], v[238:239]\n\t"
            "v_pk_mul_f32 v[156:157], v[156:157], v[238:239]\n\t"
            "v_pk_mul_f32 v[158:159], v[158:159], v[238:239]\n\t"
            "v_cvt_pk_bf16_f32 v192, v128, v129\n\t"
            "v_cvt_pk_bf16_f32 v193, v130, v131\n\t"
            "v_cvt_pk_bf16_f32 v194, v132, v133\n\t"
            "v_cvt_pk_bf16_f32 v195, v134, v135\n\t"
            "v_cvt_pk_bf16_f32 v196, v136, v137\n\t"
            "v_cvt_pk_bf16_f32 v197, v138, v139\n\t"
            "v_cvt_pk_bf16_f32 v198, v140, v141\n\t"
            "v_cvt_pk_bf16_f32 v199, v142, v143\n\t"
            "v_cvt_pk_bf16_f32 v200, v144, v145\n\t"
            "v_cvt_pk_bf16_f32 v201, v146, v147\n\t"
            "v_cvt_pk_bf16_f32 v202, v148, v149\n\t"
            "v_cvt_pk_bf16_f32 v203, v150, v151\n\t"
            "v_cvt_pk_bf16_f32 v204, v152, v153\n\t"
            "v_cvt_pk_bf16_f32 v205, v154, v155\n\t"
            "v_cvt_pk_bf16_f32 v206, v156, v157\n\t"
            "v_cvt_pk_bf16_f32 v207, v158, v159\n\t"
            "2:\n\t"
            "v_mov_b64 v[64:65], 0\n\t"
            "v_mov_b64 v[66:67], 0\n\t"
            "v_mov_b64 v[68:69], 0\n\t"
            "v_mov_b64 v[70:71], 0\n\t"
            "v_mov_b64 v[72:73], 0\n\t"
            "v_mov_b64 v[74:75], 0\n\t"
            "v_mov_b64 v[76:77], 0\n\t"
            "v_mov_b64 v[78:79], 0\n\t"
            "v_mov_b64 v[80:81], 0\n\t"
            "v_mov_b64 v[82:83], 0\n\t"
            "v_mov_b64 v[84:85], 0\n\t"
            "v_mov_b64 v[86:87], 0\n\t"
            "v_mov_b64 v[88:89], 0\n\t"
            "v_mov_b64 v[90:91], 0\n\t"
            "v_mov_b64 v[92:93], 0\n\t"
            "v_mov_b64 v[94:95], 0\n\t"
            "v_mov_b64 v[176:177], 0\n\t"
            "v_mov_b64 v[178:179], 0\n\t"
            "v_mov_b64 v[180:181], 0\n\t"
            "v_mov_b64 v[182:183], 0\n\t"
            "v_mov_b64 v[184:185], 0\n\t"
            "v_mov_b64 v[186:187], 0\n\t"
            "v_mov_b64 v[188:189], 0\n\t"
            "v_mov_b64 v[190:191], 0\n\t"
            "v_mov_b64 v[216:217], 0\n\t"
            "v_mov_b64 v[218:219], 0\n\t"
            "v_mov_b64 v[220:221], 0\n\t"
            "v_mov_b64 v[222:223], 0\n\t"
            "v_mov_b64 v[228:229], 0\n\t"
            "v_mov_b64 v[230:231], 0\n\t"
            "v_mov_b64 v[232:233], 0\n\t"
            "v_mov_b64 v[234:235], 0\n\t"
            "v_bfe_u32 v226, v240, 6, 1\n\tv_mov_b32 v236, 0xdddddddd\n\tv_mul_u32_u24 v226, 0x3f80, v226\n\tv_add_u32 v237, %[rg], v240\n\t"
            "v_lshl_or_b32 v226, v226, 16, v226\n\tv_mov_b32 v227, v226\n\tv_xor_b32 v224, 0x3f803f80, v226\n\tv_mov_b32 v225, v224\n\t"
            "s_waitcnt vmcnt(6)\n\ts_cbranch_scc1 3f\n\t"
            "v_mfma_f32_32x32x16_bf16 v[128:143], v[208:211], v[192:195], 0\n\tv_mfma_f32_32x32x16_bf16 v[160:175], v[208:211], v[200:203], 0\n\t"
            "v_mfma_f32_32x32x16_bf16 v[128:143], v[212:215], v[196:199], v[128:143]\n\tv_mfma_f32_32x32x16_bf16 v[160:175], v[212:215], v[204:207], v[160:175]\n\t"
            "s_nop 11\n\t"
            "v_max3_f32 v238, v128, v129, v130\n\tv_max3_f32 v239, v160, v161, v162\n\t"
            "v_max3_f32 v238, v238, v131, v132\n\tv_max3_f32 v239, v239, v163, v164\n\t"
            "v_max3_f32 v238, v238, v133, v134\n\tv_max3_f32 v239, v239, v165, v166\n\t"
            "v_max3_f32 v238, v238, v135, v136\n\tv_max3_f32 v239, v239, v167, v168\n\t"
            "v_max3_f32 v238, v238, v137, v138\n\tv_max3_f32 v239, v239, v169, v170\n\t"
            "v_max3_f32 v238, v238, v139, v140\n\tv_max3_f32 v239, v239, v171, v172\n\t"
            "v_max3_f32 v238, v238, v141, v142\n\tv_max3_f32 v239, v239, v173, v174\n\t"
            "v_max3_f32 v238, v238, v143, v143\n\tv_max3_f32 v239, v239, v175, v175\n\t"
            "v_mov_b32 v96, v238\n\tv_mov_b32 v97, v239\n\ts_nop 1\n\tv_permlane32_swap_b32 v238, v96\n\tv_permlane32_swap_b32 v239, v97\n\t"
            "v_max_f32 v238, v238, v96\n\tv_max_f32 v239, v239, v97\n\t"
            "3:\n\t"
            "v_xor_b32 v96, 0x80000000, v238\n\tv_xor_b32 v112, 0x80000000, v239\n\tv_mov_b32 v97, v96\n\tv_mov_b32 v113, v112\n\t"
            "v_mov_b64 v[98:99], v[96:97]\n\tv_mov_b64 v[114:115], v[112:113]\n\t"
            "v_mov_b64 v[100:101], v[96:97]\n\tv_mov_b64 v[116:117], v[112:113]\n\t"
            "v_mov_b64 v[102:103], v[96:97]\n\tv_mov_b64 v[118:119], v[112:113]\n\t"
            "v_mov_b64 v[104:105], v[96:97]\n\tv_mov_b64 v[120:121], v[112:113]\n\t"
            "v_mov_b64 v[106:107], v[96:97]\n\tv_mov_b64 v[122:123], v[112:113]\n\t"
            "v_mov_b64 v[108:109], v[96:97]\n\tv_mov_b64 v[124:125], v[112:113]\n\t"
            "v_mov_b64 v[110:111], v[96:97]\n\tv_mov_b64 v[126:127], v[112:113]\n\t"
            "s_nop 1\n\t" S0_(128, 192, 96) S1_(128, 192)
            "s_waitcnt vmcnt(4)\n\ts_barrier\n\ts_nop 8\n\t"
            "1:\n\t"
            STEP_(0, 24576, 240, 0, "s_sub_u32 %[cnt], %[cnt], 1\n\t", "") STEP_(8192, 0, 241, 1024, "", "") STEP_(16384, 8192, 242, 2048, "", "")
            STEP_(24576, 16384, 243, 3072, "s_add_u32 s40, s40, 0x8000\n\ts_addc_u32 s41, s41, 0\n\t", "s_add_u32 s42, s42, 0x4000\n\ts_addc_u32 s43, s43, 0\n\t")
            "s_add_u32 s45, s45, 0x4000\n\ts_cmp_lg_u32 %[cnt], 0\n\ts_cbranch_scc1 1b\n\t"
            L0_(232, 176) L1_(232, 176) PV0_(80, 176) PV1_(80, 176)
            "s_nop 11\n\t"
            : "=&{v[64:79]}"(oa), "=&{v[80:95]}"(ob), "=&{v[96:111]}"(t0), "=&{v[112:127]}"(t1), "+{v[128:143]}"(a0), "+{v[144:159]}"(a1), "=&{v[160:175]}"(t2),
              "=&{v[176:191]}"(t3), "+{v[192:207]}"(qq), "=&{v[208:223]}"(kvr), "+{v[224:239]}"(misc), "+{v[240:243]}"(offs),
              "+{s[40:41]}"(ks), "+{s[42:43]}"(wsr), "+{s45}"(wm0), [cnt] "+s"(cnt)
            : "{s44}"(lds0 + 8 * SLOT + w * 2048), [kv] "s"(kvf), [att] "s"(attempt), [rg] "s"(lds0 + 8 * SLOT)
            : "scc", "memory");
        const float la = __uint_as_float(misc[4]), lb = __uint_as_float(misc[8]);
        const float ia = __builtin_amdgcn_rcpf(la), ib = __builtin_amdgcn_rcpf(lb);
#pragma unroll
        for (int e = 0; e < 16; ++e) { oa[e] *= ia; ob[e] *= ib; }
        char* dst = acto + 3 * STG;
        st4_1<0>(__builtin_bit_cast(u32x4, pack8<0>(oa)), c.xv, dst); st4_1<1024>(__builtin_bit_cast(u32x4, pack8<1>(oa)), c.xv, dst);
        st4_1<2048>(__builtin_bit_cast(u32x4, pack8<0>(ob)), c.xv, dst); st4_1<3072>(__builtin_bit_cast(u32x4, pack8<1>(ob)), c.xv, dst);
        float chk = 0.f;
#pragma unroll
        for (int e = 0; e < 16; ++e) chk = __builtin_fmaf(oa[e], 0.f, __builtin_fmaf(ob[e], 0.f, chk));
        const bool bad = !(la < 1.2676506e30f) || !(lb < 1.2676506e30f) || !(chk == 0.f);
        wait_bar<4>();
        LAS volatile int* vote = (LAS volatile int*)(smem + 8 * SLOT);
        if (lane == 0) vote[w] = 0;
        if (attempt == 0 && bad) vote[w] = 1;
        lds_bar();
        const int redo = vote[0] | vote[1] | vote[2] | vote[3];
        if (!__builtin_amdgcn_readfirstlane(redo)) break;
        lds_bar();
      }
      waitv<0>();
      set_flag(facto + 3 * 8192, tag);
    }
    STAMP(15);
    REFRESH();
    {
      f32x16 a0, a1; res_to_acc(xr, a0, a1);
      poll<16>(fact + 3 * 8192, lane * 8, tag);
      STAMP(16);
      bf16x8 x[64];
      const char* xb = act + 3 * STG;
      sfor<0, XL>([&](auto s) { ld_x<s>(x, c, xb); });
      bar();
      gemm64<false, false, HookP6>(c, x, xb, a0, a1, [&](auto s) {
        if constexpr (s == 33) bar();
        if constexpr (s >= 52) pc_a(IC(a_pre(s - 52)));
      }, [](auto) {});
      STAMP(17);
      if (l == 0) {
        store_x(c, r, a0, a1, xr, acto + 4 * STG, stto + 512 * 1024);
        bar();
        sfor<12, 16>([&](auto n) { pc_a(IC(a_pre(n))); });
        waitv<4>();
        set_flag(facto + 4 * 8192, tag);
        sfor<16, 32>([&](auto n) { pc_a(IC(a_pre(n))); });
      } else {
        char* dst = out + (size_t)(tt * 32 + r) * 2048 + cb * 128 + h * 16;
        *(u32x4*)(dst) = unperm(__builtin_bit_cast(u32x4, pack8<0>(a0))); *(u32x4*)(dst + 32) = unperm(__builtin_bit_cast(u32x4, pack8<1>(a0)));
        *(u32x4*)(dst + 64) = unperm(__builtin_bit_cast(u32x4, pack8<0>(a1))); *(u32x4*)(dst + 96) = unperm(__builtin_bit_cast(u32x4, pack8<1>(a1)));
        if (tt == 0 && cb == 0) set_flag(ws + WS_EPOCH, tag);
      }
    }
    STAMP(18);
    if (l == 1) break;
    csm = 1;
    act += 4 * STG; acto += 4 * STG; fact += 4 * 8192; facto += 4 * 8192; stt += 512 * KB; stto += 512 * KB;
    kvp += 131072; zp += 8192; fkvp += 8192; kvf += 320 * KB; fkv += 2048; xu_p += 4 * STG; su_p += 512 * KB;
    l = 1;
    REFRESH();
    STAMP(0);
        poll<16>(fact, lane * 8, tag);
        STAMP(1);
        sfor<0, XL>(issue_x);
        wait_bar<XL>();
        sfor<0, 2>([&](auto s) { rd01(s, IC(0)); rd01(s, IC(1)); rd2(s); });
        sfor<0, 64>([&](auto s) { step(s, std::false_type{}); });
  }
}

__global__ __launch_bounds__(256) void k_pre(const char* a0, const char* a1, const char* a2, const char* a3, const char* a4, char* ws, int mode) {
  const int t = blockIdx.x * 256 + threadIdx.x;
  unsigned acc = 0;
  if (mode == 1) {
    acc += *(const unsigned*)(a0 + (size_t)t * 16) + *(const unsigned*)(a1 + (size_t)t * 24) + *(const unsigned*)(a2 + (size_t)t * 8) + *(const unsigned*)(a3 + (size_t)t * 8) + *(const unsigned*)(a4 + (size_t)t * 8);
  } else if (mode == 2) {
    acc += *(const unsigned*)(ws + (size_t)t * 128);
    const int cb = t >> 14, o = t & 16383; if (o < 9000) acc += *(const unsigned*)(ws + WS_COL + (size_t)cb * 2 * MB + o * 128);
    if (t < 16384) acc += *(const unsigned*)(ws + WS_ST + (size_t)t * 128);
  } else if (mode == 3) {
    if (t < 66 * 256) acc += *(const unsigned*)(ws + (size_t)t * 4096 + 64);
  }
  if (acc == 0x12345678u) *(unsigned*)(ws + 200 * MB) = acc;
}
extern "C" void kernel_launch(void* const* d_in, const int* in_sizes, int n_in, void* d_out, int out_size, void* d_ws, size_t ws_size, hipStream_t stream) {
  k_pre<<<1024, 256, 0, stream>>>((const char*)d_in[0], (const char*)d_in[1], (const char*)d_in[3], (const char*)d_in[5], (const char*)d_in[8], (char*)d_ws, 3);
  k_main<<<256, 256, 0, stream>>>((const char*)d_in[0], (const char*)d_in[1], (const char*)d_in[2], (const char*)d_in[3], (const char*)d_in[5], (const char*)d_in[6],
                                  (const char*)d_in[7], (const char*)d_in[8], (char*)d_ws, (char*)d_out);
}
```

```cpp
#include <hip/hip_runtime.h>
#include <hip/hip_bf16.h>
#include <stdint.h>
#include <type_traits>

typedef __attribute__((ext_vector_type(8))) short bf16x8;
typedef __attribute__((ext_vector_type(4))) short s16x4;
typedef __attribute__((ext_vector_type(2))) __bf16 bf2_t;
typedef __attribute__((ext_vector_type(16))) float f32x16;
typedef __attribute__((ext_vector_type(4))) float f32x4;
typedef __attribute__((ext_vector_type(2))) float f32x2;
typedef __attribute__((ext_vector_type(4))) unsigned u32x4;
typedef __attribute__((ext_vector_type(2))) unsigned u32x2;
#define LAS __attribute__((address_space(3)))
#define DI __device__ __forceinline__
#define MFMA32(a, b, c) __builtin_amdgcn_mfma_f32_32x32x16_bf16((a), (b), (c), 0, 0, 0)
#define MFMA16(a, b, c) __builtin_amdgcn_mfma_f32_16x16x32_bf16((a), (b), (c), 0, 0, 0)
#define SBAR() __builtin_amdgcn_sched_barrier(0)

constexpr int S_ = 2048, D_ = 1024;
constexpr float LOG2E = 1.4426950408889634f;
constexpr float QSCALE = 0.17677669529663687f * LOG2E;

constexpr size_t MB = 1u << 20, KB = 1024;
constexpr size_t STG = 256 * KB;
constexpr size_t WS_COL = 32 * MB;
constexpr size_t C_WT = 0;
constexpr size_t C_KVP = 768 * KB;
constexpr size_t C_ZP = 1024 * KB;
constexpr size_t C_CS = 1040 * KB;
constexpr size_t WS_ST = 64 * MB;
constexpr size_t WS_KVF = WS_ST + 1024 * KB;
constexpr size_t WS_FLAG = WS_ST + 1664 * KB;
constexpr size_t WS_EPOCH = WS_ST + 1920 * KB;
constexpr int FL_I = 2048, FL_K = 128;
typedef unsigned long long u64;

template <int I, int N, class F> DI void sfor(F&& f) { if constexpr (I < N) { f(std::integral_constant<int, I>{}); sfor<I + 1, N>(f); } }

DI unsigned cvtpk(float lo, float hi) { f32x2 v = {lo, hi}; return __builtin_bit_cast(unsigned, __builtin_convertvector(v, bf2_t)); }
DI float bflo(unsigned u) { return __uint_as_float(u << 16); }
DI float bfhi(unsigned u) { return __uint_as_float(u & 0xffff0000u); }
DI float rbf(float x) { return bflo(cvtpk(x, x)); }
DI float swap_add(float v) { auto r = __builtin_amdgcn_permlane32_swap(__float_as_uint(v), __float_as_uint(v), false, false); return __uint_as_float(r[0]) + __uint_as_float(r[1]); }
DI float swap_max(float v) { auto r = __builtin_amdgcn_permlane32_swap(__float_as_uint(v), __float_as_uint(v), false, false); return fmaxf(__uint_as_float(r[0]), __uint_as_float(r[1])); }
DI float swap_min(float v) { auto r = __builtin_amdgcn_permlane32_swap(__float_as_uint(v), __float_as_uint(v), false, false); return fminf(__uint_as_float(r[0]), __uint_as_float(r[1])); }
template <int S> DI bf16x8 pack8(const f32x16& v) {
  u32x4 p = {cvtpk(v[8 * S], v[8 * S + 1]), cvtpk(v[8 * S + 2], v[8 * S + 3]), cvtpk(v[8 * S + 4], v[8 * S + 5]), cvtpk(v[8 * S + 6], v[8 * S + 7])};
  return __builtin_bit_cast(bf16x8, p);
}
DI u32x4 unperm(u32x4 v) {
  auto r0 = __builtin_amdgcn_permlane32_swap(v[0], v[2], false, false);
  auto r1 = __builtin_amdgcn_permlane32_swap(v[1], v[3], false, false);
  return u32x4{r0[0], r1[0], r0[1], r1[1]};
}
DI float lane_get(float v, int src) { return __uint_as_float(__builtin_amdgcn_ds_bpermute(src << 2, __float_as_uint(v))); }
DI s16x4 trread(LAS const char* p) { return __builtin_amdgcn_ds_read_tr16_b64_v4i16((LAS s16x4*)p); }
DI bf16x8 cat8(s16x4 a, s16x4 b) { return __builtin_shufflevector(a, b, 0, 1, 2, 3, 4, 5, 6, 7); }
DI float dot2(unsigned a, unsigned b, float c) { return __builtin_amdgcn_fdot2_f32_bf16(__builtin_bit_cast(bf2_t, a), __builtin_bit_cast(bf2_t, b), c, false); }

template <int OFF> DI void dma(unsigned lds, unsigned v, const char* sb) {
  asm volatile("s_mov_b32 m0, %0\n\ts_nop 0\n\tglobal_load_lds_dwordx4 %1, %2 offset:%3" ::"s"(lds), "v"(v), "s"(sb), "i"(OFF) : "memory");
}
template <int OFF> DI void dma_c(unsigned lds, unsigned v, const char* sb) {
  asm volatile("s_mov_b32 m0, %0\n\ts_nop 0\n\tglobal_load_lds_dwordx4 %1, %2 offset:%3 sc1" ::"s"(lds), "v"(v), "s"(sb), "i"(OFF) : "memory");
}
template <int OFF> DI void dma_n(unsigned v, const char* sb) {
  asm volatile("global_load_lds_dwordx4 %0, %1 offset:%2 sc1" ::"v"(v), "s"(sb), "i"(OFF) : "memory");
}
constexpr int cap63(int n) { return n > 63 ? 63 : n; }
template <int N> DI void wait_bar() { asm volatile("s_waitcnt vmcnt(%0)\n\ts_barrier" ::"i"(cap63(N)) : "memory"); }
DI void lds_bar() { asm volatile("s_waitcnt lgkmcnt(0)\n\ts_barrier" ::: "memory"); }
DI void all_bar() { asm volatile("s_waitcnt vmcnt(0) lgkmcnt(0)\n\ts_barrier" ::: "memory"); }
DI void bar() { asm volatile("s_barrier" ::: "memory"); }
template <bool AG, int OFF> DI void ldx1(bf16x8& d, unsigned voff, const char* sb) {
  if constexpr (AG) asm volatile("global_load_dwordx4 %0, %1, %2 offset:%3 sc1" : "=a"(d) : "v"(voff), "s"(sb), "i"(OFF));
  else asm volatile("global_load_dwordx4 %0, %1, %2 offset:%3 sc1" : "=v"(d) : "v"(voff), "s"(sb), "i"(OFF));
}
template <int OFF> DI void ld2_1(f32x2& d, unsigned voff, const char* sb) { asm volatile("global_load_dwordx2 %0, %1, %2 offset:%3 sc1" : "=v"(d) : "v"(voff), "s"(sb), "i"(OFF)); }
DI void ld1_1(unsigned& d, unsigned voff, const void* sb) { asm volatile("global_load_dword %0, %1, %2 sc1" : "=v"(d) : "v"(voff), "s"(sb) : "memory"); }
template <int OFF> DI void st4_1(u32x4 d, unsigned voff, char* sb) { asm volatile("global_store_dwordx4 %0, %1, %2 offset:%3 sc1\n\ts_nop 1" ::"v"(voff), "v"(d), "s"(sb), "i"(OFF) : "memory"); }
DI void st2_1(f32x2 d, unsigned voff, char* sb) { asm volatile("global_store_dwordx2 %0, %1, %2 sc1" ::"v"(voff), "v"(d), "s"(sb) : "memory"); }
DI void st1_1(float d, unsigned voff, char* sb) { asm volatile("global_store_dword %0, %1, %2 sc1" ::"v"(voff), "v"(d), "s"(sb) : "memory"); }
template <int N> DI void waitv() { asm volatile("s_waitcnt vmcnt(%0)" ::"i"(cap63(N)) : "memory"); }
template <bool AG, int N, class T> DI void waitx(T& d) {
  if constexpr (AG) asm volatile("s_waitcnt vmcnt(%1)" : "+a"(d) : "i"(cap63(N))); else asm volatile("s_waitcnt vmcnt(%1)" : "+v"(d) : "i"(cap63(N)));
}
template <bool SW, bool BA, bool Z, bool NOP = false> DI void mm(f32x16& acc, const bf16x8& a, const bf16x8& b) {
#define MM_(OPS, C, OUT, BC) do { if constexpr (NOP) asm volatile("s_nop 1\n\tv_mfma_f32_32x32x16_bf16 %0, " OPS ", " C : OUT(acc) : "v"(a), BC(b)); \
                                  else asm volatile("v_mfma_f32_32x32x16_bf16 %0, " OPS ", " C : OUT(acc) : "v"(a), BC(b)); } while (0)
  if constexpr (Z) {
    if constexpr (SW) { if constexpr (BA) MM_("%2, %1", "0", "=&v", "a"); else MM_("%2, %1", "0", "=&v", "v"); }
    else { if constexpr (BA) MM_("%1, %2", "0", "=&v", "a"); else MM_("%1, %2", "0", "=&v", "v"); }
  } else {
    if constexpr (SW) { if constexpr (BA) MM_("%2, %1", "%0", "+v", "a"); else MM_("%2, %1", "%0", "+v", "v"); }
    else { if constexpr (BA) MM_("%1, %2", "%0", "+v", "a"); else MM_("%1, %2", "%0", "+v", "v"); }
  }
#undef MM_
}
DI void mdone(f32x16& a) { asm volatile("s_nop 11" : "+v"(a)); }
DI void mdone(f32x16& a, f32x16& b) { asm volatile("s_nop 11" : "+v"(a), "+v"(b)); }
DI void mdone(f32x16& a, f32x16& b, f32x16& c, f32x16& d) { asm volatile("s_nop 11" : "+v"(a), "+v"(b), "+v"(c), "+v"(d)); }
template <class T> DI T opq_v(T v) { asm volatile("" : "+v"(v)); return v; }
template <int OFF> DI void ldx0(bf16x8& d, unsigned voff, const char* sb) { asm volatile("global_load_dwordx4 %0, %1, %2 offset:%3" : "=v"(d) : "v"(voff), "s"(sb), "i"(OFF)); }
template <int OFF> DI void ldx1m(bf16x8& d, unsigned voff, const char* sb, unsigned long long m) {
  asm volatile("s_mov_b64 exec, %4\n\tglobal_load_dwordx4 %0, %1, %2 offset:%3 sc1\n\ts_mov_b64 exec, -1" : "=v"(d) : "v"(voff), "s"(sb), "i"(OFF), "s"(m));
}
template <bool AG> DI void ds_read2(bf16x8& d, unsigned addr) {
  if constexpr (AG) asm volatile("ds_read2_b64 %0, %1 offset1:2" : "=a"(d) : "v"(addr) : "memory"); else asm volatile("ds_read2_b64 %0, %1 offset1:2" : "=v"(d) : "v"(addr) : "memory");
}
template <bool AG> DI void waitl(bf16x8& d) { if constexpr (AG) asm volatile("s_waitcnt lgkmcnt(0)" : "+a"(d)::"memory"); else asm volatile("s_waitcnt lgkmcnt(0)" : "+v"(d)::"memory"); }
template <int OFF> DI void ds_write_a(unsigned addr, const bf16x8& v) { asm volatile("ds_write_b128 %0, %1 offset:%2" ::"v"(addr), "a"(v), "i"(OFF) : "memory"); }
template <int N, class T> DI void wait2(T& a, T& b) { asm volatile("s_waitcnt vmcnt(%2)" : "+v"(a), "+v"(b) : "i"(cap63(N))); }
DI void set_flag(char* flag, u64 tag) {
  unsigned z = 0;
  asm volatile("s_mov_b64 exec, 1\n\tglobal_atomic_swap_x2 %0, %1, %2 sc1\n\ts_mov_b64 exec, -1" ::"v"(z), "v"(tag), "s"(flag) : "memory");
}
DI void ld8_1(u64& d, unsigned voff, const char* sb) { asm volatile("global_load_dwordx2 %0, %1, %2 sc1" : "=v"(d) : "v"(voff), "s"(sb) : "memory"); }
template <int NL> DI void rd_flag(u64& v, unsigned voff, const char* sb) {
  const u64 zero = 0;
  if constexpr (NL == 64) asm volatile("global_atomic_or_x2 %0, %1, %3, %2 sc0 sc1" : "+v"(v) : "v"(voff), "s"(sb), "v"(zero) : "memory");
  else asm volatile("s_mov_b64 exec, %4\n\tglobal_atomic_or_x2 %0, %1, %3, %2 sc0 sc1\n\ts_mov_b64 exec, -1" : "+v"(v) : "v"(voff), "s"(sb), "v"(zero), "s"((1ull << (NL & 63)) - 1) : "memory");
}
template <int NL> DI void poll(const char* base, unsigned voff, u64 tag) {
  for (int spin = 0; spin < (1 << 15); ++spin) {
    u64 v = tag;
    rd_flag<NL>(v, voff, base);
    waitx<false, 0>(v);
    if (__all(v == tag)) break;
  }
  asm volatile("" ::: "memory");
}

template <int RBX> DI void block_map(int& rb, int& cb) {
  const int x = blockIdx.x & 7, i = blockIdx.x >> 3;
  constexpr int CBX = 32 / RBX, GC = 16 / CBX;
  rb = RBX * (x / GC) + i / CBX; cb = CBX * (x % GC) + i % CBX;
}

DI float a_fma(float a, float b, float c) { float d; asm volatile("v_fma_f32 %0, %1, %2, %3" : "=v"(d) : "v"(a), "v"(b), "v"(c)); return d; }
DI float a_fms(float a, float b, float c) { float d; asm volatile("v_fma_f32 %0, %1, -%2, %3" : "=v"(d) : "v"(a), "v"(b), "v"(c)); return d; }
DI void a_exp(float& x) { asm volatile("v_exp_f32 %0, %0" : "+v"(x)); }
DI void a_add(float& s, float x) { asm volatile("v_add_f32 %0, %0, %1" : "+v"(s) : "v"(x)); }
DI void a_mul(float& x, float s) { asm volatile("v_mul_f32 %0, %0, %1" : "+v"(x) : "v"(s)); }
DI float a_max3(float a, float b, float c) { float d; asm volatile("v_max3_f32 %0, %1, %2, %3" : "=v"(d) : "v"(a), "v"(b), "v"(c)); return d; }
DI float a_min3(float a, float b, float c) { float d; asm volatile("v_min3_f32 %0, %1, %2, %3" : "=v"(d) : "v"(a), "v"(b), "v"(c)); return d; }

DI void pin_a(bf16x8& a) { asm volatile("" : "+a"(a)); }
DI void pin2(f32x16& a, f32x16& b) { asm volatile("" : "+v"(a), "+v"(b)); }
DI bf16x8 cs_frag(float c) {
  const float hi = rbf(c), mid = rbf(c - hi), lo = (c - hi) - mid;
  return __builtin_bit_cast(bf16x8, u32x4{cvtpk(hi, mid), cvtpk(lo, 0.f), 0u, 0u});
}
template <bool CS> struct Red {
  bf16x8 p[4];
  float t[4], a, b, sd;
  template <int K> DI void run(int lane) {
    if constexpr (K == 0) asm volatile("" : "+v"(p[0]), "+v"(p[1]), "+v"(p[2]), "+v"(p[3]));
    if constexpr (K < 3) {
      const f32x4 u = __builtin_bit_cast(f32x4, p[K + 1]), v = __builtin_bit_cast(f32x4, p[0]);
#pragma unroll
      for (int e = 0; e < 4; ++e) { if (K == 0) t[e] = v[e]; a_add(t[e], u[e]); }
    }
    if constexpr (K == 3) {
      auto x = __builtin_amdgcn_permlane16_swap(__float_as_uint(t[0]), __float_as_uint(t[1]), false, false);
      auto y = __builtin_amdgcn_permlane16_swap(__float_as_uint(t[2]), __float_as_uint(t[3]), false, false);
      a = opq_v(__uint_as_float(x[0]) + __uint_as_float(x[1])); b = opq_v(__uint_as_float(y[0]) + __uint_as_float(y[1]));
    }
    if constexpr (K == 4) { auto x = __builtin_amdgcn_permlane32_swap(__float_as_uint(a), __float_as_uint(b), false, false); a = opq_v(__uint_as_float(x[0]) + __uint_as_float(x[1])); }
    if constexpr (K == 5) {
      if constexpr (CS) a = lane_get(a, (lane >> 2) + ((lane & 3) << 4));
      else { const int src = ((lane & 31) >> 1) + ((lane & 1) << 5); b = lane_get(a, src + 16); a = lane_get(a, src); }
    }
    if constexpr (CS) {
      if constexpr (K == 8) { t[0] = opq_v(rbf(a)); t[1] = opq_v(a - t[0]); }
      if constexpr (K == 9) { const float mid = rbf(t[1]); p[0] = opq_v(__builtin_bit_cast(bf16x8, u32x4{cvtpk(t[0], mid), cvtpk(t[1] - mid, 0.f), 0u, 0u})); }
    } else {
      if constexpr (K == 8) { const float m = a * (1.f / D_); b = opq_v(fmaxf(b * (1.f / D_) - m * m, 0.f)); a = opq_v(rbf(m)); }
      if constexpr (K == 9) b = opq_v(rbf(rbf(b) + 1e-5f));
      if constexpr (K == 10) { sd = opq_v(rbf(__builtin_amdgcn_sqrtf(b))); b = opq_v(__builtin_amdgcn_rcpf(sd)); }
    }
  }
};
DI void pin4(bf16x8 (&p)[4]) { asm volatile("" : "+v"(p[0]), "+v"(p[1]), "+v"(p[2]), "+v"(p[3])); }
DI bf16x8 mean_frag(float mean, bool on) { const float m = on ? -mean : 0.f; return __builtin_bit_cast(bf16x8, u32x4{cvtpk(m, m), cvtpk(m, 0.f), 0u, 0u}); }

#define STAMP(i)

constexpr int CODE_KB = 50;
constexpr int SLOT = 16384, XL = 12;
struct WinPtr {
  unsigned b[3];
  DI void init(unsigned a) { sfor<0, 3>([&](auto i) { b[i] = a + i * 65536u; asm volatile("" : "+v"(b[i])); }); }
  template <int OFF> DI bf16x8 frag() const { return *(LAS const bf16x8*)((LAS const char*)(uintptr_t)b[OFF >> 16] + (OFF & 65535)); }
};
struct Ctx { unsigned xv; WinPtr p; };
#define IC(n) std::integral_constant<int, (n)>{}

template <int S> DI void ld_x(bf16x8 (&x)[64], const Ctx& c, const char* xb) { ldx1<false, (S % 8) * 1024 - 4096>(x[S], c.xv, xb + (S / 8) * 8192 + 4096); }
template <class H> constexpr int g64_wait(int s) {
  int n = 0;
  const int t0 = s - XL;
  if (t0 < 0) n += XL - 1 - s; else n += H::n(t0);
  for (int t = t0 < 0 ? 0 : t0 + 1; t < s; ++t) n += (t + XL < 64 ? 1 : 0) + H::n(t);
  return n;
}
constexpr int a_slot(int g);
template <bool MAPA, bool ZERO, class H, class FX, class FA>
DI void gemm64(const Ctx& c, bf16x8 (&x)[64], const char* xb, f32x16& acc0, f32x16& acc1, FX&& hook, FA&& aux) {
  bf16x8 f0[3], f1[3];
  auto off = [](int s) constexpr { return (MAPA ? a_slot(24 + s / 8) : s / 8) * SLOT + (s % 8) * 2048; };
  auto rd0 = [&](auto s) { f0[s % 3] = c.p.template frag<off(s)>(); };
  auto rd1 = [&](auto s) { f1[s % 3] = c.p.template frag<off(s) + 1024>(); };
  rd0(IC(0)); rd1(IC(0)); rd0(IC(1)); rd1(IC(1));
  sfor<0, 64>([&](auto s) {
    waitx<false, g64_wait<H>(s)>(x[s]);
    SBAR();
    mm<false, false, ZERO && s == 0>(acc0, f0[s % 3], x[s]);
    SBAR();
    if constexpr (s + 2 < 64) rd0(IC(s + 2));
    if constexpr (s + XL < 64) ld_x<s + XL>(x, c, xb);
    aux(IC(2 * s));
    SBAR();
    mm<false, false, ZERO && s == 0>(acc1, f1[s % 3], x[s]);
    SBAR();
    if constexpr (s + 2 < 64) rd1(IC(s + 2));
    hook(s);
    aux(IC(2 * s + 1));
    SBAR();
  });
  mdone(acc0, acc1);
}
struct HookP6 { static constexpr int n(int s) { return s >= 52 ? 1 : 0; } };
struct HookP3 { static constexpr int n(int s) { return s >= 52 ? 1 : 0; } };
struct HookP4 { static constexpr int n(int s) { return s == 52 || s == 6 || (s >= 8 && s < 12) ? 1 : 0; } };

DI void res_to_acc(const u32x4 (&xr)[4], f32x16& a0, f32x16& a1) {
#pragma unroll
  for (int e = 0; e < 4; ++e) {
    a0[2 * e] = bflo(xr[0][e]); a0[2 * e + 1] = bfhi(xr[0][e]); a0[8 + 2 * e] = bflo(xr[1][e]); a0[9 + 2 * e] = bfhi(xr[1][e]);
    a1[2 * e] = bflo(xr[2][e]); a1[2 * e + 1] = bfhi(xr[2][e]); a1[8 + 2 * e] = bflo(xr[3][e]); a1[9 + 2 * e] = bfhi(xr[3][e]);
  }
  asm volatile("" : "+v"(a0), "+v"(a1));
}
DI void store_stats(int r, const u32x4 (&xr)[4], char* stp) {
  float s1 = 0.f, s2 = 0.f, s3 = 0.f, s4 = 0.f;
#pragma unroll
  for (int i = 0; i < 4; ++i)
#pragma unroll
    for (int e = 0; e < 4; e += 2) {
      s1 = dot2(xr[i][e], 0x3f803f80u, s1); s2 = dot2(xr[i][e], xr[i][e], s2);
      s3 = dot2(xr[i][e + 1], 0x3f803f80u, s3); s4 = dot2(xr[i][e + 1], xr[i][e + 1], s4);
    }
  st2_1(f32x2{swap_add(s1 + s3), swap_add(s2 + s4)}, (unsigned)(r * 8), stp);
}
DI void store_x(const Ctx& c, int r, const f32x16& a0, const f32x16& a1, u32x4 (&xr)[4], char* dst, char* stp) {
  SBAR();
  xr[0] = __builtin_bit_cast(u32x4, pack8<0>(a0)); st4_1<0>(xr[0], c.xv, dst); SBAR();
  xr[1] = __builtin_bit_cast(u32x4, pack8<1>(a0)); st4_1<1024>(xr[1], c.xv, dst); SBAR();
  xr[2] = __builtin_bit_cast(u32x4, pack8<0>(a1)); st4_1<2048>(xr[2], c.xv, dst); SBAR();
  xr[3] = __builtin_bit_cast(u32x4, pack8<1>(a1)); st4_1<3072>(xr[3], c.xv, dst); SBAR();
  store_stats(r, xr, stp);
  SBAR();
}

template <class FX> DI void hedge_exp(const f32x16& p, float sc, f32x16& ep, f32x16& en, FX&& hook) {
  float mx = fmaxf(p[0], p[1]), mn = fminf(p[0], p[1]);
#pragma unroll
  for (int i = 2; i < 16; i += 2) { mx = fmaxf(fmaxf(mx, p[i]), p[i + 1]); mn = fminf(fminf(mn, p[i]), p[i + 1]); }
  mx = swap_max(mx) * sc; mn = swap_min(mn) * sc;
  sfor<0, 16>([&](auto i) {
    SBAR();
    hook(i);
    constexpr int e = i;
    float a = __builtin_amdgcn_exp2f(__builtin_fmaf(p[e], sc, -mx)), b = __builtin_amdgcn_exp2f(__builtin_fmaf(p[e], -sc, mn));
    asm volatile("" : "+v"(a), "+v"(b));
    ep[e] = a; en[e] = b;
  });
  SBAR();
}

constexpr int a_slot(int g) { constexpr int m[32] = {0, 1, 2, 3, 4, 0, 1, 2, 5, 6, 7, 5, 3, 4, 6, 7, 8, 5, 3, 4, 6, 7, 0, 1, 2, 5, 3, 4, 6, 7, 0, 1}; return m[g]; }
constexpr int a_need(int g) { return g < 8 ? 8 * g - 2 : (g < 16 ? 16 * (g - 8) - (g > 12 ? 10 : 2) : 128 + 8 * (g - 16) - 2); }
constexpr bool a_bar(int s) { return (s + 2) % 8 == 0 && s >= 14 && s < 190; }
constexpr int a_pre(int n) { return n < 16 ? n : n + 16; }
constexpr int QP = 128 + 24, QC = 128 + 36;
constexpr int a_piece(int s) {
  constexpr int t[][3] = {{6, 4, 1}, {12, 10, 1}, {20, 5, 1}, {24, 6, 1}, {28, 11, 1}, {34, 7, 1}, {46, 12, 1}, {54, 13, 1}, {64, 14, 2}, {72, 15, 2}, {80, 16, 2}, {88, 17, 2}, {96, 18, 2},
                          {112, 19, 2}, {128, 20, 1}, {132, 21, 1}, {136, 22, 1}, {140, 23, 1}, {144, 24, 1}, {150, 25, 1}, {158, 26, 1}};
  for (auto& e : t) if (s >= e[0] && s < e[0] + 4 * e[2] && (s - e[0]) % e[2] == 0) return e[1] * 4 + (s - e[0]) / e[2];
  return -1;
}
constexpr int a_first(int s) { constexpr int g[6] = {0, 8, 1, 2, 9, 3}; return s < 4 ? g[(s + 20) / 4] * 4 + (s + 20) % 4 : -1; }
constexpr int a_park(int s) { return s == 148 || s == 149 ? s - 148 : (s >= 154 && s < 158 ? s - 152 : (s == 162 || s == 163 ? s - 156 : -1)); }
constexpr bool a_side(int s, bool first) { return (s >= 2 && s < (first ? 14 : 6)) || s == 40 || (s >= 104 && s < 112 && s % 2 == 0) || (s >= 120 && s < 128 && s % 2 == 0) || a_park(s) >= 0; }
struct SchedA {
  int xw[64], bw[192], sw[64], fw, cw, pw, zw, lw[32];
  constexpr SchedA(bool first) : xw{}, bw{}, sw{}, fw(0), cw(0), pw(0), zw(0), lw{} {
    int pos = 0, xpos[64] = {}, wpos[32] = {}, spos[192] = {}, fpos = 0, stpos = 0, ppos = 0, lpos[34] = {};
    if (first) { for (int s = -20; s < 0; ++s) wpos[a_first(s) / 4] = ++pos; pos += 2; }
    else for (int s = 0; s < XL; ++s) xpos[s] = ++pos;
    for (int s = 0; s < 192; ++s) {
      if (s == 128) { cw = pos + 2 - spos[126]; pos += 3; stpos = pos; }
      if (a_bar(s) || s == 6) {
        bw[s] = 63;
        for (int g = 0; g < 24; ++g) if (a_need(g) == s && wpos[g] && pos - wpos[g] < bw[s]) bw[s] = pos - wpos[g];
        if (s == 54 && pos - spos[40] < bw[s]) bw[s] = pos - spos[40];
        if (s == 142) { if (pos - stpos < bw[s]) bw[s] = pos - stpos; ++pos; }
      }
      if (s == 20 || s == 24 || s == 28) sw[s] = pos - spos[s - 15];
      if (s == 78) fw = pos - fpos;
      if (s == QC) pw = pos - ppos;
      if (s < 64) xw[s] = pos - xpos[s];
      if (!first && s + XL < 64) xpos[s + XL] = ++pos;
      if (s == 65) fpos = ++pos;
      if (s == QP) ppos = ++pos;
      if (s >= QC && s < QC + 17) lpos[2 * (s - QC)] = ++pos;
      if (a_piece(s) >= 0) wpos[a_piece(s) / 4] = ++pos;
      if (a_side(s, first)) spos[s] = ++pos;
      if (s >= QC && s < QC + 17) lpos[2 * (s - QC) + 1] = ++pos;
      if (first && a_first(s) >= 0) wpos[a_first(s) / 4] = ++pos;
      if (s == 175) zw = pos - lpos[1];
      if (s >= 176) lw[s - 176] = pos - lpos[2 + s - 176];
    }
    for (int i = 16; i < 32; ++i) lw[i] = pos - lpos[2 + i];
  }
};
constexpr int cmin(int a, int b) { return a < b ? a : b; }

#define R_OA "v[64:79]"
#define R_OB "v[80:95]"
#define S0_(n, q, m) "v_mfma_f32_32x32x16_bf16 v[" #n ":" #n "+15], v[208:211], v[" #q ":" #q "+3], v[" #m ":" #m "+15]\n\t"
#define S1_(n, q) "v_mfma_f32_32x32x16_bf16 v[" #n ":" #n "+15], v[212:215], v[" #q "+4:" #q "+7], v[" #n ":" #n "+15]\n\t"
#define PV0_(o, p) "v_smfmac_f32_32x32x32_bf16 v[" #o ":" #o "+15], v[216:219], v[" #p ":" #p "+7], v236\n\t"
#define PV1_(o, p) "v_smfmac_f32_32x32x32_bf16 v[" #o ":" #o "+15], v[220:223], v[" #p "+8:" #p "+15], v236\n\t"
#define L0_(l, p) "v_smfmac_f32_16x16x64_bf16 v[" #l ":" #l "+3], v[224:227], v[" #p ":" #p "+7], v236\n\t"
#define L1_(l, p) "v_smfmac_f32_16x16x64_bf16 v[" #l ":" #l "+3], v[224:227], v[" #p "+8:" #p "+15], v236\n\t"
#define E_(e, i) "v_exp_f32 v[" #e "+" #i "], v[" #e "+" #i "]\n\t"
#define RD_(d, off) "ds_read_b128 v[" #d ":" #d "+3], v237 offset:" #off "\n\t"
#define HALF_(e, n, q, m, p, o, l, X0, X1, X2, X3, X4, X5) \
  S0_(n, q, m) X0 E_(e, 0) E_(e, 1) E_(e, 2) S1_(n, q) X1 E_(e, 3) E_(e, 4) E_(e, 5) PV0_(o, p) X2 E_(e, 6) E_(e, 7) E_(e, 8) \
  PV1_(o, p) X3 E_(e, 9) E_(e, 10) E_(e, 11) L0_(l, p) X4 E_(e, 12) E_(e, 13) L1_(l, p) X5 E_(e, 14) E_(e, 15)
#define LG2_ "s_waitcnt lgkmcnt(2)\n\t"
#define LG0_ "s_waitcnt lgkmcnt(0)\n\t"
#define STEP_(b, nb, kv, wo, Y0, Y1) \
  HALF_(128, 160, 200, 112, 176, 80, 232, "", "", RD_(208, b + 2048), RD_(212, b + 3072), RD_(216, b), RD_(220, b + 1024) LG2_) \
  HALF_(160, 144, 192, 96, 128, 64, 228, "", LG0_, "s_add_u32 m0, s44, " #nb "\n\t", "", "global_load_lds_dwordx4 v" #kv ", s[40:41] sc1\n\t", \
        "global_load_lds_dwordx4 v" #kv ", s[40:41] offset:1024 sc1\n\t") \
  HALF_(144, 176, 200, 112, 160, 80, 232, Y0, "", RD_(208, b + 6144), RD_(212, b + 7168), RD_(216, b + 4096), RD_(220, b + 5120) LG2_) \
  HALF_(176, 128, 192, 96, 144, 64, 228, "s_mov_b32 m0, s45\n\t", LG0_, "global_load_lds_dwordx4 v240, s[42:43] offset:" #wo " sc1\n\t", Y1, \
        "s_waitcnt vmcnt(4)\n\t", "s_barrier\n\t")
DI float max16(const f32x16& s) {
  float mx = s[0];
#pragma unroll
  for (int e = 1; e < 16; ++e) mx = fmaxf(mx, s[e]);
  return mx;
}
typedef __attribute__((ext_vector_type(8))) unsigned u32x8;
typedef __attribute__((ext_vector_type(16))) unsigned u32x16;

__global__ __launch_bounds__(256, 1) void k_main(const char* xin, const char* wqkv, const char* wphi, const char* woA, const char* wq, const char* wk, const char* wv,
                                                 const char* woB, char* ws, char* out) {
  __shared__ __attribute__((aligned(16))) char smem_[163840];
  LAS char* const smem = (LAS char*)smem_;
  const unsigned lds0 = (unsigned)(uintptr_t)smem;
  Ctx c;
  int lane = threadIdx.x & 63, w = __builtin_amdgcn_readfirstlane(threadIdx.x >> 6), r = lane & 31, h = lane >> 5, rb, cb;
  block_map<4>(rb, cb);
  int tt = rb * 4 + w, blk = (lane >> 4) & 1, q4 = (lane & 15) >> 2, p4 = lane & 3;
  c.xv = lane * 16; c.p.init(lds0 + c.xv);
  char* flags = ws + WS_FLAG;
  char* col = ws + WS_COL + (size_t)cb * 2 * MB;
  const int ut = rb * 4 + (cb >> 2), ty = cb & 3;
  char* act = ws + (size_t)rb * 2 * MB + w * 65536;
  char* acto = act + cb * 4096;
  char* fact = flags + tt * FL_I;
  char* facto = fact + cb * 8;
  char* stt = ws + WS_ST + tt * 4096;
  char* stto = stt + cb * 256;
  char* kvp = col + C_KVP + w * 32768;
  char* zp = col + C_ZP + w * 2048;
  char* fkvp = flags + (cb * 4 + w) * FL_I + 8 * FL_K;
  char* fwl = flags + cb * 4 * FL_I + 11 * FL_K;
  char* kvf = ws + WS_KVF;
  char* fkv = flags + 12 * FL_K;
  const char* xu_p = ws + (size_t)rb * 2 * MB + 2 * STG + (cb >> 2) * 65536 + w * 16384 + 4096;
  const char* su_p = ws + WS_ST + 256 * KB + ut * 4096;
  const char* wkv = ((cb & 2) ? wv : wk) + w * 256 * 64;
  const char* wph = wphi + cb * 4096 + w * 1024;
  u64 tag = 0;
  u32x4 xr[4] = {};
  char* colw = col + w * 4096;
  unsigned ldsw = lds0 + w * 4096;
  auto wt = [&](int m, int ch) { return colw + C_WT + m * 131072 + ch * SLOT; };
  auto pc_w = [&](int m, int slot, int ch, auto i) { if constexpr (i == 0) dma_c<0>(ldsw + slot * SLOT, c.xv, wt(m, ch)); else dma_n<i * 1024>(c.xv, wt(m, ch)); };
  auto pc_a = [&](auto n) { constexpr int g = n / 4; pc_w(g < 8 ? 1 : (g < 16 ? 2 : (g < 24 ? 0 : 3)), a_slot(g), g % 8, IC(n % 4)); };
  u64 csm = ~0ull;
  float csv[4] = {};
  auto ld_cs = [&](bf16x8& d, int m, auto i) { ldx1m<i * 1024>(d, c.xv, col + C_CS + m * 4096, csm); };
  auto raw_in = [&](auto j, int slot) {
    const int hi = rb >> 3, pitch = (j == 0 || (j == 1 && !hi)) ? 6144 : 2048, row = lane >> 3;
    const char* src = (j == 0 ? wqkv + 2048 + hi * 2048 : (j == 1 ? (hi ? woA : wqkv) : (hi ? woB : wq))) + cb * 128 + (size_t)(rb & 7) * 128 * pitch;
    const unsigned v0 = (unsigned)((32 * w + row) * pitch + ((lane & 7) ^ (((row >> 1) & 1) << 2)) * 16);
    sfor<0, 4>([&](auto i) { dma<i * 1024>(lds0 + slot * SLOT + w * 4096, v0 + i * (8 * pitch - 1024), src); });
  };
  auto raw_out = [&](auto j, int slot) {
    const int hi = rb >> 3, ch = rb & 7, m = j == 0 ? 1 + hi : (j == 1 ? 3 * hi : 4 + hi);
    LAS const char* p = smem + slot * SLOT + (w >> 1) * 8192 + (4 * h + q4) * 128 + (((((q4 >> 1) ^ w) & 1) << 2) + 2 * blk + (p4 >> 1)) * 16 + 8 * (p4 & 1);
    const bool isv = m == 2;
    char* dst = col + C_WT + m * 131072 + (isv ? (w & 1) * 65536 + ch * 8192 + (w >> 1) * 4096 : ch * SLOT + (w >> 1) * 8192 + (w & 1) * 1024);
    float cs = 0.f;
    sfor<0, 4>([&](auto s) {
      const u32x4 f = __builtin_bit_cast(u32x4, cat8(trread(p + s * 2048), trread(p + s * 2048 + 1024)));
      st4_1<0>(f, c.xv, dst + s * (isv ? 1024 : 2048));
#pragma unroll
      for (int e = 0; e < 4; ++e) cs = dot2(f[e], 0x3f803f80u, cs);
    });
    st1_1(swap_add(cs), (unsigned)(r * 4), col + C_CS + m * 4096 + (ch * 2 + (w >> 1)) * 256 + (w & 1) * 128);
  };
  const int w0 = w; const unsigned ldsw0 = ldsw;
#define REFRESH() do { \
    w = w0; ldsw = ldsw0; \
    asm volatile("" : "+s"(w), "+s"(tag), "+s"(act), "+s"(acto), "+s"(fact), "+s"(facto), "+s"(stt), "+s"(stto), "+s"(col), "+s"(colw), "+s"(ldsw)); \
    asm volatile("" : "+v"(lane)); \
    r = lane & 31; h = lane >> 5; blk = (lane >> 4) & 1; q4 = (lane & 15) >> 2; p4 = lane & 3; c.xv = lane * 16; c.p.init(lds0 + c.xv); \
  } while (0)
  int l = 0;
  float mean, rstd;
  Red<true> cw;
  f32x16 ra0, ra1;
  bf16x8 park[16];
      constexpr int NA = 60;
      constexpr SchedA SQ(false), S1(true);
      bf16x8 x[64], f0[3], f1[3], f2[5], wf[4], kp[4], pp[32], zq[2], sel[2], mf0, mf1;
      Red<false> st; Red<true> ck, cv, cq;
      f32x16 acc[6], p[2], kvs;
      u64 fv;
      float mx[2], mn[2], e[16], se, sce;
      unsigned arow;
      LAS char* pk_img = smem;
      LAS char* v_img = smem + 2 * SLOT;
      LAS char* sh = smem + 9 * SLOT + 4096;
      auto issue_x = [&](auto s) { ldx1<(s < NA), (s % 8) * 1024 - 4096>(x[s], c.xv, act + (s / 8) * 8192 + 4096); };
      auto ld_park = [&](auto i) { ldx1<true, (i % 4) * 1024>(park[i], c.xv, wt(4, 2 + i / 4)); };
      auto ldp = [&](auto i) {
        if constexpr (i >= 2) ldx1<true, ((i - 2) % 8) * 1024 - 4096>(pp[i - 2], c.xv, kvp + ((i - 2) / 8) * 8192 + 4096); else ldx1<false, i * 1024>(zq[i], c.xv, zp);
      };
      auto rd_wf = [&](auto k) { LAS const char* wp = smem + 9 * SLOT + (4 * h + q4) * 64 + blk * 32 + p4 * 8 + k * 1024; wf[k] = cat8(trread(wp), trread(wp + 512)); };
      auto rd01 = [&](auto s, auto t) {
        if constexpr (s < 64 || (s >= 128 && s < 192)) { const bf16x8 f = c.p.template frag<a_slot(s / 8) * SLOT + (s % 8) * 2048 + t * 1024>(); if constexpr (t == 0) f0[s % 3] = f; else f1[s % 3] = f; }
      };
      auto rd2 = [&](auto s) { if constexpr (s < 128) f2[s % 5] = c.p.template frag<a_slot(8 + s / 16) * SLOT + (s % 16) * 1024>(); };
      auto v_out = [&](auto t, auto hf) {
        *(LAS bf16x8*)(v_img + t * 8192 + w * 2048 + hf * 1024 + lane * 16) = pack8<hf>(acc[2 + t]);
      };
      auto sum_mm = [&](auto i) { waitx<true, SQ.lw[i]>(pp[i]); mm<false, true, i == 0>(kvs, sel[i & 1], pp[i]); };
      auto kmap = [&](auto gi) {
        constexpr int g = gi;
        if constexpr (g == 4) { pin2(acc[0], acc[1]); }
        if constexpr (g >= 6 && g < 10) mm<false, false, g % 2 == 0>(p[(g - 6) / 2], wf[g - 6], kp[g - 6]);
        if constexpr (g >= 4 && g < 8) kp[g - 4] = opq_v(pack8<g % 2>(acc[(g - 4) / 2]));
        if constexpr (g == 10) mm<true, false, false>(acc[3], cv.p[0], mf1);
        if constexpr (g >= 12 && g < 24) {
          constexpr int hd = (g - 12) / 6, t = (g - 12) % 6;
          const f32x16& q = p[hd];
          if constexpr (t == 0) { mx[hd] = a_max3(q[0], q[1], q[2]); mn[hd] = a_min3(q[0], q[1], q[2]); mx[hd] = a_max3(mx[hd], q[3], q[4]); mn[hd] = a_min3(mn[hd], q[3], q[4]); }
          if constexpr (t >= 1 && t < 4) {
            mx[hd] = a_max3(mx[hd], q[4 * t + 1], q[4 * t + 2]); mn[hd] = a_min3(mn[hd], q[4 * t + 1], q[4 * t + 2]);
            mx[hd] = a_max3(mx[hd], q[4 * t + 3], q[(4 * t + 4) & 15]); mn[hd] = a_min3(mn[hd], q[4 * t + 3], q[(4 * t + 4) & 15]);
          }
          if constexpr (t == 4) mx[hd] = opq_v(swap_max(mx[hd]) * -sce);
          if constexpr (t == 5) mn[hd] = opq_v(swap_min(mn[hd]) * sce);
        }
        if constexpr (g >= 24) {
          constexpr int grp = (g - 24) / 26, j = (g - 24) % 26, hd = grp >> 1, sg = grp & 1;
          if constexpr (j >= 1 && j <= 16) a_exp(e[j - 1]);
          if constexpr (j < 16) e[j] = sg ? a_fms(p[hd][j], sce, mn[hd]) : a_fma(p[hd][j], sce, mx[hd]);
          if constexpr (j == 2) se = e[0];
          if constexpr (j > 2 && j <= 17) a_add(se, e[j - 2]);
          if constexpr (j == 17) { se = opq_v(__builtin_amdgcn_rcpf(swap_add(se)) * rstd); if constexpr (grp < 2) v_out(IC(0), IC(grp)); }
          if constexpr (j >= 18) {
            constexpr int gq = (j - 18) / 2;
            if constexpr (j % 2 == 0) { a_mul(e[4 * gq], se); a_mul(e[4 * gq + 1], se); a_mul(e[4 * gq + 2], se); a_mul(e[4 * gq + 3], se); }
            else *(LAS u32x2*)((LAS char*)(uintptr_t)(arow ^ ((sg * 4 + gq) << 4)) + hd * 16384) = u32x2{cvtpk(e[4 * gq], e[4 * gq + 1]), cvtpk(e[4 * gq + 2], e[4 * gq + 3])};
          }
        }
      };
      auto keep = [&](Red<true>& q, int n) { csv[n] = l == 0 ? q.a : csv[n]; q.a = csv[n]; };
      auto aux = [&](auto s, auto i, auto fl) {
        constexpr int g1 = 3 * s + i, g3 = 2 * (s - 128) + i;
        if constexpr (s < 64) {
          if constexpr (g1 >= 60 && g1 < 71) st.template run<g1 - 60>(lane);
          if constexpr (g1 == 80) { if constexpr (fl) csv[0] = ck.a; else ck.a = csv[0]; }
          if constexpr (g1 == 92) { if constexpr (fl) csv[1] = cv.a; else cv.a = csv[1]; }
          if constexpr (g1 >= (fl ? 72 : 80) && g1 < 82) ck.template run<g1 - 72>(lane);
          if constexpr (g1 >= (fl ? 84 : 92) && g1 < 94) cv.template run<g1 - 84>(lane);
          if constexpr (g1 == 71) { mean = st.a; rstd = st.b; sce = opq_v(rstd * LOG2E); }
          if constexpr (g1 == 95) mf0 = opq_v(mean_frag(mean, h == 0));
          if constexpr (g1 == 96) mf1 = opq_v(mean_frag(mean, h == 1));
          if constexpr (g1 == 99) mm<false, false, false>(acc[0], ck.p[0], mf0);
          if constexpr (g1 == 100) mm<false, false, false>(acc[1], ck.p[0], mf1);
          if constexpr (g1 == 101) mm<true, false, false>(acc[2], cv.p[0], mf0);
          if constexpr (g1 == 130) *(LAS short*)(sh + 8704 + w * 64 + (r >> 4) * 32 + ((r >> 2) & 1) * 16 + ((r >> 3) & 1) * 8 + (r & 3) * 2) = (short)(__float_as_uint(st.sd) >> 16);
          if constexpr (g1 >= 168 && g1 < 172) rd_wf(IC(g1 - 168));
          if constexpr (g1 == 174) arow = opq_v((unsigned)(uintptr_t)pk_img + (w * 32 + r) * 128 + h * 8 + ((r & 7) << 4));
        } else if constexpr (s >= 128) {
          if constexpr (g3 == 8) keep(cq, 2);
          if constexpr (g3 == 24) keep(cw, 3);
          if constexpr (g3 < 10) cq.template run<g3>(lane);
          if constexpr (g3 == 12) mm<false, false, false>(acc[4], cq.p[0], mf0);
          if constexpr (g3 == 13) mm<false, false, false>(acc[5], cq.p[0], mf1);
          if constexpr (g3 >= 16 && g3 < 26) cw.template run<g3 - 16>(lane);
          if constexpr (g3 >= 32 && g3 < 40) {
            constexpr int n = (g3 - 32) % 4, t = (g3 - 32) / 4;
            const int kr = 16 * t + 8 * (n >> 1) + 4 * h + 2 * (n & 1);
            u32x4 sv = __builtin_bit_cast(u32x4, sel[t]); sv[n] = opq_v(r == kr ? 0x3f80u : (r == kr + 1 ? 0x3f800000u : 0u)); sel[t] = __builtin_bit_cast(bf16x8, sv);
          }
          if constexpr (g3 == 40) { sel[0] = opq_v(sel[0]); sel[1] = opq_v(sel[1]); }
          if constexpr (g3 == 42) *(LAS unsigned*)(sh + 8192 + 256 + lane * 4) = opq_v(0x3f803f80u);
          if constexpr (g3 == 95) {
            wait2<SQ.zw>(zq[0], zq[1]);
#pragma unroll
            for (int n = 0; n < 4; ++n) { e[n] = __builtin_bit_cast(f32x4, zq[0])[n]; a_add(e[n], __builtin_bit_cast(f32x4, zq[1])[n]); }
          }
          if constexpr (g3 == 96) {
            auto a = __builtin_amdgcn_permlane16_swap(__float_as_uint(e[0]), __float_as_uint(e[1]), false, false);
            auto b = __builtin_amdgcn_permlane16_swap(__float_as_uint(e[2]), __float_as_uint(e[3]), false, false);
            e[0] = opq_v(__uint_as_float(a[0]) + __uint_as_float(a[1])); e[1] = opq_v(__uint_as_float(b[0]) + __uint_as_float(b[1]));
          }
          if constexpr (g3 == 98) { auto a = __builtin_amdgcn_permlane32_swap(__float_as_uint(e[0]), __float_as_uint(e[1]), false, false); e[0] = opq_v(__uint_as_float(a[0]) + __uint_as_float(a[1])); }
          if constexpr (g3 == 100) e[1] = lane_get(e[0], lane ^ 8);
          if constexpr (g3 == 106)
            *(LAS short*)(sh + 8192 + w * 64 + (lane & 1) * 32 + ((lane >> 2) & 1) * 16 + ((lane >> 1) & 1) * 8 + (lane >> 4) * 2) = (short)cvtpk(e[0] + e[1], 0.f);
          if constexpr (g3 >= 116 && g3 < 124 && g3 % 2 == 0) rd_wf(IC((g3 - 116) / 2));
        }
      };
      auto step = [&](auto s, auto fl) {
        constexpr int ps = s / 64;
        constexpr bool first = fl;
        if constexpr (a_bar(s) || (first && s == 6)) wait_bar<(ps ? cmin(S1.bw[s], SQ.bw[s]) : (first ? S1.bw[s] : SQ.bw[s]))>();
        if constexpr (s == 20 || (first && (s == 24 || s == 28))) waitv<(first ? S1.sw[s % 64] : SQ.sw[s % 64])>();
        if constexpr (s == 78) { waitx<false, SQ.fw>(fv); if (!__all(fv == tag)) poll<16>(fwl, lane * 8, tag); }
        if constexpr (s == 142) set_flag(fkvp + rb * 8, tag);
        if constexpr (s == QC) { waitx<false, SQ.pw>(fv); if (!__all(fv == tag)) poll<16>(fkvp, lane * 8, tag); }
        if constexpr (s < 64 && !first) waitx<(s < NA), SQ.xw[s % 64]>(x[s % 64]);
        SBAR();
        if constexpr (ps != 1) mm<false, (s % 64 < NA), s % 64 == 0>(acc[2 * ps], f0[s % 3], x[s % 64]);
        else mm<true, (s % 64 < NA), s % 64 == 0>(acc[3], f2[s % 5], x[s % 64]);
        SBAR();
        rd01(IC(s + 2), IC(0));
        if constexpr (ps == 1) rd2(IC(s + 4));
        if constexpr (s + XL < 64 && !first) issue_x(IC(s + XL));
        if constexpr (s == 65) ld8_1(fv, (lane & 15) * 8, fwl);
        if constexpr (s == QP) { fv = tag; rd_flag<16>(fv, lane * 8, fkvp); }
        if constexpr (s >= QC && s < QC + 17) ldp(IC(2 * (s - QC)));
        aux(s, IC(0), fl);
        SBAR();
        if constexpr (ps != 1) mm<false, (s % 64 < NA), s % 64 == 0>(acc[2 * ps + 1], f1[s % 3], x[s % 64]);
        SBAR();
        rd01(IC(s + 2), IC(1));
        if constexpr (a_piece(s) >= 0) pc_a(IC(a_piece(s)));
        if constexpr (s >= 2 && s < 6) ldx1<false, (s - 2) * 1024>(st.p[s - 2], c.xv, stt);
        if constexpr (first && s >= 6 && s < 10) ld_cs(ck.p[s - 6], 1, IC(s - 6));
        if constexpr (first && s >= 10 && s < 14) ld_cs(cv.p[s - 10], 2, IC(s - 10));
        if constexpr (s == 40) dma<0>(lds0 + 9 * SLOT + w * 1024, c.xv, wph);
        if constexpr (s >= 104 && s < 112 && s % 2 == 0) ld_cs(cq.p[(s - 104) / 2], 0, IC((s - 104) / 2));
        if constexpr (s >= 120 && s < 128 && s % 2 == 0) ld_cs(cw.p[(s - 120) / 2], 4, IC((s - 120) / 2));
        if constexpr (a_park(s) >= 0) ld_park(IC(a_park(s)));
        if constexpr (s >= QC && s < QC + 17) ldp(IC(2 * (s - QC) + 1));
        aux(s, IC(1), fl);
        if constexpr (ps == 0) {
          SBAR();
          mm<true, (s < NA), s == 0>(acc[2], f2[s % 5], x[s]);
          SBAR();
          rd2(IC(s + 2));
          if constexpr (s >= 62) rd2(IC(s + 4));
          if constexpr (first && a_first(s) >= 0) pc_a(IC(a_first(s)));
          aux(s, IC(2), fl);
        }
        if constexpr (ps == 1) { kmap(IC(2 * (s - 64))); kmap(IC(2 * (s - 64) + 1)); }
        if constexpr (s >= 176) sum_mm(IC(s - 176));
        SBAR();
      };
      {
        STAMP(30);
        bf16x8 cf;
        { const char* pc; asm volatile("s_getpc_b64 %0" : "=s"(pc));
          ldx0<0>(cf, c.xv, pc + (((blockIdx.x >> 3) * 4 + w) % CODE_KB) * 1024); }
        raw_in(IC(0), 7);
        u64 ep; ld8_1(ep, 0u, ws + WS_EPOCH);
        bf16x8 xt[4];
        const char* xw = xin + (size_t)tt * 65536;
        sfor<0, 4>([&](auto i) { ldx0<i * 32>(xt[i], (unsigned)(r * 2048 + h * 16), xw + cb * 128); });
        { const unsigned z = 0, vo = (unsigned)(lane ? cb * 4 * FL_I + 10 * FL_K : tt * FL_I);
          asm volatile("s_mov_b64 exec, 3\n\tglobal_atomic_or %0, %1, %2 sc1\n\ts_mov_b64 exec, -1" ::"v"(vo), "v"(z), "s"(flags) : "memory"); }
        wait_bar<6>();
        raw_out(IC(0), 7);
        asm volatile("s_waitcnt vmcnt(6)" : "+v"(cf), "+v"(ep), "+v"(xt[0]), "+v"(xt[1]), "+v"(xt[2]), "+v"(xt[3]));
        sfor<0, 4>([&](auto i) { xr[i] = unperm(__builtin_bit_cast(u32x4, xt[i])); });
        store_stats(r, xr, stto);
        raw_in(IC(1), 8); raw_in(IC(2), 9);
        { u64 z = ((u64)__builtin_amdgcn_readfirstlane((unsigned)(ep >> 32)) << 32 | __builtin_amdgcn_readfirstlane((unsigned)ep)) + 0x9E3779B97F4A7C15ull;
          z = (z ^ (z >> 30)) * 0xBF58476D1CE4E5B9ull; z = (z ^ (z >> 27)) * 0x94D049BB133111EBull; tag = z ^ (z >> 31); asm volatile("" : "+s"(tag)); }
        waitv<8>();
        set_flag(facto, tag);
        bar();
        if (w == 0) set_flag(fwl - FL_K + rb * 8, tag);
        const unsigned xs = (unsigned)((lane >> 3) * 2048 + ((lane & 7) ^ (((lane >> 3) & 3) << 1)) * 16), xl = lds0 + w * 28672;
        const unsigned xo = xl + r * 128 + (r >> 3) * 16 + h * 8;
        auto x_in = [&](auto g) { sfor<0, 4>([&](auto i) { dma<i * 1040>(xl + (g % 6) * 4160, xs + i * (16384 - 1040), xw + g * 128); }); };
        sfor<0, 6>(x_in);
        sfor<0, 16>([&](auto g) {
          waitv<4 * (g < 10 ? 5 : 15 - g) + (g > 2 && g < 9 ? 10 : 0)>();
          sfor<4 * g, 4 * g + 4>([&](auto s) { ds_read2<(s < NA)>(x[s], xo + (g % 6) * 4160 + (((s % 4) ^ (r & 3)) << 5)); });
          sfor<4 * g, 4 * g + 4>([&](auto s) { waitl<(s < NA)>(x[s]); });
          if constexpr (g + 6 < 16) x_in(IC(g + 6));
          if constexpr (g == 2) { bar(); raw_out(IC(1), 8); raw_out(IC(2), 9); }
        });
        unsigned tch;
        {
          const int gw = blockIdx.x * 4 + w; const unsigned z = 0;
          const unsigned vo = (unsigned)(lane < 5 ? WS_KVF - WS_ST + (gw * 5 + lane) * 128 : WS_FLAG - WS_ST + gw * 128);
          poll<32>(flags, (unsigned)((lane & 15) * 8 + (blk ? cb * 4 * FL_I + 10 * FL_K : tt * FL_I)), tag);
          bar();
          if (w == 0) set_flag(fwl + rb * 8, tag);
          sfor<-20, 0>([&](auto n) { pc_a(IC(a_first(n))); });
          asm volatile("s_mov_b64 exec, 63\n\tglobal_atomic_or %0, %1, %2 sc1\n\ts_mov_b64 exec, -1" ::"v"(vo), "v"(z), "s"(ws + WS_ST) : "memory");
          const char* sm = (gw & 512) ? ((gw & 256) ? wk : wv) : wphi;
          asm volatile("global_load_dword %0, %1, %2" : "=v"(tch) : "v"((unsigned)((gw & 255) * 256 + lane * 4)), "s"(sm));
        }
        STAMP(31); STAMP(0); STAMP(1);
        wait_bar<14>();
        sfor<0, 2>([&](auto s) { rd01(s, IC(0)); rd01(s, IC(1)); rd2(s); });
        sfor<0, 64>([&](auto s) { step(s, std::true_type{}); });
        waitx<false, 63>(tch);
      }
  asm volatile("" : "+s"(w), "+s"(tag), "+s"(act), "+s"(acto), "+s"(fact), "+s"(facto), "+s"(stt), "+s"(stto), "+s"(col), "+s"(colw), "+s"(ldsw));
  asm volatile("" : "+s"(kvp), "+s"(zp), "+s"(fkvp), "+s"(fwl), "+s"(rb), "+s"(kvf), "+s"(fkv), "+s"(xu_p), "+s"(su_p), "+s"(wkv), "+s"(csm), "+s"(l));
  for (;;) {
    {
      REFRESH();
      asm volatile("" : "+s"(kvp), "+s"(zp), "+s"(fkvp), "+s"(fwl), "+s"(rb));
      STAMP(2);
      sfor<64, 128>([&](auto s) { step(s, std::false_type{}); });
      STAMP(19);
      mdone(acc[3]);
      v_out(IC(1), IC(0)); v_out(IC(1), IC(1));
      lds_bar();
      {
        const int hd = w >> 1, fh = w & 1;
        f32x16 kv, z;
        const int ch = fh * 4 + 2 * blk + (p4 >> 1), r0 = 4 * h + q4;
        LAS const char* pa = pk_img + hd * 16384 + r0 * 128 + ((ch ^ r0) << 4) + 8 * (p4 & 1);
        LAS const char* pb = v_img + hd * 8192 + lane * 16;
        bf16x8 a[8], b[8], d[8];
#pragma unroll
        for (int s = 0; s < 8; ++s) { a[s] = cat8(trread(pa + s * 2048), trread(pa + s * 2048 + 1024)); b[s] = *(LAS const bf16x8*)(pb + s * 1024); }
#pragma unroll
        for (int s = 0; s < 8; ++s) d[s] = *(LAS const bf16x8*)(sh + 8704 + s * 32 + h * 16);
        SBAR();
        sfor<0, 8>([&](auto s) { mm<false, false, s == 0>(kv, a[s], b[s]); });
        sfor<0, 8>([&](auto s) {
          mm<false, false, s == 0>(z, d[s], a[s]);
          if constexpr (s == 1) { mdone(kv); st4_1<0>(__builtin_bit_cast(u32x4, pack8<0>(kv)), c.xv, kvp + rb * 2048); }
          if constexpr (s == 2) { st4_1<1024>(__builtin_bit_cast(u32x4, pack8<1>(kv)), c.xv, kvp + rb * 2048); waitv<SQ.cw>(); }
          SBAR();
        });
        mdone(z);
        st1_1(z[0], (unsigned)(r * 4), zp + rb * 128);
      }
      STAMP(3);
      sfor<128, 192>([&](auto s) { step(s, std::false_type{}); });
      mdone(acc[4], acc[5]);
      STAMP(4);
      {
        bf16x8 pq[2][4], ka[8], za[8];
        f32x16 ep, en, nm[4], dn[2];
        float ai[2], bi[2];
        char* dst = acto + STG;
        sfor<0, 4>([&](auto k) { mm<false, false, k % 2 == 0, true>(p[k / 2], wf[k], pack8<k % 2>(acc[4 + k / 2])); });
        mdone(p[0], p[1]);
        hedge_exp(p[0], sce, ep, en, [&](auto i) { sum_mm(IC(16 + i)); });
        pq[0][0] = pack8<0>(ep); pq[0][1] = pack8<1>(ep); pq[0][2] = pack8<0>(en); pq[0][3] = pack8<1>(en);
        pin4(pq[0]);
        asm volatile("" : "+v"(kvs));
        *(LAS bf16x8*)(sh + w * 2048 + lane * 16) = pack8<0>(kvs); *(LAS bf16x8*)(sh + w * 2048 + 1024 + lane * 16) = pack8<1>(kvs);
        lds_bar();
        STAMP(5);
        LAS const char* zb = sh + 8192 + (r & 1) * 256 + h * 32;
#pragma unroll
        for (int k = 0; k < 8; ++k) { za[k] = *(LAS const bf16x8*)(zb + (k >> 1) * 64 + (k & 1) * 16); ka[k] = *(LAS const bf16x8*)(sh + k * 1024 + lane * 16); }
        auto coef = [&](auto hd) {
          const float a = __builtin_amdgcn_rcpf(dn[0][1]), b = __builtin_amdgcn_rcpf(dn[1][1]);
          const float inv = __builtin_amdgcn_rcpf(dn[0][0] * a + dn[1][0] * b + 1e-6f);
          ai[hd] = opq_v(a * inv); bi[hd] = opq_v(b * inv);
        };
        auto fin = [&](auto hd) {
          f32x16 o;
#pragma unroll
          for (int i = 0; i < 16; ++i) o[i] = nm[2 * hd][i] * ai[hd] + nm[2 * hd + 1][i] * bi[hd];
          st4_1<hd * 2048>(__builtin_bit_cast(u32x4, pack8<0>(o)), c.xv, dst);
          st4_1<hd * 2048 + 1024>(__builtin_bit_cast(u32x4, pack8<1>(o)), c.xv, dst);
        };
        hedge_exp(p[1], sce, ep, en, [&](auto i) {
          if constexpr (i < 4) mm<false, false, i % 2 == 0>(dn[i / 2], za[i], pq[0][i]);
          else if constexpr (i < 8) mm<false, false, i % 2 == 0>(nm[i / 2 - 2], ka[i - 4], pq[0][i - 4]);
          if constexpr (i % 2) pc_a(IC(108 + i / 2));
        });
        pq[1][0] = pack8<0>(ep); pq[1][1] = pack8<1>(ep); pq[1][2] = pack8<0>(en); pq[1][3] = pack8<1>(en);
        pin4(pq[1]); pin2(dn[0], dn[1]); pin2(nm[0], nm[1]);
        coef(IC(0));
        STAMP(6);
        sfor<0, 4>([&](auto k) { mm<false, false, k % 2 == 0, k == 0>(dn[k / 2], za[4 + k], pq[1][k]); if constexpr (k % 2) pc_a(IC(116 + k / 2)); });
        sfor<0, 4>([&](auto k) { mm<false, false, k % 2 == 0>(nm[2 + k / 2], ka[4 + k], pq[1][k]); if constexpr (k % 2) pc_a(IC(118 + k / 2)); });
        fin(IC(0));
        pin2(dn[0], dn[1]); pin2(nm[2], nm[3]);
        coef(IC(1));
        fin(IC(1));
        SBAR();
        sfor<120, 124>(pc_a);
        res_to_acc(xr, ra0, ra1);
        waitv<4>();
        set_flag(facto + FL_K, tag);
        sfor<124, 128>(pc_a); sfor<8, 16>(ld_park);
      }
    }
    STAMP(7);
    REFRESH();
    LAS char* reg = smem + 8 * SLOT;
    bf16x8 fu[16], cu;
    {
      f32x16 &a0 = ra0, &a1 = ra1;
      poll<16>(fact + FL_K, lane * 8, tag);
      sfor<0, 16>([&](auto i) { pin_a(park[i]); });
      STAMP(8);
      bf16x8 x[64];
      const char* xb = act + STG;
      sfor<0, XL>([&](auto s) { ld_x<s>(x, c, xb); });
      bar();
      asm volatile("" : "+s"(wkv));
      const unsigned kvv = (unsigned)((lane >> 1) * 64 + (cb & 1) * 32 + (lane & 1) * 16);
      const unsigned wa = ldsw + c.xv, wb = wa + 65536;
      auto unpark = [&](auto i) {
        constexpr int off = a_slot(26 + i / 4) * SLOT + (i % 4) * 1024;
        ds_write_a<(off & 65535)>(off < 65536 ? wa : wb, park[i]);
      };
      gemm64<true, false, HookP3>(c, x, xb, a0, a1, [&](auto s) {
        if constexpr (s == 33) bar();
        if constexpr (s >= 36 && s < 44) unpark(IC(s - 36));
        if constexpr (s == 49) bar();
        if constexpr (s >= 50 && s < 58) unpark(IC(s - 42));
        if constexpr (s >= 52 && s < 60) dma<0>(lds0 + 8 * SLOT + w * 8192 + (s - 52) * 1024, kvv, wkv + (s - 52) * 32 * 64);
        if constexpr (s >= 60) pc_w(4, a_slot(24), 0, IC(s - 60));
      }, [](auto) {});
      STAMP(9);
      store_x(c, r, a0, a1, xr, acto + 2 * STG, stto + 256 * 1024);
      asm volatile("s_waitcnt vmcnt(5) lgkmcnt(0)\n\ts_barrier" ::: "memory");
      { LAS const char* p = reg + w * 8192 + (4 * h + q4) * 32 + p4 * 8;
#pragma unroll
        for (int i = 0; i < 16; ++i) fu[i] = cat8(trread(p + i * 512), trread(p + i * 512 + 256));
        SBAR();
        float t0 = 0.f, t1 = 0.f, t2 = 0.f, t3 = 0.f;
#pragma unroll
        for (int i = 0; i < 16; ++i) {
          const u32x4 f = __builtin_bit_cast(u32x4, fu[i]);
          t0 = dot2(f[0], 0x3f803f80u, t0); t1 = dot2(f[1], 0x3f803f80u, t1); t2 = dot2(f[2], 0x3f803f80u, t2); t3 = dot2(f[3], 0x3f803f80u, t3);
        }
        cu = opq_v(cs_frag(swap_add((t0 + t1) + (t2 + t3)))); }
      waitv<0>();
      set_flag(facto + 2 * FL_K, tag);
      auto pc_q = [&](auto i) { pc_w(4, a_slot(24 + i / 4), i / 4, IC(i % 4)); };
      sfor<4, 8>(pc_q); sfor<24, 32>(pc_q);
    }
    STAMP(10);
    REFRESH();
    f32x16 a0, a1;
    asm volatile("" : "+s"(kvf), "+s"(fkv), "+s"(xu_p), "+s"(su_p));
    {
      poll<16>(fact + 2 * FL_K, lane * 8, tag);
      bar();
      STAMP(11);
      const char* xb = act + 2 * STG; const char* stp = stt + 256 * 1024;
      bf16x8 x[64], xu[16], mf0, mf1;
      Red<false> su, sp;
      sfor<0, 4>([&](auto i) { ldx1<false, i * 1024>(su.p[i], c.xv, su_p); });
      sfor<0, 16>([&](auto i) { ldx1<true, (i % 8) * 1024 - 4096>(xu[i], c.xv, xu_p + (i / 8) * 8192); });
      f32x16 au, av;
      sfor<0, 16>([&](auto i) {
        waitx<true, 15 - i + (i < XL ? i : XL)>(xu[i]);
        SBAR();
        mm<false, true, i == 0>(au, fu[i], xu[i]);
        if constexpr (i < XL) ld_x<i>(x, c, xb);
        mm<true, true, i == 0>(av, fu[i], xu[i]);
        if constexpr (i < 11) su.template run<i>(lane);
        if constexpr (i == 12) mf0 = opq_v(mean_frag(su.a, h == 0));
        if constexpr (i == 14) { mm<false, false, false>(au, cu, mf0); mm<true, false, false>(av, cu, mf0); }
        SBAR();
      });
      STAMP(12);
      u64 fa;
      f32x4 t[4], ru;
      unsigned vo;
      gemm64<true, true, HookP4>(c, x, xb, a0, a1, [&](auto s) {
        if constexpr (s == 1) {
          pin2(au, av);
          auto put = [&](const f32x16& a) {
#pragma unroll
            for (int gq = 0; gq < 4; ++gq) *(LAS f32x4*)(reg + w * 8192 + gq * 1024 + lane * 16) = f32x4{a[4 * gq], a[4 * gq + 1], a[4 * gq + 2], a[4 * gq + 3]};
          };
          if (ty < 2) { put(au); asm volatile("" ::: "memory"); } else put(av);
        }
        if constexpr (s == 3) {
          lds_bar();
#pragma unroll
          for (int j = 0; j < 4; ++j) t[j] = *(LAS const f32x4*)(reg + j * 8192 + (ty < 2 ? w & 1 : w) * 1024 + lane * 16);
        }
        if constexpr (s == 6) st2_1(__builtin_bit_cast(f32x2, u32x2{cvtpk(t[0][0], t[0][1]), cvtpk(t[0][2], t[0][3])}), vo, kvf);
        if constexpr (s >= 8 && s < 12) ldx1<false, (s - 8) * 1024>(sp.p[s - 8], c.xv, stp);
        if constexpr (s == 7 + XL) { bar(); if (w == 0) set_flag(fkv + rb * 4 * FL_I + cb * 8, tag); }
        if constexpr (s == 52) ld8_1(fa, (lane & 15) * 8 + (w * 4 + (lane >> 4)) * 4 * FL_I, fkv);
      }, [&](auto g) {
        if constexpr (g == 0) {
#pragma unroll
          for (int e = 0; e < 4; ++e) ru[e] = lane_get(su.b, ty < 2 ? r : 8 * w + 4 * h + e);
        }
        if constexpr (g == 4) vo = opq_v(ut * 4096 + (ty < 2 ? (unsigned)(ty * 1024 + lane * 16 + (w & 1) * 8)
                                                               : (unsigned)(2048 + (w >> 1) * 1024 + ((ty & 1) * 16 + (r & 15) + 32 * (w & 1)) * 16 + h * 8)));
        if constexpr (g >= 9 && g < 13) {
#pragma unroll
          for (int e = 0; e < 4; ++e) { float v = t[0][e]; if constexpr (g < 12) a_add(v, t[g - 8][e]); else a_mul(v, ru[e]); t[0][e] = v; }
        }
        if constexpr (g >= 48 && g < 59) sp.template run<g - 48>(lane);
        if constexpr (g == 60) mf0 = opq_v(mean_frag(sp.a, h == 0));
        if constexpr (g == 61) mf1 = opq_v(mean_frag(sp.a, h == 1));
        if constexpr (g == 64) mm<false, false, false>(a0, cw.p[0], mf0);
        if constexpr (g == 65) mm<false, false, false>(a1, cw.p[0], mf1);
      });
      rstd = sp.b;
      waitx<false, 0>(fa);
#pragma unroll 1
      for (int spin = 0; spin < (1 << 15) && !__all(fa == tag); ++spin) { __builtin_amdgcn_s_sleep(8); ld8_1(fa, (lane & 15) * 8 + (w * 4 + (lane >> 4)) * 4 * FL_I, fkv); waitx<false, 0>(fa); }
      bar();
      STAMP(13);
    }
    STAMP(14);
    {
      u32x16 qq, misc;
      misc[14] = misc[15] = __float_as_uint(rstd * QSCALE);
#pragma unroll 1
      for (int attempt = 0; attempt < 2; ++attempt) {
        if (attempt == 1) {
          const bf16x8 qa0 = __builtin_bit_cast(bf16x8, __builtin_shufflevector(qq, qq, 0, 1, 2, 3)), qa1 = __builtin_bit_cast(bf16x8, __builtin_shufflevector(qq, qq, 4, 5, 6, 7));
          const bf16x8 qb0 = __builtin_bit_cast(bf16x8, __builtin_shufflevector(qq, qq, 8, 9, 10, 11)), qb1 = __builtin_bit_cast(bf16x8, __builtin_shufflevector(qq, qq, 12, 13, 14, 15));
          float mA = -INFINITY, mB = -INFINITY;
          for (int t = 0; t < 64; ++t) {
            bf16x8 k0, k1;
            ldx1<false, 0>(k0, c.xv, kvf + t * 4096); ldx1<false, 1024>(k1, c.xv, kvf + t * 4096);
            wait2<0>(k0, k1);
            mm<false, false, true>(a0, k0, qa0); mm<false, false, false>(a0, k1, qa1); mm<false, false, true>(a1, k0, qb0); mm<false, false, false>(a1, k1, qb1);
            mdone(a0, a1);
            mA = fmaxf(mA, max16(a0)); mB = fmaxf(mB, max16(a1));
          }
          misc[14] = __float_as_uint(swap_max(mA)); misc[15] = __float_as_uint(swap_max(mB));
        }
        f32x16 oa, ob, t0, t1, t2, t3;
        u32x16 kvr; u32x4 offs;
        offs[0] = c.xv;
        const char* ks = kvf + 2048 + w * 2048; const char* wsr = wt(5, 0);
        unsigned wm0 = ldsw, cnt = 8;
        asm volatile(
            "global_load_dwordx4 v[208:211], v240, %[kv] sc1\n\tglobal_load_dwordx4 v[212:215], v240, %[kv] offset:1024 sc1\n\t"
            "s_mov_b32 m0, s44\n\tv_add_u32 v241, 0x2000, v240\n\t"
            "global_load_lds_dwordx4 v240, s[40:41] sc1\n\tglobal_load_lds_dwordx4 v240, s[40:41] offset:1024 sc1\n\t"
            "s_add_u32 m0, s44, 0x2000\n\tv_add_u32 v242, 0x4000, v240\n\t"
            "global_load_lds_dwordx4 v241, s[40:41] sc1\n\tglobal_load_lds_dwordx4 v241, s[40:41] offset:1024 sc1\n\t"
            "s_add_u32 m0, s44, 0x4000\n\tv_add_u32 v243, 0x6000, v240\n\t"
            "global_load_lds_dwordx4 v242, s[40:41] sc1\n\tglobal_load_lds_dwordx4 v242, s[40:41] offset:1024 sc1\n\t"
            "s_add_u32 s40, s40, 0x6000\n\ts_addc_u32 s41, s41, 0\n\t"
            "s_cmp_lg_u32 %[att], 0\n\ts_cbranch_scc1 2f\n\t"
            "v_pk_mul_f32 v[128:129], v[128:129], v[238:239]\n\t"
            "v_pk_mul_f32 v[130:131], v[130:131], v[238:239]\n\t"
            "v_pk_mul_f32 v[132:133], v[132:133], v[238:239]\n\t"
            "v_pk_mul_f32 v[134:135], v[134:135], v[238:239]\n\t"
            "v_pk_mul_f32 v[136:137], v[136:137], v[238:239]\n\t"
            "v_pk_mul_f32 v[138:139], v[138:139], v[238:239]\n\t"
            "v_pk_mul_f32 v[140:141], v[140:141], v[238:239]\n\t"
            "v_pk_mul_f32 v[142:143], v[142:143], v[238:239]\n\t"
            "v_pk_mul_f32 v[144:145], v[144:145], v[238:239]\n\t"
            "v_pk_mul_f32 v[146:147], v[146:147], v[238:239]\n\t"
            "v_pk_mul_f32 v[148:149], v[148:149], v[238:239]\n\t"
            "v_pk_mul_f32 v[150:151], v[150:151], v[238:239]\n\t"
            "v_pk_mul_f32 v[152:153], v[152:153], v[238:239]\n\t"
            "v_pk_mul_f32 v[154:155], v[154:155], v[238:239]\n\t"
            "v_pk_mul_f32 v[156:157], v[156:157], v[238:239]\n\t"
            "v_pk_mul_f32 v[158:159], v[158:159], v[238:239]\n\t"
            "v_cvt_pk_bf16_f32 v192, v128, v129\n\t"
            "v_cvt_pk_bf16_f32 v193, v130, v131\n\t"
            "v_cvt_pk_bf16_f32 v194, v132, v133\n\t"
            "v_cvt_pk_bf16_f32 v195, v134, v135\n\t"
            "v_cvt_pk_bf16_f32 v196, v136, v137\n\t"
            "v_cvt_pk_bf16_f32 v197, v138, v139\n\t"
            "v_cvt_pk_bf16_f32 v198, v140, v141\n\t"
            "v_cvt_pk_bf16_f32 v199, v142, v143\n\t"
            "v_cvt_pk_bf16_f32 v200, v144, v145\n\t"
            "v_cvt_pk_bf16_f32 v201, v146, v147\n\t"
            "v_cvt_pk_bf16_f32 v202, v148, v149\n\t"
            "v_cvt_pk_bf16_f32 v203, v150, v151\n\t"
            "v_cvt_pk_bf16_f32 v204, v152, v153\n\t"
            "v_cvt_pk_bf16_f32 v205, v154, v155\n\t"
            "v_cvt_pk_bf16_f32 v206, v156, v157\n\t"
            "v_cvt_pk_bf16_f32 v207, v158, v159\n\t"
            "2:\n\t"
            "v_mov_b64 v[64:65], 0\n\t"
            "v_mov_b64 v[66:67], 0\n\t"
            "v_mov_b64 v[68:69], 0\n\t"
            "v_mov_b64 v[70:71], 0\n\t"
            "v_mov_b64 v[72:73], 0\n\t"
            "v_mov_b64 v[74:75], 0\n\t"
            "v_mov_b64 v[76:77], 0\n\t"
            "v_mov_b64 v[78:79], 0\n\t"
            "v_mov_b64 v[80:81], 0\n\t"
            "v_mov_b64 v[82:83], 0\n\t"
            "v_mov_b64 v[84:85], 0\n\t"
            "v_mov_b64 v[86:87], 0\n\t"
            "v_mov_b64 v[88:89], 0\n\t"
            "v_mov_b64 v[90:91], 0\n\t"
            "v_mov_b64 v[92:93], 0\n\t"
            "v_mov_b64 v[94:95], 0\n\t"
            "v_mov_b64 v[176:177], 0\n\t"
            "v_mov_b64 v[178:179], 0\n\t"
            "v_mov_b64 v[180:181], 0\n\t"
            "v_mov_b64 v[182:183], 0\n\t"
            "v_mov_b64 v[184:185], 0\n\t"
            "v_mov_b64 v[186:187], 0\n\t"
            "v_mov_b64 v[188:189], 0\n\t"
            "v_mov_b64 v[190:191], 0\n\t"
            "v_mov_b64 v[216:217], 0\n\t"
            "v_mov_b64 v[218:219], 0\n\t"
            "v_mov_b64 v[220:221], 0\n\t"
            "v_mov_b64 v[222:223], 0\n\t"
            "v_mov_b64 v[228:229], 0\n\t"
            "v_mov_b64 v[230:231], 0\n\t"
            "v_mov_b64 v[232:233], 0\n\t"
            "v_mov_b64 v[234:235], 0\n\t"
            "v_bfe_u32 v226, v240, 6, 1\n\tv_mov_b32 v236, 0xdddddddd\n\tv_mul_u32_u24 v226, 0x3f80, v226\n\tv_add_u32 v237, %[rg], v240\n\t"
            "v_lshl_or_b32 v226, v226, 16, v226\n\tv_mov_b32 v227, v226\n\tv_xor_b32 v224, 0x3f803f80, v226\n\tv_mov_b32 v225, v224\n\t"
            "s_waitcnt vmcnt(6)\n\ts_cbranch_scc1 3f\n\t"
            "v_mfma_f32_32x32x16_bf16 v[128:143], v[208:211], v[192:195], 0\n\tv_mfma_f32_32x32x16_bf16 v[160:175], v[208:211], v[200:203], 0\n\t"
            "v_mfma_f32_32x32x16_bf16 v[128:143], v[212:215], v[196:199], v[128:143]\n\tv_mfma_f32_32x32x16_bf16 v[160:175], v[212:215], v[204:207], v[160:175]\n\t"
            "s_nop 11\n\t"
            "v_max3_f32 v238, v128, v129, v130\n\tv_max3_f32 v239, v160, v161, v162\n\t"
            "v_max3_f32 v238, v238, v131, v132\n\tv_max3_f32 v239, v239, v163, v164\n\t"
            "v_max3_f32 v238, v238, v133, v134\n\tv_max3_f32 v239, v239, v165, v166\n\t"
            "v_max3_f32 v238, v238, v135, v136\n\tv_max3_f32 v239, v239, v167, v168\n\t"
            "v_max3_f32 v238, v238, v137, v138\n\tv_max3_f32 v239, v239, v169, v170\n\t"
            "v_max3_f32 v238, v238, v139, v140\n\tv_max3_f32 v239, v239, v171, v172\n\t"
            "v_max3_f32 v238, v238, v141, v142\n\tv_max3_f32 v239, v239, v173, v174\n\t"
            "v_max3_f32 v238, v238, v143, v143\n\tv_max3_f32 v239, v239, v175, v175\n\t"
            "v_mov_b32 v96, v238\n\tv_mov_b32 v97, v239\n\ts_nop 1\n\tv_permlane32_swap_b32 v238, v96\n\tv_permlane32_swap_b32 v239, v97\n\t"
            "v_max_f32 v238, v238, v96\n\tv_max_f32 v239, v239, v97\n\t"
            "3:\n\t"
            "v_xor_b32 v96, 0x80000000, v238\n\tv_xor_b32 v112, 0x80000000, v239\n\tv_mov_b32 v97, v96\n\tv_mov_b32 v113, v112\n\t"
            "v_mov_b64 v[98:99], v[96:97]\n\tv_mov_b64 v[114:115], v[112:113]\n\t"
            "v_mov_b64 v[100:101], v[96:97]\n\tv_mov_b64 v[116:117], v[112:113]\n\t"
            "v_mov_b64 v[102:103], v[96:97]\n\tv_mov_b64 v[118:119], v[112:113]\n\t"
            "v_mov_b64 v[104:105], v[96:97]\n\tv_mov_b64 v[120:121], v[112:113]\n\t"
            "v_mov_b64 v[106:107], v[96:97]\n\tv_mov_b64 v[122:123], v[112:113]\n\t"
            "v_mov_b64 v[108:109], v[96:97]\n\tv_mov_b64 v[124:125], v[112:113]\n\t"
            "v_mov_b64 v[110:111], v[96:97]\n\tv_mov_b64 v[126:127], v[112:113]\n\t"
            "s_nop 1\n\t" S0_(128, 192, 96) S1_(128, 192)
            "s_waitcnt vmcnt(4)\n\ts_barrier\n\ts_nop 8\n\t"
            "1:\n\t"
            STEP_(0, 24576, 240, 0, "s_sub_u32 %[cnt], %[cnt], 1\n\t", "") STEP_(8192, 0, 241, 1024, "", "") STEP_(16384, 8192, 242, 2048, "", "")
            STEP_(24576, 16384, 243, 3072, "s_add_u32 s40, s40, 0x8000\n\ts_addc_u32 s41, s41, 0\n\t", "s_add_u32 s42, s42, 0x4000\n\ts_addc_u32 s43, s43, 0\n\t")
            "s_add_u32 s45, s45, 0x4000\n\ts_cmp_lg_u32 %[cnt], 0\n\ts_cbranch_scc1 1b\n\t"
            L0_(232, 176) L1_(232, 176) PV0_(80, 176) PV1_(80, 176)
            "s_nop 11\n\t"
            : "=&{v[64:79]}"(oa), "=&{v[80:95]}"(ob), "=&{v[96:111]}"(t0), "=&{v[112:127]}"(t1), "+{v[128:143]}"(a0), "+{v[144:159]}"(a1), "=&{v[160:175]}"(t2),
              "=&{v[176:191]}"(t3), "+{v[192:207]}"(qq), "=&{v[208:223]}"(kvr), "+{v[224:239]}"(misc), "+{v[240:243]}"(offs),
              "+{s[40:41]}"(ks), "+{s[42:43]}"(wsr), "+{s45}"(wm0), [cnt] "+s"(cnt)
            : "{s44}"(lds0 + 8 * SLOT + w * 2048), [kv] "s"(kvf), [att] "s"(attempt), [rg] "s"(lds0 + 8 * SLOT)
            : "scc", "memory");
        const float la = __uint_as_float(misc[4]), lb = __uint_as_float(misc[8]);
        const float ia = __builtin_amdgcn_rcpf(la), ib = __builtin_amdgcn_rcpf(lb);
#pragma unroll
        for (int e = 0; e < 16; ++e) { oa[e] *= ia; ob[e] *= ib; }
        char* dst = acto + 3 * STG;
        st4_1<0>(__builtin_bit_cast(u32x4, pack8<0>(oa)), c.xv, dst); st4_1<1024>(__builtin_bit_cast(u32x4, pack8<1>(oa)), c.xv, dst);
        st4_1<2048>(__builtin_bit_cast(u32x4, pack8<0>(ob)), c.xv, dst); st4_1<3072>(__builtin_bit_cast(u32x4, pack8<1>(ob)), c.xv, dst);
        float chk = 0.f;
#pragma unroll
        for (int e = 0; e < 16; ++e) chk = __builtin_fmaf(oa[e], 0.f, __builtin_fmaf(ob[e], 0.f, chk));
        const bool bad = !(la < 1.2676506e30f) || !(lb < 1.2676506e30f) || !(chk == 0.f);
        wait_bar<4>();
        LAS volatile int* vote = (LAS volatile int*)(smem + 8 * SLOT);
        if (lane == 0) vote[w] = 0;
        if (attempt == 0 && bad) vote[w] = 1;
        lds_bar();
        const int redo = vote[0] | vote[1] | vote[2] | vote[3];
        if (!__builtin_amdgcn_readfirstlane(redo)) break;
        lds_bar();
      }
      res_to_acc(xr, ra0, ra1);
      waitv<0>();
      set_flag(facto + 3 * FL_K, tag);
    }
    STAMP(15);
    REFRESH();
    {
      f32x16 &a0 = ra0, &a1 = ra1;
      poll<16>(fact + 3 * FL_K, lane * 8, tag);
      STAMP(16);
      bf16x8 x[64];
      const char* xb = act + 3 * STG;
      sfor<0, XL>([&](auto s) { ld_x<s>(x, c, xb); });
      bar();
      gemm64<false, false, HookP6>(c, x, xb, a0, a1, [&](auto s) {
        if constexpr (s == 33) bar();
        if constexpr (s >= 52) pc_a(IC(a_pre(s - 52)));
      }, [](auto) {});
      STAMP(17);
      if (l == 0) {
        store_x(c, r, a0, a1, xr, acto + 4 * STG, stto + 512 * 1024);
        bar();
        sfor<12, 16>([&](auto n) { pc_a(IC(a_pre(n))); });
        waitv<4>();
        set_flag(facto + 4 * FL_K, tag);
        sfor<16, 24>([&](auto n) { pc_a(IC(a_pre(n))); });
      } else {
        char* dst = out + (size_t)(tt * 32 + r) * 2048 + cb * 128 + h * 16;
        *(u32x4*)(dst) = unperm(__builtin_bit_cast(u32x4, pack8<0>(a0))); *(u32x4*)(dst + 32) = unperm(__builtin_bit_cast(u32x4, pack8<1>(a0)));
        *(u32x4*)(dst + 64) = unperm(__builtin_bit_cast(u32x4, pack8<0>(a1))); *(u32x4*)(dst + 96) = unperm(__builtin_bit_cast(u32x4, pack8<1>(a1)));
        if (tt == 0 && cb == 0) set_flag(ws + WS_EPOCH, tag);
      }
    }
    STAMP(18);
    if (l == 1) break;
    csm = 1;
    act += 4 * STG; acto += 4 * STG; fact += 4 * FL_K; facto += 4 * FL_K; stt += 512 * KB; stto += 512 * KB;
    kvp += 131072; zp += 8192; fkvp += FL_K; kvf += 320 * KB; fkv += FL_K; xu_p += 4 * STG; su_p += 512 * KB;
    l = 1;
    REFRESH();
    STAMP(0);
        poll<16>(fact, lane * 8, tag);
        STAMP(1);
        sfor<0, XL>(issue_x);
        wait_bar<XL>();
        sfor<0, 2>([&](auto s) { rd01(s, IC(0)); rd01(s, IC(1)); rd2(s); });
        sfor<0, 64>([&](auto s) { step(s, std::false_type{}); });
  }
}

extern "C" void kernel_launch(void* const* d_in, const int* in_sizes, int n_in, void* d_out, int out_size, void* d_ws, size_t ws_size, hipStream_t stream) {
  k_main<<<256, 256, 0, stream>>>((const char*)d_in[0], (const char*)d_in[1], (const char*)d_in[2], (const char*)d_in[3], (const char*)d_in[5], (const char*)d_in[6],
                                  (const char*)d_in[7], (const char*)d_in[8], (char*)d_ws, (char*)d_out);
}
```

```cpp
#include <hip/hip_runtime.h>
#include <hip/hip_bf16.h>
#include <stdint.h>
#include <type_traits>

typedef __attribute__((ext_vector_type(8))) short bf16x8;
typedef __attribute__((ext_vector_type(4))) short s16x4;
typedef __attribute__((ext_vector_type(2))) __bf16 bf2_t;
typedef __attribute__((ext_vector_type(16))) float f32x16;
typedef __attribute__((ext_vector_type(4))) float f32x4;
typedef __attribute__((ext_vector_type(2))) float f32x2;
typedef __attribute__((ext_vector_type(4))) unsigned u32x4;
typedef __attribute__((ext_vector_type(2))) unsigned u32x2;
#define LAS __attribute__((address_space(3)))
#define DI __device__ __forceinline__
#define MFMA32(a, b, c) __builtin_amdgcn_mfma_f32_32x32x16_bf16((a), (b), (c), 0, 0, 0)
#define MFMA16(a, b, c) __builtin_amdgcn_mfma_f32_16x16x32_bf16((a), (b), (c), 0, 0, 0)
#define SBAR() __builtin_amdgcn_sched_barrier(0)

constexpr int S_ = 2048, D_ = 1024;
constexpr float LOG2E = 1.4426950408889634f;
constexpr float QSCALE = 0.17677669529663687f * LOG2E;

constexpr size_t MB = 1u << 20, KB = 1024;
constexpr size_t STG = 256 * KB;
constexpr size_t WS_COL = 32 * MB;
constexpr size_t C_WT = 0;
constexpr size_t C_KVP = 768 * KB;
constexpr size_t C_ZP = 1024 * KB;
constexpr size_t C_CS = 1040 * KB;
constexpr size_t WS_ST = 64 * MB;
constexpr size_t WS_KVF = WS_ST + 1024 * KB;
constexpr size_t WS_FLAG = WS_ST + 1664 * KB;
constexpr size_t WS_EPOCH = WS_ST + 1920 * KB;
constexpr int FL_I = 2048, FL_K = 128;
typedef unsigned long long u64;

template <int I, int N, class F> DI void sfor(F&& f) { if constexpr (I < N) { f(std::integral_constant<int, I>{}); sfor<I + 1, N>(f); } }

DI unsigned cvtpk(float lo, float hi) { f32x2 v = {lo, hi}; return __builtin_bit_cast(unsigned, __builtin_convertvector(v, bf2_t)); }
DI float bflo(unsigned u) { return __uint_as_float(u << 16); }
DI float bfhi(unsigned u) { return __uint_as_float(u & 0xffff0000u); }
DI float rbf(float x) { return bflo(cvtpk(x, x)); }
DI float swap_add(float v) { auto r = __builtin_amdgcn_permlane32_swap(__float_as_uint(v), __float_as_uint(v), false, false); return __uint_as_float(r[0]) + __uint_as_float(r[1]); }
DI float swap_max(float v) { auto r = __builtin_amdgcn_permlane32_swap(__float_as_uint(v), __float_as_uint(v), false, false); return fmaxf(__uint_as_float(r[0]), __uint_as_float(r[1])); }
DI float swap_min(float v) { auto r = __builtin_amdgcn_permlane32_swap(__float_as_uint(v), __float_as_uint(v), false, false); return fminf(__uint_as_float(r[0]), __uint_as_float(r[1])); }
template <int S> DI bf16x8 pack8(const f32x16& v) {
  u32x4 p = {cvtpk(v[8 * S], v[8 * S + 1]), cvtpk(v[8 * S + 2], v[8 * S + 3]), cvtpk(v[8 * S + 4], v[8 * S + 5]), cvtpk(v[8 * S + 6], v[8 * S + 7])};
  return __builtin_bit_cast(bf16x8, p);
}
DI u32x4 unperm(u32x4 v) {
  auto r0 = __builtin_amdgcn_permlane32_swap(v[0], v[2], false, false);
  auto r1 = __builtin_amdgcn_permlane32_swap(v[1], v[3], false, false);
  return u32x4{r0[0], r1[0], r0[1], r1[1]};
}
DI float lane_get(float v, int src) { return __uint_as_float(__builtin_amdgcn_ds_bpermute(src << 2, __float_as_uint(v))); }
DI s16x4 trread(LAS const char* p) { return __builtin_amdgcn_ds_read_tr16_b64_v4i16((LAS s16x4*)p); }
DI bf16x8 cat8(s16x4 a, s16x4 b) { return __builtin_shufflevector(a, b, 0, 1, 2, 3, 4, 5, 6, 7); }
DI float dot2(unsigned a, unsigned b, float c) { return __builtin_amdgcn_fdot2_f32_bf16(__builtin_bit_cast(bf2_t, a), __builtin_bit_cast(bf2_t, b), c, false); }

template <int OFF> DI void dma(unsigned lds, unsigned v, const char* sb) {
  asm volatile("s_mov_b32 m0, %0\n\ts_nop 0\n\tglobal_load_lds_dwordx4 %1, %2 offset:%3" ::"s"(lds), "v"(v), "s"(sb), "i"(OFF) : "memory");
}
template <int OFF> DI void dma_c(unsigned lds, unsigned v, const char* sb) {
  asm volatile("s_mov_b32 m0, %0\n\ts_nop 0\n\tglobal_load_lds_dwordx4 %1, %2 offset:%3 sc1" ::"s"(lds), "v"(v), "s"(sb), "i"(OFF) : "memory");
}
template <int OFF> DI void dma_n(unsigned v, const char* sb) {
  asm volatile("global_load_lds_dwordx4 %0, %1 offset:%2 sc1" ::"v"(v), "s"(sb), "i"(OFF) : "memory");
}
constexpr int cap63(int n) { return n > 63 ? 63 : n; }
template <int N> DI void wait_bar() { asm volatile("s_waitcnt vmcnt(%0)\n\ts_barrier" ::"i"(cap63(N)) : "memory"); }
DI void lds_bar() { asm volatile("s_waitcnt lgkmcnt(0)\n\ts_barrier" ::: "memory"); }
DI void all_bar() { asm volatile("s_waitcnt vmcnt(0) lgkmcnt(0)\n\ts_barrier" ::: "memory"); }
DI void bar() { asm volatile("s_barrier" ::: "memory"); }
template <bool AG, int OFF> DI void ldx1(bf16x8& d, unsigned voff, const char* sb) {
  if constexpr (AG) asm volatile("global_load_dwordx4 %0, %1, %2 offset:%3 sc1" : "=a"(d) : "v"(voff), "s"(sb), "i"(OFF));
  else asm volatile("global_load_dwordx4 %0, %1, %2 offset:%3 sc1" : "=v"(d) : "v"(voff), "s"(sb), "i"(OFF));
}
template <int OFF> DI void ld2_1(f32x2& d, unsigned voff, const char* sb) { asm volatile("global_load_dwordx2 %0, %1, %2 offset:%3 sc1" : "=v"(d) : "v"(voff), "s"(sb), "i"(OFF)); }
DI void ld1_1(unsigned& d, unsigned voff, const void* sb) { asm volatile("global_load_dword %0, %1, %2 sc1" : "=v"(d) : "v"(voff), "s"(sb) : "memory"); }
template <int OFF> DI void st4_1(u32x4 d, unsigned voff, char* sb) { asm volatile("global_store_dwordx4 %0, %1, %2 offset:%3 sc1\n\ts_nop 1" ::"v"(voff), "v"(d), "s"(sb), "i"(OFF) : "memory"); }
DI void st2_1(f32x2 d, unsigned voff, char* sb) { asm volatile("global_store_dwordx2 %0, %1, %2 sc1" ::"v"(voff), "v"(d), "s"(sb) : "memory"); }
DI void st1_1(float d, unsigned voff, char* sb) { asm volatile("global_store_dword %0, %1, %2 sc1" ::"v"(voff), "v"(d), "s"(sb) : "memory"); }
template <int N> DI void waitv() { asm volatile("s_waitcnt vmcnt(%0)" ::"i"(cap63(N)) : "memory"); }
template <bool AG, int N, class T> DI void waitx(T& d) {
  if constexpr (AG) asm volatile("s_waitcnt vmcnt(%1)" : "+a"(d) : "i"(cap63(N))); else asm volatile("s_waitcnt vmcnt(%1)" : "+v"(d) : "i"(cap63(N)));
}
template <bool SW, bool BA, bool Z, bool NOP = false> DI void mm(f32x16& acc, const bf16x8& a, const bf16x8& b) {
#define MM_(OPS, C, OUT, BC) do { if constexpr (NOP) asm volatile("s_nop 1\n\tv_mfma_f32_32x32x16_bf16 %0, " OPS ", " C : OUT(acc) : "v"(a), BC(b)); \
                                  else asm volatile("v_mfma_f32_32x32x16_bf16 %0, " OPS ", " C : OUT(acc) : "v"(a), BC(b)); } while (0)
  if constexpr (Z) {
    if constexpr (SW) { if constexpr (BA) MM_("%2, %1", "0", "=&v", "a"); else MM_("%2, %1", "0", "=&v", "v"); }
    else { if constexpr (BA) MM_("%1, %2", "0", "=&v", "a"); else MM_("%1, %2", "0", "=&v", "v"); }
  } else {
    if constexpr (SW) { if constexpr (BA) MM_("%2, %1", "%0", "+v", "a"); else MM_("%2, %1", "%0", "+v", "v"); }
    else { if constexpr (BA) MM_("%1, %2", "%0", "+v", "a"); else MM_("%1, %2", "%0", "+v", "v"); }
  }
#undef MM_
}
DI void mdone(f32x16& a) { asm volatile("s_nop 11" : "+v"(a)); }
DI void mdone(f32x16& a, f32x16& b) { asm volatile("s_nop 11" : "+v"(a), "+v"(b)); }
DI void mdone(f32x16& a, f32x16& b, f32x16& c, f32x16& d) { asm volatile("s_nop 11" : "+v"(a), "+v"(b), "+v"(c), "+v"(d)); }
template <class T> DI T opq_v(T v) { asm volatile("" : "+v"(v)); return v; }
template <int OFF> DI void ldx0(bf16x8& d, unsigned voff, const char* sb) { asm volatile("global_load_dwordx4 %0, %1, %2 offset:%3" : "=v"(d) : "v"(voff), "s"(sb), "i"(OFF)); }
template <int OFF> DI void ldx1m(bf16x8& d, unsigned voff, const char* sb, unsigned long long m) {
  asm volatile("s_mov_b64 exec, %4\n\tglobal_load_dwordx4 %0, %1, %2 offset:%3 sc1\n\ts_mov_b64 exec, -1" : "=v"(d) : "v"(voff), "s"(sb), "i"(OFF), "s"(m));
}
template <bool AG> DI void ds_read2(bf16x8& d, unsigned addr) {
  if constexpr (AG) asm volatile("ds_read2_b64 %0, %1 offset1:2" : "=a"(d) : "v"(addr) : "memory"); else asm volatile("ds_read2_b64 %0, %1 offset1:2" : "=v"(d) : "v"(addr) : "memory");
}
template <bool AG> DI void waitl(bf16x8& d) { if constexpr (AG) asm volatile("s_waitcnt lgkmcnt(0)" : "+a"(d)::"memory"); else asm volatile("s_waitcnt lgkmcnt(0)" : "+v"(d)::"memory"); }
template <int OFF> DI void ds_write_a(unsigned addr, const bf16x8& v) { asm volatile("ds_write_b128 %0, %1 offset:%2" ::"v"(addr), "a"(v), "i"(OFF) : "memory"); }
template <int N, class T> DI void wait2(T& a, T& b) { asm volatile("s_waitcnt vmcnt(%2)" : "+v"(a), "+v"(b) : "i"(cap63(N))); }
DI void set_flag(char* flag, u64 tag) {
  unsigned z = 0;
  asm volatile("s_mov_b64 exec, 1\n\tglobal_atomic_swap_x2 %0, %1, %2 sc1\n\ts_mov_b64 exec, -1" ::"v"(z), "v"(tag), "s"(flag) : "memory");
}
DI void ld8_1(u64& d, unsigned voff, const char* sb) { asm volatile("global_load_dwordx2 %0, %1, %2 sc1" : "=v"(d) : "v"(voff), "s"(sb) : "memory"); }
template <int NL> DI void rd_flag(u64& v, unsigned voff, const char* sb) {
  const u64 zero = 0;
  if constexpr (NL == 64) asm volatile("global_atomic_or_x2 %0, %1, %3, %2 sc0 sc1" : "+v"(v) : "v"(voff), "s"(sb), "v"(zero) : "memory");
  else asm volatile("s_mov_b64 exec, %4\n\tglobal_atomic_or_x2 %0, %1, %3, %2 sc0 sc1\n\ts_mov_b64 exec, -1" : "+v"(v) : "v"(voff), "s"(sb), "v"(zero), "s"((1ull << (NL & 63)) - 1) : "memory");
}
template <int NL> DI void poll(const char* base, unsigned voff, u64 tag) {
  for (int spin = 0; spin < (1 << 15); ++spin) {
    u64 v = tag;
    rd_flag<NL>(v, voff, base);
    waitx<false, 0>(v);
    if (__all(v == tag)) break;
  }
  asm volatile("" ::: "memory");
}

template <int RBX> DI void block_map(int& rb, int& cb) {
  const int x = blockIdx.x & 7, i = blockIdx.x >> 3;
  constexpr int CBX = 32 / RBX, GC = 16 / CBX;
  rb = RBX * (x / GC) + i / CBX; cb = CBX * (x % GC) + i % CBX;
}

DI float a_fma(float a, float b, float c) { float d; asm volatile("v_fma_f32 %0, %1, %2, %3" : "=v"(d) : "v"(a), "v"(b), "v"(c)); return d; }
DI float a_fms(float a, float b, float c) { float d; asm volatile("v_fma_f32 %0, %1, -%2, %3" : "=v"(d) : "v"(a), "v"(b), "v"(c)); return d; }
DI void a_exp(float& x) { asm volatile("v_exp_f32 %0, %0" : "+v"(x)); }
DI void a_add(float& s, float x) { asm volatile("v_add_f32 %0, %0, %1" : "+v"(s) : "v"(x)); }
DI void a_mul(float& x, float s) { asm volatile("v_mul_f32 %0, %0, %1" : "+v"(x) : "v"(s)); }
DI float a_max3(float a, float b, float c) { float d; asm volatile("v_max3_f32 %0, %1, %2, %3" : "=v"(d) : "v"(a), "v"(b), "v"(c)); return d; }
DI float a_min3(float a, float b, float c) { float d; asm volatile("v_min3_f32 %0, %1, %2, %3" : "=v"(d) : "v"(a), "v"(b), "v"(c)); return d; }

DI void pin_a(bf16x8& a) { asm volatile("" : "+a"(a)); }
DI void pin2(f32x16& a, f32x16& b) { asm volatile("" : "+v"(a), "+v"(b)); }
DI bf16x8 cs_frag(float c) {
  const float hi = rbf(c), mid = rbf(c - hi), lo = (c - hi) - mid;
  return __builtin_bit_cast(bf16x8, u32x4{cvtpk(hi, mid), cvtpk(lo, 0.f), 0u, 0u});
}
template <bool CS> struct Red {
  bf16x8 p[4];
  float t[4], a, b, sd;
  template <int K> DI void run(int lane) {
    if constexpr (K == 0) asm volatile("" : "+v"(p[0]), "+v"(p[1]), "+v"(p[2]), "+v"(p[3]));
    if constexpr (K < 3) {
      const f32x4 u = __builtin_bit_cast(f32x4, p[K + 1]), v = __builtin_bit_cast(f32x4, p[0]);
#pragma unroll
      for (int e = 0; e < 4; ++e) { if (K == 0) t[e] = v[e]; a_add(t[e], u[e]); }
    }
    if constexpr (K == 3) {
      auto x = __builtin_amdgcn_permlane16_swap(__float_as_uint(t[0]), __float_as_uint(t[1]), false, false);
      auto y = __builtin_amdgcn_permlane16_swap(__float_as_uint(t[2]), __float_as_uint(t[3]), false, false);
      a = opq_v(__uint_as_float(x[0]) + __uint_as_float(x[1])); b = opq_v(__uint_as_float(y[0]) + __uint_as_float(y[1]));
    }
    if constexpr (K == 4) { auto x = __builtin_amdgcn_permlane32_swap(__float_as_uint(a), __float_as_uint(b), false, false); a = opq_v(__uint_as_float(x[0]) + __uint_as_float(x[1])); }
    if constexpr (K == 5) {
      if constexpr (CS) a = lane_get(a, (lane >> 2) + ((lane & 3) << 4));
      else { const int src = ((lane & 31) >> 1) + ((lane & 1) << 5); b = lane_get(a, src + 16); a = lane_get(a, src); }
    }
    if constexpr (CS) {
      if constexpr (K == 8) { t[0] = opq_v(rbf(a)); t[1] = opq_v(a - t[0]); }
      if constexpr (K == 9) { const float mid = rbf(t[1]); p[0] = opq_v(__builtin_bit_cast(bf16x8, u32x4{cvtpk(t[0], mid), cvtpk(t[1] - mid, 0.f), 0u, 0u})); }
    } else {
      if constexpr (K == 8) { const float m = a * (1.f / D_); b = opq_v(fmaxf(b * (1.f / D_) - m * m, 0.f)); a = opq_v(rbf(m)); }
      if constexpr (K == 9) b = opq_v(rbf(rbf(b) + 1e-5f));
      if constexpr (K == 10) { sd = opq_v(rbf(__builtin_amdgcn_sqrtf(b))); b = opq_v(__builtin_amdgcn_rcpf(sd)); }
    }
  }
};
DI void pin4(bf16x8 (&p)[4]) { asm volatile("" : "+v"(p[0]), "+v"(p[1]), "+v"(p[2]), "+v"(p[3])); }
DI bf16x8 mean_frag(float mean, bool on) { const float m = on ? -mean : 0.f; return __builtin_bit_cast(bf16x8, u32x4{cvtpk(m, m), cvtpk(m, 0.f), 0u, 0u}); }

#define STAMP(i)

constexpr int CODE_KB = 50;
constexpr int SLOT = 16384, XL = 12;
struct WinPtr {
  unsigned b[3];
  DI void init(unsigned a) { sfor<0, 3>([&](auto i) { b[i] = a + i * 65536u; asm volatile("" : "+v"(b[i])); }); }
  template <int OFF> DI bf16x8 frag() const { return *(LAS const bf16x8*)((LAS const char*)(uintptr_t)b[OFF >> 16] + (OFF & 65535)); }
};
struct Ctx { unsigned xv; WinPtr p; };
#define IC(n) std::integral_constant<int, (n)>{}

template <int S> DI void ld_x(bf16x8 (&x)[64], const Ctx& c, const char* xb) { ldx1<false, (S % 8) * 1024 - 4096>(x[S], c.xv, xb + (S / 8) * 8192 + 4096); }
template <class H> constexpr int g64_wait(int s) {
  int n = 0;
  const int t0 = s - XL;
  if (t0 < 0) n += XL - 1 - s; else n += H::n(t0);
  for (int t = t0 < 0 ? 0 : t0 + 1; t < s; ++t) n += (t + XL < 64 ? 1 : 0) + H::n(t);
  return n;
}
constexpr int a_slot(int g);
template <bool MAPA, bool ZERO, class H, class FX, class FA>
DI void gemm64(const Ctx& c, bf16x8 (&x)[64], const char* xb, f32x16& acc0, f32x16& acc1, FX&& hook, FA&& aux) {
  bf16x8 f0[3], f1[3];
  auto off = [](int s) constexpr { return (MAPA ? a_slot(24 + s / 8) : s / 8) * SLOT + (s % 8) * 2048; };
  auto rd0 = [&](auto s) { f0[s % 3] = c.p.template frag<off(s)>(); };
  auto rd1 = [&](auto s) { f1[s % 3] = c.p.template frag<off(s) + 1024>(); };
  rd0(IC(0)); rd1(IC(0)); rd0(IC(1)); rd1(IC(1));
  sfor<0, 64>([&](auto s) {
    waitx<false, g64_wait<H>(s)>(x[s]);
    SBAR();
    mm<false, false, ZERO && s == 0>(acc0, f0[s % 3], x[s]);
    SBAR();
    if constexpr (s + 2 < 64) rd0(IC(s + 2));
    if constexpr (s + XL < 64) ld_x<s + XL>(x, c, xb);
    aux(IC(2 * s));
    SBAR();
    mm<false, false, ZERO && s == 0>(acc1, f1[s % 3], x[s]);
    SBAR();
    if constexpr (s + 2 < 64) rd1(IC(s + 2));
    hook(s);
    aux(IC(2 * s + 1));
    SBAR();
  });
  mdone(acc0, acc1);
}
struct HookP6 { static constexpr int n(int s) { return s >= 52 ? 1 : 0; } };
struct HookP3 { static constexpr int n(int s) { return s >= 52 ? 1 : 0; } };
struct HookP4 { static constexpr int n(int s) { return s == 52 || s == 6 || (s >= 8 && s < 12) ? 1 : 0; } };

DI void res_to_acc(const u32x4 (&xr)[4], f32x16& a0, f32x16& a1) {
#pragma unroll
  for (int e = 0; e < 4; ++e) {
    a0[2 * e] = bflo(xr[0][e]); a0[2 * e + 1] = bfhi(xr[0][e]); a0[8 + 2 * e] = bflo(xr[1][e]); a0[9 + 2 * e] = bfhi(xr[1][e]);
    a1[2 * e] = bflo(xr[2][e]); a1[2 * e + 1] = bfhi(xr[2][e]); a1[8 + 2 * e] = bflo(xr[3][e]); a1[9 + 2 * e] = bfhi(xr[3][e]);
  }
  asm volatile("" : "+v"(a0), "+v"(a1));
}
DI void store_stats(int r, const u32x4 (&xr)[4], char* stp) {
  float s1 = 0.f, s2 = 0.f, s3 = 0.f, s4 = 0.f;
#pragma unroll
  for (int i = 0; i < 4; ++i)
#pragma unroll
    for (int e = 0; e < 4; e += 2) {
      s1 = dot2(xr[i][e], 0x3f803f80u, s1); s2 = dot2(xr[i][e], xr[i][e], s2);
      s3 = dot2(xr[i][e + 1], 0x3f803f80u, s3); s4 = dot2(xr[i][e + 1], xr[i][e + 1], s4);
    }
  st2_1(f32x2{swap_add(s1 + s3), swap_add(s2 + s4)}, (unsigned)(r * 8), stp);
}
DI void store_x(const Ctx& c, int r, const f32x16& a0, const f32x16& a1, u32x4 (&xr)[4], char* dst, char* stp) {
  SBAR();
  xr[0] = __builtin_bit_cast(u32x4, pack8<0>(a0)); st4_1<0>(xr[0], c.xv, dst); SBAR();
  xr[1] = __builtin_bit_cast(u32x4, pack8<1>(a0)); st4_1<1024>(xr[1], c.xv, dst); SBAR();
  xr[2] = __builtin_bit_cast(u32x4, pack8<0>(a1)); st4_1<2048>(xr[2], c.xv, dst); SBAR();
  xr[3] = __builtin_bit_cast(u32x4, pack8<1>(a1)); st4_1<3072>(xr[3], c.xv, dst); SBAR();
  store_stats(r, xr, stp);
  SBAR();
}

template <class FX> DI void hedge_exp(const f32x16& p, float sc, f32x16& ep, f32x16& en, FX&& hook) {
  float mx = fmaxf(p[0], p[1]), mn = fminf(p[0], p[1]);
#pragma unroll
  for (int i = 2; i < 16; i += 2) { mx = fmaxf(fmaxf(mx, p[i]), p[i + 1]); mn = fminf(fminf(mn, p[i]), p[i + 1]); }
  mx = swap_max(mx) * sc; mn = swap_min(mn) * sc;
  sfor<0, 16>([&](auto i) {
    SBAR();
    hook(i);
    constexpr int e = i;
    float a = __builtin_amdgcn_exp2f(__builtin_fmaf(p[e], sc, -mx)), b = __builtin_amdgcn_exp2f(__builtin_fmaf(p[e], -sc, mn));
    asm volatile("" : "+v"(a), "+v"(b));
    ep[e] = a; en[e] = b;
  });
  SBAR();
}

constexpr int a_slot(int g) { constexpr int m[32] = {0, 1, 2, 3, 4, 0, 1, 2, 5, 6, 7, 5, 3, 4, 6, 7, 8, 5, 3, 4, 6, 7, 0, 1, 2, 5, 3, 4, 6, 7, 0, 1}; return m[g]; }
constexpr int a_need(int g) { return g < 8 ? 8 * g - 2 : (g < 16 ? 16 * (g - 8) - (g > 12 ? 10 : 2) : 128 + 8 * (g - 16) - 2); }
constexpr bool a_bar(int s) { return (s + 2) % 8 == 0 && s >= 14 && s < 190; }
constexpr int a_pre(int n) { return n < 16 ? n : n + 16; }
constexpr int QP = 128 + 24, QC = 128 + 36;
constexpr int a_piece(int s) {
  constexpr int t[][3] = {{6, 4, 1}, {12, 10, 1}, {20, 5, 1}, {24, 6, 1}, {28, 11, 1}, {34, 7, 1}, {46, 12, 1}, {54, 13, 1}, {64, 14, 2}, {72, 15, 2}, {94, 16, 2}, {102, 17, 2}, {110, 18, 2},
                          {118, 19, 2}, {128, 20, 1}, {132, 21, 1}, {136, 22, 1}, {140, 23, 1}, {144, 24, 1}, {150, 25, 1}, {158, 26, 1}};
  for (auto& e : t) if (s >= e[0] && s < e[0] + 4 * e[2] && (s - e[0]) % e[2] == 0) return e[1] * 4 + (s - e[0]) / e[2];
  return -1;
}
constexpr int a_first(int s) { constexpr int g[6] = {0, 8, 1, 2, 9, 3}; return s < 4 ? g[(s + 20) / 4] * 4 + (s + 20) % 4 : -1; }
constexpr int a_raw(int s) { constexpr int t[8] = {4, 5, 10, 11, 16, 17, 18, 19}; for (int i = 0; i < 8; ++i) if (t[i] == s) return i; return -1; }
constexpr int a_park(int s) { return s == 148 || s == 149 ? s - 148 : (s >= 154 && s < 158 ? s - 152 : (s == 162 || s == 163 ? s - 156 : -1)); }
constexpr bool a_side(int s, bool first) { return (s >= 2 && s < (first ? 14 : 6)) || s == 50 || (s >= 96 && s < 128 && s % 4 == 3) || a_park(s) >= 0; }
struct SchedA {
  int xw[64], bw[192], sw[64], fw, cw, pw, zw, lw[32];
  constexpr SchedA(bool first) : xw{}, bw{}, sw{}, fw(0), cw(0), pw(0), zw(0), lw{} {
    int pos = 0, xpos[64] = {}, wpos[32] = {}, spos[192] = {}, fpos = 0, stpos = 0, ppos = 0, rpos = 0, lpos[34] = {};
    if (first) { for (int s = -20; s < 0; ++s) wpos[a_first(s) / 4] = ++pos; pos += 2; }
    else for (int s = 0; s < XL; ++s) xpos[s] = ++pos;
    for (int s = 0; s < 192; ++s) {
      if (s == 128) { cw = pos + 2 - spos[127]; pos += 3; stpos = pos; }
      if (a_bar(s) || s == 6) {
        bw[s] = 63;
        for (int g = 0; g < 24; ++g) if (a_need(g) == s && wpos[g] && pos - wpos[g] < bw[s]) bw[s] = pos - wpos[g];
        if (s == 62 && pos - spos[50] < bw[s]) bw[s] = pos - spos[50];
        if (s == 38 && first && pos - rpos < bw[s]) bw[s] = pos - rpos;
        if (s == 142) { if (pos - stpos < bw[s]) bw[s] = pos - stpos; ++pos; }
      }
      if (s == 20 || s == 24 || s == 28) sw[s] = pos - spos[s - 15];
      if (s == 94) fw = pos - fpos;
      if (s == QC) pw = pos - ppos;
      if (s < 64) xw[s] = pos - xpos[s];
      if (!first && s + XL < 64) xpos[s + XL] = ++pos;
      if (s == 81) fpos = ++pos;
      if (s == QP) ppos = ++pos;
      if (s >= QC && s < QC + 17) lpos[2 * (s - QC)] = ++pos;
      if (a_piece(s) >= 0) wpos[a_piece(s) / 4] = ++pos;
      if (a_side(s, first)) spos[s] = ++pos;
      if (s >= QC && s < QC + 17) lpos[2 * (s - QC) + 1] = ++pos;
      if (first && a_first(s) >= 0) wpos[a_first(s) / 4] = ++pos;
      if (first && a_raw(s) >= 0) rpos = ++pos;
      if (first && (s == 39 || s == 42)) pos += 5;
      if (s == 175) zw = pos - lpos[1];
      if (s >= 176) lw[s - 176] = pos - lpos[2 + s - 176];
    }
    for (int i = 16; i < 32; ++i) lw[i] = pos - lpos[2 + i];
  }
};
constexpr int cmin(int a, int b) { return a < b ? a : b; }

#define R_OA "v[64:79]"
#define R_OB "v[80:95]"
#define S0_(n, q, m) "v_mfma_f32_32x32x16_bf16 v[" #n ":" #n "+15], v[208:211], v[" #q ":" #q "+3], v[" #m ":" #m "+15]\n\t"
#define S1_(n, q) "v_mfma_f32_32x32x16_bf16 v[" #n ":" #n "+15], v[212:215], v[" #q "+4:" #q "+7], v[" #n ":" #n "+15]\n\t"
#define PV0_(o, p) "v_smfmac_f32_32x32x32_bf16 v[" #o ":" #o "+15], v[216:219], v[" #p ":" #p "+7], v236\n\t"
#define PV1_(o, p) "v_smfmac_f32_32x32x32_bf16 v[" #o ":" #o "+15], v[220:223], v[" #p "+8:" #p "+15], v236\n\t"
#define L0_(l, p) "v_smfmac_f32_16x16x64_bf16 v[" #l ":" #l "+3], v[224:227], v[" #p ":" #p "+7], v236\n\t"
#define L1_(l, p) "v_smfmac_f32_16x16x64_bf16 v[" #l ":" #l "+3], v[224:227], v[" #p "+8:" #p "+15], v236\n\t"
#define E_(e, i) "v_exp_f32 v[" #e "+" #i "], v[" #e "+" #i "]\n\t"
#define RD_(d, off) "ds_read_b128 v[" #d ":" #d "+3], v237 offset:" #off "\n\t"
#define HALF_(e, n, q, m, p, o, l, X0, X1, X2, X3, X4, X5) \
  S0_(n, q, m) X0 E_(e, 0) E_(e, 1) E_(e, 2) S1_(n, q) X1 E_(e, 3) E_(e, 4) E_(e, 5) PV0_(o, p) X2 E_(e, 6) E_(e, 7) E_(e, 8) \
  PV1_(o, p) X3 E_(e, 9) E_(e, 10) E_(e, 11) L0_(l, p) X4 E_(e, 12) E_(e, 13) L1_(l, p) X5 E_(e, 14) E_(e, 15)
#define LG2_ "s_waitcnt lgkmcnt(2)\n\t"
#define LG0_ "s_waitcnt lgkmcnt(0)\n\t"
#define STEP_(b, nb, kv, wo, Y0, Y1) \
  HALF_(128, 160, 200, 112, 176, 80, 232, "", "", RD_(208, b + 2048), RD_(212, b + 3072), RD_(216, b), RD_(220, b + 1024) LG2_) \
  HALF_(160, 144, 192, 96, 128, 64, 228, "", LG0_, "s_add_u32 m0, s44, " #nb "\n\t", "", "global_load_lds_dwordx4 v" #kv ", s[40:41] sc1\n\t", \
        "global_load_lds_dwordx4 v" #kv ", s[40:41] offset:1024 sc1\n\t") \
  HALF_(144, 176, 200, 112, 160, 80, 232, Y0, "", RD_(208, b + 6144), RD_(212, b + 7168), RD_(216, b + 4096), RD_(220, b + 5120) LG2_) \
  HALF_(176, 128, 192, 96, 144, 64, 228, "s_mov_b32 m0, s45\n\t", LG0_, "global_load_lds_dwordx4 v240, s[42:43] offset:" #wo " sc1\n\t", Y1, \
        "s_waitcnt vmcnt(4)\n\t", "s_barrier\n\t")
DI float max16(const f32x16& s) {
  float mx = s[0];
#pragma unroll
  for (int e = 1; e < 16; ++e) mx = fmaxf(mx, s[e]);
  return mx;
}
typedef __attribute__((ext_vector_type(8))) unsigned u32x8;
typedef __attribute__((ext_vector_type(16))) unsigned u32x16;

__global__ __launch_bounds__(256, 1) void k_main(const char* xin, const char* wqkv, const char* wphi, const char* woA, const char* wq, const char* wk, const char* wv,
                                                 const char* woB, char* ws, char* out) {
  __shared__ __attribute__((aligned(16))) char smem_[163840];
  LAS char* const smem = (LAS char*)smem_;
  const unsigned lds0 = (unsigned)(uintptr_t)smem;
  Ctx c;
  int lane = threadIdx.x & 63, w = __builtin_amdgcn_readfirstlane(threadIdx.x >> 6), r = lane & 31, h = lane >> 5, rb, cb;
  block_map<4>(rb, cb);
  int tt = rb * 4 + w, blk = (lane >> 4) & 1, q4 = (lane & 15) >> 2, p4 = lane & 3;
  c.xv = lane * 16; c.p.init(lds0 + c.xv);
  char* flags = ws + WS_FLAG;
  char* col = ws + WS_COL + (size_t)cb * 2 * MB;
  const int ut = rb * 4 + (cb >> 2), ty = cb & 3;
  char* act = ws + (size_t)rb * 2 * MB + w * 65536;
  char* acto = act + cb * 4096;
  char* fact = flags + tt * FL_I;
  char* facto = fact + cb * 8;
  char* stt = ws + WS_ST + tt * 4096;
  char* stto = stt + cb * 256;
  char* kvp = col + C_KVP + w * 32768;
  char* zp = col + C_ZP + w * 2048;
  char* fkvp = flags + (cb * 4 + w) * FL_I + 8 * FL_K;
  char* fwl = flags + cb * 4 * FL_I + 11 * FL_K;
  char* kvf = ws + WS_KVF;
  char* fkv = flags + 12 * FL_K;
  const char* xu_p = ws + (size_t)rb * 2 * MB + 2 * STG + (cb >> 2) * 65536 + w * 16384 + 4096;
  const char* su_p = ws + WS_ST + 256 * KB + ut * 4096;
  const char* wkv = ((cb & 2) ? wv : wk) + w * 256 * 64;
  const char* wph = wphi + cb * 4096 + w * 1024;
  u64 tag = 0;
  u32x4 xr[4] = {};
  char* colw = col + w * 4096;
  unsigned ldsw = lds0 + w * 4096;
  auto wt = [&](int m, int ch) { return colw + C_WT + m * 131072 + ch * SLOT; };
  auto pc_w = [&](int m, int slot, int ch, auto i) { if constexpr (i == 0) dma_c<0>(ldsw + slot * SLOT, c.xv, wt(m, ch)); else dma_n<i * 1024>(c.xv, wt(m, ch)); };
  auto pc_a = [&](auto n) { constexpr int g = n / 4; pc_w(g < 8 ? 1 : (g < 16 ? 2 : (g < 24 ? 0 : 3)), a_slot(g), g % 8, IC(n % 4)); };
  u64 csm = ~0ull;
  float csv[4] = {};
  auto ld_cs = [&](bf16x8& d, int m, auto i) { ldx1m<i * 1024>(d, c.xv, col + C_CS + m * 4096, csm); };
  auto raw_in = [&](auto j, int slot, auto i) {
    const int hi = rb >> 3, pitch = (j == 0 || (j == 1 && !hi)) ? 6144 : 2048, row = lane >> 3;
    const char* src = (j == 0 ? wqkv + 2048 + hi * 2048 : (j == 1 ? (hi ? woA : wqkv) : (hi ? woB : wq))) + cb * 128 + (size_t)(rb & 7) * 128 * pitch;
    const unsigned v0 = (unsigned)((32 * w + row) * pitch + ((lane & 7) ^ (((row >> 1) & 1) << 2)) * 16);
    dma<i * 1024>(lds0 + slot * SLOT + w * 4096, v0 + i * (8 * pitch - 1024), src);
  };
  auto raw_out = [&](auto j, int slot) {
    const int hi = rb >> 3, ch = rb & 7, m = j == 0 ? 1 + hi : (j == 1 ? 3 * hi : 4 + hi);
    LAS const char* p = smem + slot * SLOT + (w >> 1) * 8192 + (4 * h + q4) * 128 + (((((q4 >> 1) ^ w) & 1) << 2) + 2 * blk + (p4 >> 1)) * 16 + 8 * (p4 & 1);
    const bool isv = m == 2;
    char* dst = col + C_WT + m * 131072 + (isv ? (w & 1) * 65536 + ch * 8192 + (w >> 1) * 4096 : ch * SLOT + (w >> 1) * 8192 + (w & 1) * 1024);
    float cs = 0.f;
    sfor<0, 4>([&](auto s) {
      const u32x4 f = __builtin_bit_cast(u32x4, cat8(trread(p + s * 2048), trread(p + s * 2048 + 1024)));
      st4_1<0>(f, c.xv, dst + s * (isv ? 1024 : 2048));
#pragma unroll
      for (int e = 0; e < 4; ++e) cs = dot2(f[e], 0x3f803f80u, cs);
    });
    st1_1(swap_add(cs), (unsigned)(r * 4), col + C_CS + m * 4096 + (ch * 2 + (w >> 1)) * 256 + (w & 1) * 128);
  };
  const int w0 = w; const unsigned ldsw0 = ldsw;
#define REFRESH() do { \
    w = w0; ldsw = ldsw0; \
    asm volatile("" : "+s"(w), "+s"(tag), "+s"(act), "+s"(acto), "+s"(fact), "+s"(facto), "+s"(stt), "+s"(stto), "+s"(col), "+s"(colw), "+s"(ldsw)); \
    asm volatile("" : "+v"(lane)); \
    r = lane & 31; h = lane >> 5; blk = (lane >> 4) & 1; q4 = (lane & 15) >> 2; p4 = lane & 3; c.xv = lane * 16; c.p.init(lds0 + c.xv); \
  } while (0)
  int l = 0;
  float mean, rstd;
  Red<true> cw;
  f32x16 ra0, ra1;
  bf16x8 park[16];
      constexpr int NA = 60;
      constexpr SchedA SQ(false), S1(true);
      bf16x8 x[64], f0[3], f1[3], f2[5], wf[4], kp[4], pp[32], zq[2], sel[2], mf0, mf1;
      Red<false> st; Red<true> ck, cv, cq;
      f32x16 acc[6], p[2], kvs;
      u64 fv;
      float mx[2], mn[2], e[16], se, sce;
      unsigned arow;
      LAS char* pk_img = smem;
      LAS char* v_img = smem + 2 * SLOT;
      LAS char* sh = smem + 9 * SLOT + 4096;
      auto issue_x = [&](auto s) { ldx1<(s < NA), (s % 8) * 1024 - 4096>(x[s], c.xv, act + (s / 8) * 8192 + 4096); };
      auto ld_park = [&](auto i) { ldx1<true, (i % 4) * 1024>(park[i], c.xv, wt(4, 2 + i / 4)); };
      auto ldp = [&](auto i) {
        if constexpr (i >= 2) ldx1<true, ((i - 2) % 8) * 1024 - 4096>(pp[i - 2], c.xv, kvp + ((i - 2) / 8) * 8192 + 4096); else ldx1<false, i * 1024>(zq[i], c.xv, zp);
      };
      auto rd_wf = [&](auto k) { LAS const char* wp = smem + 9 * SLOT + (4 * h + q4) * 64 + blk * 32 + p4 * 8 + k * 1024; wf[k] = cat8(trread(wp), trread(wp + 512)); };
      auto rd01 = [&](auto s, auto t) {
        if constexpr (s < 64 || (s >= 128 && s < 192)) { const bf16x8 f = c.p.template frag<a_slot(s / 8) * SLOT + (s % 8) * 2048 + t * 1024>(); if constexpr (t == 0) f0[s % 3] = f; else f1[s % 3] = f; }
      };
      auto rd2 = [&](auto s) { if constexpr (s < 128) f2[s % 5] = c.p.template frag<a_slot(8 + s / 16) * SLOT + (s % 16) * 1024>(); };
      auto v_out = [&](auto t, auto hf) {
        *(LAS bf16x8*)(v_img + t * 8192 + w * 2048 + hf * 1024 + lane * 16) = pack8<hf>(acc[2 + t]);
      };
      auto sum_mm = [&](auto i) { waitx<true, SQ.lw[i]>(pp[i]); mm<false, true, i == 0>(kvs, sel[i & 1], pp[i]); };
      auto kmap = [&](auto gi) {
        constexpr int g = gi;
        if constexpr (g == 4) { pin2(acc[0], acc[1]); }
        if constexpr (g >= 6 && g < 10) mm<false, false, g % 2 == 0>(p[(g - 6) / 2], wf[g - 6], kp[g - 6]);
        if constexpr (g >= 4 && g < 8) kp[g - 4] = opq_v(pack8<g % 2>(acc[(g - 4) / 2]));
        if constexpr (g == 10) mm<true, false, false>(acc[3], cv.p[0], mf1);
        if constexpr (g >= 12 && g < 24) {
          constexpr int hd = (g - 12) / 6, t = (g - 12) % 6;
          const f32x16& q = p[hd];
          if constexpr (t == 0) { mx[hd] = a_max3(q[0], q[1], q[2]); mn[hd] = a_min3(q[0], q[1], q[2]); mx[hd] = a_max3(mx[hd], q[3], q[4]); mn[hd] = a_min3(mn[hd], q[3], q[4]); }
          if constexpr (t >= 1 && t < 4) {
            mx[hd] = a_max3(mx[hd], q[4 * t + 1], q[4 * t + 2]); mn[hd] = a_min3(mn[hd], q[4 * t + 1], q[4 * t + 2]);
            mx[hd] = a_max3(mx[hd], q[4 * t + 3], q[(4 * t + 4) & 15]); mn[hd] = a_min3(mn[hd], q[4 * t + 3], q[(4 * t + 4) & 15]);
          }
          if constexpr (t == 4) mx[hd] = opq_v(swap_max(mx[hd]) * -sce);
          if constexpr (t == 5) mn[hd] = opq_v(swap_min(mn[hd]) * sce);
        }
        if constexpr (g >= 24) {
          constexpr int grp = (g - 24) / 26, j = (g - 24) % 26, hd = grp >> 1, sg = grp & 1;
          if constexpr (j >= 1 && j <= 16) a_exp(e[j - 1]);
          if constexpr (j < 16) e[j] = sg ? a_fms(p[hd][j], sce, mn[hd]) : a_fma(p[hd][j], sce, mx[hd]);
          if constexpr (j == 2) se = e[0];
          if constexpr (j > 2 && j <= 17) a_add(se, e[j - 2]);
          if constexpr (j == 17) { se = opq_v(__builtin_amdgcn_rcpf(swap_add(se)) * rstd); if constexpr (grp < 2) v_out(IC(0), IC(grp)); }
          if constexpr (j >= 18) {
            constexpr int gq = (j - 18) / 2;
            if constexpr (j % 2 == 0) { a_mul(e[4 * gq], se); a_mul(e[4 * gq + 1], se); a_mul(e[4 * gq + 2], se); a_mul(e[4 * gq + 3], se); }
            else *(LAS u32x2*)((LAS char*)(uintptr_t)(arow ^ ((sg * 4 + gq) << 4)) + hd * 16384) = u32x2{cvtpk(e[4 * gq], e[4 * gq + 1]), cvtpk(e[4 * gq + 2], e[4 * gq + 3])};
          }
        }
      };
      auto keep = [&](Red<true>& q, int n) { csv[n] = l == 0 ? q.a : csv[n]; q.a = csv[n]; };
      auto aux = [&](auto s, auto i, auto fl) {
        constexpr int g1 = 3 * s + i, g3 = 2 * (s - 128) + i;
        if constexpr (s < 64) {
          if constexpr (g1 >= 60 && g1 < 71) st.template run<g1 - 60>(lane);
          if constexpr (g1 == 80) { if constexpr (fl) csv[0] = ck.a; else ck.a = csv[0]; }
          if constexpr (g1 == 92) { if constexpr (fl) csv[1] = cv.a; else cv.a = csv[1]; }
          if constexpr (g1 >= (fl ? 72 : 80) && g1 < 82) ck.template run<g1 - 72>(lane);
          if constexpr (g1 >= (fl ? 84 : 92) && g1 < 94) cv.template run<g1 - 84>(lane);
          if constexpr (g1 == 71) { mean = st.a; rstd = st.b; sce = opq_v(rstd * LOG2E); }
          if constexpr (g1 == 95) mf0 = opq_v(mean_frag(mean, h == 0));
          if constexpr (g1 == 96) mf1 = opq_v(mean_frag(mean, h == 1));
          if constexpr (g1 == 99) mm<false, false, false>(acc[0], ck.p[0], mf0);
          if constexpr (g1 == 100) mm<false, false, false>(acc[1], ck.p[0], mf1);
          if constexpr (g1 == 101) mm<true, false, false>(acc[2], cv.p[0], mf0);
          if constexpr (g1 == 150) *(LAS short*)(sh + 8704 + w * 64 + (r >> 4) * 32 + ((r >> 2) & 1) * 16 + ((r >> 3) & 1) * 8 + (r & 3) * 2) = (short)(__float_as_uint(st.sd) >> 16);
          if constexpr (g1 >= 186 && g1 < 190) rd_wf(IC(g1 - 186));
          if constexpr (g1 == 174) arow = opq_v((unsigned)(uintptr_t)pk_img + (w * 32 + r) * 128 + h * 8 + ((r & 7) << 4));
        } else if constexpr (s >= 128) {
          if constexpr (g3 == 8) keep(cq, 2);
          if constexpr (g3 == 24) keep(cw, 3);
          if constexpr (g3 < 10) cq.template run<g3>(lane);
          if constexpr (g3 == 12) mm<false, false, false>(acc[4], cq.p[0], mf0);
          if constexpr (g3 == 13) mm<false, false, false>(acc[5], cq.p[0], mf1);
          if constexpr (g3 >= 16 && g3 < 26) cw.template run<g3 - 16>(lane);
          if constexpr (g3 >= 32 && g3 < 40) {
            constexpr int n = (g3 - 32) % 4, t = (g3 - 32) / 4;
            const int kr = 16 * t + 8 * (n >> 1) + 4 * h + 2 * (n & 1);
            u32x4 sv = __builtin_bit_cast(u32x4, sel[t]); sv[n] = opq_v(r == kr ? 0x3f80u : (r == kr + 1 ? 0x3f800000u : 0u)); sel[t] = __builtin_bit_cast(bf16x8, sv);
          }
          if constexpr (g3 == 40) { sel[0] = opq_v(sel[0]); sel[1] = opq_v(sel[1]); }
          if constexpr (g3 == 42) *(LAS unsigned*)(sh + 8192 + 256 + lane * 4) = opq_v(0x3f803f80u);
          if constexpr (g3 == 95) {
            wait2<SQ.zw>(zq[0], zq[1]);
#pragma unroll
            for (int n = 0; n < 4; ++n) { e[n] = __builtin_bit_cast(f32x4, zq[0])[n]; a_add(e[n], __builtin_bit_cast(f32x4, zq[1])[n]); }
          }
          if constexpr (g3 == 96) {
            auto a = __builtin_amdgcn_permlane16_swap(__float_as_uint(e[0]), __float_as_uint(e[1]), false, false);
            auto b = __builtin_amdgcn_permlane16_swap(__float_as_uint(e[2]), __float_as_uint(e[3]), false, false);
            e[0] = opq_v(__uint_as_float(a[0]) + __uint_as_float(a[1])); e[1] = opq_v(__uint_as_float(b[0]) + __uint_as_float(b[1]));
          }
          if constexpr (g3 == 98) { auto a = __builtin_amdgcn_permlane32_swap(__float_as_uint(e[0]), __float_as_uint(e[1]), false, false); e[0] = opq_v(__uint_as_float(a[0]) + __uint_as_float(a[1])); }
          if constexpr (g3 == 100) e[1] = lane_get(e[0], lane ^ 8);
          if constexpr (g3 == 106)
            *(LAS short*)(sh + 8192 + w * 64 + (lane & 1) * 32 + ((lane >> 2) & 1) * 16 + ((lane >> 1) & 1) * 8 + (lane >> 4) * 2) = (short)cvtpk(e[0] + e[1], 0.f);
          if constexpr (g3 >= 116 && g3 < 124 && g3 % 2 == 0) rd_wf(IC((g3 - 116) / 2));
        }
      };
      auto step = [&](auto s, auto fl) {
        constexpr int ps = s / 64;
        constexpr bool first = fl;
        if constexpr (a_bar(s) || (first && s == 6)) wait_bar<(ps ? cmin(S1.bw[s], SQ.bw[s]) : (first ? S1.bw[s] : SQ.bw[s]))>();
        if constexpr (s == 20 || (first && (s == 24 || s == 28))) waitv<(first ? S1.sw[s % 64] : SQ.sw[s % 64])>();
        if constexpr (first && s == 62) { if (w == 0) set_flag(fwl + rb * 8, tag); }
        if constexpr (s == 94) { waitx<false, SQ.fw>(fv); if (!__all(fv == tag)) poll<16>(fwl, lane * 8, tag); }
        if constexpr (s == 142) set_flag(fkvp + rb * 8, tag);
        if constexpr (s == QC) { waitx<false, SQ.pw>(fv); if (!__all(fv == tag)) poll<16>(fkvp, lane * 8, tag); }
        if constexpr (s < 64 && !first) waitx<(s < NA), SQ.xw[s % 64]>(x[s % 64]);
        SBAR();
        if constexpr (ps != 1) mm<false, (s % 64 < NA), s % 64 == 0>(acc[2 * ps], f0[s % 3], x[s % 64]);
        else mm<true, (s % 64 < NA), s % 64 == 0>(acc[3], f2[s % 5], x[s % 64]);
        SBAR();
        rd01(IC(s + 2), IC(0));
        if constexpr (ps == 1) rd2(IC(s + 4));
        if constexpr (s + XL < 64 && !first) issue_x(IC(s + XL));
        if constexpr (s == 81) ld8_1(fv, (lane & 15) * 8, fwl);
        if constexpr (s == QP) { fv = tag; rd_flag<16>(fv, lane * 8, fkvp); }
        if constexpr (s >= QC && s < QC + 17) ldp(IC(2 * (s - QC)));
        aux(s, IC(0), fl);
        SBAR();
        if constexpr (ps != 1) mm<false, (s % 64 < NA), s % 64 == 0>(acc[2 * ps + 1], f1[s % 3], x[s % 64]);
        SBAR();
        rd01(IC(s + 2), IC(1));
        if constexpr (a_piece(s) >= 0) pc_a(IC(a_piece(s)));
        if constexpr (s >= 2 && s < 6) ldx1<false, (s - 2) * 1024>(st.p[s - 2], c.xv, stt);
        if constexpr (first && s >= 6 && s < 10) ld_cs(ck.p[s - 6], 1, IC(s - 6));
        if constexpr (first && s >= 10 && s < 14) ld_cs(cv.p[s - 10], 2, IC(s - 10));
        if constexpr (s == 50) dma<0>(lds0 + 9 * SLOT + w * 1024, c.xv, wph);
        if constexpr (s >= 96 && s < 112 && s % 4 == 3) ld_cs(cq.p[(s - 96) / 4], 0, IC((s - 96) / 4));
        if constexpr (s >= 112 && s < 128 && s % 4 == 3) ld_cs(cw.p[(s - 112) / 4], 4, IC((s - 112) / 4));
        if constexpr (a_park(s) >= 0) ld_park(IC(a_park(s)));
        if constexpr (s >= QC && s < QC + 17) ldp(IC(2 * (s - QC) + 1));
        aux(s, IC(1), fl);
        if constexpr (ps == 0) {
          SBAR();
          mm<true, (s < NA), s == 0>(acc[2], f2[s % 5], x[s]);
          SBAR();
          rd2(IC(s + 2));
          if constexpr (s >= 62) rd2(IC(s + 4));
          if constexpr (first && a_first(s) >= 0) pc_a(IC(a_first(s)));
          if constexpr (first && a_raw(s) >= 0) raw_in(IC(1 + a_raw(s) / 4), 8 + a_raw(s) / 4, IC(a_raw(s) % 4));
          if constexpr (first && s == 39) raw_out(IC(2), 9);
          if constexpr (first && s == 42) raw_out(IC(1), 8);
          aux(s, IC(2), fl);
        }
        if constexpr (ps == 1) { kmap(IC(2 * (s - 64))); kmap(IC(2 * (s - 64) + 1)); }
        if constexpr (s >= 176) sum_mm(IC(s - 176));
        SBAR();
      };
      {
        STAMP(30);
        bf16x8 cf;
        { const char* pc; asm volatile("s_getpc_b64 %0" : "=s"(pc));
          ldx0<0>(cf, c.xv, pc + (((blockIdx.x >> 3) * 4 + w) % CODE_KB) * 1024); }
        sfor<0, 4>([&](auto i) { raw_in(IC(0), 7, i); });
        u64 ep; ld8_1(ep, 0u, ws + WS_EPOCH);
        bf16x8 xt[4];
        const char* xw = xin + (size_t)tt * 65536;
        sfor<0, 4>([&](auto i) { ldx0<i * 32>(xt[i], (unsigned)(r * 2048 + h * 16), xw + cb * 128); });
        { const unsigned z = 0, vo = (unsigned)(lane ? cb * 4 * FL_I + 10 * FL_K : tt * FL_I);
          asm volatile("s_mov_b64 exec, 3\n\tglobal_atomic_or %0, %1, %2 sc1\n\ts_mov_b64 exec, -1" ::"v"(vo), "v"(z), "s"(flags) : "memory"); }
        wait_bar<6>();
        raw_out(IC(0), 7);
        asm volatile("s_waitcnt vmcnt(6)" : "+v"(cf), "+v"(ep), "+v"(xt[0]), "+v"(xt[1]), "+v"(xt[2]), "+v"(xt[3]));
        sfor<0, 4>([&](auto i) { xr[i] = unperm(__builtin_bit_cast(u32x4, xt[i])); });
        store_stats(r, xr, stto);
        { u64 z = ((u64)__builtin_amdgcn_readfirstlane((unsigned)(ep >> 32)) << 32 | __builtin_amdgcn_readfirstlane((unsigned)ep)) + 0x9E3779B97F4A7C15ull;
          z = (z ^ (z >> 30)) * 0xBF58476D1CE4E5B9ull; z = (z ^ (z >> 27)) * 0x94D049BB133111EBull; tag = z ^ (z >> 31); asm volatile("" : "+s"(tag)); }
        const unsigned xs = (unsigned)((lane >> 3) * 2048 + ((lane & 7) ^ (((lane >> 3) & 3) << 1)) * 16), xl = lds0 + w * 28672;
        const unsigned xo = xl + r * 128 + (r >> 3) * 16 + h * 8;
        auto x_in = [&](auto g) { sfor<0, 4>([&](auto i) { dma<i * 1040>(xl + (g % 6) * 4160, xs + i * (16384 - 1040), xw + g * 128); }); };
        sfor<0, 2>(x_in);
        waitv<8>();
        set_flag(facto, tag);
        bar();
        if (w == 0) set_flag(fwl - FL_K + rb * 8, tag);
        sfor<2, 6>(x_in);
        const unsigned fo = (unsigned)((lane & 15) * 8 + (blk ? cb * 4 * FL_I + 10 * FL_K : tt * FL_I));
        sfor<0, 16>([&](auto g) {
          waitv<4 * (g < 10 ? 5 : 15 - g) + (g > 11 ? 1 : 0)>();
          sfor<4 * g, 4 * g + 4>([&](auto s) { ds_read2<(s < NA)>(x[s], xo + (g % 6) * 4160 + (((s % 4) ^ (r & 3)) << 5)); });
          sfor<4 * g, 4 * g + 4>([&](auto s) { waitl<(s < NA)>(x[s]); });
          if constexpr (g + 6 < 16) x_in(IC(g + 6));
          if constexpr (g == 11) { fv = tag; rd_flag<32>(fv, fo, flags); }
        });
        unsigned tch;
        {
          const int gw = blockIdx.x * 4 + w; const unsigned z = 0;
          const unsigned vo = (unsigned)(lane < 5 ? WS_KVF - WS_ST + (gw * 5 + lane) * 128 : WS_FLAG - WS_ST + gw * 128);
          waitx<false, 0>(fv);
          if (!__all(fv == tag)) poll<32>(flags, fo, tag);
          bar();
          sfor<-20, 0>([&](auto n) { pc_a(IC(a_first(n))); });
          asm volatile("s_mov_b64 exec, 63\n\tglobal_atomic_or %0, %1, %2 sc1\n\ts_mov_b64 exec, -1" ::"v"(vo), "v"(z), "s"(ws + WS_ST) : "memory");
          const char* sm = (gw & 512) ? ((gw & 256) ? wk : wv) : wphi;
          asm volatile("global_load_dword %0, %1, %2" : "=v"(tch) : "v"((unsigned)((gw & 255) * 256 + lane * 4)), "s"(sm));
        }
        STAMP(31); STAMP(0); STAMP(1);
        wait_bar<14>();
        sfor<0, 2>([&](auto s) { rd01(s, IC(0)); rd01(s, IC(1)); rd2(s); });
        sfor<0, 64>([&](auto s) { step(s, std::true_type{}); });
        waitx<false, 63>(tch);
      }
  asm volatile("" : "+s"(w), "+s"(tag), "+s"(act), "+s"(acto), "+s"(fact), "+s"(facto), "+s"(stt), "+s"(stto), "+s"(col), "+s"(colw), "+s"(ldsw));
  asm volatile("" : "+s"(kvp), "+s"(zp), "+s"(fkvp), "+s"(fwl), "+s"(rb), "+s"(kvf), "+s"(fkv), "+s"(xu_p), "+s"(su_p), "+s"(wkv), "+s"(csm), "+s"(l));
  for (;;) {
    {
      REFRESH();
      asm volatile("" : "+s"(kvp), "+s"(zp), "+s"(fkvp), "+s"(fwl), "+s"(rb));
      STAMP(2);
      sfor<64, 128>([&](auto s) { step(s, std::false_type{}); });
      STAMP(19);
      mdone(acc[3]);
      v_out(IC(1), IC(0)); v_out(IC(1), IC(1));
      lds_bar();
      {
        const int hd = w >> 1, fh = w & 1;
        f32x16 kv, z;
        const int ch = fh * 4 + 2 * blk + (p4 >> 1), r0 = 4 * h + q4;
        LAS const char* pa = pk_img + hd * 16384 + r0 * 128 + ((ch ^ r0) << 4) + 8 * (p4 & 1);
        LAS const char* pb = v_img + hd * 8192 + lane * 16;
        bf16x8 a[8], b[8], d[8];
#pragma unroll
        for (int s = 0; s < 8; ++s) { a[s] = cat8(trread(pa + s * 2048), trread(pa + s * 2048 + 1024)); b[s] = *(LAS const bf16x8*)(pb + s * 1024); }
#pragma unroll
        for (int s = 0; s < 8; ++s) d[s] = *(LAS const bf16x8*)(sh + 8704 + s * 32 + h * 16);
        SBAR();
        sfor<0, 8>([&](auto s) { mm<false, false, s == 0>(kv, a[s], b[s]); });
        sfor<0, 8>([&](auto s) {
          mm<false, false, s == 0>(z, d[s], a[s]);
          if constexpr (s == 1) { mdone(kv); st4_1<0>(__builtin_bit_cast(u32x4, pack8<0>(kv)), c.xv, kvp + rb * 2048); }
          if constexpr (s == 2) { st4_1<1024>(__builtin_bit_cast(u32x4, pack8<1>(kv)), c.xv, kvp + rb * 2048); waitv<SQ.cw>(); }
          SBAR();
        });
        mdone(z);
        st1_1(z[0], (unsigned)(r * 4), zp + rb * 128);
      }
      STAMP(3);
      sfor<128, 192>([&](auto s) { step(s, std::false_type{}); });
      mdone(acc[4], acc[5]);
      STAMP(4);
      {
        bf16x8 pq[2][4], ka[8], za[8];
        f32x16 ep, en, nm[4], dn[2];
        float ai[2], bi[2];
        char* dst = acto + STG;
        sfor<0, 4>([&](auto k) { mm<false, false, k % 2 == 0, true>(p[k / 2], wf[k], pack8<k % 2>(acc[4 + k / 2])); });
        mdone(p[0], p[1]);
        hedge_exp(p[0], sce, ep, en, [&](auto i) { sum_mm(IC(16 + i)); });
        pq[0][0] = pack8<0>(ep); pq[0][1] = pack8<1>(ep); pq[0][2] = pack8<0>(en); pq[0][3] = pack8<1>(en);
        pin4(pq[0]);
        asm volatile("" : "+v"(kvs));
        *(LAS bf16x8*)(sh + w * 2048 + lane * 16) = pack8<0>(kvs); *(LAS bf16x8*)(sh + w * 2048 + 1024 + lane * 16) = pack8<1>(kvs);
        lds_bar();
        STAMP(5);
        LAS const char* zb = sh + 8192 + (r & 1) * 256 + h * 32;
#pragma unroll
        for (int k = 0; k < 8; ++k) { za[k] = *(LAS const bf16x8*)(zb + (k >> 1) * 64 + (k & 1) * 16); ka[k] = *(LAS const bf16x8*)(sh + k * 1024 + lane * 16); }
        auto coef = [&](auto hd) {
          const float a = __builtin_amdgcn_rcpf(dn[0][1]), b = __builtin_amdgcn_rcpf(dn[1][1]);
          const float inv = __builtin_amdgcn_rcpf(dn[0][0] * a + dn[1][0] * b + 1e-6f);
          ai[hd] = opq_v(a * inv); bi[hd] = opq_v(b * inv);
        };
        auto fin = [&](auto hd) {
          f32x16 o;
#pragma unroll
          for (int i = 0; i < 16; ++i) o[i] = nm[2 * hd][i] * ai[hd] + nm[2 * hd + 1][i] * bi[hd];
          st4_1<hd * 2048>(__builtin_bit_cast(u32x4, pack8<0>(o)), c.xv, dst);
          st4_1<hd * 2048 + 1024>(__builtin_bit_cast(u32x4, pack8<1>(o)), c.xv, dst);
        };
        hedge_exp(p[1], sce, ep, en, [&](auto i) {
          if constexpr (i < 4) mm<false, false, i % 2 == 0>(dn[i / 2], za[i], pq[0][i]);
          else if constexpr (i < 8) mm<false, false, i % 2 == 0>(nm[i / 2 - 2], ka[i - 4], pq[0][i - 4]);
          if constexpr (i % 2) pc_a(IC(108 + i / 2));
        });
        pq[1][0] = pack8<0>(ep); pq[1][1] = pack8<1>(ep); pq[1][2] = pack8<0>(en); pq[1][3] = pack8<1>(en);
        pin4(pq[1]); pin2(dn[0], dn[1]); pin2(nm[0], nm[1]);
        coef(IC(0));
        STAMP(6);
        sfor<0, 4>([&](auto k) { mm<false, false, k % 2 == 0, k == 0>(dn[k / 2], za[4 + k], pq[1][k]); if constexpr (k % 2) pc_a(IC(116 + k / 2)); });
        sfor<0, 4>([&](auto k) { mm<false, false, k % 2 == 0>(nm[2 + k / 2], ka[4 + k], pq[1][k]); if constexpr (k % 2) pc_a(IC(118 + k / 2)); });
        fin(IC(0));
        pin2(dn[0], dn[1]); pin2(nm[2], nm[3]);
        coef(IC(1));
        fin(IC(1));
        SBAR();
        sfor<120, 124>(pc_a);
        res_to_acc(xr, ra0, ra1);
        waitv<4>();
        set_flag(facto + FL_K, tag);
        sfor<124, 128>(pc_a); sfor<8, 16>(ld_park);
      }
    }
    STAMP(7);
    REFRESH();
    LAS char* reg = smem + 8 * SLOT;
    bf16x8 fu[16], cu;
    {
      f32x16 &a0 = ra0, &a1 = ra1;
      poll<16>(fact + FL_K, lane * 8, tag);
      sfor<0, 16>([&](auto i) { pin_a(park[i]); });
      STAMP(8);
      bf16x8 x[64];
      const char* xb = act + STG;
      sfor<0, XL>([&](auto s) { ld_x<s>(x, c, xb); });
      bar();
      asm volatile("" : "+s"(wkv));
      const unsigned kvv = (unsigned)((lane >> 1) * 64 + (cb & 1) * 32 + (lane & 1) * 16);
      const unsigned wa = ldsw + c.xv, wb = wa + 65536;
      auto unpark = [&](auto i) {
        constexpr int off = a_slot(26 + i / 4) * SLOT + (i % 4) * 1024;
        ds_write_a<(off & 65535)>(off < 65536 ? wa : wb, park[i]);
      };
      gemm64<true, false, HookP3>(c, x, xb, a0, a1, [&](auto s) {
        if constexpr (s == 33) bar();
        if constexpr (s >= 36 && s < 44) unpark(IC(s - 36));
        if constexpr (s == 49) bar();
        if constexpr (s >= 50 && s < 58) unpark(IC(s - 42));
        if constexpr (s >= 52 && s < 60) dma<0>(lds0 + 8 * SLOT + w * 8192 + (s - 52) * 1024, kvv, wkv + (s - 52) * 32 * 64);
        if constexpr (s >= 60) pc_w(4, a_slot(24), 0, IC(s - 60));
      }, [](auto) {});
      STAMP(9);
      store_x(c, r, a0, a1, xr, acto + 2 * STG, stto + 256 * 1024);
      asm volatile("s_waitcnt vmcnt(5) lgkmcnt(0)\n\ts_barrier" ::: "memory");
      { LAS const char* p = reg + w * 8192 + (4 * h + q4) * 32 + p4 * 8;
#pragma unroll
        for (int i = 0; i < 16; ++i) fu[i] = cat8(trread(p + i * 512), trread(p + i * 512 + 256));
        SBAR();
        float t0 = 0.f, t1 = 0.f, t2 = 0.f, t3 = 0.f;
#pragma unroll
        for (int i = 0; i < 16; ++i) {
          const u32x4 f = __builtin_bit_cast(u32x4, fu[i]);
          t0 = dot2(f[0], 0x3f803f80u, t0); t1 = dot2(f[1], 0x3f803f80u, t1); t2 = dot2(f[2], 0x3f803f80u, t2); t3 = dot2(f[3], 0x3f803f80u, t3);
        }
        cu = opq_v(cs_frag(swap_add((t0 + t1) + (t2 + t3)))); }
      waitv<0>();
      set_flag(facto + 2 * FL_K, tag);
      auto pc_q = [&](auto i) { pc_w(4, a_slot(24 + i / 4), i / 4, IC(i % 4)); };
      sfor<4, 8>(pc_q); sfor<24, 32>(pc_q);
    }
    STAMP(10);
    REFRESH();
    f32x16 a0, a1;
    asm volatile("" : "+s"(kvf), "+s"(fkv), "+s"(xu_p), "+s"(su_p));
    {
      poll<16>(fact + 2 * FL_K, lane * 8, tag);
      bar();
      STAMP(11);
      const char* xb = act + 2 * STG; const char* stp = stt + 256 * 1024;
      bf16x8 x[64], xu[16], mf0, mf1;
      Red<false> su, sp;
      sfor<0, 4>([&](auto i) { ldx1<false, i * 1024>(su.p[i], c.xv, su_p); });
      sfor<0, 16>([&](auto i) { ldx1<true, (i % 8) * 1024 - 4096>(xu[i], c.xv, xu_p + (i / 8) * 8192); });
      f32x16 au, av;
      sfor<0, 16>([&](auto i) {
        waitx<true, 15 - i + (i < XL ? i : XL)>(xu[i]);
        SBAR();
        mm<false, true, i == 0>(au, fu[i], xu[i]);
        if constexpr (i < XL) ld_x<i>(x, c, xb);
        mm<true, true, i == 0>(av, fu[i], xu[i]);
        if constexpr (i < 11) su.template run<i>(lane);
        if constexpr (i == 12) mf0 = opq_v(mean_frag(su.a, h == 0));
        if constexpr (i == 14) { mm<false, false, false>(au, cu, mf0); mm<true, false, false>(av, cu, mf0); }
        SBAR();
      });
      STAMP(12);
      u64 fa;
      f32x4 t[4], ru;
      unsigned vo;
      gemm64<true, true, HookP4>(c, x, xb, a0, a1, [&](auto s) {
        if constexpr (s == 1) {
          pin2(au, av);
          auto put = [&](const f32x16& a) {
#pragma unroll
            for (int gq = 0; gq < 4; ++gq) *(LAS f32x4*)(reg + w * 8192 + gq * 1024 + lane * 16) = f32x4{a[4 * gq], a[4 * gq + 1], a[4 * gq + 2], a[4 * gq + 3]};
          };
          if (ty < 2) { put(au); asm volatile("" ::: "memory"); } else put(av);
        }
        if constexpr (s == 3) {
          lds_bar();
#pragma unroll
          for (int j = 0; j < 4; ++j) t[j] = *(LAS const f32x4*)(reg + j * 8192 + (ty < 2 ? w & 1 : w) * 1024 + lane * 16);
        }
        if constexpr (s == 6) st2_1(__builtin_bit_cast(f32x2, u32x2{cvtpk(t[0][0], t[0][1]), cvtpk(t[0][2], t[0][3])}), vo, kvf);
        if constexpr (s >= 8 && s < 12) ldx1<false, (s - 8) * 1024>(sp.p[s - 8], c.xv, stp);
        if constexpr (s == 7 + XL) { bar(); if (w == 0) set_flag(fkv + rb * 4 * FL_I + cb * 8, tag); }
        if constexpr (s == 52) ld8_1(fa, (lane & 15) * 8 + (w * 4 + (lane >> 4)) * 4 * FL_I, fkv);
      }, [&](auto g) {
        if constexpr (g == 0) {
#pragma unroll
          for (int e = 0; e < 4; ++e) ru[e] = lane_get(su.b, ty < 2 ? r : 8 * w + 4 * h + e);
        }
        if constexpr (g == 4) vo = opq_v(ut * 4096 + (ty < 2 ? (unsigned)(ty * 1024 + lane * 16 + (w & 1) * 8)
                                                               : (unsigned)(2048 + (w >> 1) * 1024 + ((ty & 1) * 16 + (r & 15) + 32 * (w & 1)) * 16 + h * 8)));
        if constexpr (g >= 9 && g < 13) {
#pragma unroll
          for (int e = 0; e < 4; ++e) { float v = t[0][e]; if constexpr (g < 12) a_add(v, t[g - 8][e]); else a_mul(v, ru[e]); t[0][e] = v; }
        }
        if constexpr (g >= 48 && g < 59) sp.template run<g - 48>(lane);
        if constexpr (g == 60) mf0 = opq_v(mean_frag(sp.a, h == 0));
        if constexpr (g == 61) mf1 = opq_v(mean_frag(sp.a, h == 1));
        if constexpr (g == 64) mm<false, false, false>(a0, cw.p[0], mf0);
        if constexpr (g == 65) mm<false, false, false>(a1, cw.p[0], mf1);
      });
      rstd = sp.b;
      waitx<false, 0>(fa);
#pragma unroll 1
      for (int spin = 0; spin < (1 << 15) && !__all(fa == tag); ++spin) { __builtin_amdgcn_s_sleep(8); ld8_1(fa, (lane & 15) * 8 + (w * 4 + (lane >> 4)) * 4 * FL_I, fkv); waitx<false, 0>(fa); }
      bar();
      STAMP(13);
    }
    STAMP(14);
    {
      u32x16 qq, misc;
      misc[14] = misc[15] = __float_as_uint(rstd * QSCALE);
#pragma unroll 1
      for (int attempt = 0; attempt < 2; ++attempt) {
        if (attempt == 1) {
          const bf16x8 qa0 = __builtin_bit_cast(bf16x8, __builtin_shufflevector(qq, qq, 0, 1, 2, 3)), qa1 = __builtin_bit_cast(bf16x8, __builtin_shufflevector(qq, qq, 4, 5, 6, 7));
          const bf16x8 qb0 = __builtin_bit_cast(bf16x8, __builtin_shufflevector(qq, qq, 8, 9, 10, 11)), qb1 = __builtin_bit_cast(bf16x8, __builtin_shufflevector(qq, qq, 12, 13, 14, 15));
          float mA = -INFINITY, mB = -INFINITY;
          for (int t = 0; t < 64; ++t) {
            bf16x8 k0, k1;
            ldx1<false, 0>(k0, c.xv, kvf + t * 4096); ldx1<false, 1024>(k1, c.xv, kvf + t * 4096);
            wait2<0>(k0, k1);
            mm<false, false, true>(a0, k0, qa0); mm<false, false, false>(a0, k1, qa1); mm<false, false, true>(a1, k0, qb0); mm<false, false, false>(a1, k1, qb1);
            mdone(a0, a1);
            mA = fmaxf(mA, max16(a0)); mB = fmaxf(mB, max16(a1));
          }
          misc[14] = __float_as_uint(swap_max(mA)); misc[15] = __float_as_uint(swap_max(mB));
        }
        f32x16 oa, ob, t0, t1, t2, t3;
        u32x16 kvr; u32x4 offs;
        offs[0] = c.xv;
        const char* ks = kvf + 2048 + w * 2048; const char* wsr = wt(5, 0);
        unsigned wm0 = ldsw, cnt = 8;
        asm volatile(
            "global_load_dwordx4 v[208:211], v240, %[kv] sc1\n\tglobal_load_dwordx4 v[212:215], v240, %[kv] offset:1024 sc1\n\t"
            "s_mov_b32 m0, s44\n\tv_add_u32 v241, 0x2000, v240\n\t"
            "global_load_lds_dwordx4 v240, s[40:41] sc1\n\tglobal_load_lds_dwordx4 v240, s[40:41] offset:1024 sc1\n\t"
            "s_add_u32 m0, s44, 0x2000\n\tv_add_u32 v242, 0x4000, v240\n\t"
            "global_load_lds_dwordx4 v241, s[40:41] sc1\n\tglobal_load_lds_dwordx4 v241, s[40:41] offset:1024 sc1\n\t"
            "s_add_u32 m0, s44, 0x4000\n\tv_add_u32 v243, 0x6000, v240\n\t"
            "global_load_lds_dwordx4 v242, s[40:41] sc1\n\tglobal_load_lds_dwordx4 v242, s[40:41] offset:1024 sc1\n\t"
            "s_add_u32 s40, s40, 0x6000\n\ts_addc_u32 s41, s41, 0\n\t"
            "s_cmp_lg_u32 %[att], 0\n\ts_cbranch_scc1 2f\n\t"
            "v_pk_mul_f32 v[128:129], v[128:129], v[238:239]\n\t"
            "v_pk_mul_f32 v[130:131], v[130:131], v[238:239]\n\t"
            "v_pk_mul_f32 v[132:133], v[132:133], v[238:239]\n\t"
            "v_pk_mul_f32 v[134:135], v[134:135], v[238:239]\n\t"
            "v_pk_mul_f32 v[136:137], v[136:137], v[238:239]\n\t"
            "v_pk_mul_f32 v[138:139], v[138:139], v[238:239]\n\t"
            "v_pk_mul_f32 v[140:141], v[140:141], v[238:239]\n\t"
            "v_pk_mul_f32 v[142:143], v[142:143], v[238:239]\n\t"
            "v_pk_mul_f32 v[144:145], v[144:145], v[238:239]\n\t"
            "v_pk_mul_f32 v[146:147], v[146:147], v[238:239]\n\t"
            "v_pk_mul_f32 v[148:149], v[148:149], v[238:239]\n\t"
            "v_pk_mul_f32 v[150:151], v[150:151], v[238:239]\n\t"
            "v_pk_mul_f32 v[152:153], v[152:153], v[238:239]\n\t"
            "v_pk_mul_f32 v[154:155], v[154:155], v[238:239]\n\t"
            "v_pk_mul_f32 v[156:157], v[156:157], v[238:239]\n\t"
            "v_pk_mul_f32 v[158:159], v[158:159], v[238:239]\n\t"
            "v_cvt_pk_bf16_f32 v192, v128, v129\n\t"
            "v_cvt_pk_bf16_f32 v193, v130, v131\n\t"
            "v_cvt_pk_bf16_f32 v194, v132, v133\n\t"
            "v_cvt_pk_bf16_f32 v195, v134, v135\n\t"
            "v_cvt_pk_bf16_f32 v196, v136, v137\n\t"
            "v_cvt_pk_bf16_f32 v197, v138, v139\n\t"
            "v_cvt_pk_bf16_f32 v198, v140, v141\n\t"
            "v_cvt_pk_bf16_f32 v199, v142, v143\n\t"
            "v_cvt_pk_bf16_f32 v200, v144, v145\n\t"
            "v_cvt_pk_bf16_f32 v201, v146, v147\n\t"
            "v_cvt_pk_bf16_f32 v202, v148, v149\n\t"
            "v_cvt_pk_bf16_f32 v203, v150, v151\n\t"
            "v_cvt_pk_bf16_f32 v204, v152, v153\n\t"
            "v_cvt_pk_bf16_f32 v205, v154, v155\n\t"
            "v_cvt_pk_bf16_f32 v206, v156, v157\n\t"
            "v_cvt_pk_bf16_f32 v207, v158, v159\n\t"
            "2:\n\t"
            "v_mov_b64 v[64:65], 0\n\t"
            "v_mov_b64 v[66:67], 0\n\t"
            "v_mov_b64 v[68:69], 0\n\t"
            "v_mov_b64 v[70:71], 0\n\t"
            "v_mov_b64 v[72:73], 0\n\t"
            "v_mov_b64 v[74:75], 0\n\t"
            "v_mov_b64 v[76:77], 0\n\t"
            "v_mov_b64 v[78:79], 0\n\t"
            "v_mov_b64 v[80:81], 0\n\t"
            "v_mov_b64 v[82:83], 0\n\t"
            "v_mov_b64 v[84:85], 0\n\t"
            "v_mov_b64 v[86:87], 0\n\t"
            "v_mov_b64 v[88:89], 0\n\t"
            "v_mov_b64 v[90:91], 0\n\t"
            "v_mov_b64 v[92:93], 0\n\t"
            "v_mov_b64 v[94:95], 0\n\t"
            "v_mov_b64 v[176:177], 0\n\t"
            "v_mov_b64 v[178:179], 0\n\t"
            "v_mov_b64 v[180:181], 0\n\t"
            "v_mov_b64 v[182:183], 0\n\t"
            "v_mov_b64 v[184:185], 0\n\t"
            "v_mov_b64 v[186:187], 0\n\t"
            "v_mov_b64 v[188:189], 0\n\t"
            "v_mov_b64 v[190:191], 0\n\t"
            "v_mov_b64 v[216:217], 0\n\t"
            "v_mov_b64 v[218:219], 0\n\t"
            "v_mov_b64 v[220:221], 0\n\t"
            "v_mov_b64 v[222:223], 0\n\t"
            "v_mov_b64 v[228:229], 0\n\t"
            "v_mov_b64 v[230:231], 0\n\t"
            "v_mov_b64 v[232:233], 0\n\t"
            "v_mov_b64 v[234:235], 0\n\t"
            "v_bfe_u32 v226, v240, 6, 1\n\tv_mov_b32 v236, 0xdddddddd\n\tv_mul_u32_u24 v226, 0x3f80, v226\n\tv_add_u32 v237, %[rg], v240\n\t"
            "v_lshl_or_b32 v226, v226, 16, v226\n\tv_mov_b32 v227, v226\n\tv_xor_b32 v224, 0x3f803f80, v226\n\tv_mov_b32 v225, v224\n\t"
            "s_waitcnt vmcnt(6)\n\ts_cbranch_scc1 3f\n\t"
            "v_mfma_f32_32x32x16_bf16 v[128:143], v[208:211], v[192:195], 0\n\tv_mfma_f32_32x32x16_bf16 v[160:175], v[208:211], v[200:203], 0\n\t"
            "v_mfma_f32_32x32x16_bf16 v[128:143], v[212:215], v[196:199], v[128:143]\n\tv_mfma_f32_32x32x16_bf16 v[160:175], v[212:215], v[204:207], v[160:175]\n\t"
            "s_nop 11\n\t"
            "v_max3_f32 v238, v128, v129, v130\n\tv_max3_f32 v239, v160, v161, v162\n\t"
            "v_max3_f32 v238, v238, v131, v132\n\tv_max3_f32 v239, v239, v163, v164\n\t"
            "v_max3_f32 v238, v238, v133, v134\n\tv_max3_f32 v239, v239, v165, v166\n\t"
            "v_max3_f32 v238, v238, v135, v136\n\tv_max3_f32 v239, v239, v167, v168\n\t"
            "v_max3_f32 v238, v238, v137, v138\n\tv_max3_f32 v239, v239, v169, v170\n\t"
            "v_max3_f32 v238, v238, v139, v140\n\tv_max3_f32 v239, v239, v171, v172\n\t"
            "v_max3_f32 v238, v238, v141, v142\n\tv_max3_f32 v239, v239, v173, v174\n\t"
            "v_max3_f32 v238, v238, v143, v143\n\tv_max3_f32 v239, v239, v175, v175\n\t"
            "v_mov_b32 v96, v238\n\tv_mov_b32 v97, v239\n\ts_nop 1\n\tv_permlane32_swap_b32 v238, v96\n\tv_permlane32_swap_b32 v239, v97\n\t"
            "v_max_f32 v238, v238, v96\n\tv_max_f32 v239, v239, v97\n\t"
            "3:\n\t"
            "v_xor_b32 v96, 0x80000000, v238\n\tv_xor_b32 v112, 0x80000000, v239\n\tv_mov_b32 v97, v96\n\tv_mov_b32 v113, v112\n\t"
            "v_mov_b64 v[98:99], v[96:97]\n\tv_mov_b64 v[114:115], v[112:113]\n\t"
            "v_mov_b64 v[100:101], v[96:97]\n\tv_mov_b64 v[116:117], v[112:113]\n\t"
            "v_mov_b64 v[102:103], v[96:97]\n\tv_mov_b64 v[118:119], v[112:113]\n\t"
            "v_mov_b64 v[104:105], v[96:97]\n\tv_mov_b64 v[120:121], v[112:113]\n\t"
            "v_mov_b64 v[106:107], v[96:97]\n\tv_mov_b64 v[122:123], v[112:113]\n\t"
            "v_mov_b64 v[108:109], v[96:97]\n\tv_mov_b64 v[124:125], v[112:113]\n\t"
            "v_mov_b64 v[110:111], v[96:97]\n\tv_mov_b64 v[126:127], v[112:113]\n\t"
            "s_nop 1\n\t" S0_(128, 192, 96) S1_(128, 192)
            "s_waitcnt vmcnt(4)\n\ts_barrier\n\ts_nop 8\n\t"
            "1:\n\t"
            STEP_(0, 24576, 240, 0, "s_sub_u32 %[cnt], %[cnt], 1\n\t", "") STEP_(8192, 0, 241, 1024, "", "") STEP_(16384, 8192, 242, 2048, "", "")
            STEP_(24576, 16384, 243, 3072, "s_add_u32 s40, s40, 0x8000\n\ts_addc_u32 s41, s41, 0\n\t", "s_add_u32 s42, s42, 0x4000\n\ts_addc_u32 s43, s43, 0\n\t")
            "s_add_u32 s45, s45, 0x4000\n\ts_cmp_lg_u32 %[cnt], 0\n\ts_cbranch_scc1 1b\n\t"
            L0_(232, 176) L1_(232, 176) PV0_(80, 176) PV1_(80, 176)
            "s_nop 11\n\t"
            : "=&{v[64:79]}"(oa), "=&{v[80:95]}"(ob), "=&{v[96:111]}"(t0), "=&{v[112:127]}"(t1), "+{v[128:143]}"(a0), "+{v[144:159]}"(a1), "=&{v[160:175]}"(t2),
              "=&{v[176:191]}"(t3), "+{v[192:207]}"(qq), "=&{v[208:223]}"(kvr), "+{v[224:239]}"(misc), "+{v[240:243]}"(offs),
              "+{s[40:41]}"(ks), "+{s[42:43]}"(wsr), "+{s45}"(wm0), [cnt] "+s"(cnt)
            : "{s44}"(lds0 + 8 * SLOT + w * 2048), [kv] "s"(kvf), [att] "s"(attempt), [rg] "s"(lds0 + 8 * SLOT)
            : "scc", "memory");
        const float la = __uint_as_float(misc[4]), lb = __uint_as_float(misc[8]);
        const float ia = __builtin_amdgcn_rcpf(la), ib = __builtin_amdgcn_rcpf(lb);
#pragma unroll
        for (int e = 0; e < 16; ++e) { oa[e] *= ia; ob[e] *= ib; }
        char* dst = acto + 3 * STG;
        st4_1<0>(__builtin_bit_cast(u32x4, pack8<0>(oa)), c.xv, dst); st4_1<1024>(__builtin_bit_cast(u32x4, pack8<1>(oa)), c.xv, dst);
        st4_1<2048>(__builtin_bit_cast(u32x4, pack8<0>(ob)), c.xv, dst); st4_1<3072>(__builtin_bit_cast(u32x4, pack8<1>(ob)), c.xv, dst);
        float chk = 0.f;
#pragma unroll
        for (int e = 0; e < 16; ++e) chk = __builtin_fmaf(oa[e], 0.f, __builtin_fmaf(ob[e], 0.f, chk));
        const bool bad = !(la < 1.2676506e30f) || !(lb < 1.2676506e30f) || !(chk == 0.f);
        wait_bar<4>();
        LAS volatile int* vote = (LAS volatile int*)(smem + 8 * SLOT);
        if (lane == 0) vote[w] = 0;
        if (attempt == 0 && bad) vote[w] = 1;
        lds_bar();
        const int redo = vote[0] | vote[1] | vote[2] | vote[3];
        if (!__builtin_amdgcn_readfirstlane(redo)) break;
        lds_bar();
      }
      res_to_acc(xr, ra0, ra1);
      waitv<0>();
      set_flag(facto + 3 * FL_K, tag);
    }
    STAMP(15);
    REFRESH();
    {
      f32x16 &a0 = ra0, &a1 = ra1;
      poll<16>(fact + 3 * FL_K, lane * 8, tag);
      STAMP(16);
      bf16x8 x[64];
      const char* xb = act + 3 * STG;
      sfor<0, XL>([&](auto s) { ld_x<s>(x, c, xb); });
      bar();
      gemm64<false, false, HookP6>(c, x, xb, a0, a1, [&](auto s) {
        if constexpr (s == 33) bar();
        if constexpr (s >= 52) pc_a(IC(a_pre(s - 52)));
      }, [](auto) {});
      STAMP(17);
      if (l == 0) {
        store_x(c, r, a0, a1, xr, acto + 4 * STG, stto + 512 * 1024);
        bar();
        sfor<12, 16>([&](auto n) { pc_a(IC(a_pre(n))); });
        waitv<4>();
        set_flag(facto + 4 * FL_K, tag);
        sfor<16, 24>([&](auto n) { pc_a(IC(a_pre(n))); });
      } else {
        char* dst = out + (size_t)(tt * 32 + r) * 2048 + cb * 128 + h * 16;
        *(u32x4*)(dst) = unperm(__builtin_bit_cast(u32x4, pack8<0>(a0))); *(u32x4*)(dst + 32) = unperm(__builtin_bit_cast(u32x4, pack8<1>(a0)));
        *(u32x4*)(dst + 64) = unperm(__builtin_bit_cast(u32x4, pack8<0>(a1))); *(u32x4*)(dst + 96) = unperm(__builtin_bit_cast(u32x4, pack8<1>(a1)));
        if (tt == 0 && cb == 0) set_flag(ws + WS_EPOCH, tag);
      }
    }
    STAMP(18);
    if (l == 1) break;
    csm = 1;
    act += 4 * STG; acto += 4 * STG; fact += 4 * FL_K; facto += 4 * FL_K; stt += 512 * KB; stto += 512 * KB;
    kvp += 131072; zp += 8192; fkvp += FL_K; kvf += 320 * KB; fkv += FL_K; xu_p += 4 * STG; su_p += 512 * KB;
    l = 1;
    REFRESH();
    STAMP(0);
        poll<16>(fact, lane * 8, tag);
        STAMP(1);
        sfor<0, XL>(issue_x);
        wait_bar<XL>();
        sfor<0, 2>([&](auto s) { rd01(s, IC(0)); rd01(s, IC(1)); rd2(s); });
        sfor<0, 64>([&](auto s) { step(s, std::false_type{}); });
  }
}

extern "C" void kernel_launch(void* const* d_in, const int* in_sizes, int n_in, void* d_out, int out_size, void* d_ws, size_t ws_size, hipStream_t stream) {
  k_main<<<256, 256, 0, stream>>>((const char*)d_in[0], (const char*)d_in[1], (const char*)d_in[2], (const char*)d_in[3], (const char*)d_in[5], (const char*)d_in[6],
                                  (const char*)d_in[7], (const char*)d_in[8], (char*)d_ws, (char*)d_out);
}
```

```cpp
#include <hip/hip_runtime.h>
#include <hip/hip_bf16.h>
#include <stdint.h>
#include <type_traits>

typedef __attribute__((ext_vector_type(8))) short bf16x8;
typedef __attribute__((ext_vector_type(4))) short s16x4;
typedef __attribute__((ext_vector_type(2))) __bf16 bf2_t;
typedef __attribute__((ext_vector_type(16))) float f32x16;
typedef __attribute__((ext_vector_type(4))) float f32x4;
typedef __attribute__((ext_vector_type(2))) float f32x2;
typedef __attribute__((ext_vector_type(4))) unsigned u32x4;
typedef __attribute__((ext_vector_type(2))) unsigned u32x2;
#define LAS __attribute__((address_space(3)))
#define DI __device__ __forceinline__
#define MFMA32(a, b, c) __builtin_amdgcn_mfma_f32_32x32x16_bf16((a), (b), (c), 0, 0, 0)
#define MFMA16(a, b, c) __builtin_amdgcn_mfma_f32_16x16x32_bf16((a), (b), (c), 0, 0, 0)
#define SBAR() __builtin_amdgcn_sched_barrier(0)

constexpr int S_ = 2048, D_ = 1024;
constexpr float LOG2E = 1.4426950408889634f;
constexpr float QSCALE = 0.17677669529663687f * LOG2E;

constexpr size_t MB = 1u << 20, KB = 1024;
constexpr size_t STG = 256 * KB;
constexpr size_t WS_COL = 32 * MB;
constexpr size_t C_WT = 0;
constexpr size_t C_KVP = 768 * KB;
constexpr size_t C_ZP = 1024 * KB;
constexpr size_t C_CS = 1040 * KB;
constexpr size_t WS_ST = 64 * MB;
constexpr size_t WS_KVF = WS_ST + 1024 * KB;
constexpr size_t WS_FLAG = WS_ST + 1664 * KB;
constexpr size_t WS_EPOCH = WS_ST + 1920 * KB;
constexpr int FL_I = 2048, FL_K = 128;
typedef unsigned long long u64;

template <int I, int N, class F> DI void sfor(F&& f) { if constexpr (I < N) { f(std::integral_constant<int, I>{}); sfor<I + 1, N>(f); } }

DI unsigned cvtpk(float lo, float hi) { f32x2 v = {lo, hi}; return __builtin_bit_cast(unsigned, __builtin_convertvector(v, bf2_t)); }
DI float bflo(unsigned u) { return __uint_as_float(u << 16); }
DI float bfhi(unsigned u) { return __uint_as_float(u & 0xffff0000u); }
DI float rbf(float x) { return bflo(cvtpk(x, x)); }
DI float swap_add(float v) { auto r = __builtin_amdgcn_permlane32_swap(__float_as_uint(v), __float_as_uint(v), false, false); return __uint_as_float(r[0]) + __uint_as_float(r[1]); }
DI float swap_max(float v) { auto r = __builtin_amdgcn_permlane32_swap(__float_as_uint(v), __float_as_uint(v), false, false); return fmaxf(__uint_as_float(r[0]), __uint_as_float(r[1])); }
DI float swap_min(float v) { auto r = __builtin_amdgcn_permlane32_swap(__float_as_uint(v), __float_as_uint(v), false, false); return fminf(__uint_as_float(r[0]), __uint_as_float(r[1])); }
template <int S> DI bf16x8 pack8(const f32x16& v) {
  u32x4 p = {cvtpk(v[8 * S], v[8 * S + 1]), cvtpk(v[8 * S + 2], v[8 * S + 3]), cvtpk(v[8 * S + 4], v[8 * S + 5]), cvtpk(v[8 * S + 6], v[8 * S + 7])};
  return __builtin_bit_cast(bf16x8, p);
}
DI u32x4 unperm(u32x4 v) {
  auto r0 = __builtin_amdgcn_permlane32_swap(v[0], v[2], false, false);
  auto r1 = __builtin_amdgcn_permlane32_swap(v[1], v[3], false, false);
  return u32x4{r0[0], r1[0], r0[1], r1[1]};
}
DI float lane_get(float v, int src) { return __uint_as_float(__builtin_amdgcn_ds_bpermute(src << 2, __float_as_uint(v))); }
DI s16x4 trread(LAS const char* p) { return __builtin_amdgcn_ds_read_tr16_b64_v4i16((LAS s16x4*)p); }
DI bf16x8 cat8(s16x4 a, s16x4 b) { return __builtin_shufflevector(a, b, 0, 1, 2, 3, 4, 5, 6, 7); }
DI float dot2(unsigned a, unsigned b, float c) { return __builtin_amdgcn_fdot2_f32_bf16(__builtin_bit_cast(bf2_t, a), __builtin_bit_cast(bf2_t, b), c, false); }

template <int OFF> DI void dma(unsigned lds, unsigned v, const char* sb) {
  asm volatile("s_mov_b32 m0, %0\n\ts_nop 0\n\tglobal_load_lds_dwordx4 %1, %2 offset:%3" ::"s"(lds), "v"(v), "s"(sb), "i"(OFF) : "memory");
}
template <int OFF> DI void dma_c(unsigned lds, unsigned v, const char* sb) {
  asm volatile("s_mov_b32 m0, %0\n\ts_nop 0\n\tglobal_load_lds_dwordx4 %1, %2 offset:%3 sc1" ::"s"(lds), "v"(v), "s"(sb), "i"(OFF) : "memory");
}
template <int OFF> DI void dma_n(unsigned v, const char* sb) {
  asm volatile("global_load_lds_dwordx4 %0, %1 offset:%2 sc1" ::"v"(v), "s"(sb), "i"(OFF) : "memory");
}
constexpr int cap63(int n) { return n > 63 ? 63 : n; }
template <int N> DI void wait_bar() { asm volatile("s_waitcnt vmcnt(%0)\n\ts_barrier" ::"i"(cap63(N)) : "memory"); }
DI void lds_bar() { asm volatile("s_waitcnt lgkmcnt(0)\n\ts_barrier" ::: "memory"); }
DI void all_bar() { asm volatile("s_waitcnt vmcnt(0) lgkmcnt(0)\n\ts_barrier" ::: "memory"); }
DI void bar() { asm volatile("s_barrier" ::: "memory"); }
template <bool AG, int OFF> DI void ldx1(bf16x8& d, unsigned voff, const char* sb) {
  if constexpr (AG) asm volatile("global_load_dwordx4 %0, %1, %2 offset:%3 sc1" : "=a"(d) : "v"(voff), "s"(sb), "i"(OFF));
  else asm volatile("global_load_dwordx4 %0, %1, %2 offset:%3 sc1" : "=v"(d) : "v"(voff), "s"(sb), "i"(OFF));
}
template <int OFF> DI void ld2_1(f32x2& d, unsigned voff, const char* sb) { asm volatile("global_load_dwordx2 %0, %1, %2 offset:%3 sc1" : "=v"(d) : "v"(voff), "s"(sb), "i"(OFF)); }
DI void ld1_1(unsigned& d, unsigned voff, const void* sb) { asm volatile("global_load_dword %0, %1, %2 sc1" : "=v"(d) : "v"(voff), "s"(sb) : "memory"); }
template <int OFF> DI void st4_1(u32x4 d, unsigned voff, char* sb) { asm volatile("global_store_dwordx4 %0, %1, %2 offset:%3 sc1\n\ts_nop 1" ::"v"(voff), "v"(d), "s"(sb), "i"(OFF) : "memory"); }
DI void st2_1(f32x2 d, unsigned voff, char* sb) { asm volatile("global_store_dwordx2 %0, %1, %2 sc1" ::"v"(voff), "v"(d), "s"(sb) : "memory"); }
DI void st1_1(float d, unsigned voff, char* sb) { asm volatile("global_store_dword %0, %1, %2 sc1" ::"v"(voff), "v"(d), "s"(sb) : "memory"); }
template <int N> DI void waitv() { asm volatile("s_waitcnt vmcnt(%0)" ::"i"(cap63(N)) : "memory"); }
template <bool AG, int N, class T> DI void waitx(T& d) {
  if constexpr (AG) asm volatile("s_waitcnt vmcnt(%1)" : "+a"(d) : "i"(cap63(N))); else asm volatile("s_waitcnt vmcnt(%1)" : "+v"(d) : "i"(cap63(N)));
}
template <bool SW, bool BA, bool Z, bool NOP = false> DI void mm(f32x16& acc, const bf16x8& a, const bf16x8& b) {
#define MM_(OPS, C, OUT, BC) do { if constexpr (NOP) asm volatile("s_nop 1\n\tv_mfma_f32_32x32x16_bf16 %0, " OPS ", " C : OUT(acc) : "v"(a), BC(b)); \
                                  else asm volatile("v_mfma_f32_32x32x16_bf16 %0, " OPS ", " C : OUT(acc) : "v"(a), BC(b)); } while (0)
  if constexpr (Z) {
    if constexpr (SW) { if constexpr (BA) MM_("%2, %1", "0", "=&v", "a"); else MM_("%2, %1", "0", "=&v", "v"); }
    else { if constexpr (BA) MM_("%1, %2", "0", "=&v", "a"); else MM_("%1, %2", "0", "=&v", "v"); }
  } else {
    if constexpr (SW) { if constexpr (BA) MM_("%2, %1", "%0", "+v", "a"); else MM_("%2, %1", "%0", "+v", "v"); }
    else { if constexpr (BA) MM_("%1, %2", "%0", "+v", "a"); else MM_("%1, %2", "%0", "+v", "v"); }
  }
#undef MM_
}
DI void mdone(f32x16& a) { asm volatile("s_nop 11" : "+v"(a)); }
DI void mdone(f32x16& a, f32x16& b) { asm volatile("s_nop 11" : "+v"(a), "+v"(b)); }
DI void mdone(f32x16& a, f32x16& b, f32x16& c, f32x16& d) { asm volatile("s_nop 11" : "+v"(a), "+v"(b), "+v"(c), "+v"(d)); }
template <class T> DI T opq_v(T v) { asm volatile("" : "+v"(v)); return v; }
template <int OFF> DI void ldx0(bf16x8& d, unsigned voff, const char* sb) { asm volatile("global_load_dwordx4 %0, %1, %2 offset:%3" : "=v"(d) : "v"(voff), "s"(sb), "i"(OFF)); }
template <int OFF> DI void ldx1m(bf16x8& d, unsigned voff, const char* sb, unsigned long long m) {
  asm volatile("s_mov_b64 exec, %4\n\tglobal_load_dwordx4 %0, %1, %2 offset:%3 sc1\n\ts_mov_b64 exec, -1" : "=v"(d) : "v"(voff), "s"(sb), "i"(OFF), "s"(m));
}
template <bool AG> DI void ds_read2(bf16x8& d, unsigned addr) {
  if constexpr (AG) asm volatile("ds_read2_b64 %0, %1 offset1:2" : "=a"(d) : "v"(addr) : "memory"); else asm volatile("ds_read2_b64 %0, %1 offset1:2" : "=v"(d) : "v"(addr) : "memory");
}
template <bool AG> DI void waitl(bf16x8& d) { if constexpr (AG) asm volatile("s_waitcnt lgkmcnt(0)" : "+a"(d)::"memory"); else asm volatile("s_waitcnt lgkmcnt(0)" : "+v"(d)::"memory"); }
template <int OFF> DI void ds_write_a(unsigned addr, const bf16x8& v) { asm volatile("ds_write_b128 %0, %1 offset:%2" ::"v"(addr), "a"(v), "i"(OFF) : "memory"); }
template <int N, class T> DI void wait2(T& a, T& b) { asm volatile("s_waitcnt vmcnt(%2)" : "+v"(a), "+v"(b) : "i"(cap63(N))); }
DI void set_flag(char* flag, u64 tag) {
  unsigned z = 0;
  asm volatile("s_mov_b64 exec, 1\n\tglobal_atomic_swap_x2 %0, %1, %2 sc1\n\ts_mov_b64 exec, -1" ::"v"(z), "v"(tag), "s"(flag) : "memory");
}
DI void ld8_1(u64& d, unsigned voff, const char* sb) { asm volatile("global_load_dwordx2 %0, %1, %2 sc1" : "=v"(d) : "v"(voff), "s"(sb) : "memory"); }
template <int NL> DI void rd_flag(u64& v, unsigned voff, const char* sb) {
  const u64 zero = 0;
  if constexpr (NL == 64) asm volatile("global_atomic_or_x2 %0, %1, %3, %2 sc0 sc1" : "+v"(v) : "v"(voff), "s"(sb), "v"(zero) : "memory");
  else asm volatile("s_mov_b64 exec, %4\n\tglobal_atomic_or_x2 %0, %1, %3, %2 sc0 sc1\n\ts_mov_b64 exec, -1" : "+v"(v) : "v"(voff), "s"(sb), "v"(zero), "s"((1ull << (NL & 63)) - 1) : "memory");
}
template <int NL> DI void poll(const char* base, unsigned voff, u64 tag) {
  for (int spin = 0; spin < (1 << 15); ++spin) {
    u64 v = tag;
    rd_flag<NL>(v, voff, base);
    waitx<false, 0>(v);
    if (__all(v == tag)) break;
  }
  asm volatile("" ::: "memory");
}

template <int RBX> DI void block_map(int& rb, int& cb) {
  const int x = blockIdx.x & 7, i = blockIdx.x >> 3;
  constexpr int CBX = 32 / RBX, GC = 16 / CBX;
  rb = RBX * (x / GC) + i / CBX; cb = CBX * (x % GC) + i % CBX;
}

DI float a_fma(float a, float b, float c) { float d; asm volatile("v_fma_f32 %0, %1, %2, %3" : "=v"(d) : "v"(a), "v"(b), "v"(c)); return d; }
DI float a_fms(float a, float b, float c) { float d; asm volatile("v_fma_f32 %0, %1, -%2, %3" : "=v"(d) : "v"(a), "v"(b), "v"(c)); return d; }
DI void a_exp(float& x) { asm volatile("v_exp_f32 %0, %0" : "+v"(x)); }
DI void a_add(float& s, float x) { asm volatile("v_add_f32 %0, %0, %1" : "+v"(s) : "v"(x)); }
DI void a_mul(float& x, float s) { asm volatile("v_mul_f32 %0, %0, %1" : "+v"(x) : "v"(s)); }
DI void a_hedge(float& ca, float& cb, float& na, float& nb, float& sa, float& sb, float pa, float pb, float p, float sc, float mx, float mn) {
  asm volatile("v_exp_f32 %0, %0\n\tv_fma_f32 %2, %8, %9, %10\n\tv_add_f32 %4, %4, %6\n\tv_exp_f32 %1, %1\n\tv_fma_f32 %3, %8, -%9, %11\n\tv_add_f32 %5, %5, %7"
               : "+v"(ca), "+v"(cb), "=&v"(na), "=&v"(nb), "+v"(sa), "+v"(sb) : "v"(pa), "v"(pb), "v"(p), "v"(sc), "v"(mx), "v"(mn));
}
DI unsigned long long a_norm(float a, float b, float c, float d, float s) {
  u32x2 o; float t0, t1;
  asm volatile("v_mul_f32 %2, %4, %8\n\tv_mul_f32 %3, %5, %8\n\tv_mul_f32 %1, %6, %8\n\tv_cvt_pk_bf16_f32 %0, %2, %3\n\tv_mul_f32 %2, %7, %8\n\tv_cvt_pk_bf16_f32 %1, %1, %2"
               : "=&v"(o[0]), "=&v"(o[1]), "=&v"(t0), "=&v"(t1) : "v"(a), "v"(b), "v"(c), "v"(d), "v"(s));
  return __builtin_bit_cast(unsigned long long, o);
}
DI void a_mm4(float& mx, float& mn, float a, float b, float c, float d) {
  asm volatile("v_max3_f32 %0, %0, %2, %3\n\tv_min3_f32 %1, %1, %2, %3\n\tv_max3_f32 %0, %0, %4, %5\n\tv_min3_f32 %1, %1, %4, %5" : "+v"(mx), "+v"(mn) : "v"(a), "v"(b), "v"(c), "v"(d));
}
DI float a_max3(float a, float b, float c) { float d; asm volatile("v_max3_f32 %0, %1, %2, %3" : "=v"(d) : "v"(a), "v"(b), "v"(c)); return d; }
DI float a_min3(float a, float b, float c) { float d; asm volatile("v_min3_f32 %0, %1, %2, %3" : "=v"(d) : "v"(a), "v"(b), "v"(c)); return d; }

DI void pin_a(bf16x8& a) { asm volatile("" : "+a"(a)); }
DI void pin2(f32x16& a, f32x16& b) { asm volatile("" : "+v"(a), "+v"(b)); }
DI bf16x8 cs_frag(float c) {
  const float hi = rbf(c), mid = rbf(c - hi), lo = (c - hi) - mid;
  return __builtin_bit_cast(bf16x8, u32x4{cvtpk(hi, mid), cvtpk(lo, 0.f), 0u, 0u});
}
template <bool CS> struct Red {
  bf16x8 p[4];
  float t[4], a, b, sd;
  template <int K> DI void run(int lane) {
    if constexpr (K == 0) asm volatile("" : "+v"(p[0]), "+v"(p[1]), "+v"(p[2]), "+v"(p[3]));
    if constexpr (K < 3) {
      const f32x4 u = __builtin_bit_cast(f32x4, p[K + 1]), v = __builtin_bit_cast(f32x4, p[0]);
#pragma unroll
      for (int e = 0; e < 4; ++e) { if (K == 0) t[e] = v[e]; a_add(t[e], u[e]); }
    }
    if constexpr (K == 3) {
      auto x = __builtin_amdgcn_permlane16_swap(__float_as_uint(t[0]), __float_as_uint(t[1]), false, false);
      auto y = __builtin_amdgcn_permlane16_swap(__float_as_uint(t[2]), __float_as_uint(t[3]), false, false);
      a = opq_v(__uint_as_float(x[0]) + __uint_as_float(x[1])); b = opq_v(__uint_as_float(y[0]) + __uint_as_float(y[1]));
    }
    if constexpr (K == 4) { auto x = __builtin_amdgcn_permlane32_swap(__float_as_uint(a), __float_as_uint(b), false, false); a = opq_v(__uint_as_float(x[0]) + __uint_as_float(x[1])); }
    if constexpr (K == 5) {
      if constexpr (CS) a = lane_get(a, (lane >> 2) + ((lane & 3) << 4));
      else { const int src = ((lane & 31) >> 1) + ((lane & 1) << 5); b = lane_get(a, src + 16); a = lane_get(a, src); }
    }
    if constexpr (CS) {
      if constexpr (K == 8) { t[0] = opq_v(rbf(a)); t[1] = opq_v(a - t[0]); }
      if constexpr (K == 9) { const float mid = rbf(t[1]); p[0] = opq_v(__builtin_bit_cast(bf16x8, u32x4{cvtpk(t[0], mid), cvtpk(t[1] - mid, 0.f), 0u, 0u})); }
    } else {
      if constexpr (K == 8) { const float m = a * (1.f / D_); b = opq_v(fmaxf(b * (1.f / D_) - m * m, 0.f)); a = opq_v(rbf(m)); }
      if constexpr (K == 9) b = opq_v(rbf(rbf(b) + 1e-5f));
      if constexpr (K == 10) { sd = opq_v(rbf(__builtin_amdgcn_sqrtf(b))); b = opq_v(__builtin_amdgcn_rcpf(sd)); }
    }
  }
};
DI void pin4(bf16x8 (&p)[4]) { asm volatile("" : "+v"(p[0]), "+v"(p[1]), "+v"(p[2]), "+v"(p[3])); }
DI bf16x8 mean_frag(float mean, bool on) { const float m = on ? -mean : 0.f; return __builtin_bit_cast(bf16x8, u32x4{cvtpk(m, m), cvtpk(m, 0.f), 0u, 0u}); }

#define STAMP(i)

constexpr int CODE_KB = 50;
constexpr int SLOT = 16384, XL = 12;
struct WinPtr {
  unsigned b[3];
  DI void init(unsigned a) { sfor<0, 3>([&](auto i) { b[i] = a + i * 65536u; asm volatile("" : "+v"(b[i])); }); }
  template <int OFF> DI bf16x8 frag() const { return *(LAS const bf16x8*)((LAS const char*)(uintptr_t)b[OFF >> 16] + (OFF & 65535)); }
};
struct Ctx { unsigned xv; WinPtr p; };
#define IC(n) std::integral_constant<int, (n)>{}

template <int S> DI void ld_x(bf16x8 (&x)[64], const Ctx& c, const char* xb) { ldx1<false, (S % 8) * 1024 - 4096>(x[S], c.xv, xb + (S / 8) * 8192 + 4096); }
template <class H> constexpr int g64_wait(int s) {
  int n = 0;
  const int t0 = s - XL;
  if (t0 < 0) n += XL - 1 - s; else n += H::n(t0);
  for (int t = t0 < 0 ? 0 : t0 + 1; t < s; ++t) n += (t + XL < 64 ? 1 : 0) + H::n(t);
  return n;
}
constexpr int a_slot(int g);
template <bool MAPA, bool ZERO, class H, class FX, class FA>
DI void gemm64(const Ctx& c, bf16x8 (&x)[64], const char* xb, f32x16& acc0, f32x16& acc1, FX&& hook, FA&& aux) {
  bf16x8 f0[3], f1[3];
  auto off = [](int s) constexpr { return (MAPA ? a_slot(24 + s / 8) : s / 8) * SLOT + (s % 8) * 2048; };
  auto rd0 = [&](auto s) { f0[s % 3] = c.p.template frag<off(s)>(); };
  auto rd1 = [&](auto s) { f1[s % 3] = c.p.template frag<off(s) + 1024>(); };
  rd0(IC(0)); rd1(IC(0)); rd0(IC(1)); rd1(IC(1));
  sfor<0, 64>([&](auto s) {
    waitx<false, g64_wait<H>(s)>(x[s]);
    SBAR();
    mm<false, false, ZERO && s == 0>(acc0, f0[s % 3], x[s]);
    SBAR();
    if constexpr (s + 2 < 64) rd0(IC(s + 2));
    if constexpr (s + XL < 64) ld_x<s + XL>(x, c, xb);
    aux(IC(2 * s));
    SBAR();
    mm<false, false, ZERO && s == 0>(acc1, f1[s % 3], x[s]);
    SBAR();
    if constexpr (s + 2 < 64) rd1(IC(s + 2));
    hook(s);
    aux(IC(2 * s + 1));
    SBAR();
  });
  mdone(acc0, acc1);
}
struct HookP6 { static constexpr int n(int s) { return s >= 52 ? 1 : 0; } };
struct HookP3 { static constexpr int n(int s) { return s >= 52 ? 1 : 0; } };
struct HookP4 { static constexpr int n(int s) { return s == 52 || s == 6 || (s >= 8 && s < 12) ? 1 : 0; } };

DI void res_to_acc(const u32x4 (&xr)[4], f32x16& a0, f32x16& a1) {
#pragma unroll
  for (int e = 0; e < 4; ++e) {
    a0[2 * e] = bflo(xr[0][e]); a0[2 * e + 1] = bfhi(xr[0][e]); a0[8 + 2 * e] = bflo(xr[1][e]); a0[9 + 2 * e] = bfhi(xr[1][e]);
    a1[2 * e] = bflo(xr[2][e]); a1[2 * e + 1] = bfhi(xr[2][e]); a1[8 + 2 * e] = bflo(xr[3][e]); a1[9 + 2 * e] = bfhi(xr[3][e]);
  }
  asm volatile("" : "+v"(a0), "+v"(a1));
}
DI void store_stats(int r, const u32x4 (&xr)[4], char* stp) {
  float s1 = 0.f, s2 = 0.f, s3 = 0.f, s4 = 0.f;
#pragma unroll
  for (int i = 0; i < 4; ++i)
#pragma unroll
    for (int e = 0; e < 4; e += 2) {
      s1 = dot2(xr[i][e], 0x3f803f80u, s1); s2 = dot2(xr[i][e], xr[i][e], s2);
      s3 = dot2(xr[i][e + 1], 0x3f803f80u, s3); s4 = dot2(xr[i][e + 1], xr[i][e + 1], s4);
    }
  st2_1(f32x2{swap_add(s1 + s3), swap_add(s2 + s4)}, (unsigned)(r * 8), stp);
}
DI void store_x(const Ctx& c, int r, const f32x16& a0, const f32x16& a1, u32x4 (&xr)[4], char* dst, char* stp) {
  SBAR();
  xr[0] = __builtin_bit_cast(u32x4, pack8<0>(a0)); st4_1<0>(xr[0], c.xv, dst); SBAR();
  xr[1] = __builtin_bit_cast(u32x4, pack8<1>(a0)); st4_1<1024>(xr[1], c.xv, dst); SBAR();
  xr[2] = __builtin_bit_cast(u32x4, pack8<0>(a1)); st4_1<2048>(xr[2], c.xv, dst); SBAR();
  xr[3] = __builtin_bit_cast(u32x4, pack8<1>(a1)); st4_1<3072>(xr[3], c.xv, dst); SBAR();
  store_stats(r, xr, stp);
  SBAR();
}

template <class FX> DI void hedge_exp(const f32x16& p, float sc, f32x16& ep, f32x16& en, FX&& hook) {
  float mx = fmaxf(p[0], p[1]), mn = fminf(p[0], p[1]);
#pragma unroll
  for (int i = 2; i < 16; i += 2) { mx = fmaxf(fmaxf(mx, p[i]), p[i + 1]); mn = fminf(fminf(mn, p[i]), p[i + 1]); }
  mx = swap_max(mx) * sc; mn = swap_min(mn) * sc;
  sfor<0, 16>([&](auto i) {
    SBAR();
    hook(i);
    constexpr int e = i;
    float a = __builtin_amdgcn_exp2f(__builtin_fmaf(p[e], sc, -mx)), b = __builtin_amdgcn_exp2f(__builtin_fmaf(p[e], -sc, mn));
    asm volatile("" : "+v"(a), "+v"(b));
    ep[e] = a; en[e] = b;
  });
  SBAR();
}

constexpr int a_slot(int g) { constexpr int m[32] = {0, 1, 2, 3, 4, 0, 1, 2, 5, 6, 7, 5, 3, 4, 6, 7, 8, 5, 3, 4, 6, 7, 0, 1, 2, 5, 3, 4, 6, 7, 0, 1}; return m[g]; }
constexpr int a_need(int g) { return g < 8 ? 8 * g - 2 : (g < 16 ? 16 * (g - 8) - (g > 12 ? 10 : 2) : 128 + 8 * (g - 16) - 2); }
constexpr bool a_bar(int s) { return (s + 2) % 8 == 0 && s >= 14 && s < 190; }
constexpr int a_pre(int n) { return n < 16 ? n : n + 16; }
constexpr int QP = 128 + 24, QC = 128 + 36;
constexpr int a_piece(int s) {
  constexpr int t[][3] = {{6, 4, 1}, {12, 10, 1}, {20, 5, 1}, {24, 6, 1}, {28, 11, 1}, {34, 7, 1}, {46, 12, 1}, {54, 13, 1}, {64, 14, 2}, {72, 15, 2}, {94, 16, 2}, {102, 17, 2}, {110, 18, 2},
                          {118, 19, 2}, {128, 20, 1}, {132, 21, 1}, {136, 22, 1}, {140, 23, 1}, {144, 24, 1}, {150, 25, 1}, {158, 26, 1}};
  for (auto& e : t) if (s >= e[0] && s < e[0] + 4 * e[2] && (s - e[0]) % e[2] == 0) return e[1] * 4 + (s - e[0]) / e[2];
  return -1;
}
constexpr int a_first(int s) { constexpr int g[6] = {0, 8, 1, 2, 9, 3}; return s < 4 ? g[(s + 20) / 4] * 4 + (s + 20) % 4 : -1; }
constexpr int a_raw(int s) { constexpr int t[8] = {4, 5, 10, 11, 16, 17, 18, 19}; for (int i = 0; i < 8; ++i) if (t[i] == s) return i; return -1; }
constexpr int a_park(int s) { return s == 148 || s == 149 ? s - 148 : (s >= 154 && s < 158 ? s - 152 : (s == 162 || s == 163 ? s - 156 : -1)); }
constexpr bool a_side(int s, bool first) { return (s >= 2 && s < (first ? 14 : 6)) || s == 50 || (s >= 96 && s < 128 && s % 4 == 3) || a_park(s) >= 0; }
struct SchedA {
  int xw[64], bw[192], sw[64], fw, cw, pw, zw, lw[32];
  constexpr SchedA(bool first) : xw{}, bw{}, sw{}, fw(0), cw(0), pw(0), zw(0), lw{} {
    int pos = 0, xpos[64] = {}, wpos[32] = {}, spos[192] = {}, fpos = 0, stpos = 0, ppos = 0, rpos = 0, lpos[34] = {};
    if (first) { for (int s = -20; s < 0; ++s) wpos[a_first(s) / 4] = ++pos; pos += 2; }
    else for (int s = 0; s < XL; ++s) xpos[s] = ++pos;
    for (int s = 0; s < 192; ++s) {
      if (s == 128) { cw = pos + 2 - spos[127]; pos += 3; stpos = pos; }
      if (a_bar(s) || s == 6) {
        bw[s] = 63;
        for (int g = 0; g < 24; ++g) if (a_need(g) == s && wpos[g] && pos - wpos[g] < bw[s]) bw[s] = pos - wpos[g];
        if (s == 62 && pos - spos[50] < bw[s]) bw[s] = pos - spos[50];
        if (s == 38 && first && pos - rpos < bw[s]) bw[s] = pos - rpos;
        if (s == 142) { if (pos - stpos < bw[s]) bw[s] = pos - stpos; ++pos; }
      }
      if (s == 20 || s == 24 || s == 28) sw[s] = pos - spos[s - 15];
      if (s == 94) fw = pos - fpos;
      if (s == QC) pw = pos - ppos;
      if (s < 64) xw[s] = pos - xpos[s];
      if (!first && s + XL < 64) xpos[s + XL] = ++pos;
      if (s == 81) fpos = ++pos;
      if (s == QP) ppos = ++pos;
      if (s >= QC && s < QC + 17) lpos[2 * (s - QC)] = ++pos;
      if (a_piece(s) >= 0) wpos[a_piece(s) / 4] = ++pos;
      if (a_side(s, first)) spos[s] = ++pos;
      if (s >= QC && s < QC + 17) lpos[2 * (s - QC) + 1] = ++pos;
      if (first && a_first(s) >= 0) wpos[a_first(s) / 4] = ++pos;
      if (first && a_raw(s) >= 0) rpos = ++pos;
      if (first && (s == 39 || s == 42)) pos += 5;
      if (s == 175) zw = pos - lpos[1];
      if (s >= 176) lw[s - 176] = pos - lpos[2 + s - 176];
    }
    for (int i = 16; i < 32; ++i) lw[i] = pos - lpos[2 + i];
  }
};
constexpr int cmin(int a, int b) { return a < b ? a : b; }

#define R_OA "v[64:79]"
#define R_OB "v[80:95]"
#define S0_(n, q, m) "v_mfma_f32_32x32x16_bf16 v[" #n ":" #n "+15], v[208:211], v[" #q ":" #q "+3], v[" #m ":" #m "+15]\n\t"
#define S1_(n, q) "v_mfma_f32_32x32x16_bf16 v[" #n ":" #n "+15], v[212:215], v[" #q "+4:" #q "+7], v[" #n ":" #n "+15]\n\t"
#define PV0_(o, p) "v_smfmac_f32_32x32x32_bf16 v[" #o ":" #o "+15], v[216:219], v[" #p ":" #p "+7], v236\n\t"
#define PV1_(o, p) "v_smfmac_f32_32x32x32_bf16 v[" #o ":" #o "+15], v[220:223], v[" #p "+8:" #p "+15], v236\n\t"
#define L0_(l, p) "v_smfmac_f32_16x16x64_bf16 v[" #l ":" #l "+3], v[224:227], v[" #p ":" #p "+7], v236\n\t"
#define L1_(l, p) "v_smfmac_f32_16x16x64_bf16 v[" #l ":" #l "+3], v[224:227], v[" #p "+8:" #p "+15], v236\n\t"
#define E_(e, i) "v_exp_f32 v[" #e "+" #i "], v[" #e "+" #i "]\n\t"
#define RD_(d, off) "ds_read_b128 v[" #d ":" #d "+3], v237 offset:" #off "\n\t"
#define HALF_(e, n, q, m, p, o, l, X0, X1, X2, X3, X4, X5) \
  S0_(n, q, m) X0 E_(e, 0) E_(e, 1) E_(e, 2) S1_(n, q) X1 E_(e, 3) E_(e, 4) E_(e, 5) PV0_(o, p) X2 E_(e, 6) E_(e, 7) E_(e, 8) \
  PV1_(o, p) X3 E_(e, 9) E_(e, 10) E_(e, 11) L0_(l, p) X4 E_(e, 12) E_(e, 13) L1_(l, p) X5 E_(e, 14) E_(e, 15)
#define LG2_ "s_waitcnt lgkmcnt(2)\n\t"
#define LG0_ "s_waitcnt lgkmcnt(0)\n\t"
#define STEP_(b, nb, kv, wo, Y0, Y1) \
  HALF_(128, 160, 200, 112, 176, 80, 232, "", "", RD_(208, b + 2048), RD_(212, b + 3072), RD_(216, b), RD_(220, b + 1024) LG2_) \
  HALF_(160, 144, 192, 96, 128, 64, 228, "", LG0_, "s_add_u32 m0, s44, " #nb "\n\t", "", "global_load_lds_dwordx4 v" #kv ", s[40:41] sc1\n\t", \
        "global_load_lds_dwordx4 v" #kv ", s[40:41] offset:1024 sc1\n\t") \
  HALF_(144, 176, 200, 112, 160, 80, 232, Y0, "", RD_(208, b + 6144), RD_(212, b + 7168), RD_(216, b + 4096), RD_(220, b + 5120) LG2_) \
  HALF_(176, 128, 192, 96, 144, 64, 228, "s_mov_b32 m0, s45\n\t", LG0_, "global_load_lds_dwordx4 v240, s[42:43] offset:" #wo " sc1\n\t", Y1, \
        "s_waitcnt vmcnt(4)\n\t", "s_barrier\n\t")
DI float max16(const f32x16& s) {
  float mx = s[0];
#pragma unroll
  for (int e = 1; e < 16; ++e) mx = fmaxf(mx, s[e]);
  return mx;
}
typedef __attribute__((ext_vector_type(8))) unsigned u32x8;
typedef __attribute__((ext_vector_type(16))) unsigned u32x16;

__global__ __launch_bounds__(256, 1) void k_main(const char* xin, const char* wqkv, const char* wphi, const char* woA, const char* wq, const char* wk, const char* wv,
                                                 const char* woB, char* ws, char* out) {
  __shared__ __attribute__((aligned(16))) char smem_[163840];
  LAS char* const smem = (LAS char*)smem_;
  const unsigned lds0 = (unsigned)(uintptr_t)smem;
  Ctx c;
  int lane = threadIdx.x & 63, w = __builtin_amdgcn_readfirstlane(threadIdx.x >> 6), r = lane & 31, h = lane >> 5, rb, cb;
  block_map<4>(rb, cb);
  int tt = rb * 4 + w, blk = (lane >> 4) & 1, q4 = (lane & 15) >> 2, p4 = lane & 3;
  c.xv = lane * 16; c.p.init(lds0 + c.xv);
  char* flags = ws + WS_FLAG;
  char* col = ws + WS_COL + (size_t)cb * 2 * MB;
  const int ut = rb * 4 + (cb >> 2), ty = cb & 3;
  char* act = ws + (size_t)rb * 2 * MB + w * 65536;
  char* acto = act + cb * 4096;
  char* fact = flags + tt * FL_I;
  char* facto = fact + cb * 8;
  char* stt = ws + WS_ST + tt * 4096;
  char* stto = stt + cb * 256;
  char* kvp = col + C_KVP + w * 32768;
  char* zp = col + C_ZP + w * 2048;
  char* fkvp = flags + (cb * 4 + w) * FL_I + 8 * FL_K;
  char* fwl = flags + cb * 4 * FL_I + 11 * FL_K;
  char* kvf = ws + WS_KVF;
  char* fkv = flags + 12 * FL_K;
  const char* xu_p = ws + (size_t)rb * 2 * MB + 2 * STG + (cb >> 2) * 65536 + w * 16384 + 4096;
  const char* su_p = ws + WS_ST + 256 * KB + ut * 4096;
  const char* wkv = ((cb & 2) ? wv : wk) + w * 256 * 64;
  const char* wph = wphi + cb * 4096 + w * 1024;
  u64 tag = 0;
  u32x4 xr[4] = {};
  char* colw = col + w * 4096;
  unsigned ldsw = lds0 + w * 4096;
  auto wt = [&](int m, int ch) { return colw + C_WT + m * 131072 + ch * SLOT; };
  auto pc_w = [&](int m, int slot, int ch, auto i) { if constexpr (i == 0) dma_c<0>(ldsw + slot * SLOT, c.xv, wt(m, ch)); else dma_n<i * 1024>(c.xv, wt(m, ch)); };
  auto pc_a = [&](auto n) { constexpr int g = n / 4; pc_w(g < 8 ? 1 : (g < 16 ? 2 : (g < 24 ? 0 : 3)), a_slot(g), g % 8, IC(n % 4)); };
  u64 csm = ~0ull;
  float csv[4] = {};
  auto ld_cs = [&](bf16x8& d, int m, auto i) { ldx1m<i * 1024>(d, c.xv, col + C_CS + m * 4096, csm); };
  auto raw_in = [&](auto j, int slot, auto i) {
    const int hi = rb >> 3, pitch = (j == 0 || (j == 1 && !hi)) ? 6144 : 2048, row = lane >> 3;
    const char* src = (j == 0 ? wqkv + 2048 + hi * 2048 : (j == 1 ? (hi ? woA : wqkv) : (hi ? woB : wq))) + cb * 128 + (size_t)(rb & 7) * 128 * pitch;
    const unsigned v0 = (unsigned)((32 * w + row) * pitch + ((lane & 7) ^ (((row >> 1) & 1) << 2)) * 16);
    dma<i * 1024>(lds0 + slot * SLOT + w * 4096, v0 + i * (8 * pitch - 1024), src);
  };
  auto raw_out = [&](auto j, int slot) {
    const int hi = rb >> 3, ch = rb & 7, m = j == 0 ? 1 + hi : (j == 1 ? 3 * hi : 4 + hi);
    LAS const char* p = smem + slot * SLOT + (w >> 1) * 8192 + (4 * h + q4) * 128 + (((((q4 >> 1) ^ w) & 1) << 2) + 2 * blk + (p4 >> 1)) * 16 + 8 * (p4 & 1);
    const bool isv = m == 2;
    char* dst = col + C_WT + m * 131072 + (isv ? (w & 1) * 65536 + ch * 8192 + (w >> 1) * 4096 : ch * SLOT + (w >> 1) * 8192 + (w & 1) * 1024);
    float cs = 0.f;
    sfor<0, 4>([&](auto s) {
      const u32x4 f = __builtin_bit_cast(u32x4, cat8(trread(p + s * 2048), trread(p + s * 2048 + 1024)));
      st4_1<0>(f, c.xv, dst + s * (isv ? 1024 : 2048));
#pragma unroll
      for (int e = 0; e < 4; ++e) cs = dot2(f[e], 0x3f803f80u, cs);
    });
    st1_1(swap_add(cs), (unsigned)(r * 4), col + C_CS + m * 4096 + (ch * 2 + (w >> 1)) * 256 + (w & 1) * 128);
  };
  const int w0 = w; const unsigned ldsw0 = ldsw;
#define REFRESH() do { \
    w = w0; ldsw = ldsw0; \
    asm volatile("" : "+s"(w), "+s"(tag), "+s"(act), "+s"(acto), "+s"(fact), "+s"(facto), "+s"(stt), "+s"(stto), "+s"(col), "+s"(colw), "+s"(ldsw)); \
    asm volatile("" : "+v"(lane)); \
    r = lane & 31; h = lane >> 5; blk = (lane >> 4) & 1; q4 = (lane & 15) >> 2; p4 = lane & 3; c.xv = lane * 16; c.p.init(lds0 + c.xv); \
  } while (0)
  int l = 0;
  float mean, rstd;
  Red<true> cw;
  f32x16 ra0, ra1;
  bf16x8 park[16];
      constexpr int NA = 60;
      constexpr SchedA SQ(false), S1(true);
      bf16x8 x[64], f0[3], f1[3], f2[5], wf[4], kp[4], pp[32], zq[2], sel[2], mf0, mf1;
      Red<false> st; Red<true> ck, cv, cq;
      f32x16 acc[6], p[2], kvs;
      u64 fv;
      float mx[1], mn[1], e[16], en[16], se, sn, sce;
      unsigned arow;
      LAS char* pk_img = smem;
      LAS char* v_img = smem + 2 * SLOT;
      LAS char* sh = smem + 9 * SLOT + 4096;
      auto issue_x = [&](auto s) { ldx1<(s < NA), (s % 8) * 1024 - 4096>(x[s], c.xv, act + (s / 8) * 8192 + 4096); };
      auto ld_park = [&](auto i) { ldx1<true, (i % 4) * 1024>(park[i], c.xv, wt(4, 2 + i / 4)); };
      auto ldp = [&](auto i) {
        if constexpr (i >= 2) ldx1<true, ((i - 2) % 8) * 1024 - 4096>(pp[i - 2], c.xv, kvp + ((i - 2) / 8) * 8192 + 4096); else ldx1<false, i * 1024>(zq[i], c.xv, zp);
      };
      auto rd_wf = [&](auto k) { LAS const char* wp = smem + 9 * SLOT + (4 * h + q4) * 64 + blk * 32 + p4 * 8 + k * 1024; wf[k] = cat8(trread(wp), trread(wp + 512)); };
      auto rd01 = [&](auto s, auto t) {
        if constexpr (s < 64 || (s >= 128 && s < 192)) { const bf16x8 f = c.p.template frag<a_slot(s / 8) * SLOT + (s % 8) * 2048 + t * 1024>(); if constexpr (t == 0) f0[s % 3] = f; else f1[s % 3] = f; }
      };
      auto rd2 = [&](auto s) { if constexpr (s < 128) f2[s % 5] = c.p.template frag<a_slot(8 + s / 16) * SLOT + (s % 16) * 1024>(); };
      auto v_out = [&](auto t, auto hf) {
        *(LAS bf16x8*)(v_img + t * 8192 + w * 2048 + hf * 1024 + lane * 16) = pack8<hf>(acc[2 + t]);
      };
      auto sum_mm = [&](auto i) { waitx<true, SQ.lw[i]>(pp[i]); mm<false, true, i == 0>(kvs, sel[i & 1], pp[i]); };
      auto kmap = [&](auto gi) {
        constexpr int g = gi;
        if constexpr (g == 2) pin2(acc[0], acc[1]);
        if constexpr (g == 3 || g == 4) { mm<false, false, true>(p[g - 3], wf[2 * g - 6], kp[2 * g - 6]); mm<false, false, false>(p[g - 3], wf[2 * g - 5], kp[2 * g - 5]); }
        if constexpr (g == 2 || g == 3) { kp[2 * g - 4] = opq_v(pack8<0>(acc[g - 2])); kp[2 * g - 3] = opq_v(pack8<1>(acc[g - 2])); }
        if constexpr (g == 5) mm<true, false, false, true>(acc[3], cv.p[0], mf1);
        if constexpr (g >= 6) {
          constexpr int hd = (g - 6) / 29, j = (g - 6) % 29;
          const f32x16& q = p[hd];
          if constexpr (j == 0) { mx[0] = a_max3(q[0], q[1], q[2]); mn[0] = a_min3(q[0], q[1], q[2]); a_mm4(mx[0], mn[0], q[3], q[4], q[5], q[6]); a_mm4(mx[0], mn[0], q[7], q[8], q[9], q[10]); }
          if constexpr (j == 1) { a_mm4(mx[0], mn[0], q[11], q[12], q[13], q[14]); mx[0] = opq_v(swap_max(fmaxf(mx[0], q[15])) * -sce); }
          if constexpr (j == 2) {
            mn[0] = opq_v(swap_min(fminf(mn[0], q[15])) * sce);
            e[0] = a_fma(q[0], sce, mx[0]); en[0] = a_fms(q[0], sce, mn[0]); se = 0.f; sn = 0.f;
          }
          if constexpr (j == 3) { float z = 0.f; a_hedge(e[0], en[0], e[1], en[1], se, sn, z, z, q[1], sce, mx[0], mn[0]); }
          if constexpr (j >= 4 && j < 18) a_hedge(e[j - 3], en[j - 3], e[j - 2], en[j - 2], se, sn, e[j - 4], en[j - 4], q[j - 2], sce, mx[0], mn[0]);
          if constexpr (j == 18) { a_exp(e[15]); a_exp(en[15]); a_add(se, e[14]); a_add(sn, en[14]); if constexpr (hd == 0) v_out(IC(0), IC(0)); }
          if constexpr (j == 19) se = opq_v(__builtin_amdgcn_rcpf(swap_add(se + e[15])) * rstd);
          if constexpr (j == 20) { sn = opq_v(__builtin_amdgcn_rcpf(swap_add(sn + en[15])) * rstd); if constexpr (hd == 0) v_out(IC(0), IC(1)); }
          if constexpr (j >= 21) {
            constexpr int n = j - 21, gq = n & 3;
            const float* v = n < 4 ? e : en;
            *(LAS u64*)((LAS char*)(uintptr_t)(arow ^ (n << 4)) + hd * 16384) = a_norm(v[4 * gq], v[4 * gq + 1], v[4 * gq + 2], v[4 * gq + 3], n < 4 ? se : sn);
          }
        }
      };
      auto keep = [&](Red<true>& q, int n) { csv[n] = l == 0 ? q.a : csv[n]; q.a = csv[n]; };
      auto aux = [&](auto s, auto i, auto fl) {
        constexpr int g1 = 3 * s + i, g3 = 2 * (s - 128) + i;
        if constexpr (s < 64) {
          if constexpr (g1 >= 60 && g1 < 71) st.template run<g1 - 60>(lane);
          if constexpr (g1 == 80) { if constexpr (fl) csv[0] = ck.a; else ck.a = csv[0]; }
          if constexpr (g1 == 92) { if constexpr (fl) csv[1] = cv.a; else cv.a = csv[1]; }
          if constexpr (g1 >= (fl ? 72 : 80) && g1 < 82) ck.template run<g1 - 72>(lane);
          if constexpr (g1 >= (fl ? 84 : 92) && g1 < 94) cv.template run<g1 - 84>(lane);
          if constexpr (g1 == 71) { mean = st.a; rstd = st.b; sce = opq_v(rstd * LOG2E); }
          if constexpr (g1 == 95) mf0 = opq_v(mean_frag(mean, h == 0));
          if constexpr (g1 == 96) mf1 = opq_v(mean_frag(mean, h == 1));
          if constexpr (g1 == 99) mm<false, false, false>(acc[0], ck.p[0], mf0);
          if constexpr (g1 == 100) mm<false, false, false>(acc[1], ck.p[0], mf1);
          if constexpr (g1 == 101) mm<true, false, false>(acc[2], cv.p[0], mf0);
          if constexpr (g1 == 150) *(LAS short*)(sh + 8704 + w * 64 + (r >> 4) * 32 + ((r >> 2) & 1) * 16 + ((r >> 3) & 1) * 8 + (r & 3) * 2) = (short)(__float_as_uint(st.sd) >> 16);
          if constexpr (g1 >= 186 && g1 < 190) rd_wf(IC(g1 - 186));
          if constexpr (g1 == 174) arow = opq_v((unsigned)(uintptr_t)pk_img + (w * 32 + r) * 128 + h * 8 + ((r & 7) << 4));
        } else if constexpr (s >= 128) {
          if constexpr (g3 == 8) keep(cq, 2);
          if constexpr (g3 == 24) keep(cw, 3);
          if constexpr (g3 < 10) cq.template run<g3>(lane);
          if constexpr (g3 == 12) mm<false, false, false>(acc[4], cq.p[0], mf0);
          if constexpr (g3 == 13) mm<false, false, false>(acc[5], cq.p[0], mf1);
          if constexpr (g3 >= 16 && g3 < 26) cw.template run<g3 - 16>(lane);
          if constexpr (g3 >= 32 && g3 < 40) {
            constexpr int n = (g3 - 32) % 4, t = (g3 - 32) / 4;
            const int kr = 16 * t + 8 * (n >> 1) + 4 * h + 2 * (n & 1);
            u32x4 sv = __builtin_bit_cast(u32x4, sel[t]); sv[n] = opq_v(r == kr ? 0x3f80u : (r == kr + 1 ? 0x3f800000u : 0u)); sel[t] = __builtin_bit_cast(bf16x8, sv);
          }
          if constexpr (g3 == 40) { sel[0] = opq_v(sel[0]); sel[1] = opq_v(sel[1]); }
          if constexpr (g3 == 42) *(LAS unsigned*)(sh + 8192 + 256 + lane * 4) = opq_v(0x3f803f80u);
          if constexpr (g3 == 95) {
            wait2<SQ.zw>(zq[0], zq[1]);
#pragma unroll
            for (int n = 0; n < 4; ++n) { e[n] = __builtin_bit_cast(f32x4, zq[0])[n]; a_add(e[n], __builtin_bit_cast(f32x4, zq[1])[n]); }
          }
          if constexpr (g3 == 96) {
            auto a = __builtin_amdgcn_permlane16_swap(__float_as_uint(e[0]), __float_as_uint(e[1]), false, false);
            auto b = __builtin_amdgcn_permlane16_swap(__float_as_uint(e[2]), __float_as_uint(e[3]), false, false);
            e[0] = opq_v(__uint_as_float(a[0]) + __uint_as_float(a[1])); e[1] = opq_v(__uint_as_float(b[0]) + __uint_as_float(b[1]));
          }
          if constexpr (g3 == 98) { auto a = __builtin_amdgcn_permlane32_swap(__float_as_uint(e[0]), __float_as_uint(e[1]), false, false); e[0] = opq_v(__uint_as_float(a[0]) + __uint_as_float(a[1])); }
          if constexpr (g3 == 100) e[1] = lane_get(e[0], lane ^ 8);
          if constexpr (g3 == 106)
            *(LAS short*)(sh + 8192 + w * 64 + (lane & 1) * 32 + ((lane >> 2) & 1) * 16 + ((lane >> 1) & 1) * 8 + (lane >> 4) * 2) = (short)cvtpk(e[0] + e[1], 0.f);
          if constexpr (g3 >= 116 && g3 < 124 && g3 % 2 == 0) rd_wf(IC((g3 - 116) / 2));
        }
      };
      auto step = [&](auto s, auto fl) {
        constexpr int ps = s / 64;
        constexpr bool first = fl;
        if constexpr (a_bar(s) || (first && s == 6)) wait_bar<(ps ? cmin(S1.bw[s], SQ.bw[s]) : (first ? S1.bw[s] : SQ.bw[s]))>();
        if constexpr (s == 20 || (first && (s == 24 || s == 28))) waitv<(first ? S1.sw[s % 64] : SQ.sw[s % 64])>();
        if constexpr (first && s == 62) { if (w == 0) set_flag(fwl + rb * 8, tag); }
        if constexpr (s == 94) { waitx<false, SQ.fw>(fv); if (!__all(fv == tag)) poll<16>(fwl, lane * 8, tag); }
        if constexpr (s == 142) set_flag(fkvp + rb * 8, tag);
        if constexpr (s == QC) { waitx<false, SQ.pw>(fv); if (!__all(fv == tag)) poll<16>(fkvp, lane * 8, tag); }
        if constexpr (s < 64 && !first) waitx<(s < NA), SQ.xw[s % 64]>(x[s % 64]);
        SBAR();
        if constexpr (ps != 1) mm<false, (s % 64 < NA), s % 64 == 0>(acc[2 * ps], f0[s % 3], x[s % 64]);
        else mm<true, (s % 64 < NA), s % 64 == 0>(acc[3], f2[s % 5], x[s % 64]);
        SBAR();
        rd01(IC(s + 2), IC(0));
        if constexpr (ps == 1) rd2(IC(s + 4));
        if constexpr (s + XL < 64 && !first) issue_x(IC(s + XL));
        if constexpr (s == 81) ld8_1(fv, (lane & 15) * 8, fwl);
        if constexpr (s == QP) { fv = tag; rd_flag<16>(fv, lane * 8, fkvp); }
        if constexpr (s >= QC && s < QC + 17) ldp(IC(2 * (s - QC)));
        aux(s, IC(0), fl);
        SBAR();
        if constexpr (ps != 1) mm<false, (s % 64 < NA), s % 64 == 0>(acc[2 * ps + 1], f1[s % 3], x[s % 64]);
        SBAR();
        rd01(IC(s + 2), IC(1));
        if constexpr (a_piece(s) >= 0) pc_a(IC(a_piece(s)));
        if constexpr (s >= 2 && s < 6) ldx1<false, (s - 2) * 1024>(st.p[s - 2], c.xv, stt);
        if constexpr (first && s >= 6 && s < 10) ld_cs(ck.p[s - 6], 1, IC(s - 6));
        if constexpr (first && s >= 10 && s < 14) ld_cs(cv.p[s - 10], 2, IC(s - 10));
        if constexpr (s == 50) dma<0>(lds0 + 9 * SLOT + w * 1024, c.xv, wph);
        if constexpr (s >= 96 && s < 112 && s % 4 == 3) ld_cs(cq.p[(s - 96) / 4], 0, IC((s - 96) / 4));
        if constexpr (s >= 112 && s < 128 && s % 4 == 3) ld_cs(cw.p[(s - 112) / 4], 4, IC((s - 112) / 4));
        if constexpr (a_park(s) >= 0) ld_park(IC(a_park(s)));
        if constexpr (s >= QC && s < QC + 17) ldp(IC(2 * (s - QC) + 1));
        aux(s, IC(1), fl);
        if constexpr (ps == 0) {
          SBAR();
          mm<true, (s < NA), s == 0>(acc[2], f2[s % 5], x[s]);
          SBAR();
          rd2(IC(s + 2));
          if constexpr (s >= 62) rd2(IC(s + 4));
          if constexpr (first && a_first(s) >= 0) pc_a(IC(a_first(s)));
          if constexpr (first && a_raw(s) >= 0) raw_in(IC(1 + a_raw(s) / 4), 8 + a_raw(s) / 4, IC(a_raw(s) % 4));
          if constexpr (first && s == 39) raw_out(IC(2), 9);
          if constexpr (first && s == 42) raw_out(IC(1), 8);
          aux(s, IC(2), fl);
        }
        if constexpr (ps == 1) kmap(IC(s - 64));
        if constexpr (s >= 176) sum_mm(IC(s - 176));
        SBAR();
      };
      {
        STAMP(30);
        bf16x8 cf;
        { const char* pc; asm volatile("s_getpc_b64 %0" : "=s"(pc));
          ldx0<0>(cf, c.xv, pc + (((blockIdx.x >> 3) * 4 + w) % CODE_KB) * 1024); }
        sfor<0, 4>([&](auto i) { raw_in(IC(0), 7, i); });
        u64 ep; ld8_1(ep, 0u, ws + WS_EPOCH);
        bf16x8 xt[4];
        const char* xw = xin + (size_t)tt * 65536;
        sfor<0, 4>([&](auto i) { ldx0<i * 32>(xt[i], (unsigned)(r * 2048 + h * 16), xw + cb * 128); });
        { const unsigned z = 0, vo = (unsigned)(lane ? cb * 4 * FL_I + 10 * FL_K : tt * FL_I);
          asm volatile("s_mov_b64 exec, 3\n\tglobal_atomic_or %0, %1, %2 sc1\n\ts_mov_b64 exec, -1" ::"v"(vo), "v"(z), "s"(flags) : "memory"); }
        wait_bar<6>();
        raw_out(IC(0), 7);
        asm volatile("s_waitcnt vmcnt(6)" : "+v"(cf), "+v"(ep), "+v"(xt[0]), "+v"(xt[1]), "+v"(xt[2]), "+v"(xt[3]));
        sfor<0, 4>([&](auto i) { xr[i] = unperm(__builtin_bit_cast(u32x4, xt[i])); });
        store_stats(r, xr, stto);
        { u64 z = ((u64)__builtin_amdgcn_readfirstlane((unsigned)(ep >> 32)) << 32 | __builtin_amdgcn_readfirstlane((unsigned)ep)) + 0x9E3779B97F4A7C15ull;
          z = (z ^ (z >> 30)) * 0xBF58476D1CE4E5B9ull; z = (z ^ (z >> 27)) * 0x94D049BB133111EBull; tag = z ^ (z >> 31); asm volatile("" : "+s"(tag)); }
        const unsigned xs = (unsigned)((lane >> 3) * 2048 + ((lane & 7) ^ (((lane >> 3) & 3) << 1)) * 16), xl = lds0 + w * 28672;
        const unsigned xo = xl + r * 128 + (r >> 3) * 16 + h * 8;
        auto x_in = [&](auto g) { sfor<0, 4>([&](auto i) { dma<i * 1040>(xl + (g % 6) * 4160, xs + i * (16384 - 1040), xw + g * 128); }); };
        sfor<0, 2>(x_in);
        waitv<8>();
        set_flag(facto, tag);
        bar();
        if (w == 0) set_flag(fwl - FL_K + rb * 8, tag);
        sfor<2, 6>(x_in);
        const unsigned fo = (unsigned)((lane & 15) * 8 + (blk ? cb * 4 * FL_I + 10 * FL_K : tt * FL_I));
        sfor<0, 16>([&](auto g) {
          waitv<4 * (g < 10 ? 5 : 15 - g) + (g > 11 ? 1 : 0)>();
          sfor<4 * g, 4 * g + 4>([&](auto s) { ds_read2<(s < NA)>(x[s], xo + (g % 6) * 4160 + (((s % 4) ^ (r & 3)) << 5)); });
          sfor<4 * g, 4 * g + 4>([&](auto s) { waitl<(s < NA)>(x[s]); });
          if constexpr (g + 6 < 16) x_in(IC(g + 6));
          if constexpr (g == 11) { fv = tag; rd_flag<32>(fv, fo, flags); }
        });
        unsigned tch;
        {
          const int gw = blockIdx.x * 4 + w; const unsigned z = 0;
          const unsigned vo = (unsigned)(lane < 5 ? WS_KVF - WS_ST + (gw * 5 + lane) * 128 : WS_FLAG - WS_ST + gw * 128);
          waitx<false, 0>(fv);
          if (!__all(fv == tag)) poll<32>(flags, fo, tag);
          bar();
          sfor<-20, 0>([&](auto n) { pc_a(IC(a_first(n))); });
          asm volatile("s_mov_b64 exec, 63\n\tglobal_atomic_or %0, %1, %2 sc1\n\ts_mov_b64 exec, -1" ::"v"(vo), "v"(z), "s"(ws + WS_ST) : "memory");
          const char* sm = (gw & 512) ? ((gw & 256) ? wk : wv) : wphi;
          asm volatile("global_load_dword %0, %1, %2" : "=v"(tch) : "v"((unsigned)((gw & 255) * 256 + lane * 4)), "s"(sm));
        }
        STAMP(31); STAMP(0); STAMP(1);
        wait_bar<14>();
        sfor<0, 2>([&](auto s) { rd01(s, IC(0)); rd01(s, IC(1)); rd2(s); });
        sfor<0, 64>([&](auto s) { step(s, std::true_type{}); });
        waitx<false, 63>(tch);
      }
  asm volatile("" : "+s"(w), "+s"(tag), "+s"(act), "+s"(acto), "+s"(fact), "+s"(facto), "+s"(stt), "+s"(stto), "+s"(col), "+s"(colw), "+s"(ldsw));
  asm volatile("" : "+s"(kvp), "+s"(zp), "+s"(fkvp), "+s"(fwl), "+s"(rb), "+s"(kvf), "+s"(fkv), "+s"(xu_p), "+s"(su_p), "+s"(wkv), "+s"(csm), "+s"(l));
  for (;;) {
    {
      REFRESH();
      asm volatile("" : "+s"(kvp), "+s"(zp), "+s"(fkvp), "+s"(fwl), "+s"(rb));
      STAMP(2);
      sfor<64, 128>([&](auto s) { step(s, std::false_type{}); });
      STAMP(19);
      mdone(acc[3]);
      v_out(IC(1), IC(0)); v_out(IC(1), IC(1));
      lds_bar();
      {
        const int hd = w >> 1, fh = w & 1;
        f32x16 kv, z;
        const int ch = fh * 4 + 2 * blk + (p4 >> 1), r0 = 4 * h + q4;
        LAS const char* pa = pk_img + hd * 16384 + r0 * 128 + ((ch ^ r0) << 4) + 8 * (p4 & 1);
        LAS const char* pb = v_img + hd * 8192 + lane * 16;
        bf16x8 a[8], b[8], d[8];
#pragma unroll
        for (int s = 0; s < 8; ++s) { a[s] = cat8(trread(pa + s * 2048), trread(pa + s * 2048 + 1024)); b[s] = *(LAS const bf16x8*)(pb + s * 1024); }
#pragma unroll
        for (int s = 0; s < 8; ++s) d[s] = *(LAS const bf16x8*)(sh + 8704 + s * 32 + h * 16);
        SBAR();
        sfor<0, 8>([&](auto s) { mm<false, false, s == 0>(kv, a[s], b[s]); });
        sfor<0, 8>([&](auto s) {
          mm<false, false, s == 0>(z, d[s], a[s]);
          if constexpr (s == 1) { mdone(kv); st4_1<0>(__builtin_bit_cast(u32x4, pack8<0>(kv)), c.xv, kvp + rb * 2048); }
          if constexpr (s == 2) { st4_1<1024>(__builtin_bit_cast(u32x4, pack8<1>(kv)), c.xv, kvp + rb * 2048); waitv<SQ.cw>(); }
          SBAR();
        });
        mdone(z);
        st1_1(z[0], (unsigned)(r * 4), zp + rb * 128);
      }
      STAMP(3);
      sfor<128, 192>([&](auto s) { step(s, std::false_type{}); });
      mdone(acc[4], acc[5]);
      STAMP(4);
      {
        bf16x8 pq[2][4], ka[8], za[8];
        f32x16 ep, em, nm[4], dn[2];
        float ai[2], bi[2];
        char* dst = acto + STG;
        sfor<0, 4>([&](auto k) { mm<false, false, k % 2 == 0, true>(p[k / 2], wf[k], pack8<k % 2>(acc[4 + k / 2])); });
        mdone(p[0], p[1]);
        hedge_exp(p[0], sce, ep, em, [&](auto i) { sum_mm(IC(16 + i)); });
        pq[0][0] = pack8<0>(ep); pq[0][1] = pack8<1>(ep); pq[0][2] = pack8<0>(em); pq[0][3] = pack8<1>(em);
        pin4(pq[0]);
        asm volatile("" : "+v"(kvs));
        *(LAS bf16x8*)(sh + w * 2048 + lane * 16) = pack8<0>(kvs); *(LAS bf16x8*)(sh + w * 2048 + 1024 + lane * 16) = pack8<1>(kvs);
        lds_bar();
        STAMP(5);
        LAS const char* zb = sh + 8192 + (r & 1) * 256 + h * 32;
#pragma unroll
        for (int k = 0; k < 8; ++k) { za[k] = *(LAS const bf16x8*)(zb + (k >> 1) * 64 + (k & 1) * 16); ka[k] = *(LAS const bf16x8*)(sh + k * 1024 + lane * 16); }
        auto coef = [&](auto hd) {
          const float a = __builtin_amdgcn_rcpf(dn[0][1]), b = __builtin_amdgcn_rcpf(dn[1][1]);
          const float inv = __builtin_amdgcn_rcpf(dn[0][0] * a + dn[1][0] * b + 1e-6f);
          ai[hd] = opq_v(a * inv); bi[hd] = opq_v(b * inv);
        };
        auto fin = [&](auto hd) {
          f32x16 o;
#pragma unroll
          for (int i = 0; i < 16; ++i) o[i] = nm[2 * hd][i] * ai[hd] + nm[2 * hd + 1][i] * bi[hd];
          st4_1<hd * 2048>(__builtin_bit_cast(u32x4, pack8<0>(o)), c.xv, dst);
          st4_1<hd * 2048 + 1024>(__builtin_bit_cast(u32x4, pack8<1>(o)), c.xv, dst);
        };
        hedge_exp(p[1], sce, ep, em, [&](auto i) {
          if constexpr (i < 4) mm<false, false, i % 2 == 0>(dn[i / 2], za[i], pq[0][i]);
          else if constexpr (i < 8) mm<false, false, i % 2 == 0>(nm[i / 2 - 2], ka[i - 4], pq[0][i - 4]);
          if constexpr (i % 2) pc_a(IC(108 + i / 2));
        });
        pq[1][0] = pack8<0>(ep); pq[1][1] = pack8<1>(ep); pq[1][2] = pack8<0>(em); pq[1][3] = pack8<1>(em);
        pin4(pq[1]); pin2(dn[0], dn[1]); pin2(nm[0], nm[1]);
        coef(IC(0));
        STAMP(6);
        sfor<0, 4>([&](auto k) { mm<false, false, k % 2 == 0, k == 0>(dn[k / 2], za[4 + k], pq[1][k]); if constexpr (k % 2) pc_a(IC(116 + k / 2)); });
        sfor<0, 4>([&](auto k) { mm<false, false, k % 2 == 0>(nm[2 + k / 2], ka[4 + k], pq[1][k]); if constexpr (k % 2) pc_a(IC(118 + k / 2)); });
        fin(IC(0));
        pin2(dn[0], dn[1]); pin2(nm[2], nm[3]);
        coef(IC(1));
        fin(IC(1));
        SBAR();
        sfor<120, 124>(pc_a);
        res_to_acc(xr, ra0, ra1);
        waitv<4>();
        set_flag(facto + FL_K, tag);
        sfor<124, 128>(pc_a); sfor<8, 16>(ld_park);
      }
    }
    STAMP(7);
    REFRESH();
    LAS char* reg = smem + 8 * SLOT;
    bf16x8 fu[16], cu;
    {
      f32x16 &a0 = ra0, &a1 = ra1;
      poll<16>(fact + FL_K, lane * 8, tag);
      sfor<0, 16>([&](auto i) { pin_a(park[i]); });
      STAMP(8);
      bf16x8 x[64];
      const char* xb = act + STG;
      sfor<0, XL>([&](auto s) { ld_x<s>(x, c, xb); });
      bar();
      asm volatile("" : "+s"(wkv));
      const unsigned kvv = (unsigned)((lane >> 1) * 64 + (cb & 1) * 32 + (lane & 1) * 16);
      const unsigned wa = ldsw + c.xv, wb = wa + 65536;
      auto unpark = [&](auto i) {
        constexpr int off = a_slot(26 + i / 4) * SLOT + (i % 4) * 1024;
        ds_write_a<(off & 65535)>(off < 65536 ? wa : wb, park[i]);
      };
      gemm64<true, false, HookP3>(c, x, xb, a0, a1, [&](auto s) {
        if constexpr (s == 33) bar();
        if constexpr (s >= 36 && s < 44) unpark(IC(s - 36));
        if constexpr (s == 49) bar();
        if constexpr (s >= 50 && s < 58) unpark(IC(s - 42));
        if constexpr (s >= 52 && s < 60) dma<0>(lds0 + 8 * SLOT + w * 8192 + (s - 52) * 1024, kvv, wkv + (s - 52) * 32 * 64);
        if constexpr (s >= 60) pc_w(4, a_slot(24), 0, IC(s - 60));
      }, [](auto) {});
      STAMP(9);
      store_x(c, r, a0, a1, xr, acto + 2 * STG, stto + 256 * 1024);
      asm volatile("s_waitcnt vmcnt(5) lgkmcnt(0)\n\ts_barrier" ::: "memory");
      { LAS const char* p = reg + w * 8192 + (4 * h + q4) * 32 + p4 * 8;
#pragma unroll
        for (int i = 0; i < 16; ++i) fu[i] = cat8(trread(p + i * 512), trread(p + i * 512 + 256));
        SBAR();
        float t0 = 0.f, t1 = 0.f, t2 = 0.f, t3 = 0.f;
#pragma unroll
        for (int i = 0; i < 16; ++i) {
          const u32x4 f = __builtin_bit_cast(u32x4, fu[i]);
          t0 = dot2(f[0], 0x3f803f80u, t0); t1 = dot2(f[1], 0x3f803f80u, t1); t2 = dot2(f[2], 0x3f803f80u, t2); t3 = dot2(f[3], 0x3f803f80u, t3);
        }
        cu = opq_v(cs_frag(swap_add((t0 + t1) + (t2 + t3)))); }
      waitv<0>();
      set_flag(facto + 2 * FL_K, tag);
      auto pc_q = [&](auto i) { pc_w(4, a_slot(24 + i / 4), i / 4, IC(i % 4)); };
      sfor<4, 8>(pc_q); sfor<24, 32>(pc_q);
    }
    STAMP(10);
    REFRESH();
    f32x16 a0, a1;
    asm volatile("" : "+s"(kvf), "+s"(fkv), "+s"(xu_p), "+s"(su_p));
    {
      poll<16>(fact + 2 * FL_K, lane * 8, tag);
      bar();
      STAMP(11);
      const char* xb = act + 2 * STG; const char* stp = stt + 256 * 1024;
      bf16x8 x[64], xu[16], mf0, mf1;
      Red<false> su, sp;
      sfor<0, 4>([&](auto i) { ldx1<false, i * 1024>(su.p[i], c.xv, su_p); });
      sfor<0, 16>([&](auto i) { ldx1<true, (i % 8) * 1024 - 4096>(xu[i], c.xv, xu_p + (i / 8) * 8192); });
      f32x16 au, av;
      sfor<0, 16>([&](auto i) {
        waitx<true, 15 - i + (i < XL ? i : XL)>(xu[i]);
        SBAR();
        mm<false, true, i == 0>(au, fu[i], xu[i]);
        if constexpr (i < XL) ld_x<i>(x, c, xb);
        mm<true, true, i == 0>(av, fu[i], xu[i]);
        if constexpr (i < 11) su.template run<i>(lane);
        if constexpr (i == 12) mf0 = opq_v(mean_frag(su.a, h == 0));
        if constexpr (i == 14) { mm<false, false, false>(au, cu, mf0); mm<true, false, false>(av, cu, mf0); }
        SBAR();
      });
      STAMP(12);
      u64 fa;
      f32x4 t[4], ru;
      unsigned vo;
      gemm64<true, true, HookP4>(c, x, xb, a0, a1, [&](auto s) {
        if constexpr (s == 1) {
          pin2(au, av);
          auto put = [&](const f32x16& a) {
#pragma unroll
            for (int gq = 0; gq < 4; ++gq) *(LAS f32x4*)(reg + w * 8192 + gq * 1024 + lane * 16) = f32x4{a[4 * gq], a[4 * gq + 1], a[4 * gq + 2], a[4 * gq + 3]};
          };
          if (ty < 2) { put(au); asm volatile("" ::: "memory"); } else put(av);
        }
        if constexpr (s == 3) {
          lds_bar();
#pragma unroll
          for (int j = 0; j < 4; ++j) t[j] = *(LAS const f32x4*)(reg + j * 8192 + (ty < 2 ? w & 1 : w) * 1024 + lane * 16);
        }
        if constexpr (s == 6) st2_1(__builtin_bit_cast(f32x2, u32x2{cvtpk(t[0][0], t[0][1]), cvtpk(t[0][2], t[0][3])}), vo, kvf);
        if constexpr (s >= 8 && s < 12) ldx1<false, (s - 8) * 1024>(sp.p[s - 8], c.xv, stp);
        if constexpr (s == 7 + XL) { bar(); if (w == 0) set_flag(fkv + rb * 4 * FL_I + cb * 8, tag); }
        if constexpr (s == 52) ld8_1(fa, (lane & 15) * 8 + (w * 4 + (lane >> 4)) * 4 * FL_I, fkv);
      }, [&](auto g) {
        if constexpr (g == 0) {
#pragma unroll
          for (int e = 0; e < 4; ++e) ru[e] = lane_get(su.b, ty < 2 ? r : 8 * w + 4 * h + e);
        }
        if constexpr (g == 4) vo = opq_v(ut * 4096 + (ty < 2 ? (unsigned)(ty * 1024 + lane * 16 + (w & 1) * 8)
                                                               : (unsigned)(2048 + (w >> 1) * 1024 + ((ty & 1) * 16 + (r & 15) + 32 * (w & 1)) * 16 + h * 8)));
        if constexpr (g >= 9 && g < 13) {
#pragma unroll
          for (int e = 0; e < 4; ++e) { float v = t[0][e]; if constexpr (g < 12) a_add(v, t[g - 8][e]); else a_mul(v, ru[e]); t[0][e] = v; }
        }
        if constexpr (g >= 48 && g < 59) sp.template run<g - 48>(lane);
        if constexpr (g == 60) mf0 = opq_v(mean_frag(sp.a, h == 0));
        if constexpr (g == 61) mf1 = opq_v(mean_frag(sp.a, h == 1));
        if constexpr (g == 64) mm<false, false, false>(a0, cw.p[0], mf0);
        if constexpr (g == 65) mm<false, false, false>(a1, cw.p[0], mf1);
      });
      rstd = sp.b;
      waitx<false, 0>(fa);
#pragma unroll 1
      for (int spin = 0; spin < (1 << 15) && !__all(fa == tag); ++spin) { __builtin_amdgcn_s_sleep(8); ld8_1(fa, (lane & 15) * 8 + (w * 4 + (lane >> 4)) * 4 * FL_I, fkv); waitx<false, 0>(fa); }
      bar();
      STAMP(13);
    }
    STAMP(14);
    {
      u32x16 qq, misc;
      misc[14] = misc[15] = __float_as_uint(rstd * QSCALE);
#pragma unroll 1
      for (int attempt = 0; attempt < 2; ++attempt) {
        if (attempt == 1) {
          const bf16x8 qa0 = __builtin_bit_cast(bf16x8, __builtin_shufflevector(qq, qq, 0, 1, 2, 3)), qa1 = __builtin_bit_cast(bf16x8, __builtin_shufflevector(qq, qq, 4, 5, 6, 7));
          const bf16x8 qb0 = __builtin_bit_cast(bf16x8, __builtin_shufflevector(qq, qq, 8, 9, 10, 11)), qb1 = __builtin_bit_cast(bf16x8, __builtin_shufflevector(qq, qq, 12, 13, 14, 15));
          float mA = -INFINITY, mB = -INFINITY;
          for (int t = 0; t < 64; ++t) {
            bf16x8 k0, k1;
            ldx1<false, 0>(k0, c.xv, kvf + t * 4096); ldx1<false, 1024>(k1, c.xv, kvf + t * 4096);
            wait2<0>(k0, k1);
            mm<false, false, true>(a0, k0, qa0); mm<false, false, false>(a0, k1, qa1); mm<false, false, true>(a1, k0, qb0); mm<false, false, false>(a1, k1, qb1);
            mdone(a0, a1);
            mA = fmaxf(mA, max16(a0)); mB = fmaxf(mB, max16(a1));
          }
          misc[14] = __float_as_uint(swap_max(mA)); misc[15] = __float_as_uint(swap_max(mB));
        }
        f32x16 oa, ob, t0, t1, t2, t3;
        u32x16 kvr; u32x4 offs;
        offs[0] = c.xv;
        const char* ks = kvf + 2048 + w * 2048; const char* wsr = wt(5, 0);
        unsigned wm0 = ldsw, cnt = 8;
        asm volatile(
            "global_load_dwordx4 v[208:211], v240, %[kv] sc1\n\tglobal_load_dwordx4 v[212:215], v240, %[kv] offset:1024 sc1\n\t"
            "s_mov_b32 m0, s44\n\tv_add_u32 v241, 0x2000, v240\n\t"
            "global_load_lds_dwordx4 v240, s[40:41] sc1\n\tglobal_load_lds_dwordx4 v240, s[40:41] offset:1024 sc1\n\t"
            "s_add_u32 m0, s44, 0x2000\n\tv_add_u32 v242, 0x4000, v240\n\t"
            "global_load_lds_dwordx4 v241, s[40:41] sc1\n\tglobal_load_lds_dwordx4 v241, s[40:41] offset:1024 sc1\n\t"
            "s_add_u32 m0, s44, 0x4000\n\tv_add_u32 v243, 0x6000, v240\n\t"
            "global_load_lds_dwordx4 v242, s[40:41] sc1\n\tglobal_load_lds_dwordx4 v242, s[40:41] offset:1024 sc1\n\t"
            "s_add_u32 s40, s40, 0x6000\n\ts_addc_u32 s41, s41, 0\n\t"
            "s_cmp_lg_u32 %[att], 0\n\ts_cbranch_scc1 2f\n\t"
            "v_pk_mul_f32 v[128:129], v[128:129], v[238:239]\n\t"
            "v_pk_mul_f32 v[130:131], v[130:131], v[238:239]\n\t"
            "v_pk_mul_f32 v[132:133], v[132:133], v[238:239]\n\t"
            "v_pk_mul_f32 v[134:135], v[134:135], v[238:239]\n\t"
            "v_pk_mul_f32 v[136:137], v[136:137], v[238:239]\n\t"
            "v_pk_mul_f32 v[138:139], v[138:139], v[238:239]\n\t"
            "v_pk_mul_f32 v[140:141], v[140:141], v[238:239]\n\t"
            "v_pk_mul_f32 v[142:143], v[142:143], v[238:239]\n\t"
            "v_pk_mul_f32 v[144:145], v[144:145], v[238:239]\n\t"
            "v_pk_mul_f32 v[146:147], v[146:147], v[238:239]\n\t"
            "v_pk_mul_f32 v[148:149], v[148:149], v[238:239]\n\t"
            "v_pk_mul_f32 v[150:151], v[150:151], v[238:239]\n\t"
            "v_pk_mul_f32 v[152:153], v[152:153], v[238:239]\n\t"
            "v_pk_mul_f32 v[154:155], v[154:155], v[238:239]\n\t"
            "v_pk_mul_f32 v[156:157], v[156:157], v[238:239]\n\t"
            "v_pk_mul_f32 v[158:159], v[158:159], v[238:239]\n\t"
            "v_cvt_pk_bf16_f32 v192, v128, v129\n\t"
            "v_cvt_pk_bf16_f32 v193, v130, v131\n\t"
            "v_cvt_pk_bf16_f32 v194, v132, v133\n\t"
            "v_cvt_pk_bf16_f32 v195, v134, v135\n\t"
            "v_cvt_pk_bf16_f32 v196, v136, v137\n\t"
            "v_cvt_pk_bf16_f32 v197, v138, v139\n\t"
            "v_cvt_pk_bf16_f32 v198, v140, v141\n\t"
            "v_cvt_pk_bf16_f32 v199, v142, v143\n\t"
            "v_cvt_pk_bf16_f32 v200, v144, v145\n\t"
            "v_cvt_pk_bf16_f32 v201, v146, v147\n\t"
            "v_cvt_pk_bf16_f32 v202, v148, v149\n\t"
            "v_cvt_pk_bf16_f32 v203, v150, v151\n\t"
            "v_cvt_pk_bf16_f32 v204, v152, v153\n\t"
            "v_cvt_pk_bf16_f32 v205, v154, v155\n\t"
            "v_cvt_pk_bf16_f32 v206, v156, v157\n\t"
            "v_cvt_pk_bf16_f32 v207, v158, v159\n\t"
            "2:\n\t"
            "v_mov_b64 v[64:65], 0\n\t"
            "v_mov_b64 v[66:67], 0\n\t"
            "v_mov_b64 v[68:69], 0\n\t"
            "v_mov_b64 v[70:71], 0\n\t"
            "v_mov_b64 v[72:73], 0\n\t"
            "v_mov_b64 v[74:75], 0\n\t"
            "v_mov_b64 v[76:77], 0\n\t"
            "v_mov_b64 v[78:79], 0\n\t"
            "v_mov_b64 v[80:81], 0\n\t"
            "v_mov_b64 v[82:83], 0\n\t"
            "v_mov_b64 v[84:85], 0\n\t"
            "v_mov_b64 v[86:87], 0\n\t"
            "v_mov_b64 v[88:89], 0\n\t"
            "v_mov_b64 v[90:91], 0\n\t"
            "v_mov_b64 v[92:93], 0\n\t"
            "v_mov_b64 v[94:95], 0\n\t"
            "v_mov_b64 v[176:177], 0\n\t"
            "v_mov_b64 v[178:179], 0\n\t"
            "v_mov_b64 v[180:181], 0\n\t"
            "v_mov_b64 v[182:183], 0\n\t"
            "v_mov_b64 v[184:185], 0\n\t"
            "v_mov_b64 v[186:187], 0\n\t"
            "v_mov_b64 v[188:189], 0\n\t"
            "v_mov_b64 v[190:191], 0\n\t"
            "v_mov_b64 v[216:217], 0\n\t"
            "v_mov_b64 v[218:219], 0\n\t"
            "v_mov_b64 v[220:221], 0\n\t"
            "v_mov_b64 v[222:223], 0\n\t"
            "v_mov_b64 v[228:229], 0\n\t"
            "v_mov_b64 v[230:231], 0\n\t"
            "v_mov_b64 v[232:233], 0\n\t"
            "v_mov_b64 v[234:235], 0\n\t"
            "v_bfe_u32 v226, v240, 6, 1\n\tv_mov_b32 v236, 0xdddddddd\n\tv_mul_u32_u24 v226, 0x3f80, v226\n\tv_add_u32 v237, %[rg], v240\n\t"
            "v_lshl_or_b32 v226, v226, 16, v226\n\tv_mov_b32 v227, v226\n\tv_xor_b32 v224, 0x3f803f80, v226\n\tv_mov_b32 v225, v224\n\t"
            "s_waitcnt vmcnt(6)\n\ts_cbranch_scc1 3f\n\t"
            "v_mfma_f32_32x32x16_bf16 v[128:143], v[208:211], v[192:195], 0\n\tv_mfma_f32_32x32x16_bf16 v[160:175], v[208:211], v[200:203], 0\n\t"
            "v_mfma_f32_32x32x16_bf16 v[128:143], v[212:215], v[196:199], v[128:143]\n\tv_mfma_f32_32x32x16_bf16 v[160:175], v[212:215], v[204:207], v[160:175]\n\t"
            "s_nop 11\n\t"
            "v_max3_f32 v238, v128, v129, v130\n\tv_max3_f32 v239, v160, v161, v162\n\t"
            "v_max3_f32 v238, v238, v131, v132\n\tv_max3_f32 v239, v239, v163, v164\n\t"
            "v_max3_f32 v238, v238, v133, v134\n\tv_max3_f32 v239, v239, v165, v166\n\t"
            "v_max3_f32 v238, v238, v135, v136\n\tv_max3_f32 v239, v239, v167, v168\n\t"
            "v_max3_f32 v238, v238, v137, v138\n\tv_max3_f32 v239, v239, v169, v170\n\t"
            "v_max3_f32 v238, v238, v139, v140\n\tv_max3_f32 v239, v239, v171, v172\n\t"
            "v_max3_f32 v238, v238, v141, v142\n\tv_max3_f32 v239, v239, v173, v174\n\t"
            "v_max3_f32 v238, v238, v143, v143\n\tv_max3_f32 v239, v239, v175, v175\n\t"
            "v_mov_b32 v96, v238\n\tv_mov_b32 v97, v239\n\ts_nop 1\n\tv_permlane32_swap_b32 v238, v96\n\tv_permlane32_swap_b32 v239, v97\n\t"
            "v_max_f32 v238, v238, v96\n\tv_max_f32 v239, v239, v97\n\t"
            "3:\n\t"
            "v_xor_b32 v96, 0x80000000, v238\n\tv_xor_b32 v112, 0x80000000, v239\n\tv_mov_b32 v97, v96\n\tv_mov_b32 v113, v112\n\t"
            "v_mov_b64 v[98:99], v[96:97]\n\tv_mov_b64 v[114:115], v[112:113]\n\t"
            "v_mov_b64 v[100:101], v[96:97]\n\tv_mov_b64 v[116:117], v[112:113]\n\t"
            "v_mov_b64 v[102:103], v[96:97]\n\tv_mov_b64 v[118:119], v[112:113]\n\t"
            "v_mov_b64 v[104:105], v[96:97]\n\tv_mov_b64 v[120:121], v[112:113]\n\t"
            "v_mov_b64 v[106:107], v[96:97]\n\tv_mov_b64 v[122:123], v[112:113]\n\t"
            "v_mov_b64 v[108:109], v[96:97]\n\tv_mov_b64 v[124:125], v[112:113]\n\t"
            "v_mov_b64 v[110:111], v[96:97]\n\tv_mov_b64 v[126:127], v[112:113]\n\t"
            "s_nop 1\n\t" S0_(128, 192, 96) S1_(128, 192)
            "s_waitcnt vmcnt(4)\n\ts_barrier\n\ts_nop 8\n\t"
            "1:\n\t"
            STEP_(0, 24576, 240, 0, "s_sub_u32 %[cnt], %[cnt], 1\n\t", "") STEP_(8192, 0, 241, 1024, "", "") STEP_(16384, 8192, 242, 2048, "", "")
            STEP_(24576, 16384, 243, 3072, "s_add_u32 s40, s40, 0x8000\n\ts_addc_u32 s41, s41, 0\n\t", "s_add_u32 s42, s42, 0x4000\n\ts_addc_u32 s43, s43, 0\n\t")
            "s_add_u32 s45, s45, 0x4000\n\ts_cmp_lg_u32 %[cnt], 0\n\ts_cbranch_scc1 1b\n\t"
            L0_(232, 176) L1_(232, 176) PV0_(80, 176) PV1_(80, 176)
            "s_nop 11\n\t"
            : "=&{v[64:79]}"(oa), "=&{v[80:95]}"(ob), "=&{v[96:111]}"(t0), "=&{v[112:127]}"(t1), "+{v[128:143]}"(a0), "+{v[144:159]}"(a1), "=&{v[160:175]}"(t2),
              "=&{v[176:191]}"(t3), "+{v[192:207]}"(qq), "=&{v[208:223]}"(kvr), "+{v[224:239]}"(misc), "+{v[240:243]}"(offs),
              "+{s[40:41]}"(ks), "+{s[42:43]}"(wsr), "+{s45}"(wm0), [cnt] "+s"(cnt)
            : "{s44}"(lds0 + 8 * SLOT + w * 2048), [kv] "s"(kvf), [att] "s"(attempt), [rg] "s"(lds0 + 8 * SLOT)
            : "scc", "memory");
        const float la = __uint_as_float(misc[4]), lb = __uint_as_float(misc[8]);
        const float ia = __builtin_amdgcn_rcpf(la), ib = __builtin_amdgcn_rcpf(lb);
#pragma unroll
        for (int e = 0; e < 16; ++e) { oa[e] *= ia; ob[e] *= ib; }
        char* dst = acto + 3 * STG;
        st4_1<0>(__builtin_bit_cast(u32x4, pack8<0>(oa)), c.xv, dst); st4_1<1024>(__builtin_bit_cast(u32x4, pack8<1>(oa)), c.xv, dst);
        st4_1<2048>(__builtin_bit_cast(u32x4, pack8<0>(ob)), c.xv, dst); st4_1<3072>(__builtin_bit_cast(u32x4, pack8<1>(ob)), c.xv, dst);
        float chk = 0.f;
#pragma unroll
        for (int e = 0; e < 16; ++e) chk = __builtin_fmaf(oa[e], 0.f, __builtin_fmaf(ob[e], 0.f, chk));
        const bool bad = !(la < 1.2676506e30f) || !(lb < 1.2676506e30f) || !(chk == 0.f);
        wait_bar<4>();
        LAS volatile int* vote = (LAS volatile int*)(smem + 8 * SLOT);
        if (lane == 0) vote[w] = 0;
        if (attempt == 0 && bad) vote[w] = 1;
        lds_bar();
        const int redo = vote[0] | vote[1] | vote[2] | vote[3];
        if (!__builtin_amdgcn_readfirstlane(redo)) break;
        lds_bar();
      }
      res_to_acc(xr, ra0, ra1);
      waitv<0>();
      set_flag(facto + 3 * FL_K, tag);
    }
    STAMP(15);
    REFRESH();
    {
      f32x16 &a0 = ra0, &a1 = ra1;
      poll<16>(fact + 3 * FL_K, lane * 8, tag);
      STAMP(16);
      bf16x8 x[64];
      const char* xb = act + 3 * STG;
      sfor<0, XL>([&](auto s) { ld_x<s>(x, c, xb); });
      bar();
      gemm64<false, false, HookP6>(c, x, xb, a0, a1, [&](auto s) {
        if constexpr (s == 33) bar();
        if constexpr (s >= 52) pc_a(IC(a_pre(s - 52)));
      }, [](auto) {});
      STAMP(17);
      if (l == 0) {
        store_x(c, r, a0, a1, xr, acto + 4 * STG, stto + 512 * 1024);
        bar();
        sfor<12, 16>([&](auto n) { pc_a(IC(a_pre(n))); });
        waitv<4>();
        set_flag(facto + 4 * FL_K, tag);
        sfor<16, 24>([&](auto n) { pc_a(IC(a_pre(n))); });
      } else {
        LAS char* im = smem + 8 * SLOT + w * 4096;
        sfor<0, 8>([&](auto n) {
          constexpr int g = n;
          const f32x16& a = g < 4 ? a0 : a1;
          *(LAS u32x2*)(im + r * 128 + ((g ^ (r & 7)) << 4) + h * 8) = u32x2{cvtpk(a[4 * (g & 3)], a[4 * (g & 3) + 1]), cvtpk(a[4 * (g & 3) + 2], a[4 * (g & 3) + 3])};
        });
        char* dst = out + (size_t)tt * 65536 + cb * 128;
        sfor<0, 4>([&](auto i) {
          const u32x4 v = *(LAS const u32x4*)(im + i * 1024 + lane * 16);
          st4_1<0>(v, (unsigned)((i * 8 + (lane >> 3)) * 2048 + (((lane & 7) ^ (lane >> 3)) << 4)), dst);
        });
        if (tt == 0 && cb == 0) set_flag(ws + WS_EPOCH, tag);
      }
    }
    STAMP(18);
    if (l == 1) break;
    csm = 1;
    act += 4 * STG; acto += 4 * STG; fact += 4 * FL_K; facto += 4 * FL_K; stt += 512 * KB; stto += 512 * KB;
    kvp += 131072; zp += 8192; fkvp += FL_K; kvf += 320 * KB; fkv += FL_K; xu_p += 4 * STG; su_p += 512 * KB;
    l = 1;
    REFRESH();
    STAMP(0);
        poll<16>(fact, lane * 8, tag);
        STAMP(1);
        sfor<0, XL>(issue_x);
        wait_bar<XL>();
        sfor<0, 2>([&](auto s) { rd01(s, IC(0)); rd01(s, IC(1)); rd2(s); });
        sfor<0, 64>([&](auto s) { step(s, std::false_type{}); });
  }
}

extern "C" void kernel_launch(void* const* d_in, const int* in_sizes, int n_in, void* d_out, int out_size, void* d_ws, size_t ws_size, hipStream_t stream) {
  k_main<<<256, 256, 0, stream>>>((const char*)d_in[0], (const char*)d_in[1], (const char*)d_in[2], (const char*)d_in[3], (const char*)d_in[5], (const char*)d_in[6],
                                  (const char*)d_in[7], (const char*)d_in[8], (char*)d_ws, (char*)d_out);
}
```

```cpp
#include <hip/hip_runtime.h>
#include <hip/hip_bf16.h>
#include <stdint.h>
#include <type_traits>

typedef __attribute__((ext_vector_type(8))) short bf16x8;
typedef __attribute__((ext_vector_type(4))) short s16x4;
typedef __attribute__((ext_vector_type(2))) __bf16 bf2_t;
typedef __attribute__((ext_vector_type(16))) float f32x16;
typedef __attribute__((ext_vector_type(4))) float f32x4;
typedef __attribute__((ext_vector_type(2))) float f32x2;
typedef __attribute__((ext_vector_type(4))) unsigned u32x4;
typedef __attribute__((ext_vector_type(2))) unsigned u32x2;
#define LAS __attribute__((address_space(3)))
#define DI __device__ __forceinline__
#define MFMA32(a, b, c) __builtin_amdgcn_mfma_f32_32x32x16_bf16((a), (b), (c), 0, 0, 0)
#define MFMA16(a, b, c) __builtin_amdgcn_mfma_f32_16x16x32_bf16((a), (b), (c), 0, 0, 0)
#define SBAR() __builtin_amdgcn_sched_barrier(0)

constexpr int S_ = 2048, D_ = 1024;
constexpr float LOG2E = 1.4426950408889634f;
constexpr float QSCALE = 0.17677669529663687f * LOG2E;

constexpr size_t MB = 1u << 20, KB = 1024;
constexpr size_t STG = 256 * KB;
constexpr size_t WS_COL = 32 * MB;
constexpr size_t C_WT = 0;
constexpr size_t C_KVP = 768 * KB;
constexpr size_t C_ZP = 1024 * KB;
constexpr size_t C_CS = 1040 * KB;
constexpr size_t WS_ST = 64 * MB;
constexpr size_t WS_KVF = WS_ST + 1024 * KB;
constexpr size_t WS_FLAG = WS_ST + 1664 * KB;
constexpr size_t WS_EPOCH = WS_ST + 1920 * KB;
constexpr int FL_I = 2048, FL_K = 128;
typedef unsigned long long u64;

template <int I, int N, class F> DI void sfor(F&& f) { if constexpr (I < N) { f(std::integral_constant<int, I>{}); sfor<I + 1, N>(f); } }

DI unsigned cvtpk(float lo, float hi) { f32x2 v = {lo, hi}; return __builtin_bit_cast(unsigned, __builtin_convertvector(v, bf2_t)); }
DI float bflo(unsigned u) { return __uint_as_float(u << 16); }
DI float bfhi(unsigned u) { return __uint_as_float(u & 0xffff0000u); }
DI float rbf(float x) { return bflo(cvtpk(x, x)); }
DI float swap_add(float v) { auto r = __builtin_amdgcn_permlane32_swap(__float_as_uint(v), __float_as_uint(v), false, false); return __uint_as_float(r[0]) + __uint_as_float(r[1]); }
DI float swap_max(float v) { auto r = __builtin_amdgcn_permlane32_swap(__float_as_uint(v), __float_as_uint(v), false, false); return fmaxf(__uint_as_float(r[0]), __uint_as_float(r[1])); }
DI float swap_min(float v) { auto r = __builtin_amdgcn_permlane32_swap(__float_as_uint(v), __float_as_uint(v), false, false); return fminf(__uint_as_float(r[0]), __uint_as_float(r[1])); }
template <int S> DI bf16x8 pack8(const f32x16& v) {
  u32x4 p = {cvtpk(v[8 * S], v[8 * S + 1]), cvtpk(v[8 * S + 2], v[8 * S + 3]), cvtpk(v[8 * S + 4], v[8 * S + 5]), cvtpk(v[8 * S + 6], v[8 * S + 7])};
  return __builtin_bit_cast(bf16x8, p);
}
DI u32x4 unperm(u32x4 v) {
  auto r0 = __builtin_amdgcn_permlane32_swap(v[0], v[2], false, false);
  auto r1 = __builtin_amdgcn_permlane32_swap(v[1], v[3], false, false);
  return u32x4{r0[0], r1[0], r0[1], r1[1]};
}
DI float lane_get(float v, int src) { return __uint_as_float(__builtin_amdgcn_ds_bpermute(src << 2, __float_as_uint(v))); }
DI s16x4 trread(LAS const char* p) { return __builtin_amdgcn_ds_read_tr16_b64_v4i16((LAS s16x4*)p); }
DI bf16x8 cat8(s16x4 a, s16x4 b) { return __builtin_shufflevector(a, b, 0, 1, 2, 3, 4, 5, 6, 7); }
DI float dot2(unsigned a, unsigned b, float c) { return __builtin_amdgcn_fdot2_f32_bf16(__builtin_bit_cast(bf2_t, a), __builtin_bit_cast(bf2_t, b), c, false); }

template <int OFF> DI void dma(unsigned lds, unsigned v, const char* sb) {
  asm volatile("s_mov_b32 m0, %0\n\ts_nop 0\n\tglobal_load_lds_dwordx4 %1, %2 offset:%3" ::"s"(lds), "v"(v), "s"(sb), "i"(OFF) : "memory");
}
template <int OFF> DI void dma_c(unsigned lds, unsigned v, const char* sb) {
  asm volatile("s_mov_b32 m0, %0\n\ts_nop 0\n\tglobal_load_lds_dwordx4 %1, %2 offset:%3 sc1" ::"s"(lds), "v"(v), "s"(sb), "i"(OFF) : "memory");
}
template <int OFF> DI void dma_n(unsigned v, const char* sb) {
  asm volatile("global_load_lds_dwordx4 %0, %1 offset:%2 sc1" ::"v"(v), "s"(sb), "i"(OFF) : "memory");
}
constexpr int cap63(int n) { return n > 63 ? 63 : n; }
template <int N> DI void wait_bar() { asm volatile("s_waitcnt vmcnt(%0)\n\ts_barrier" ::"i"(cap63(N)) : "memory"); }
DI void lds_bar() { asm volatile("s_waitcnt lgkmcnt(0)\n\ts_barrier" ::: "memory"); }
DI void all_bar() { asm volatile("s_waitcnt vmcnt(0) lgkmcnt(0)\n\ts_barrier" ::: "memory"); }
DI void bar() { asm volatile("s_barrier" ::: "memory"); }
template <bool AG, int OFF> DI void ldx1(bf16x8& d, unsigned voff, const char* sb) {
  if constexpr (AG) asm volatile("global_load_dwordx4 %0, %1, %2 offset:%3 sc1" : "=a"(d) : "v"(voff), "s"(sb), "i"(OFF));
  else asm volatile("global_load_dwordx4 %0, %1, %2 offset:%3 sc1" : "=v"(d) : "v"(voff), "s"(sb), "i"(OFF));
}
template <int OFF> DI void ld2_1(f32x2& d, unsigned voff, const char* sb) { asm volatile("global_load_dwordx2 %0, %1, %2 offset:%3 sc1" : "=v"(d) : "v"(voff), "s"(sb), "i"(OFF)); }
DI void ld1_1(unsigned& d, unsigned voff, const void* sb) { asm volatile("global_load_dword %0, %1, %2 sc1" : "=v"(d) : "v"(voff), "s"(sb) : "memory"); }
template <int OFF> DI void st4_1(u32x4 d, unsigned voff, char* sb) { asm volatile("global_store_dwordx4 %0, %1, %2 offset:%3 sc1\n\ts_nop 1" ::"v"(voff), "v"(d), "s"(sb), "i"(OFF) : "memory"); }
DI void st2_1(f32x2 d, unsigned voff, char* sb) { asm volatile("global_store_dwordx2 %0, %1, %2 sc1" ::"v"(voff), "v"(d), "s"(sb) : "memory"); }
DI void st1_1(float d, unsigned voff, char* sb) { asm volatile("global_store_dword %0, %1, %2 sc1" ::"v"(voff), "v"(d), "s"(sb) : "memory"); }
template <int N> DI void waitv() { asm volatile("s_waitcnt vmcnt(%0)" ::"i"(cap63(N)) : "memory"); }
template <bool AG, int N, class T> DI void waitx(T& d) {
  if constexpr (AG) asm volatile("s_waitcnt vmcnt(%1)" : "+a"(d) : "i"(cap63(N))); else asm volatile("s_waitcnt vmcnt(%1)" : "+v"(d) : "i"(cap63(N)));
}
template <bool SW, bool BA, bool Z, bool NOP = false> DI void mm(f32x16& acc, const bf16x8& a, const bf16x8& b) {
#define MM_(OPS, C, OUT, BC) do { if constexpr (NOP) asm volatile("s_nop 1\n\tv_mfma_f32_32x32x16_bf16 %0, " OPS ", " C : OUT(acc) : "v"(a), BC(b)); \
                                  else asm volatile("v_mfma_f32_32x32x16_bf16 %0, " OPS ", " C : OUT(acc) : "v"(a), BC(b)); } while (0)
  if constexpr (Z) {
    if constexpr (SW) { if constexpr (BA) MM_("%2, %1", "0", "=&v", "a"); else MM_("%2, %1", "0", "=&v", "v"); }
    else { if constexpr (BA) MM_("%1, %2", "0", "=&v", "a"); else MM_("%1, %2", "0", "=&v", "v"); }
  } else {
    if constexpr (SW) { if constexpr (BA) MM_("%2, %1", "%0", "+v", "a"); else MM_("%2, %1", "%0", "+v", "v"); }
    else { if constexpr (BA) MM_("%1, %2", "%0", "+v", "a"); else MM_("%1, %2", "%0", "+v", "v"); }
  }
#undef MM_
}
DI void mdone(f32x16& a) { asm volatile("s_nop 11" : "+v"(a)); }
DI void mdone(f32x16& a, f32x16& b) { asm volatile("s_nop 11" : "+v"(a), "+v"(b)); }
DI void mdone(f32x16& a, f32x16& b, f32x16& c, f32x16& d) { asm volatile("s_nop 11" : "+v"(a), "+v"(b), "+v"(c), "+v"(d)); }
template <class T> DI T opq_v(T v) { asm volatile("" : "+v"(v)); return v; }
template <int OFF> DI void ldx0(bf16x8& d, unsigned voff, const char* sb) { asm volatile("global_load_dwordx4 %0, %1, %2 offset:%3" : "=v"(d) : "v"(voff), "s"(sb), "i"(OFF)); }
template <int OFF> DI void ldx1m(bf16x8& d, unsigned voff, const char* sb, unsigned long long m) {
  asm volatile("s_mov_b64 exec, %4\n\tglobal_load_dwordx4 %0, %1, %2 offset:%3 sc1\n\ts_mov_b64 exec, -1" : "=v"(d) : "v"(voff), "s"(sb), "i"(OFF), "s"(m));
}
template <bool AG, int OFF8> DI void ds_read2(bf16x8& d, unsigned addr) {
  addr += OFF8 * 8;
  if constexpr (AG) asm volatile("ds_read2_b64 %0, %1 offset1:2" : "=a"(d) : "v"(addr) : "memory"); else asm volatile("ds_read2_b64 %0, %1 offset1:2" : "=v"(d) : "v"(addr) : "memory");
}
template <bool AG, int N> DI void waitl(bf16x8& d) {
  if constexpr (AG) asm volatile("s_waitcnt lgkmcnt(%1)" : "+a"(d) : "i"(N) : "memory"); else asm volatile("s_waitcnt lgkmcnt(%1)" : "+v"(d) : "i"(N) : "memory");
}
template <int OFF> DI void ds_write_a(unsigned addr, const bf16x8& v) { asm volatile("ds_write_b128 %0, %1 offset:%2" ::"v"(addr), "a"(v), "i"(OFF) : "memory"); }
template <int N, class T> DI void wait2(T& a, T& b) { asm volatile("s_waitcnt vmcnt(%2)" : "+v"(a), "+v"(b) : "i"(cap63(N))); }
DI void set_flag(char* flag, u64 tag) {
  unsigned z = 0;
  asm volatile("s_mov_b64 exec, 1\n\tglobal_atomic_swap_x2 %0, %1, %2 sc1\n\ts_mov_b64 exec, -1" ::"v"(z), "v"(tag), "s"(flag) : "memory");
}
DI void ld8_1(u64& d, unsigned voff, const char* sb) { asm volatile("global_load_dwordx2 %0, %1, %2 sc1" : "=v"(d) : "v"(voff), "s"(sb) : "memory"); }
template <int NL> DI void rd_flag(u64& v, unsigned voff, const char* sb) {
  const u64 zero = 0;
  if constexpr (NL == 64) asm volatile("global_atomic_or_x2 %0, %1, %3, %2 sc0 sc1" : "+v"(v) : "v"(voff), "s"(sb), "v"(zero) : "memory");
  else asm volatile("s_mov_b64 exec, %4\n\tglobal_atomic_or_x2 %0, %1, %3, %2 sc0 sc1\n\ts_mov_b64 exec, -1" : "+v"(v) : "v"(voff), "s"(sb), "v"(zero), "s"((1ull << (NL & 63)) - 1) : "memory");
}
template <int NL> DI void poll(const char* base, unsigned voff, u64 tag) {
  for (int spin = 0; spin < (1 << 15); ++spin) {
    u64 v = tag;
    rd_flag<NL>(v, voff, base);
    waitx<false, 0>(v);
    if (__all(v == tag)) break;
  }
  asm volatile("" ::: "memory");
}

template <int RBX> DI void block_map(int& rb, int& cb) {
  const int x = blockIdx.x & 7, i = blockIdx.x >> 3;
  constexpr int CBX = 32 / RBX, GC = 16 / CBX;
  rb = RBX * (x / GC) + i / CBX; cb = CBX * (x % GC) + i % CBX;
}

DI float a_fma(float a, float b, float c) { float d; asm volatile("v_fma_f32 %0, %1, %2, %3" : "=v"(d) : "v"(a), "v"(b), "v"(c)); return d; }
DI float a_fms(float a, float b, float c) { float d; asm volatile("v_fma_f32 %0, %1, -%2, %3" : "=v"(d) : "v"(a), "v"(b), "v"(c)); return d; }
DI void a_exp(float& x) { asm volatile("v_exp_f32 %0, %0" : "+v"(x)); }
DI void a_add(float& s, float x) { asm volatile("v_add_f32 %0, %0, %1" : "+v"(s) : "v"(x)); }
DI void a_mul(float& x, float s) { asm volatile("v_mul_f32 %0, %0, %1" : "+v"(x) : "v"(s)); }
template <bool NEG> DI void a_hedge(float& c, float& n, float& s, float prev, float p, float sc, float m) {
  if constexpr (NEG) asm volatile("v_exp_f32 %0, %0\n\tv_fma_f32 %1, %4, -%5, %6\n\tv_add_f32 %2, %2, %3" : "+v"(c), "=&v"(n), "+v"(s) : "v"(prev), "v"(p), "v"(sc), "v"(m));
  else asm volatile("v_exp_f32 %0, %0\n\tv_fma_f32 %1, %4, %5, %6\n\tv_add_f32 %2, %2, %3" : "+v"(c), "=&v"(n), "+v"(s) : "v"(prev), "v"(p), "v"(sc), "v"(m));
}
DI unsigned a_norm2(float a, float b, float s) {
  unsigned o; float t;
  asm volatile("v_mul_f32 %0, %2, %4\n\tv_mul_f32 %1, %3, %4\n\tv_cvt_pk_bf16_f32 %0, %0, %1" : "=&v"(o), "=&v"(t) : "v"(a), "v"(b), "v"(s));
  return o;
}
DI void a_sum16(f32x4& s, const bf16x8& sel, const bf16x8& f) { asm volatile("v_mfma_f32_16x16x32_bf16 %0, %1, %2, %0" : "+v"(s) : "v"(sel), "v"(f)); }
DI void a_mm4(float& mx, float& mn, float a, float b, float c, float d) {
  asm volatile("v_max3_f32 %0, %0, %2, %3\n\tv_min3_f32 %1, %1, %2, %3\n\tv_max3_f32 %0, %0, %4, %5\n\tv_min3_f32 %1, %1, %4, %5" : "+v"(mx), "+v"(mn) : "v"(a), "v"(b), "v"(c), "v"(d));
}
DI float a_max3(float a, float b, float c) { float d; asm volatile("v_max3_f32 %0, %1, %2, %3" : "=v"(d) : "v"(a), "v"(b), "v"(c)); return d; }
DI float a_min3(float a, float b, float c) { float d; asm volatile("v_min3_f32 %0, %1, %2, %3" : "=v"(d) : "v"(a), "v"(b), "v"(c)); return d; }

DI float opq_a(float v) { asm volatile("" : "+a"(v)); return v; }
DI void pin_a(bf16x8& a) { asm volatile("" : "+a"(a)); }
DI void pin2(f32x16& a, f32x16& b) { asm volatile("" : "+v"(a), "+v"(b)); }
DI bf16x8 cs_frag(float c) {
  const float hi = rbf(c), mid = rbf(c - hi), lo = (c - hi) - mid;
  return __builtin_bit_cast(bf16x8, u32x4{cvtpk(hi, mid), cvtpk(lo, 0.f), 0u, 0u});
}
template <bool CS> struct Red {
  bf16x8 p[4];
  float t[4], a, b, sd;
  template <int K> DI void run(int lane) {
    if constexpr (K == 0) asm volatile("" : "+v"(p[0]), "+v"(p[1]), "+v"(p[2]), "+v"(p[3]));
    if constexpr (K < 3) {
      const f32x4 u = __builtin_bit_cast(f32x4, p[K + 1]), v = __builtin_bit_cast(f32x4, p[0]);
#pragma unroll
      for (int e = 0; e < 4; ++e) { if (K == 0) t[e] = v[e]; a_add(t[e], u[e]); }
    }
    if constexpr (K == 3) {
      auto x = __builtin_amdgcn_permlane16_swap(__float_as_uint(t[0]), __float_as_uint(t[1]), false, false);
      auto y = __builtin_amdgcn_permlane16_swap(__float_as_uint(t[2]), __float_as_uint(t[3]), false, false);
      a = opq_v(__uint_as_float(x[0]) + __uint_as_float(x[1])); b = opq_v(__uint_as_float(y[0]) + __uint_as_float(y[1]));
    }
    if constexpr (K == 4) { auto x = __builtin_amdgcn_permlane32_swap(__float_as_uint(a), __float_as_uint(b), false, false); a = opq_v(__uint_as_float(x[0]) + __uint_as_float(x[1])); }
    if constexpr (K == 5) {
      if constexpr (CS) a = lane_get(a, (lane >> 2) + ((lane & 3) << 4));
      else { const int src = ((lane & 31) >> 1) + ((lane & 1) << 5); b = lane_get(a, src + 16); a = lane_get(a, src); }
    }
    if constexpr (CS) {
      if constexpr (K == 8) { t[0] = opq_v(rbf(a)); t[1] = opq_v(a - t[0]); }
      if constexpr (K == 9) { const float mid = rbf(t[1]); p[0] = opq_v(__builtin_bit_cast(bf16x8, u32x4{cvtpk(t[0], mid), cvtpk(t[1] - mid, 0.f), 0u, 0u})); }
    } else {
      if constexpr (K == 8) { const float m = a * (1.f / D_); b = opq_v(fmaxf(b * (1.f / D_) - m * m, 0.f)); a = opq_v(rbf(m)); }
      if constexpr (K == 9) b = opq_v(rbf(rbf(b) + 1e-5f));
      if constexpr (K == 10) { sd = opq_v(rbf(__builtin_amdgcn_sqrtf(b))); b = opq_v(__builtin_amdgcn_rcpf(sd)); }
    }
  }
};
DI void pin4(bf16x8 (&p)[4]) { asm volatile("" : "+v"(p[0]), "+v"(p[1]), "+v"(p[2]), "+v"(p[3])); }
DI bf16x8 mean_frag(float mean, bool on) { const float m = on ? -mean : 0.f; return __builtin_bit_cast(bf16x8, u32x4{cvtpk(m, m), cvtpk(m, 0.f), 0u, 0u}); }

#define STAMP(i)

constexpr int CODE_KB = 50;
constexpr int SLOT = 16384, XL = 12;
struct WinPtr {
  unsigned b[3];
  DI void init(unsigned a) { sfor<0, 3>([&](auto i) { b[i] = a + i * 65536u; asm volatile("" : "+v"(b[i])); }); }
  template <int OFF> DI bf16x8 frag() const { return *(LAS const bf16x8*)((LAS const char*)(uintptr_t)b[OFF >> 16] + (OFF & 65535)); }
};
struct Ctx { unsigned xv; WinPtr p; };
#define IC(n) std::integral_constant<int, (n)>{}

template <int S> DI void ld_x(bf16x8 (&x)[64], const Ctx& c, const char* xb) { ldx1<false, (S % 8) * 1024 - 4096>(x[S], c.xv, xb + (S / 8) * 8192 + 4096); }
template <class H> constexpr int g64_wait(int s) {
  int n = 0;
  const int t0 = s - XL;
  if (t0 < 0) n += XL - 1 - s; else n += H::n(t0);
  for (int t = t0 < 0 ? 0 : t0 + 1; t < s; ++t) n += (t + XL < 64 ? 1 : 0) + H::n(t);
  return n;
}
constexpr int a_slot(int g);
template <bool MAPA, bool ZERO, class H, class FX, class FA>
DI void gemm64(const Ctx& c, bf16x8 (&x)[64], const char* xb, f32x16& acc0, f32x16& acc1, FX&& hook, FA&& aux) {
  bf16x8 f0[3], f1[3];
  auto off = [](int s) constexpr { return (MAPA ? a_slot(24 + s / 8) : s / 8) * SLOT + (s % 8) * 2048; };
  auto rd0 = [&](auto s) { f0[s % 3] = c.p.template frag<off(s)>(); };
  auto rd1 = [&](auto s) { f1[s % 3] = c.p.template frag<off(s) + 1024>(); };
  rd0(IC(0)); rd1(IC(0)); rd0(IC(1)); rd1(IC(1));
  sfor<0, 64>([&](auto s) {
    waitx<false, g64_wait<H>(s)>(x[s]);
    SBAR();
    mm<false, false, ZERO && s == 0>(acc0, f0[s % 3], x[s]);
    SBAR();
    if constexpr (s + 2 < 64) rd0(IC(s + 2));
    if constexpr (s + XL < 64) ld_x<s + XL>(x, c, xb);
    aux(IC(2 * s));
    SBAR();
    mm<false, false, ZERO && s == 0>(acc1, f1[s % 3], x[s]);
    SBAR();
    if constexpr (s + 2 < 64) rd1(IC(s + 2));
    hook(s);
    aux(IC(2 * s + 1));
    SBAR();
  });
  mdone(acc0, acc1);
}
struct HookP6 { static constexpr int n(int s) { return s >= 52 ? 1 : 0; } };
struct HookP3 { static constexpr int n(int s) { return s >= 52 ? 1 : 0; } };
struct HookP4 { static constexpr int n(int s) { return s == 52 || s == 6 || (s >= 8 && s < 12) ? 1 : 0; } };

DI void park4(u32x4 (&v)[4]) { asm volatile("" : "+a"(v[0]), "+a"(v[1]), "+a"(v[2]), "+a"(v[3])); }
DI void res_to_acc(u32x4 (&xr)[4], f32x16& a0, f32x16& a1) {
  park4(xr);
#pragma unroll
  for (int e = 0; e < 4; ++e) {
    a0[2 * e] = bflo(xr[0][e]); a0[2 * e + 1] = bfhi(xr[0][e]); a0[8 + 2 * e] = bflo(xr[1][e]); a0[9 + 2 * e] = bfhi(xr[1][e]);
    a1[2 * e] = bflo(xr[2][e]); a1[2 * e + 1] = bfhi(xr[2][e]); a1[8 + 2 * e] = bflo(xr[3][e]); a1[9 + 2 * e] = bfhi(xr[3][e]);
  }
  asm volatile("" : "+v"(a0), "+v"(a1));
}
DI void store_stats(int r, const u32x4 (&xr)[4], char* stp) {
  float s1 = 0.f, s2 = 0.f, s3 = 0.f, s4 = 0.f;
#pragma unroll
  for (int i = 0; i < 4; ++i)
#pragma unroll
    for (int e = 0; e < 4; e += 2) {
      s1 = dot2(xr[i][e], 0x3f803f80u, s1); s2 = dot2(xr[i][e], xr[i][e], s2);
      s3 = dot2(xr[i][e + 1], 0x3f803f80u, s3); s4 = dot2(xr[i][e + 1], xr[i][e + 1], s4);
    }
  st2_1(f32x2{swap_add(s1 + s3), swap_add(s2 + s4)}, (unsigned)(r * 8), stp);
}
DI void store_x(const Ctx& c, int r, const f32x16& a0, const f32x16& a1, u32x4 (&xr)[4], char* dst, char* stp) {
  SBAR();
  xr[0] = __builtin_bit_cast(u32x4, pack8<0>(a0)); st4_1<0>(xr[0], c.xv, dst); SBAR();
  xr[1] = __builtin_bit_cast(u32x4, pack8<1>(a0)); st4_1<1024>(xr[1], c.xv, dst); SBAR();
  xr[2] = __builtin_bit_cast(u32x4, pack8<0>(a1)); st4_1<2048>(xr[2], c.xv, dst); SBAR();
  xr[3] = __builtin_bit_cast(u32x4, pack8<1>(a1)); st4_1<3072>(xr[3], c.xv, dst); SBAR();
  store_stats(r, xr, stp);
  SBAR();
  park4(xr);
}

template <class FX> DI void hedge_exp(const f32x16& p, float sc, f32x16& ep, f32x16& en, FX&& hook) {
  float mx = fmaxf(p[0], p[1]), mn = fminf(p[0], p[1]);
#pragma unroll
  for (int i = 2; i < 16; i += 2) { mx = fmaxf(fmaxf(mx, p[i]), p[i + 1]); mn = fminf(fminf(mn, p[i]), p[i + 1]); }
  mx = swap_max(mx) * sc; mn = swap_min(mn) * sc;
  sfor<0, 16>([&](auto i) {
    SBAR();
    hook(i);
    constexpr int e = i;
    float a = __builtin_amdgcn_exp2f(__builtin_fmaf(p[e], sc, -mx)), b = __builtin_amdgcn_exp2f(__builtin_fmaf(p[e], -sc, mn));
    asm volatile("" : "+v"(a), "+v"(b));
    ep[e] = a; en[e] = b;
  });
  SBAR();
}

constexpr int a_slot(int g) { return g == 8 || g == 15 ? 8 : (g > 8 && g < 15 ? g - 9 : g % 8); }
constexpr bool a_bar(int s) { return (s + 2) % 8 == 0 && s < 190; }
constexpr int QP = 128 + 24, QC = 128 + 36;
constexpr int a_piece(int s, bool first) {
  constexpr int t[][3] = {{46, 8, 1}, {54, 9, 1}, {58, 10, 1}, {64, 11, 2}, {72, 12, 2}, {80, 13, 2}, {88, 14, 2}, {96, 15, 2}, {104, 16, 1}, {108, 17, 1}, {112, 18, 1},
                          {116, 19, 1}, {120, 20, 1}, {134, 21, 1}, {138, 22, 1}, {142, 23, 1}, {146, 24, 1}, {150, 25, 1}, {158, 26, 1}};
  if (!first && s >= 8 && s < 24 && s % 2 == 0) return 24 + (s - 8) / 2;
  for (auto& e : t) if (s >= e[0] && s < e[0] + 4 * e[2] && (s - e[0]) % e[2] == 0) return e[1] * 4 + (s - e[0]) / e[2];
  return -1;
}
constexpr int a_park(int s) { return s >= 129 && s < 135 ? s - 129 : (s == 162 || s == 163 ? s - 156 : -1); }
constexpr int a_ld0(bool first) { return first ? 32 : 2; }
constexpr bool a_side(int s, bool first) { return (s >= a_ld0(first) && s < a_ld0(first) + (first ? 12 : 4)) || s == 50 || (s >= 154 && s < 158) || a_park(s) >= 0; }
constexpr int f_rd(int n) { return n % 6 < 4 && n / 6 + 1 < 16 ? (n / 6 + 1) * 4 + n % 6 : -1; }
constexpr int f_in(int n) { return (n + 1) % 6 < 4 && (n + 1) / 6 >= 1 && (n + 1) / 6 + 4 < 16 ? ((n + 1) / 6 + 4) * 4 + (n + 1) % 6 : -1; }
constexpr int f_raw(int s) { return s >= 6 && s < 46 && s % 2 == 0 ? ((s + 2) / 8 + 2) * 4 + ((s + 2) % 8) / 2 : -1; }
struct SchedA {
  int xw[64], bw[192], sw[3], aw, fw, cw, pw, zw, uw, vw, lw[32];
  constexpr SchedA(bool first) : xw{}, bw{}, sw{}, aw(0), fw(0), cw(0), pw(0), zw(0), uw(0), vw(0), lw{} {
    int pos = 0, xpos[64] = {}, wpos[32] = {}, spos[192] = {}, fpos = 0, stpos = 0, ppos = 0, upos = 0, vpos = 0, lpos[34] = {};
    if (first) { xpos[3] = 4; xpos[4] = pos = 8; } else for (int s = 0; s < XL; ++s) xpos[s] = ++pos;
    for (int s = 0; s < 192; ++s) {
      if (s == 128) { pos += 3; stpos = pos; }
      if (a_bar(s)) {
        const int g = (s + 2) / 8;
        bw[s] = wpos[g] ? pos - wpos[g] : 63;
        if (s == 62 && pos - spos[50] < bw[s]) bw[s] = pos - spos[50];
        if (s == 142) { if (pos - stpos < bw[s]) bw[s] = pos - stpos; ++pos; }
      }
      if (s == 102) uw = pos - upos;
      if (s == 118) vw = pos - vpos;
      for (int k = 0; k < 3; ++k) if (s == a_ld0(first) + 14 + 4 * k) sw[k] = pos - spos[a_ld0(first) + 3 + 4 * k];
      if (s == 32) aw = pos - fpos;
      if (s == 128) fw = pos - fpos;
      if (s == 170) cw = pos - spos[157];
      if (s == QC) pw = pos - ppos;
      if (s < 64) xw[s] = first ? (s % 3 == 0 && s / 3 + 1 >= 3 && s / 3 + 1 < 16 ? pos - xpos[s / 3 + 1] : 63) : pos - xpos[s];
      if (first && s < 64 && f_in(2 * s) >= 0) xpos[f_in(2 * s) / 4] = ++pos;
      if (!first && s + XL < 64) xpos[s + XL] = ++pos;
      if ((first && s == 20) || s == 127) fpos = ++pos;
      if (s == QP) ppos = ++pos;
      if (s >= QC) lpos[s - QC] = ++pos;
      if (a_piece(s, first) >= 0) wpos[a_piece(s, first) / 4] = ++pos;
      if (a_side(s, first)) spos[s] = ++pos;
      if (first && s < 64 && f_in(2 * s + 1) >= 0) xpos[f_in(2 * s + 1) / 4] = ++pos;
      if (first && f_raw(s) >= 0) wpos[f_raw(s) / 4] = ++pos;
      if (s == 87) upos = pos;
      if (s == 103) vpos = pos;
      if (s == 177) zw = pos - lpos[1];
    }
    for (int i = 0; i < 32; ++i) {
      lw[i] = pos - lpos[2 + i];
      if (i % 2 == 0 && 192 - QC + i / 2 < 34) lpos[192 - QC + i / 2] = ++pos;
      if (i % 2 == 0 && i >= 16) ++pos;
    }
  }
};
constexpr int cmin(int a, int b) { return a < b ? a : b; }

#define R_OA "v[64:79]"
#define R_OB "v[80:95]"
#define S0_(n, q, m) "v_mfma_f32_32x32x16_bf16 v[" #n ":" #n "+15], v[208:211], v[" #q ":" #q "+3], v[" #m ":" #m "+15]\n\t"
#define S1_(n, q) "v_mfma_f32_32x32x16_bf16 v[" #n ":" #n "+15], v[212:215], v[" #q "+4:" #q "+7], v[" #n ":" #n "+15]\n\t"
#define PV0_(o, p) "v_smfmac_f32_32x32x32_bf16 v[" #o ":" #o "+15], v[216:219], v[" #p ":" #p "+7], v236\n\t"
#define PV1_(o, p) "v_smfmac_f32_32x32x32_bf16 v[" #o ":" #o "+15], v[220:223], v[" #p "+8:" #p "+15], v236\n\t"
#define L0_(l, p) "v_smfmac_f32_16x16x64_bf16 v[" #l ":" #l "+3], v[224:227], v[" #p ":" #p "+7], v236\n\t"
#define L1_(l, p) "v_smfmac_f32_16x16x64_bf16 v[" #l ":" #l "+3], v[224:227], v[" #p "+8:" #p "+15], v236\n\t"
#define E_(e, i) "v_exp_f32 v[" #e "+" #i "], v[" #e "+" #i "]\n\t"
#define RD_(d, off) "ds_read_b128 v[" #d ":" #d "+3], v237 offset:" #off "\n\t"
#define HALF_(e, n, q, m, p, o, l, X0, X1, X2, X3, X4, X5) \
  S0_(n, q, m) X0 E_(e, 0) E_(e, 1) E_(e, 2) S1_(n, q) X1 E_(e, 3) E_(e, 4) E_(e, 5) PV0_(o, p) X2 E_(e, 6) E_(e, 7) E_(e, 8) \
  PV1_(o, p) X3 E_(e, 9) E_(e, 10) E_(e, 11) L0_(l, p) X4 E_(e, 12) E_(e, 13) L1_(l, p) X5 E_(e, 14) E_(e, 15)
#define LG2_ "s_waitcnt lgkmcnt(2)\n\t"
#define LG0_ "s_waitcnt lgkmcnt(0)\n\t"
#define STEP_(b, nb, kv, wo, Y0, Y1) \
  HALF_(128, 160, 200, 112, 176, 80, 232, "", "", RD_(208, b + 2048), RD_(212, b + 3072), RD_(216, b), RD_(220, b + 1024) LG2_) \
  HALF_(160, 144, 192, 96, 128, 64, 228, "", LG0_, "s_add_u32 m0, s44, " #nb "\n\t", "", "global_load_lds_dwordx4 v" #kv ", s[40:41] sc1\n\t", \
        "global_load_lds_dwordx4 v" #kv ", s[40:41] offset:1024 sc1\n\t") \
  HALF_(144, 176, 200, 112, 160, 80, 232, Y0, "", RD_(208, b + 6144), RD_(212, b + 7168), RD_(216, b + 4096), RD_(220, b + 5120) LG2_) \
  HALF_(176, 128, 192, 96, 144, 64, 228, "s_mov_b32 m0, s45\n\t", LG0_, "global_load_lds_dwordx4 v240, s[42:43] offset:" #wo " sc1\n\t", Y1, \
        "s_waitcnt vmcnt(4)\n\t", "s_barrier\n\t")
DI float max16(const f32x16& s) {
  float mx = s[0];
#pragma unroll
  for (int e = 1; e < 16; ++e) mx = fmaxf(mx, s[e]);
  return mx;
}
typedef __attribute__((ext_vector_type(8))) unsigned u32x8;
typedef __attribute__((ext_vector_type(16))) unsigned u32x16;

__global__ __launch_bounds__(256, 1) void k_main(const char* xin, const char* wqkv, const char* wphi, const char* woA, const char* wq, const char* wk, const char* wv,
                                                 const char* woB, char* ws, char* out) {
  __shared__ __attribute__((aligned(16))) char smem_[163840];
  LAS char* const smem = (LAS char*)smem_;
  const unsigned lds0 = (unsigned)(uintptr_t)smem;
  Ctx c;
  int lane = threadIdx.x & 63, w = __builtin_amdgcn_readfirstlane(threadIdx.x >> 6), r = lane & 31, h = lane >> 5, rb, cb;
  block_map<4>(rb, cb);
  int tt = rb * 4 + w, blk = (lane >> 4) & 1, q4 = (lane & 15) >> 2, p4 = lane & 3;
  c.xv = lane * 16; c.p.init(lds0 + c.xv);
  char* flags = ws + WS_FLAG;
  char* col = ws + WS_COL + (size_t)cb * 2 * MB;
  const int ut = rb * 4 + (cb >> 2), ty = cb & 3;
  char* act = ws + (size_t)rb * 2 * MB + w * 65536;
  char* acto = act + cb * 4096;
  char* fact = flags + tt * FL_I;
  char* facto = fact + cb * 8;
  char* stt = ws + WS_ST + tt * 4096;
  char* stto = stt + cb * 256;
  char* kvp = col + C_KVP + w * 32768;
  char* zp = col + C_ZP + w * 2048;
  char* fkvp = flags + (cb * 4 + w) * FL_I + 8 * FL_K;
  char* fwl = flags + cb * 4 * FL_I + 11 * FL_K;
  char* kvf = ws + WS_KVF;
  char* fkv = flags + 12 * FL_K;
  const char* xu_p = ws + (size_t)rb * 2 * MB + 2 * STG + (cb >> 2) * 65536 + w * 16384 + 4096;
  const char* su_p = ws + WS_ST + 256 * KB + ut * 4096;
  const char* wkv = ((cb & 2) ? wv : wk) + w * 256 * 64;
  const char* wph = wphi + cb * 4096 + w * 1024;
  u64 tag = 0;
  u32x4 xr[4] = {};
  char* colw = col + w * 4096;
  unsigned ldsw = lds0 + w * 4096;
  auto wt = [&](int m, int ch) { return colw + C_WT + m * 131072 + ch * SLOT; };
  auto pc_w = [&](int m, int slot, int ch, auto i) { if constexpr (i == 0) dma_c<0>(ldsw + slot * SLOT, c.xv, wt(m, ch)); else dma_n<i * 1024>(c.xv, wt(m, ch)); };
  auto pc_a = [&](auto n) { constexpr int g = n / 4; pc_w(g < 8 ? 1 : (g < 16 ? 2 : (g < 24 ? 0 : 3)), a_slot(g), g % 8, IC(n % 4)); };
  u64 csm = ~0ull;
  float csv[4] = {};
  auto pc_m = [&](auto n) { constexpr int g = n / 4; dma_c<(n % 4) * 1024>(ldsw + a_slot(g) * SLOT, c.xv, wt(2, g % 8)); };
  auto ld_cs = [&](bf16x8& d, int m, auto i) { ldx1m<i * 1024>(d, c.xv, col + C_CS + m * 4096, csm); };
  auto raw_src = [&](int pitch, auto i) { const int row = lane >> 3; return (unsigned)((32 * w + 8 * i + row) * pitch + ((lane & 7) ^ (((row >> 1) & 1) << 2)) * 16 - i * 1024); };
  auto raw_in = [&](auto j, int slot, auto i) {
    const int hi = rb >> 3, pitch = (j == 0 || (j == 2 && hi)) ? 6144 : 2048;
    const char* src = (j == 0 ? wqkv + (hi ? 0 : 4096) : (j == 1 ? (hi ? wq : woA) : (hi ? wqkv + 2048 : woB))) + cb * 128 + (size_t)(rb & 7) * 128 * pitch;
    dma<i * 1024>(ldsw + slot * SLOT, raw_src(pitch, i), src);
  };
  auto raw_out = [&](auto j, int slot) {
    const int hi = rb >> 3, ch = rb & 7, m = j == 0 ? 2 - 2 * hi : (j == 1 ? 3 + hi : 5 - 4 * hi);
    LAS const char* p = smem + slot * SLOT + (w >> 1) * 8192 + (4 * h + q4) * 128 + (((((q4 >> 1) ^ w) & 1) << 2) + 2 * blk + (p4 >> 1)) * 16 + 8 * (p4 & 1);
    char* dst = col + C_WT + m * 131072 + ch * SLOT + (w >> 1) * 8192 + (w & 1) * 1024 + 4096;
    float cs = 0.f;
    sfor<0, 4>([&](auto s) {
      const u32x4 f = __builtin_bit_cast(u32x4, cat8(trread(p + s * 2048), trread(p + s * 2048 + 1024)));
      st4_1<s * 2048 - 4096>(f, c.xv, dst);
#pragma unroll
      for (int e = 0; e < 4; ++e) cs = dot2(f[e], 0x3f803f80u, cs);
    });
    st1_1(swap_add(cs), (unsigned)(r * 4), col + C_CS + m * 4096 + (ch * 2 + (w >> 1)) * 256 + (w & 1) * 128);
  };
  const int w0 = w; const unsigned ldsw0 = ldsw;
#define REFRESH() do { \
    w = w0; ldsw = ldsw0; \
    asm volatile("" : "+s"(w), "+s"(tag), "+s"(act), "+s"(acto), "+s"(fact), "+s"(facto), "+s"(stt), "+s"(stto), "+s"(col), "+s"(colw), "+s"(ldsw)); \
    asm volatile("" : "+v"(lane)); \
    r = lane & 31; h = lane >> 5; blk = (lane >> 4) & 1; q4 = (lane & 15) >> 2; p4 = lane & 3; c.xv = lane * 16; c.p.init(lds0 + c.xv); \
  } while (0)
  int l = 0;
  float mean, rstd;
  Red<true> cw;
  f32x16 ra0, ra1;
  bf16x8 park[16];
      constexpr int NA = 58;
      constexpr SchedA SQ(false), S1(true);
      bf16x8 x[64], f0[3], f1[3], wf[4], kp[4], pp[32], zq[2], sel[2], mf0, mf1;
      Red<false> st; Red<true> ck, cv, cq;
      f32x16 acc[6], p[2], kvs;
      u64 fv;
      float mx, mn, e[16], en[16], se, sn, sce;
      f32x4 ks0 = {}, ks1 = {};
      unsigned arow, pk0, ra, xo[4], xs;
      const char* xw = xin + (size_t)tt * 65536;
      LAS char* pk_img = smem + 6 * SLOT;
      LAS char* v_img = smem + 5 * SLOT;
      LAS char* sh = smem + 9 * SLOT + 4096;
      auto issue_x = [&](auto s) { ldx1<(s < NA), (s % 8) * 1024 - 4096>(x[s], c.xv, act + (s / 8) * 8192 + 4096); };
      auto ld_park = [&](auto i) { ldx1<true, (i % 4) * 1024>(park[i], c.xv, wt(4, 2 + i / 4)); };
      auto ldp = [&](auto i) {
        if constexpr (i >= 2) ldx1<true, ((i - 2) % 8) * 1024 - 4096>(pp[i - 2], c.xv, kvp + ((i - 2) / 8) * 8192 + 4096); else ldx1<false, i * 1024>(zq[i], c.xv, zp);
      };
      auto rd_wf = [&](auto k) { LAS const char* wp = smem + 9 * SLOT + (4 * h + q4) * 64 + blk * 32 + p4 * 8 + k * 1024; wf[k] = cat8(trread(wp), trread(wp + 512)); };
      auto rd01 = [&](auto s, auto t, auto fl) {
        if constexpr (s < 192) {
          bf16x8 f;
          if constexpr (fl && s < 64) { LAS const char* q = (LAS const char*)(uintptr_t)(ra ^ (t * 64u)) + ((s / 8 + 3) % 4) * SLOT + (s % 8) * 2048; f = cat8(trread(q), trread(q + 1024)); }
          else f = c.p.template frag<a_slot(s / 8) * SLOT + (s % 8) * 2048 + t * 1024>();
          if constexpr (t == 0) f0[s % 3] = f; else f1[s % 3] = f;
        }
      };
      auto x_in = [&](auto n) { dma<(n % 4) * 1040>(lds0 + w * 20480 + ((n / 4) % 4) * 4160, xs + (n % 4) * (16384 - 1040), xw + (n / 4) * 128); };
      auto x_out = [&](auto s) { ds_read2<(s < NA), ((s / 4) % 4) * 520>(x[s], xo[s % 4]); };
      auto x_fin = [&](auto g, auto n) { waitl<(4 * g < NA), n>(x[4 * g]); sfor<4 * g + 1, 4 * g + 4>([&](auto s) { if constexpr (s < NA) pin_a(x[s]); else x[s] = opq_v(x[s]); }); };
      auto k_in = [&](auto n) { dma<(n % 4) * 1024>(ldsw + (5 + (n / 4 + 3) % 4) * SLOT, raw_src(6144, IC(n % 4)), wqkv + 2048 + cb * 128 + (size_t)(n / 4) * 128 * 6144); };
      auto v_out = [&](auto t, auto hf) {
        *(LAS bf16x8*)(v_img + t * 8192 + w * 2048 + hf * 1024 + lane * 16) = pack8<hf>(acc[2 + t]);
      };
      auto sum_mm = [&](auto i) { waitx<true, SQ.lw[i]>(pp[i]); mm<false, true, i == 0>(kvs, sel[i & 1], pp[i]); };
      auto kmap = [&](auto gi) {
        constexpr int g = gi;
        if constexpr (g == 4) pin2(acc[0], acc[1]);
        if constexpr (g >= 6 && g < 10) mm<false, false, g % 2 == 0>(p[(g - 6) / 2], wf[g - 6], kp[g - 6]);
        if constexpr (g >= 4 && g < 8) kp[g - 4] = opq_v(pack8<g % 2>(acc[(g - 4) / 2]));
        if constexpr (g == 10) mm<true, false, false, true>(acc[2], cv.p[0], mf0);
        if constexpr (g == 11) mm<true, false, false, true>(acc[3], cv.p[0], mf1);
        if constexpr (g >= 12) {
          constexpr int hd = (g - 12) / 58, j = (g - 12) % 58, n = (j - 7) / 2;
          const f32x16& q = p[hd];
          if constexpr (j == 0) { mx = a_max3(q[0], q[1], q[2]); mn = a_min3(q[0], q[1], q[2]); mx = a_max3(mx, q[3], q[3]); mn = a_min3(mn, q[3], q[3]); }
          if constexpr (j >= 1 && j < 4) a_mm4(mx, mn, q[4 * j], q[4 * j + 1], q[4 * j + 2], q[4 * j + 3]);
          if constexpr (j == 4) mx = opq_v(swap_max(mx) * -sce);
          if constexpr (j == 5) mn = opq_v(swap_min(mn) * sce);
          if constexpr (j == 6) { e[0] = a_fma(q[0], sce, mx); en[0] = a_fms(q[0], sce, mn); se = opq_v(0.f); sn = opq_v(0.f); }
          if constexpr (j >= 7 && j < 37) {
            if constexpr (j % 2) a_hedge<false>(e[n], e[n + 1], se, n ? e[n ? n - 1 : 0] : sn, q[n + 1], sce, mx);
            else a_hedge<true>(en[n], en[n + 1], sn, n ? en[n ? n - 1 : 0] : se, q[n + 1], sce, mn);
          }
          if constexpr (j == 37) { a_exp(e[15]); a_add(se, e[14]); }
          if constexpr (j == 38) { a_exp(en[15]); a_add(sn, en[14]); }
          if constexpr (j == 39) se = opq_v(swap_add(se + e[15]));
          if constexpr (j == 40) sn = opq_v(swap_add(sn + en[15]));
          if constexpr (j == 41) { se = opq_v(__builtin_amdgcn_rcpf(se) * rstd); sn = opq_v(__builtin_amdgcn_rcpf(sn) * rstd); }
          if constexpr (j >= 42) {
            constexpr int c8 = (j - 42) / 2, o = 4 * (c8 & 3) + 2 * (j % 2);
            const unsigned u = c8 < 4 ? a_norm2(e[o], e[o + 1], se) : a_norm2(en[o], en[o + 1], sn);
            if constexpr (j % 2 == 0) pk0 = u; else *(LAS u32x2*)((LAS char*)(uintptr_t)(arow ^ (c8 << 4)) + hd * 16384) = u32x2{pk0, u};
          }
        }
      };
      auto aux = [&](auto s, auto i, auto fl) {
        constexpr int g1 = 2 * (s - a_ld0(fl) - 14) + i, g3 = 2 * (s - 128) + i;
        if constexpr (s < 64) {
          if constexpr (fl) { if constexpr (i == 0) a_sum16(ks0, sel[0], f0[s % 3]); else a_sum16(ks1, sel[0], f1[s % 3]); }
          if constexpr (g1 >= 0 && g1 < 11) st.template run<g1>(lane);
          if constexpr (g1 == 11) { mean = st.a; rstd = st.b; sce = opq_v(rstd * LOG2E); }
          if constexpr (g1 == 12) mf0 = opq_v(mean_frag(mean, h == 0));
          if constexpr (g1 == 13) mf1 = opq_v(mean_frag(mean, h == 1));
          if constexpr (fl && g1 >= 8 && g1 < 14) cv.template run<g1 - 8>(lane);
          if constexpr (fl && g1 >= 16 && g1 < 22) cq.template run<g1 - 16>(lane);
          if constexpr (fl && g1 == 18) csv[1] = opq_a(cv.a);
          if constexpr (fl && g1 == 26) csv[2] = opq_a(cq.a);
          if constexpr (g1 == 22) cv.a = csv[1] = opq_a(csv[1]);
          if constexpr (g1 == 22 || g1 == 23) cv.template run<g1 - 14>(lane);
          if constexpr (!fl) {
            if constexpr (g1 == 14) ck.a = opq_v(csv[0]);
            if constexpr (g1 == 14 || g1 == 15) ck.template run<g1 - 6>(lane);
            if constexpr (g1 == 18) mm<false, false, false, true>(acc[0], ck.p[0], mf0);
            if constexpr (g1 == 19) mm<false, false, false, true>(acc[1], ck.p[0], mf1);
          }
          if constexpr (s == 60 && i == 0) *(LAS short*)(sh + 8704 + w * 64 + (r >> 4) * 32 + ((r >> 2) & 1) * 16 + ((r >> 3) & 1) * 8 + (r & 3) * 2) = (short)(__float_as_uint(st.sd) >> 16);
          if constexpr (s == 61 && i == 0) arow = opq_v((unsigned)(uintptr_t)pk_img + (w * 32 + r) * 128 + h * 8 + ((r & 7) << 4));
          if constexpr (s >= 62) rd_wf(IC(2 * (s - 62) + i));
        } else if constexpr (s < 128) {
          kmap(IC(2 * (s - 64) + i));
        } else {
          if constexpr (g3 == 2) cq.a = csv[2] = opq_a(csv[2]);
          if constexpr (g3 == 2 || g3 == 3) cq.template run<g3 + 6>(lane);
          if constexpr (g3 == 6) mm<false, false, false, true>(acc[4], cq.p[0], mf0);
          if constexpr (g3 == 7) mm<false, false, false, true>(acc[5], cq.p[0], mf1);
          if constexpr (g3 == 92) { csv[3] = l == 0 ? cw.a : csv[3]; cw.a = csv[3]; }
          if constexpr (g3 >= 84 && g3 < 94) cw.template run<g3 - 84>(lane);
          if constexpr (g3 >= 32 && g3 < 40) {
            constexpr int n = (g3 - 32) % 4, t = (g3 - 32) / 4;
            const int kr = 16 * t + 8 * (n >> 1) + 4 * h + 2 * (n & 1);
            u32x4 sv = __builtin_bit_cast(u32x4, sel[t]); sv[n] = opq_v(r == kr ? 0x3f80u : (r == kr + 1 ? 0x3f800000u : 0u)); sel[t] = __builtin_bit_cast(bf16x8, sv);
          }
          if constexpr (g3 == 40) { sel[0] = opq_v(sel[0]); sel[1] = opq_v(sel[1]); }
          if constexpr (g3 == 42) *(LAS unsigned*)(sh + 8192 + 256 + lane * 4) = opq_v(0x3f803f80u);
          if constexpr (g3 == 99) {
            wait2<SQ.zw>(zq[0], zq[1]);
#pragma unroll
            for (int n = 0; n < 4; ++n) { e[n] = __builtin_bit_cast(f32x4, zq[0])[n]; a_add(e[n], __builtin_bit_cast(f32x4, zq[1])[n]); }
          }
          if constexpr (g3 == 101) {
            auto a = __builtin_amdgcn_permlane16_swap(__float_as_uint(e[0]), __float_as_uint(e[1]), false, false);
            auto b = __builtin_amdgcn_permlane16_swap(__float_as_uint(e[2]), __float_as_uint(e[3]), false, false);
            e[0] = opq_v(__uint_as_float(a[0]) + __uint_as_float(a[1])); e[1] = opq_v(__uint_as_float(b[0]) + __uint_as_float(b[1]));
          }
          if constexpr (g3 == 103) { auto a = __builtin_amdgcn_permlane32_swap(__float_as_uint(e[0]), __float_as_uint(e[1]), false, false); e[0] = opq_v(__uint_as_float(a[0]) + __uint_as_float(a[1])); }
          if constexpr (g3 == 105) e[1] = lane_get(e[0], lane ^ 8);
          if constexpr (g3 == 111)
            *(LAS short*)(sh + 8192 + w * 64 + (lane & 1) * 32 + ((lane >> 2) & 1) * 16 + ((lane >> 1) & 1) * 8 + (lane >> 4) * 2) = (short)cvtpk(e[0] + e[1], 0.f);
          if constexpr (g3 >= 116 && g3 < 124 && g3 % 2 == 1) rd_wf(IC((g3 - 116) / 2));
        }
      };
      const unsigned fo = (unsigned)((lane & 15) * 8 + (blk ? cb * 4 * FL_I + 10 * FL_K : tt * FL_I));
      auto step = [&](auto s, auto fl) {
        constexpr int ps = s / 64, L0 = a_ld0(fl);
        constexpr bool first = fl;
        if constexpr (s == 102) { if (l == 0) waitv<SQ.uw>(); }
        if constexpr (s == 118) { if (l == 0) waitv<SQ.vw>(); }
        if constexpr (s == 70 || s == 78) { if (l == 0) waitv<cmin(S1.bw[s], SQ.bw[s]) + 8>(); else waitv<cmin(S1.bw[s], SQ.bw[s])>(); bar(); }
        else if constexpr (a_bar(s) && s >= (first ? 14 : 38)) wait_bar<(ps ? cmin(S1.bw[s], SQ.bw[s]) : (first ? S1.bw[s] : SQ.bw[s]))>();
        if constexpr (s == L0 + 14 || (first && (s == L0 + 18 || s == L0 + 22))) waitv<(first ? S1.sw[(s - L0 - 14) / 4] : SQ.sw[0])>();
        if constexpr (first && s == 32) { waitx<false, S1.aw>(fv); if (!__all(fv == tag)) poll<32>(flags, fo, tag); }
        if constexpr (s == 118) { if (l == 0 && w == 0) set_flag(fwl + rb * 8, tag); }
        if constexpr (s == 128) { waitx<false, SQ.fw>(fv); if (!__all(fv == tag)) poll<16>(fwl, lane * 8, tag); }
        if constexpr (s == 142) set_flag(fkvp + rb * 8, tag);
        if constexpr (s == QC) { waitx<false, SQ.pw>(fv); if (!__all(fv == tag)) poll<16>(fkvp, lane * 8, tag); }
        if constexpr (s == 170) waitv<SQ.cw>();
        if constexpr (s < 64 && !first) waitx<(s < NA), SQ.xw[s % 64]>(x[s % 64]);
        if constexpr (first && s % 3 == 0) waitv<S1.xw[s % 64]>();
        SBAR();
        mm<ps == 1, (s % 64 < NA), s % 64 == 0>(acc[2 * ps], f0[s % 3], x[s % 64]);
        SBAR();
        if constexpr (first && f_rd(2 * s) >= 0) x_out(IC(f_rd(2 * s)));
        rd01(IC(s + 2), IC(0), fl);
        if constexpr (first && f_in(2 * s) >= 0) x_in(IC(f_in(2 * s)));
        if constexpr (s + XL < 64 && !first) issue_x(IC(s + XL));
        if constexpr (first && s == 20) { fv = tag; rd_flag<32>(fv, fo, flags); }
        if constexpr (s == 127) ld8_1(fv, (lane & 15) * 8, fwl);
        if constexpr (s == QP) { fv = tag; rd_flag<16>(fv, lane * 8, fkvp); }
        if constexpr (s >= QC) ldp(IC(s - QC));
        aux(s, IC(0), fl);
        SBAR();
        mm<ps == 1, (s % 64 < NA), s % 64 == 0>(acc[2 * ps + 1], f1[s % 3], x[s % 64]);
        SBAR();
        if constexpr (first && f_rd(2 * s + 1) >= 0) x_out(IC(f_rd(2 * s + 1)));
        if constexpr (first && s % 3 == 2 && s / 3 + 1 < 16) x_fin(IC(s / 3 + 1), IC(4));
        rd01(IC(s + 2), IC(1), fl);
        if constexpr (a_piece(s, first) >= 0) { if constexpr (first) pc_m(IC(a_piece(s, first))); else pc_a(IC(a_piece(s, first))); }
        if constexpr (s >= L0 && s < L0 + 4) ldx1<false, (s - L0) * 1024>(st.p[s - L0], c.xv, stt);
        if constexpr (first && s >= L0 + 4 && s < L0 + 8) ld_cs(cv.p[s - L0 - 4], 2, IC(s - L0 - 4));
        if constexpr (first && s >= L0 + 8 && s < L0 + 12) ld_cs(cq.p[s - L0 - 8], 0, IC(s - L0 - 8));
        if constexpr (s == 50) dma<0>(lds0 + 9 * SLOT + w * 1024, c.xv, wph);
        if constexpr (s >= 154 && s < 158) ld_cs(cw.p[s - 154], 4, IC(s - 154));
        if constexpr (a_park(s) >= 0) ld_park(IC(a_park(s)));
        if constexpr (first && f_in(2 * s + 1) >= 0) x_in(IC(f_in(2 * s + 1)));
        if constexpr (first && f_raw(s) >= 0) k_in(IC(f_raw(s)));
        aux(s, IC(1), fl);
        if constexpr (s == 87) { if (l == 0) sfor<0, 4>([&](auto i) { raw_in(IC(1), 7, i); }); }
        if constexpr (s == 103) { if (l == 0) raw_out(IC(1), 7); }
        if constexpr (s == 133) { if (l == 0) sfor<0, 4>([&](auto i) { raw_in(IC(2), 8, i); }); }
        SBAR();
      };
      {
        STAMP(30);
        bf16x8 cf, kt[2];
        { const char* pc; asm volatile("s_getpc_b64 %0" : "=s"(pc));
          ldx0<0>(cf, c.xv, pc + (((blockIdx.x >> 3) * 4 + w) % CODE_KB) * 1024); }
        bf16x8 xt[4];
        sfor<0, 4>([&](auto i) { ldx0<i * 32>(xt[i], (unsigned)(r * 2048 + h * 16), xw + cb * 128); });
        sfor<0, 2>([&](auto i) { ldx0<0>(kt[i], (unsigned)((lane >> 3) * 6144 + (lane & 7) * 16), wqkv + 2048 + cb * 128 + (size_t)(tt * 16 + i * 8) * 6144); });
        u64 ep; ld8_1(ep, 0u, ws + WS_EPOCH);
        sfor<0, 4>([&](auto i) { raw_in(IC(0), 9, i); });
        xs = (unsigned)((lane >> 3) * 2048 + ((lane & 7) ^ (((lane >> 3) & 3) << 1)) * 16);
        sfor<0, 4>([&](auto i) { xo[i] = opq_v(lds0 + w * 20480 + r * 128 + (((r & 3) ^ i) << 5) + h * 8 + (r >> 3) * 16); });
        ra = opq_v(lds0 + 5 * SLOT + (4 * h + q4) * 128 + (((q4 >> 1) << 2) + 2 * blk + (p4 >> 1)) * 16 + 8 * (p4 & 1));
        { const unsigned o = (((lane >> 2) ^ (lane >> 4)) & 1) ? 0u : 0x3f803f80u; sel[0] = opq_v(__builtin_bit_cast(bf16x8, u32x4{o, o, o, o})); ks0 = opq_v(ks0); ks1 = opq_v(ks1); }
        sfor<0, 4>(x_in); sfor<0, 4>(k_in);
        asm volatile("s_waitcnt vmcnt(12)" : "+v"(cf), "+v"(ep), "+v"(xt[0]), "+v"(xt[1]), "+v"(xt[2]), "+v"(xt[3]), "+v"(kt[0]), "+v"(kt[1]));
        sfor<0, 4>([&](auto i) { xr[i] = unperm(__builtin_bit_cast(u32x4, xt[i])); });
        store_stats(r, xr, stto);
        park4(xr);
        { u64 z = ((u64)__builtin_amdgcn_readfirstlane((unsigned)(ep >> 32)) << 32 | __builtin_amdgcn_readfirstlane((unsigned)ep)) + 0x9E3779B97F4A7C15ull;
          z = (z ^ (z >> 30)) * 0xBF58476D1CE4E5B9ull; z = (z ^ (z >> 27)) * 0x94D049BB133111EBull; tag = z ^ (z >> 31); asm volatile("" : "+s"(tag)); }
        wait_bar<9>();
        raw_out(IC(0), 9);
        sfor<4, 12>(x_in); sfor<4, 12>(k_in);
        waitv<22>();
        sfor<0, 4>(x_out);
        wait_bar<0>();
        set_flag(facto, tag);
        if (w == 0) set_flag(fwl - FL_K + rb * 8, tag);
        x_fin(IC(0), IC(0));
        sfor<12, 20>(x_in);
        STAMP(31); STAMP(0); STAMP(1);
        sfor<0, 2>([&](auto s) { rd01(s, IC(0), std::true_type{}); rd01(s, IC(1), std::true_type{}); });
        sfor<0, 64>([&](auto s) { step(s, std::true_type{}); });
        {
          const int gw = blockIdx.x * 4 + w; const unsigned z = 0;
          const unsigned vo = (unsigned)(lane < 5 ? WS_KVF - WS_ST + (gw * 5 + lane) * 128 : WS_FLAG - WS_ST + gw * 128);
          asm volatile("s_mov_b64 exec, 63\n\tglobal_atomic_or %0, %1, %2 sc1\n\ts_mov_b64 exec, -1" ::"v"(vo), "v"(z), "s"(ws + WS_ST) : "memory");
          const unsigned dump = lds0 + 9 * SLOT + 4096 + w * 2048;
          dma<0>(dump, c.xv, woA + gw * 2048); dma<1024>(dump, c.xv, woA + gw * 2048);
          dma<0>(dump, c.xv, wq + gw * 2048); dma<1024>(dump, c.xv, wq + gw * 2048);
          dma<0>(dump, c.xv, woB + gw * 2048); dma<1024>(dump, c.xv, woB + gw * 2048);
          dma<0>(dump, c.xv, ((gw & 64) ? wk : wv) + (gw & 63) * 1024);
        }
        asm volatile("s_nop 7" : "+v"(ks0), "+v"(ks1));
        csv[0] = h ? ks1[0] : ks0[0];
        const bf16x8 kf = cs_frag(csv[0]);
        mm<false, false, false, true>(acc[0], kf, mf0); mm<false, false, false, true>(acc[1], kf, mf1);
      }
  asm volatile("" : "+s"(w), "+s"(tag), "+s"(act), "+s"(acto), "+s"(fact), "+s"(facto), "+s"(stt), "+s"(stto), "+s"(col), "+s"(colw), "+s"(ldsw));
  asm volatile("" : "+s"(kvp), "+s"(zp), "+s"(fkvp), "+s"(fwl), "+s"(rb), "+s"(kvf), "+s"(fkv), "+s"(xu_p), "+s"(su_p), "+s"(wkv), "+s"(csm), "+s"(l));
  for (;;) {
    {
      REFRESH();
      asm volatile("" : "+s"(kvp), "+s"(zp), "+s"(fkvp), "+s"(fwl), "+s"(rb));
      STAMP(2);
      sfor<64, 128>([&](auto s) { step(s, std::false_type{}); });
      STAMP(19);
      mdone(acc[2], acc[3]);
      v_out(IC(0), IC(0)); v_out(IC(0), IC(1)); v_out(IC(1), IC(0)); v_out(IC(1), IC(1));
      lds_bar();
      {
        const int hd = w >> 1, fh = w & 1;
        f32x16 kv, z;
        const int ch = fh * 4 + 2 * blk + (p4 >> 1), r0 = 4 * h + q4;
        LAS const char* pa = pk_img + hd * 16384 + r0 * 128 + ((ch ^ r0) << 4) + 8 * (p4 & 1);
        LAS const char* pb = v_img + hd * 8192 + lane * 16;
        bf16x8 a[8], b[8], d[8];
#pragma unroll
        for (int s = 0; s < 8; ++s) { a[s] = cat8(trread(pa + s * 2048), trread(pa + s * 2048 + 1024)); b[s] = *(LAS const bf16x8*)(pb + s * 1024); }
#pragma unroll
        for (int s = 0; s < 8; ++s) d[s] = *(LAS const bf16x8*)(sh + 8704 + s * 32 + h * 16);
        SBAR();
        sfor<0, 8>([&](auto s) { mm<false, false, s == 0>(kv, a[s], b[s]); });
        sfor<0, 8>([&](auto s) {
          mm<false, false, s == 0>(z, d[s], a[s]);
          if constexpr (s == 1) { mdone(kv); st4_1<0>(__builtin_bit_cast(u32x4, pack8<0>(kv)), c.xv, kvp + rb * 2048); }
          if constexpr (s == 2) { st4_1<1024>(__builtin_bit_cast(u32x4, pack8<1>(kv)), c.xv, kvp + rb * 2048); }
          SBAR();
        });
        mdone(z);
        st1_1(z[0], (unsigned)(r * 4), zp + rb * 128);
      }
      STAMP(3);
      sfor<128, 192>([&](auto s) { step(s, std::false_type{}); });
      mdone(acc[4], acc[5]);
      STAMP(4);
      {
        bf16x8 pq[2][4], ka[8], za[8];
        f32x16 ep, em, nm[4], dn[4];
        float ai[2], bi[2];
        char* dst = acto + STG;
        sfor<0, 4>([&](auto k) { mm<false, false, k % 2 == 0, true>(p[k / 2], wf[k], pack8<k % 2>(acc[4 + k / 2])); });
        mdone(p[0], p[1]);
        bar();
        if (l == 0) raw_out(IC(2), 8);
        sfor<0, 2>([&](auto hd) {
          hedge_exp(p[hd], sce, ep, em, [&](auto e) {
            constexpr int i = hd * 16 + e;
            sum_mm(IC(i));
            if constexpr (i % 2 == 0 && 192 - QC + i / 2 < 34) ldp(IC(192 - QC + i / 2));
            if constexpr (i % 2 == 0 && i >= 16) pc_a(IC(108 + (i - 16) / 2));
          });
          if constexpr (hd == 1) {
            mdone(kvs);
            *(LAS bf16x8*)(sh + w * 2048 + lane * 16) = pack8<0>(kvs); *(LAS bf16x8*)(sh + w * 2048 + 1024 + lane * 16) = pack8<1>(kvs);
            SBAR();
          }
          pq[hd][0] = pack8<0>(ep); pq[hd][1] = pack8<1>(ep); pq[hd][2] = pack8<0>(em); pq[hd][3] = pack8<1>(em);
          pin4(pq[hd]);
        });
        STAMP(5);
        lds_bar();
        LAS const char* zb = sh + 8192 + (r & 1) * 256 + h * 32;
#pragma unroll
        for (int k = 0; k < 8; ++k) za[k] = *(LAS const bf16x8*)(zb + (k >> 1) * 64 + (k & 1) * 16);
#pragma unroll
        for (int k = 0; k < 8; ++k) ka[k] = *(LAS const bf16x8*)(sh + k * 1024 + lane * 16);
        SBAR();
        auto coef = [&](auto hd) {
          const float a = __builtin_amdgcn_rcpf(dn[2 * hd][1]), b = __builtin_amdgcn_rcpf(dn[2 * hd + 1][1]);
          const float inv = __builtin_amdgcn_rcpf(dn[2 * hd][0] * a + dn[2 * hd + 1][0] * b + 1e-6f);
          ai[hd] = opq_v(a * inv); bi[hd] = opq_v(b * inv);
        };
        auto fin = [&](auto hd) {
          f32x16 o;
#pragma unroll
          for (int i = 0; i < 16; ++i) o[i] = nm[2 * hd][i] * ai[hd] + nm[2 * hd + 1][i] * bi[hd];
          st4_1<hd * 2048>(__builtin_bit_cast(u32x4, pack8<0>(o)), c.xv, dst);
          st4_1<hd * 2048 + 1024>(__builtin_bit_cast(u32x4, pack8<1>(o)), c.xv, dst);
        };
        sfor<0, 8>([&](auto k) { mm<false, false, k % 2 == 0>(dn[k / 2], za[k], pq[k / 4][k % 4]); if constexpr (k % 2) pc_a(IC(116 + k / 2)); });
        sfor<0, 4>([&](auto k) { mm<false, false, k % 2 == 0>(nm[k / 2], ka[k], pq[0][k]); });
        mdone(dn[0], dn[1], dn[2], dn[3]);
        sfor<4, 8>([&](auto k) { mm<false, false, k % 2 == 0>(nm[k / 2], ka[k], pq[1][k - 4]); if constexpr (k == 4) coef(IC(0)); if constexpr (k == 6) coef(IC(1)); });
        STAMP(6);
        pin2(nm[0], nm[1]);
        fin(IC(0));
        pin2(nm[2], nm[3]);
        fin(IC(1));
        SBAR();
        sfor<120, 124>(pc_a);
        res_to_acc(xr, ra0, ra1);
        waitv<4>();
        set_flag(facto + FL_K, tag);
        sfor<124, 128>(pc_a); sfor<8, 16>(ld_park);
      }
    }
    STAMP(7);
    REFRESH();
    LAS char* reg = smem + 8 * SLOT;
    bf16x8 fu[16], cu;
    {
      f32x16 &a0 = ra0, &a1 = ra1;
      poll<16>(fact + FL_K, lane * 8, tag);
      sfor<0, 16>([&](auto i) { pin_a(park[i]); });
      STAMP(8);
      bf16x8 x[64];
      const char* xb = act + STG;
      sfor<0, XL>([&](auto s) { ld_x<s>(x, c, xb); });
      bar();
      asm volatile("" : "+s"(wkv));
      const unsigned kvv = (unsigned)((lane >> 1) * 64 + (cb & 1) * 32 + (lane & 1) * 16);
      const unsigned wa = ldsw + c.xv, wb = wa + 65536;
      auto unpark = [&](auto i) {
        constexpr int off = a_slot(26 + i / 4) * SLOT + (i % 4) * 1024;
        ds_write_a<(off & 65535)>(off < 65536 ? wa : wb, park[i]);
      };
      gemm64<true, false, HookP3>(c, x, xb, a0, a1, [&](auto s) {
        if constexpr (s == 33) bar();
        if constexpr (s >= 36 && s < 44) unpark(IC(s - 36));
        if constexpr (s == 49) bar();
        if constexpr (s >= 50 && s < 58) unpark(IC(s - 42));
        if constexpr (s >= 52 && s < 60) dma<0>(lds0 + 8 * SLOT + w * 8192 + (s - 52) * 1024, kvv, wkv + (s - 52) * 32 * 64);
        if constexpr (s >= 60) pc_w(4, a_slot(24), 0, IC(s - 60));
      }, [](auto) {});
      STAMP(9);
      store_x(c, r, a0, a1, xr, acto + 2 * STG, stto + 256 * 1024);
      asm volatile("s_waitcnt vmcnt(5) lgkmcnt(0)\n\ts_barrier" ::: "memory");
      { LAS const char* p = reg + w * 8192 + (4 * h + q4) * 32 + p4 * 8;
#pragma unroll
        for (int i = 0; i < 16; ++i) fu[i] = cat8(trread(p + i * 512), trread(p + i * 512 + 256));
        SBAR();
        float t0 = 0.f, t1 = 0.f, t2 = 0.f, t3 = 0.f;
#pragma unroll
        for (int i = 0; i < 16; ++i) {
          const u32x4 f = __builtin_bit_cast(u32x4, fu[i]);
          t0 = dot2(f[0], 0x3f803f80u, t0); t1 = dot2(f[1], 0x3f803f80u, t1); t2 = dot2(f[2], 0x3f803f80u, t2); t3 = dot2(f[3], 0x3f803f80u, t3);
        }
        cu = opq_v(cs_frag(swap_add((t0 + t1) + (t2 + t3)))); }
      waitv<0>();
      set_flag(facto + 2 * FL_K, tag);
      auto pc_q = [&](auto i) { pc_w(4, a_slot(24 + i / 4), i / 4, IC(i % 4)); };
      sfor<4, 8>(pc_q); sfor<24, 32>(pc_q);
    }
    STAMP(10);
    REFRESH();
    f32x16 a0, a1;
    asm volatile("" : "+s"(kvf), "+s"(fkv), "+s"(xu_p), "+s"(su_p));
    {
      poll<16>(fact + 2 * FL_K, lane * 8, tag);
      bar();
      STAMP(11);
      const char* xb = act + 2 * STG; const char* stp = stt + 256 * 1024;
      bf16x8 x[64], xu[16], mf0, mf1;
      Red<false> su, sp;
      sfor<0, 4>([&](auto i) { ldx1<false, i * 1024>(su.p[i], c.xv, su_p); });
      sfor<0, 16>([&](auto i) { ldx1<true, (i % 8) * 1024 - 4096>(xu[i], c.xv, xu_p + (i / 8) * 8192); });
      f32x16 au, av;
      sfor<0, 16>([&](auto i) {
        waitx<true, 15 - i + (i < XL ? i : XL)>(xu[i]);
        SBAR();
        mm<false, true, i == 0>(au, fu[i], xu[i]);
        if constexpr (i < XL) ld_x<i>(x, c, xb);
        mm<true, true, i == 0>(av, fu[i], xu[i]);
        if constexpr (i < 11) su.template run<i>(lane);
        if constexpr (i == 12) mf0 = opq_v(mean_frag(su.a, h == 0));
        if constexpr (i == 14) { mm<false, false, false>(au, cu, mf0); mm<true, false, false>(av, cu, mf0); }
        SBAR();
      });
      STAMP(12);
      u64 fa;
      f32x4 t[4], ru;
      unsigned vo;
      gemm64<true, true, HookP4>(c, x, xb, a0, a1, [&](auto s) {
        if constexpr (s == 1) {
          pin2(au, av);
          auto put = [&](const f32x16& a) {
#pragma unroll
            for (int gq = 0; gq < 4; ++gq) *(LAS f32x4*)(reg + w * 8192 + gq * 1024 + lane * 16) = f32x4{a[4 * gq], a[4 * gq + 1], a[4 * gq + 2], a[4 * gq + 3]};
          };
          if (ty < 2) { put(au); asm volatile("" ::: "memory"); } else put(av);
        }
        if constexpr (s == 3) {
          lds_bar();
#pragma unroll
          for (int j = 0; j < 4; ++j) t[j] = *(LAS const f32x4*)(reg + j * 8192 + (ty < 2 ? w & 1 : w) * 1024 + lane * 16);
        }
        if constexpr (s == 6) st2_1(__builtin_bit_cast(f32x2, u32x2{cvtpk(t[0][0], t[0][1]), cvtpk(t[0][2], t[0][3])}), vo, kvf);
        if constexpr (s >= 8 && s < 12) ldx1<false, (s - 8) * 1024>(sp.p[s - 8], c.xv, stp);
        if constexpr (s == 7 + XL) { bar(); if (w == 0) set_flag(fkv + rb * 4 * FL_I + cb * 8, tag); }
        if constexpr (s == 52) ld8_1(fa, (lane & 15) * 8 + (w * 4 + (lane >> 4)) * 4 * FL_I, fkv);
      }, [&](auto g) {
        if constexpr (g == 0) {
#pragma unroll
          for (int e = 0; e < 4; ++e) ru[e] = lane_get(su.b, ty < 2 ? r : 8 * w + 4 * h + e);
        }
        if constexpr (g == 4) vo = opq_v(ut * 4096 + (ty < 2 ? (unsigned)(ty * 1024 + lane * 16 + (w & 1) * 8)
                                                               : (unsigned)(2048 + (w >> 1) * 1024 + ((ty & 1) * 16 + (r & 15) + 32 * (w & 1)) * 16 + h * 8)));
        if constexpr (g >= 9 && g < 13) {
#pragma unroll
          for (int e = 0; e < 4; ++e) { float v = t[0][e]; if constexpr (g < 12) a_add(v, t[g - 8][e]); else a_mul(v, ru[e]); t[0][e] = v; }
        }
        if constexpr (g >= 48 && g < 59) sp.template run<g - 48>(lane);
        if constexpr (g == 60) mf0 = opq_v(mean_frag(sp.a, h == 0));
        if constexpr (g == 61) mf1 = opq_v(mean_frag(sp.a, h == 1));
        if constexpr (g == 64) mm<false, false, false>(a0, cw.p[0], mf0);
        if constexpr (g == 65) mm<false, false, false>(a1, cw.p[0], mf1);
      });
      rstd = sp.b;
      waitx<false, 0>(fa);
#pragma unroll 1
      for (int spin = 0; spin < (1 << 15) && !__all(fa == tag); ++spin) { __builtin_amdgcn_s_sleep(8); ld8_1(fa, (lane & 15) * 8 + (w * 4 + (lane >> 4)) * 4 * FL_I, fkv); waitx<false, 0>(fa); }
      bar();
      STAMP(13);
    }
    STAMP(14);
    {
      u32x16 qq, misc;
      misc[14] = misc[15] = __float_as_uint(rstd * QSCALE);
#pragma unroll 1
      for (int attempt = 0; attempt < 2; ++attempt) {
        if (attempt == 1) {
          const bf16x8 qa0 = __builtin_bit_cast(bf16x8, __builtin_shufflevector(qq, qq, 0, 1, 2, 3)), qa1 = __builtin_bit_cast(bf16x8, __builtin_shufflevector(qq, qq, 4, 5, 6, 7));
          const bf16x8 qb0 = __builtin_bit_cast(bf16x8, __builtin_shufflevector(qq, qq, 8, 9, 10, 11)), qb1 = __builtin_bit_cast(bf16x8, __builtin_shufflevector(qq, qq, 12, 13, 14, 15));
          float mA = -INFINITY, mB = -INFINITY;
          for (int t = 0; t < 64; ++t) {
            bf16x8 k0, k1;
            ldx1<false, 0>(k0, c.xv, kvf + t * 4096); ldx1<false, 1024>(k1, c.xv, kvf + t * 4096);
            wait2<0>(k0, k1);
            mm<false, false, true>(a0, k0, qa0); mm<false, false, false>(a0, k1, qa1); mm<false, false, true>(a1, k0, qb0); mm<false, false, false>(a1, k1, qb1);
            mdone(a0, a1);
            mA = fmaxf(mA, max16(a0)); mB = fmaxf(mB, max16(a1));
          }
          misc[14] = __float_as_uint(swap_max(mA)); misc[15] = __float_as_uint(swap_max(mB));
        }
        f32x16 oa, ob, t0, t1, t2, t3;
        u32x16 kvr; u32x4 offs;
        offs[0] = c.xv;
        const char* ks = kvf + 2048 + w * 2048; const char* wsr = wt(5, 0);
        unsigned wm0 = ldsw, cnt = 8;
        asm volatile(
            "global_load_dwordx4 v[208:211], v240, %[kv] sc1\n\tglobal_load_dwordx4 v[212:215], v240, %[kv] offset:1024 sc1\n\t"
            "s_mov_b32 m0, s44\n\tv_add_u32 v241, 0x2000, v240\n\t"
            "global_load_lds_dwordx4 v240, s[40:41] sc1\n\tglobal_load_lds_dwordx4 v240, s[40:41] offset:1024 sc1\n\t"
            "s_add_u32 m0, s44, 0x2000\n\tv_add_u32 v242, 0x4000, v240\n\t"
            "global_load_lds_dwordx4 v241, s[40:41] sc1\n\tglobal_load_lds_dwordx4 v241, s[40:41] offset:1024 sc1\n\t"
            "s_add_u32 m0, s44, 0x4000\n\tv_add_u32 v243, 0x6000, v240\n\t"
            "global_load_lds_dwordx4 v242, s[40:41] sc1\n\tglobal_load_lds_dwordx4 v242, s[40:41] offset:1024 sc1\n\t"
            "s_add_u32 s40, s40, 0x6000\n\ts_addc_u32 s41, s41, 0\n\t"
            "s_cmp_lg_u32 %[att], 0\n\ts_cbranch_scc1 2f\n\t"
            "v_pk_mul_f32 v[128:129], v[128:129], v[238:239]\n\t"
            "v_pk_mul_f32 v[130:131], v[130:131], v[238:239]\n\t"
            "v_pk_mul_f32 v[132:133], v[132:133], v[238:239]\n\t"
            "v_pk_mul_f32 v[134:135], v[134:135], v[238:239]\n\t"
            "v_pk_mul_f32 v[136:137], v[136:137], v[238:239]\n\t"
            "v_pk_mul_f32 v[138:139], v[138:139], v[238:239]\n\t"
            "v_pk_mul_f32 v[140:141], v[140:141], v[238:239]\n\t"
            "v_pk_mul_f32 v[142:143], v[142:143], v[238:239]\n\t"
            "v_pk_mul_f32 v[144:145], v[144:145], v[238:239]\n\t"
            "v_pk_mul_f32 v[146:147], v[146:147], v[238:239]\n\t"
            "v_pk_mul_f32 v[148:149], v[148:149], v[238:239]\n\t"
            "v_pk_mul_f32 v[150:151], v[150:151], v[238:239]\n\t"
            "v_pk_mul_f32 v[152:153], v[152:153], v[238:239]\n\t"
            "v_pk_mul_f32 v[154:155], v[154:155], v[238:239]\n\t"
            "v_pk_mul_f32 v[156:157], v[156:157], v[238:239]\n\t"
            "v_pk_mul_f32 v[158:159], v[158:159], v[238:239]\n\t"
            "v_cvt_pk_bf16_f32 v192, v128, v129\n\t"
            "v_cvt_pk_bf16_f32 v193, v130, v131\n\t"
            "v_cvt_pk_bf16_f32 v194, v132, v133\n\t"
            "v_cvt_pk_bf16_f32 v195, v134, v135\n\t"
            "v_cvt_pk_bf16_f32 v196, v136, v137\n\t"
            "v_cvt_pk_bf16_f32 v197, v138, v139\n\t"
            "v_cvt_pk_bf16_f32 v198, v140, v141\n\t"
            "v_cvt_pk_bf16_f32 v199, v142, v143\n\t"
            "v_cvt_pk_bf16_f32 v200, v144, v145\n\t"
            "v_cvt_pk_bf16_f32 v201, v146, v147\n\t"
            "v_cvt_pk_bf16_f32 v202, v148, v149\n\t"
            "v_cvt_pk_bf16_f32 v203, v150, v151\n\t"
            "v_cvt_pk_bf16_f32 v204, v152, v153\n\t"
            "v_cvt_pk_bf16_f32 v205, v154, v155\n\t"
            "v_cvt_pk_bf16_f32 v206, v156, v157\n\t"
            "v_cvt_pk_bf16_f32 v207, v158, v159\n\t"
            "2:\n\t"
            "v_mov_b64 v[64:65], 0\n\t"
            "v_mov_b64 v[66:67], 0\n\t"
            "v_mov_b64 v[68:69], 0\n\t"
            "v_mov_b64 v[70:71], 0\n\t"
            "v_mov_b64 v[72:73], 0\n\t"
            "v_mov_b64 v[74:75], 0\n\t"
            "v_mov_b64 v[76:77], 0\n\t"
            "v_mov_b64 v[78:79], 0\n\t"
            "v_mov_b64 v[80:81], 0\n\t"
            "v_mov_b64 v[82:83], 0\n\t"
            "v_mov_b64 v[84:85], 0\n\t"
            "v_mov_b64 v[86:87], 0\n\t"
            "v_mov_b64 v[88:89], 0\n\t"
            "v_mov_b64 v[90:91], 0\n\t"
            "v_mov_b64 v[92:93], 0\n\t"
            "v_mov_b64 v[94:95], 0\n\t"
            "v_mov_b64 v[176:177], 0\n\t"
            "v_mov_b64 v[178:179], 0\n\t"
            "v_mov_b64 v[180:181], 0\n\t"
            "v_mov_b64 v[182:183], 0\n\t"
            "v_mov_b64 v[184:185], 0\n\t"
            "v_mov_b64 v[186:187], 0\n\t"
            "v_mov_b64 v[188:189], 0\n\t"
            "v_mov_b64 v[190:191], 0\n\t"
            "v_mov_b64 v[216:217], 0\n\t"
            "v_mov_b64 v[218:219], 0\n\t"
            "v_mov_b64 v[220:221], 0\n\t"
            "v_mov_b64 v[222:223], 0\n\t"
            "v_mov_b64 v[228:229], 0\n\t"
            "v_mov_b64 v[230:231], 0\n\t"
            "v_mov_b64 v[232:233], 0\n\t"
            "v_mov_b64 v[234:235], 0\n\t"
            "v_bfe_u32 v226, v240, 6, 1\n\tv_mov_b32 v236, 0xdddddddd\n\tv_mul_u32_u24 v226, 0x3f80, v226\n\tv_add_u32 v237, %[rg], v240\n\t"
            "v_lshl_or_b32 v226, v226, 16, v226\n\tv_mov_b32 v227, v226\n\tv_xor_b32 v224, 0x3f803f80, v226\n\tv_mov_b32 v225, v224\n\t"
            "s_waitcnt vmcnt(6)\n\ts_cbranch_scc1 3f\n\t"
            "v_mfma_f32_32x32x16_bf16 v[128:143], v[208:211], v[192:195], 0\n\tv_mfma_f32_32x32x16_bf16 v[160:175], v[208:211], v[200:203], 0\n\t"
            "v_mfma_f32_32x32x16_bf16 v[128:143], v[212:215], v[196:199], v[128:143]\n\tv_mfma_f32_32x32x16_bf16 v[160:175], v[212:215], v[204:207], v[160:175]\n\t"
            "s_nop 11\n\t"
            "v_max3_f32 v238, v128, v129, v130\n\tv_max3_f32 v239, v160, v161, v162\n\t"
            "v_max3_f32 v238, v238, v131, v132\n\tv_max3_f32 v239, v239, v163, v164\n\t"
            "v_max3_f32 v238, v238, v133, v134\n\tv_max3_f32 v239, v239, v165, v166\n\t"
            "v_max3_f32 v238, v238, v135, v136\n\tv_max3_f32 v239, v239, v167, v168\n\t"
            "v_max3_f32 v238, v238, v137, v138\n\tv_max3_f32 v239, v239, v169, v170\n\t"
            "v_max3_f32 v238, v238, v139, v140\n\tv_max3_f32 v239, v239, v171, v172\n\t"
            "v_max3_f32 v238, v238, v141, v142\n\tv_max3_f32 v239, v239, v173, v174\n\t"
            "v_max3_f32 v238, v238, v143, v143\n\tv_max3_f32 v239, v239, v175, v175\n\t"
            "v_mov_b32 v96, v238\n\tv_mov_b32 v97, v239\n\ts_nop 1\n\tv_permlane32_swap_b32 v238, v96\n\tv_permlane32_swap_b32 v239, v97\n\t"
            "v_max_f32 v238, v238, v96\n\tv_max_f32 v239, v239, v97\n\t"
            "3:\n\t"
            "v_xor_b32 v96, 0x80000000, v238\n\tv_xor_b32 v112, 0x80000000, v239\n\tv_mov_b32 v97, v96\n\tv_mov_b32 v113, v112\n\t"
            "v_mov_b64 v[98:99], v[96:97]\n\tv_mov_b64 v[114:115], v[112:113]\n\t"
            "v_mov_b64 v[100:101], v[96:97]\n\tv_mov_b64 v[116:117], v[112:113]\n\t"
            "v_mov_b64 v[102:103], v[96:97]\n\tv_mov_b64 v[118:119], v[112:113]\n\t"
            "v_mov_b64 v[104:105], v[96:97]\n\tv_mov_b64 v[120:121], v[112:113]\n\t"
            "v_mov_b64 v[106:107], v[96:97]\n\tv_mov_b64 v[122:123], v[112:113]\n\t"
            "v_mov_b64 v[108:109], v[96:97]\n\tv_mov_b64 v[124:125], v[112:113]\n\t"
            "v_mov_b64 v[110:111], v[96:97]\n\tv_mov_b64 v[126:127], v[112:113]\n\t"
            "s_nop 1\n\t" S0_(128, 192, 96) S1_(128, 192)
            "s_waitcnt vmcnt(4)\n\ts_barrier\n\ts_nop 8\n\t"
            "1:\n\t"
            STEP_(0, 24576, 240, 0, "s_sub_u32 %[cnt], %[cnt], 1\n\t", "") STEP_(8192, 0, 241, 1024, "", "") STEP_(16384, 8192, 242, 2048, "", "")
            STEP_(24576, 16384, 243, 3072, "s_add_u32 s40, s40, 0x8000\n\ts_addc_u32 s41, s41, 0\n\t", "s_add_u32 s42, s42, 0x4000\n\ts_addc_u32 s43, s43, 0\n\t")
            "s_add_u32 s45, s45, 0x4000\n\ts_cmp_lg_u32 %[cnt], 0\n\ts_cbranch_scc1 1b\n\t"
            L0_(232, 176) L1_(232, 176) PV0_(80, 176) PV1_(80, 176)
            "s_nop 11\n\t"
            : "=&{v[64:79]}"(oa), "=&{v[80:95]}"(ob), "=&{v[96:111]}"(t0), "=&{v[112:127]}"(t1), "+{v[128:143]}"(a0), "+{v[144:159]}"(a1), "=&{v[160:175]}"(t2),
              "=&{v[176:191]}"(t3), "+{v[192:207]}"(qq), "=&{v[208:223]}"(kvr), "+{v[224:239]}"(misc), "+{v[240:243]}"(offs),
              "+{s[40:41]}"(ks), "+{s[42:43]}"(wsr), "+{s45}"(wm0), [cnt] "+s"(cnt)
            : "{s44}"(lds0 + 8 * SLOT + w * 2048), [kv] "s"(kvf), [att] "s"(attempt), [rg] "s"(lds0 + 8 * SLOT)
            : "scc", "memory");
        const float la = __uint_as_float(misc[4]), lb = __uint_as_float(misc[8]);
        const float ia = __builtin_amdgcn_rcpf(la), ib = __builtin_amdgcn_rcpf(lb);
#pragma unroll
        for (int e = 0; e < 16; ++e) { oa[e] *= ia; ob[e] *= ib; }
        char* dst = acto + 3 * STG;
        st4_1<0>(__builtin_bit_cast(u32x4, pack8<0>(oa)), c.xv, dst); st4_1<1024>(__builtin_bit_cast(u32x4, pack8<1>(oa)), c.xv, dst);
        st4_1<2048>(__builtin_bit_cast(u32x4, pack8<0>(ob)), c.xv, dst); st4_1<3072>(__builtin_bit_cast(u32x4, pack8<1>(ob)), c.xv, dst);
        float chk = 0.f;
#pragma unroll
        for (int e = 0; e < 16; ++e) chk = __builtin_fmaf(oa[e], 0.f, __builtin_fmaf(ob[e], 0.f, chk));
        const bool bad = !(la < 1.2676506e30f) || !(lb < 1.2676506e30f) || !(chk == 0.f);
        wait_bar<4>();
        LAS volatile int* vote = (LAS volatile int*)(smem + 8 * SLOT);
        if (lane == 0) vote[w] = 0;
        if (attempt == 0 && bad) vote[w] = 1;
        lds_bar();
        const int redo = vote[0] | vote[1] | vote[2] | vote[3];
        if (!__builtin_amdgcn_readfirstlane(redo)) break;
        lds_bar();
      }
      res_to_acc(xr, ra0, ra1);
      waitv<0>();
      set_flag(facto + 3 * FL_K, tag);
    }
    STAMP(15);
    REFRESH();
    {
      f32x16 &a0 = ra0, &a1 = ra1;
      poll<16>(fact + 3 * FL_K, lane * 8, tag);
      STAMP(16);
      bf16x8 x[64];
      const char* xb = act + 3 * STG;
      sfor<0, XL>([&](auto s) { ld_x<s>(x, c, xb); });
      bar();
      gemm64<false, false, HookP6>(c, x, xb, a0, a1, [&](auto s) {
        if constexpr (s == 33) bar();
        if constexpr (s >= 52) pc_a(IC(s - 52));
      }, [](auto) {});
      STAMP(17);
      if (l == 0) {
        store_x(c, r, a0, a1, xr, acto + 4 * STG, stto + 512 * 1024);
        bar();
        sfor<12, 16>(pc_a);
        waitv<4>();
        set_flag(facto + 4 * FL_K, tag);
        sfor<16, 24>(pc_a);
      } else {
        LAS char* im = smem + 8 * SLOT + w * 4096;
        sfor<0, 8>([&](auto n) {
          constexpr int g = n;
          const f32x16& a = g < 4 ? a0 : a1;
          *(LAS u32x2*)(im + r * 128 + ((g ^ (r & 7)) << 4) + h * 8) = u32x2{cvtpk(a[4 * (g & 3)], a[4 * (g & 3) + 1]), cvtpk(a[4 * (g & 3) + 2], a[4 * (g & 3) + 3])};
        });
        char* dst = out + (size_t)tt * 65536 + cb * 128;
        sfor<0, 4>([&](auto i) {
          const u32x4 v = *(LAS const u32x4*)(im + i * 1024 + lane * 16);
          st4_1<0>(v, (unsigned)((i * 8 + (lane >> 3)) * 2048 + (((lane & 7) ^ (lane >> 3)) << 4)), dst);
        });
        if (tt == 0 && cb == 0) set_flag(ws + WS_EPOCH, tag);
      }
    }
    STAMP(18);
    if (l == 1) break;
    csm = 1;
    act += 4 * STG; acto += 4 * STG; fact += 4 * FL_K; facto += 4 * FL_K; stt += 512 * KB; stto += 512 * KB;
    kvp += 131072; zp += 8192; fkvp += FL_K; kvf += 320 * KB; fkv += FL_K; xu_p += 4 * STG; su_p += 512 * KB;
    l = 1;
    REFRESH();
    STAMP(0);
        poll<16>(fact, lane * 8, tag);
        STAMP(1);
        sfor<0, XL>(issue_x);
        wait_bar<XL>();
        sfor<0, 2>([&](auto s) { rd01(s, IC(0), std::false_type{}); rd01(s, IC(1), std::false_type{}); });
        sfor<0, 64>([&](auto s) { step(s, std::false_type{}); });
  }
}

extern "C" void kernel_launch(void* const* d_in, const int* in_sizes, int n_in, void* d_out, int out_size, void* d_ws, size_t ws_size, hipStream_t stream) {
  k_main<<<256, 256, 0, stream>>>((const char*)d_in[0], (const char*)d_in[1], (const char*)d_in[2], (const char*)d_in[3], (const char*)d_in[5], (const char*)d_in[6],
                                  (const char*)d_in[7], (const char*)d_in[8], (char*)d_ws, (char*)d_out);
}
```

```cpp
#include <hip/hip_runtime.h>
#include <hip/hip_bf16.h>
#include <stdint.h>
#include <type_traits>

typedef __attribute__((ext_vector_type(8))) short bf16x8;
typedef __attribute__((ext_vector_type(4))) short s16x4;
typedef __attribute__((ext_vector_type(2))) __bf16 bf2_t;
typedef __attribute__((ext_vector_type(16))) float f32x16;
typedef __attribute__((ext_vector_type(4))) float f32x4;
typedef __attribute__((ext_vector_type(2))) float f32x2;
typedef __attribute__((ext_vector_type(4))) unsigned u32x4;
typedef __attribute__((ext_vector_type(2))) unsigned u32x2;
#define LAS __attribute__((address_space(3)))
#define DI __device__ __forceinline__
#define MFMA32(a, b, c) __builtin_amdgcn_mfma_f32_32x32x16_bf16((a), (b), (c), 0, 0, 0)
#define MFMA16(a, b, c) __builtin_amdgcn_mfma_f32_16x16x32_bf16((a), (b), (c), 0, 0, 0)
#define SBAR() __builtin_amdgcn_sched_barrier(0)

constexpr int S_ = 2048, D_ = 1024;
constexpr float LOG2E = 1.4426950408889634f;
constexpr float QSCALE = 0.17677669529663687f * LOG2E;

constexpr size_t MB = 1u << 20, KB = 1024;
constexpr size_t STG = 256 * KB;
constexpr size_t WS_COL = 32 * MB;
constexpr size_t C_WT = 0;
constexpr size_t C_KVP = 768 * KB;
constexpr size_t C_ZP = 1024 * KB;
constexpr size_t C_CS = 1040 * KB;
constexpr size_t WS_ST = 64 * MB;
constexpr size_t WS_KVF = WS_ST + 1024 * KB;
constexpr size_t WS_FLAG = WS_ST + 1664 * KB;
constexpr size_t WS_EPOCH = WS_ST + 1920 * KB;
constexpr int FL_I = 2048, FL_K = 128;
typedef unsigned long long u64;

template <int I, int N, class F> DI void sfor(F&& f) { if constexpr (I < N) { f(std::integral_constant<int, I>{}); sfor<I + 1, N>(f); } }

DI unsigned cvtpk(float lo, float hi) { f32x2 v = {lo, hi}; return __builtin_bit_cast(unsigned, __builtin_convertvector(v, bf2_t)); }
DI float bflo(unsigned u) { return __uint_as_float(u << 16); }
DI float bfhi(unsigned u) { return __uint_as_float(u & 0xffff0000u); }
DI float rbf(float x) { return bflo(cvtpk(x, x)); }
DI float swap_add(float v) { auto r = __builtin_amdgcn_permlane32_swap(__float_as_uint(v), __float_as_uint(v), false, false); return __uint_as_float(r[0]) + __uint_as_float(r[1]); }
DI float swap_max(float v) { auto r = __builtin_amdgcn_permlane32_swap(__float_as_uint(v), __float_as_uint(v), false, false); return fmaxf(__uint_as_float(r[0]), __uint_as_float(r[1])); }
DI float swap_min(float v) { auto r = __builtin_amdgcn_permlane32_swap(__float_as_uint(v), __float_as_uint(v), false, false); return fminf(__uint_as_float(r[0]), __uint_as_float(r[1])); }
template <int S> DI bf16x8 pack8(const f32x16& v) {
  u32x4 p = {cvtpk(v[8 * S], v[8 * S + 1]), cvtpk(v[8 * S + 2], v[8 * S + 3]), cvtpk(v[8 * S + 4], v[8 * S + 5]), cvtpk(v[8 * S + 6], v[8 * S + 7])};
  return __builtin_bit_cast(bf16x8, p);
}
DI u32x4 unperm(u32x4 v) {
  auto r0 = __builtin_amdgcn_permlane32_swap(v[0], v[2], false, false);
  auto r1 = __builtin_amdgcn_permlane32_swap(v[1], v[3], false, false);
  return u32x4{r0[0], r1[0], r0[1], r1[1]};
}
DI float lane_get(float v, int src) { return __uint_as_float(__builtin_amdgcn_ds_bpermute(src << 2, __float_as_uint(v))); }
DI s16x4 trread(LAS const char* p) { return __builtin_amdgcn_ds_read_tr16_b64_v4i16((LAS s16x4*)p); }
DI bf16x8 cat8(s16x4 a, s16x4 b) { return __builtin_shufflevector(a, b, 0, 1, 2, 3, 4, 5, 6, 7); }
DI float dot2(unsigned a, unsigned b, float c) { return __builtin_amdgcn_fdot2_f32_bf16(__builtin_bit_cast(bf2_t, a), __builtin_bit_cast(bf2_t, b), c, false); }

template <int OFF> DI void dma(unsigned lds, unsigned v, const char* sb) {
  asm volatile("s_mov_b32 m0, %0\n\ts_nop 0\n\tglobal_load_lds_dwordx4 %1, %2 offset:%3" ::"s"(lds), "v"(v), "s"(sb), "i"(OFF) : "memory");
}
template <int OFF> DI void dma_c(unsigned lds, unsigned v, const char* sb) {
  asm volatile("s_mov_b32 m0, %0\n\ts_nop 0\n\tglobal_load_lds_dwordx4 %1, %2 offset:%3 sc1" ::"s"(lds), "v"(v), "s"(sb), "i"(OFF) : "memory");
}
template <int OFF> DI void dma_n(unsigned v, const char* sb) {
  asm volatile("global_load_lds_dwordx4 %0, %1 offset:%2 sc1" ::"v"(v), "s"(sb), "i"(OFF) : "memory");
}
constexpr int cap63(int n) { return n > 63 ? 63 : n; }
template <int N> DI void wait_bar() { asm volatile("s_waitcnt vmcnt(%0)\n\ts_barrier" ::"i"(cap63(N)) : "memory"); }
DI void lds_bar() { asm volatile("s_waitcnt lgkmcnt(0)\n\ts_barrier" ::: "memory"); }
DI void all_bar() { asm volatile("s_waitcnt vmcnt(0) lgkmcnt(0)\n\ts_barrier" ::: "memory"); }
DI void bar() { asm volatile("s_barrier" ::: "memory"); }
template <bool AG, int OFF> DI void ldx1(bf16x8& d, unsigned voff, const char* sb) {
  if constexpr (AG) asm volatile("global_load_dwordx4 %0, %1, %2 offset:%3 sc1" : "=a"(d) : "v"(voff), "s"(sb), "i"(OFF));
  else asm volatile("global_load_dwordx4 %0, %1, %2 offset:%3 sc1" : "=v"(d) : "v"(voff), "s"(sb), "i"(OFF));
}
template <int OFF> DI void ld2_1(f32x2& d, unsigned voff, const char* sb) { asm volatile("global_load_dwordx2 %0, %1, %2 offset:%3 sc1" : "=v"(d) : "v"(voff), "s"(sb), "i"(OFF)); }
DI void ld1_1(unsigned& d, unsigned voff, const void* sb) { asm volatile("global_load_dword %0, %1, %2 sc1" : "=v"(d) : "v"(voff), "s"(sb) : "memory"); }
template <int OFF> DI void st4_1(u32x4 d, unsigned voff, char* sb) { asm volatile("global_store_dwordx4 %0, %1, %2 offset:%3 sc1\n\ts_nop 1" ::"v"(voff), "v"(d), "s"(sb), "i"(OFF) : "memory"); }
DI void st2_1(f32x2 d, unsigned voff, char* sb) { asm volatile("global_store_dwordx2 %0, %1, %2 sc1" ::"v"(voff), "v"(d), "s"(sb) : "memory"); }
DI void st1_1(float d, unsigned voff, char* sb) { asm volatile("global_store_dword %0, %1, %2 sc1" ::"v"(voff), "v"(d), "s"(sb) : "memory"); }
template <int N> DI void waitv() { asm volatile("s_waitcnt vmcnt(%0)" ::"i"(cap63(N)) : "memory"); }
template <bool AG, int N, class T> DI void waitx(T& d) {
  if constexpr (AG) asm volatile("s_waitcnt vmcnt(%1)" : "+a"(d) : "i"(cap63(N))); else asm volatile("s_waitcnt vmcnt(%1)" : "+v"(d) : "i"(cap63(N)));
}
template <bool SW, bool BA, bool Z, bool NOP = false> DI void mm(f32x16& acc, const bf16x8& a, const bf16x8& b) {
#define MM_(OPS, C, OUT, BC) do { if constexpr (NOP) asm volatile("s_nop 1\n\tv_mfma_f32_32x32x16_bf16 %0, " OPS ", " C : OUT(acc) : "v"(a), BC(b)); \
                                  else asm volatile("v_mfma_f32_32x32x16_bf16 %0, " OPS ", " C : OUT(acc) : "v"(a), BC(b)); } while (0)
  if constexpr (Z) {
    if constexpr (SW) { if constexpr (BA) MM_("%2, %1", "0", "=&v", "a"); else MM_("%2, %1", "0", "=&v", "v"); }
    else { if constexpr (BA) MM_("%1, %2", "0", "=&v", "a"); else MM_("%1, %2", "0", "=&v", "v"); }
  } else {
    if constexpr (SW) { if constexpr (BA) MM_("%2, %1", "%0", "+v", "a"); else MM_("%2, %1", "%0", "+v", "v"); }
    else { if constexpr (BA) MM_("%1, %2", "%0", "+v", "a"); else MM_("%1, %2", "%0", "+v", "v"); }
  }
#undef MM_
}
DI void mdone(f32x16& a) { asm volatile("s_nop 11" : "+v"(a)); }
DI void mdone(f32x16& a, f32x16& b) { asm volatile("s_nop 11" : "+v"(a), "+v"(b)); }
DI void mdone(f32x16& a, f32x16& b, f32x16& c, f32x16& d) { asm volatile("s_nop 11" : "+v"(a), "+v"(b), "+v"(c), "+v"(d)); }
template <class T> DI T opq_v(T v) { asm volatile("" : "+v"(v)); return v; }
template <int OFF> DI void ldx0(bf16x8& d, unsigned voff, const char* sb) { asm volatile("global_load_dwordx4 %0, %1, %2 offset:%3" : "=v"(d) : "v"(voff), "s"(sb), "i"(OFF)); }
template <int OFF> DI void ldx1m(bf16x8& d, unsigned voff, const char* sb, unsigned long long m) {
  asm volatile("s_mov_b64 exec, %4\n\tglobal_load_dwordx4 %0, %1, %2 offset:%3 sc1\n\ts_mov_b64 exec, -1" : "=v"(d) : "v"(voff), "s"(sb), "i"(OFF), "s"(m));
}
template <bool AG, int OFF8> DI void ds_read2(bf16x8& d, unsigned addr) {
  addr += OFF8 * 8;
  if constexpr (AG) asm volatile("ds_read2_b64 %0, %1 offset1:2" : "=a"(d) : "v"(addr) : "memory"); else asm volatile("ds_read2_b64 %0, %1 offset1:2" : "=v"(d) : "v"(addr) : "memory");
}
template <bool AG, int N> DI void waitl(bf16x8& d) {
  if constexpr (AG) asm volatile("s_waitcnt lgkmcnt(%1)" : "+a"(d) : "i"(N) : "memory"); else asm volatile("s_waitcnt lgkmcnt(%1)" : "+v"(d) : "i"(N) : "memory");
}
template <int OFF> DI void ds_write_a(unsigned addr, const bf16x8& v) { asm volatile("ds_write_b128 %0, %1 offset:%2" ::"v"(addr), "a"(v), "i"(OFF) : "memory"); }
template <int N, class T> DI void wait2(T& a, T& b) { asm volatile("s_waitcnt vmcnt(%2)" : "+v"(a), "+v"(b) : "i"(cap63(N))); }
DI void set_flag(char* flag, u64 tag) {
  unsigned z = 0;
  asm volatile("s_mov_b64 exec, 1\n\tglobal_atomic_swap_x2 %0, %1, %2 sc1\n\ts_mov_b64 exec, -1" ::"v"(z), "v"(tag), "s"(flag) : "memory");
}
DI void ld8_1(u64& d, unsigned voff, const char* sb) { asm volatile("global_load_dwordx2 %0, %1, %2 sc1" : "=v"(d) : "v"(voff), "s"(sb) : "memory"); }
template <int NL> DI void rd_flag(u64& v, unsigned voff, const char* sb) {
  const u64 zero = 0;
  if constexpr (NL == 64) asm volatile("global_atomic_or_x2 %0, %1, %3, %2 sc0 sc1" : "+v"(v) : "v"(voff), "s"(sb), "v"(zero) : "memory");
  else asm volatile("s_mov_b64 exec, %4\n\tglobal_atomic_or_x2 %0, %1, %3, %2 sc0 sc1\n\ts_mov_b64 exec, -1" : "+v"(v) : "v"(voff), "s"(sb), "v"(zero), "s"((1ull << (NL & 63)) - 1) : "memory");
}
template <int NL> DI void poll(const char* base, unsigned voff, u64 tag) {
  for (int spin = 0; spin < (1 << 15); ++spin) {
    u64 v = tag;
    rd_flag<NL>(v, voff, base);
    waitx<false, 0>(v);
    if (__all(v == tag)) break;
  }
  asm volatile("" ::: "memory");
}

template <int RBX> DI void block_map(int& rb, int& cb) {
  const int x = blockIdx.x & 7, i = blockIdx.x >> 3;
  constexpr int CBX = 32 / RBX, GC = 16 / CBX;
  rb = RBX * (x / GC) + i / CBX; cb = CBX * (x % GC) + i % CBX;
}

DI float a_fma(float a, float b, float c) { float d; asm volatile("v_fma_f32 %0, %1, %2, %3" : "=v"(d) : "v"(a), "v"(b), "v"(c)); return d; }
DI float a_fms(float a, float b, float c) { float d; asm volatile("v_fma_f32 %0, %1, -%2, %3" : "=v"(d) : "v"(a), "v"(b), "v"(c)); return d; }
DI void a_exp(float& x) { asm volatile("v_exp_f32 %0, %0" : "+v"(x)); }
DI void a_add(float& s, float x) { asm volatile("v_add_f32 %0, %0, %1" : "+v"(s) : "v"(x)); }
DI void a_mul(float& x, float s) { asm volatile("v_mul_f32 %0, %0, %1" : "+v"(x) : "v"(s)); }
template <bool NEG> DI void a_hedge(float& c, float& n, float& s, float prev, float p, float sc, float m) {
  if constexpr (NEG) asm volatile("v_exp_f32 %0, %0\n\tv_fma_f32 %1, %4, -%5, %6\n\tv_add_f32 %2, %2, %3" : "+v"(c), "=&v"(n), "+v"(s) : "v"(prev), "v"(p), "v"(sc), "v"(m));
  else asm volatile("v_exp_f32 %0, %0\n\tv_fma_f32 %1, %4, %5, %6\n\tv_add_f32 %2, %2, %3" : "+v"(c), "=&v"(n), "+v"(s) : "v"(prev), "v"(p), "v"(sc), "v"(m));
}
DI unsigned a_norm2(float a, float b, float s) {
  unsigned o; float t;
  asm volatile("v_mul_f32 %0, %2, %4\n\tv_mul_f32 %1, %3, %4\n\tv_cvt_pk_bf16_f32 %0, %0, %1" : "=&v"(o), "=&v"(t) : "v"(a), "v"(b), "v"(s));
  return o;
}
DI void a_sum16(f32x4& s, const bf16x8& sel, const bf16x8& f) { asm volatile("v_mfma_f32_16x16x32_bf16 %0, %1, %2, %0" : "+v"(s) : "v"(sel), "v"(f)); }
DI void a_mm4(float& mx, float& mn, float a, float b, float c, float d) {
  asm volatile("v_max3_f32 %0, %0, %2, %3\n\tv_min3_f32 %1, %1, %2, %3\n\tv_max3_f32 %0, %0, %4, %5\n\tv_min3_f32 %1, %1, %4, %5" : "+v"(mx), "+v"(mn) : "v"(a), "v"(b), "v"(c), "v"(d));
}
DI float a_max3(float a, float b, float c) { float d; asm volatile("v_max3_f32 %0, %1, %2, %3" : "=v"(d) : "v"(a), "v"(b), "v"(c)); return d; }
DI float a_min3(float a, float b, float c) { float d; asm volatile("v_min3_f32 %0, %1, %2, %3" : "=v"(d) : "v"(a), "v"(b), "v"(c)); return d; }

DI float opq_a(float v) { asm volatile("" : "+a"(v)); return v; }
DI void pin_a(bf16x8& a) { asm volatile("" : "+a"(a)); }
DI void pin2(f32x16& a, f32x16& b) { asm volatile("" : "+v"(a), "+v"(b)); }
DI bf16x8 cs_frag(float c) {
  const float hi = rbf(c), mid = rbf(c - hi), lo = (c - hi) - mid;
  return __builtin_bit_cast(bf16x8, u32x4{cvtpk(hi, mid), cvtpk(lo, 0.f), 0u, 0u});
}
template <bool CS> struct Red {
  bf16x8 p[4];
  float t[4], a, b, sd;
  template <int K> DI void run(int lane) {
    if constexpr (K == 0) asm volatile("" : "+v"(p[0]), "+v"(p[1]), "+v"(p[2]), "+v"(p[3]));
    if constexpr (K < 3) {
      const f32x4 u = __builtin_bit_cast(f32x4, p[K + 1]), v = __builtin_bit_cast(f32x4, p[0]);
#pragma unroll
      for (int e = 0; e < 4; ++e) { if (K == 0) t[e] = v[e]; a_add(t[e], u[e]); }
    }
    if constexpr (K == 3) {
      auto x = __builtin_amdgcn_permlane16_swap(__float_as_uint(t[0]), __float_as_uint(t[1]), false, false);
      auto y = __builtin_amdgcn_permlane16_swap(__float_as_uint(t[2]), __float_as_uint(t[3]), false, false);
      a = opq_v(__uint_as_float(x[0]) + __uint_as_float(x[1])); b = opq_v(__uint_as_float(y[0]) + __uint_as_float(y[1]));
    }
    if constexpr (K == 4) { auto x = __builtin_amdgcn_permlane32_swap(__float_as_uint(a), __float_as_uint(b), false, false); a = opq_v(__uint_as_float(x[0]) + __uint_as_float(x[1])); }
    if constexpr (K == 5) {
      if constexpr (CS) a = lane_get(a, (lane >> 2) + ((lane & 3) << 4));
      else { const int src = ((lane & 31) >> 1) + ((lane & 1) << 5); b = lane_get(a, src + 16); a = lane_get(a, src); }
    }
    if constexpr (CS) {
      if constexpr (K == 8) { t[0] = opq_v(rbf(a)); t[1] = opq_v(a - t[0]); }
      if constexpr (K == 9) { const float mid = rbf(t[1]); p[0] = opq_v(__builtin_bit_cast(bf16x8, u32x4{cvtpk(t[0], mid), cvtpk(t[1] - mid, 0.f), 0u, 0u})); }
    } else {
      if constexpr (K == 8) { const float m = a * (1.f / D_); b = opq_v(fmaxf(b * (1.f / D_) - m * m, 0.f)); a = opq_v(rbf(m)); }
      if constexpr (K == 9) b = opq_v(rbf(rbf(b) + 1e-5f));
      if constexpr (K == 10) { sd = opq_v(rbf(__builtin_amdgcn_sqrtf(b))); b = opq_v(__builtin_amdgcn_rcpf(sd)); }
    }
  }
};
DI void pin4(bf16x8 (&p)[4]) { asm volatile("" : "+v"(p[0]), "+v"(p[1]), "+v"(p[2]), "+v"(p[3])); }
DI bf16x8 mean_frag(float mean, bool on) { const float m = on ? -mean : 0.f; return __builtin_bit_cast(bf16x8, u32x4{cvtpk(m, m), cvtpk(m, 0.f), 0u, 0u}); }

#define STAMP(i)

constexpr int SLOT = 16384, XL = 12;
struct WinPtr {
  unsigned b[3];
  DI void init(unsigned a) { sfor<0, 3>([&](auto i) { b[i] = a + i * 65536u; asm volatile("" : "+v"(b[i])); }); }
  template <int OFF> DI bf16x8 frag() const { return *(LAS const bf16x8*)((LAS const char*)(uintptr_t)b[OFF >> 16] + (OFF & 65535)); }
};
struct Ctx { unsigned xv; WinPtr p; };
#define AI __attribute__((always_inline))
#define RARE(c) __builtin_expect(!!(c), 0)
#define IC(n) std::integral_constant<int, (n)>{}

template <int S> DI void ld_x(bf16x8 (&x)[64], const Ctx& c, const char* xb) { ldx1<false, (S % 8) * 1024 - 4096>(x[S], c.xv, xb + (S / 8) * 8192 + 4096); }
template <class H> constexpr int g64_wait(int s) {
  int n = 0;
  const int t0 = s - XL;
  if (t0 < 0) n += XL - 1 - s; else n += H::n(t0);
  for (int t = t0 < 0 ? 0 : t0 + 1; t < s; ++t) n += (t + XL < 64 ? 1 : 0) + H::n(t);
  return n;
}
constexpr int a_slot(int g);
template <bool MAPA, bool ZERO, class H, class FX, class FA>
DI void gemm64(const Ctx& c, bf16x8 (&x)[64], const char* xb, f32x16& acc0, f32x16& acc1, FX&& hook, FA&& aux) {
  bf16x8 f0[3], f1[3];
  auto off = [](int s) constexpr { return (MAPA ? a_slot(24 + s / 8) : s / 8) * SLOT + (s % 8) * 2048; };
  auto rd0 = [&](auto s) { f0[s % 3] = c.p.template frag<off(s)>(); };
  auto rd1 = [&](auto s) { f1[s % 3] = c.p.template frag<off(s) + 1024>(); };
  rd0(IC(0)); rd1(IC(0)); rd0(IC(1)); rd1(IC(1));
  sfor<0, 64>([&](auto s) {
    waitx<false, g64_wait<H>(s)>(x[s]);
    SBAR();
    mm<false, false, ZERO && s == 0>(acc0, f0[s % 3], x[s]);
    SBAR();
    if constexpr (s + 2 < 64) rd0(IC(s + 2));
    if constexpr (s + XL < 64) ld_x<s + XL>(x, c, xb);
    aux(IC(2 * s));
    SBAR();
    mm<false, false, ZERO && s == 0>(acc1, f1[s % 3], x[s]);
    SBAR();
    if constexpr (s + 2 < 64) rd1(IC(s + 2));
    hook(s);
    aux(IC(2 * s + 1));
    SBAR();
  });
  mdone(acc0, acc1);
}
struct HookP6 { static constexpr int n(int s) { return s >= 52 ? 1 : 0; } };
struct HookP3 { static constexpr int n(int s) { return s >= 52 ? 1 : 0; } };
struct HookP4 { static constexpr int n(int s) { return s == 52 || s == 6 || (s >= 8 && s < 12) || (s >= 14 && s < 30 && s % 2 == 0) ? 1 : 0; } };

DI void park4(u32x4 (&v)[4]) { asm volatile("" : "+a"(v[0]), "+a"(v[1]), "+a"(v[2]), "+a"(v[3])); }
DI void res_to_acc(u32x4 (&xr)[4], f32x16& a0, f32x16& a1) {
  park4(xr);
#pragma unroll
  for (int e = 0; e < 4; ++e) {
    a0[2 * e] = bflo(xr[0][e]); a0[2 * e + 1] = bfhi(xr[0][e]); a0[8 + 2 * e] = bflo(xr[1][e]); a0[9 + 2 * e] = bfhi(xr[1][e]);
    a1[2 * e] = bflo(xr[2][e]); a1[2 * e + 1] = bfhi(xr[2][e]); a1[8 + 2 * e] = bflo(xr[3][e]); a1[9 + 2 * e] = bfhi(xr[3][e]);
  }
  asm volatile("" : "+v"(a0), "+v"(a1));
}
DI void store_stats(int r, const u32x4 (&xr)[4], char* stp) {
  float s1 = 0.f, s2 = 0.f, s3 = 0.f, s4 = 0.f;
#pragma unroll
  for (int i = 0; i < 4; ++i)
#pragma unroll
    for (int e = 0; e < 4; e += 2) {
      s1 = dot2(xr[i][e], 0x3f803f80u, s1); s2 = dot2(xr[i][e], xr[i][e], s2);
      s3 = dot2(xr[i][e + 1], 0x3f803f80u, s3); s4 = dot2(xr[i][e + 1], xr[i][e + 1], s4);
    }
  st2_1(f32x2{swap_add(s1 + s3), swap_add(s2 + s4)}, (unsigned)(r * 8), stp);
}
DI void store_x(const Ctx& c, int r, const f32x16& a0, const f32x16& a1, u32x4 (&xr)[4], char* dst, char* stp) {
  SBAR();
  xr[0] = __builtin_bit_cast(u32x4, pack8<0>(a0)); st4_1<0>(xr[0], c.xv, dst); SBAR();
  xr[1] = __builtin_bit_cast(u32x4, pack8<1>(a0)); st4_1<1024>(xr[1], c.xv, dst); SBAR();
  xr[2] = __builtin_bit_cast(u32x4, pack8<0>(a1)); st4_1<2048>(xr[2], c.xv, dst); SBAR();
  xr[3] = __builtin_bit_cast(u32x4, pack8<1>(a1)); st4_1<3072>(xr[3], c.xv, dst); SBAR();
  store_stats(r, xr, stp);
  SBAR();
  park4(xr);
}

template <class FX> DI void hedge_exp(const f32x16& p, float sc, f32x16& ep, f32x16& en, FX&& hook) {
  float mx = fmaxf(p[0], p[1]), mn = fminf(p[0], p[1]);
#pragma unroll
  for (int i = 2; i < 16; i += 2) { mx = fmaxf(fmaxf(mx, p[i]), p[i + 1]); mn = fminf(fminf(mn, p[i]), p[i + 1]); }
  mx = swap_max(mx) * sc; mn = swap_min(mn) * sc;
  sfor<0, 16>([&](auto i) {
    SBAR();
    hook(i);
    constexpr int e = i;
    float a = __builtin_amdgcn_exp2f(__builtin_fmaf(p[e], sc, -mx)), b = __builtin_amdgcn_exp2f(__builtin_fmaf(p[e], -sc, mn));
    asm volatile("" : "+v"(a), "+v"(b));
    ep[e] = a; en[e] = b;
  });
  SBAR();
}

constexpr int a_slot(int g) { return g == 8 || g == 15 ? 8 : (g > 8 && g < 15 ? g - 9 : g % 8); }
constexpr bool a_bar(int s) { return (s + 2) % 8 == 0 && s < 190; }
constexpr int QP = 128 + 24, QC = 128 + 36;
constexpr int a_piece(int s, bool first) {
  constexpr int t[][3] = {{46, 8, 1}, {54, 9, 1}, {58, 10, 1}, {64, 11, 2}, {72, 12, 2}, {80, 13, 2}, {88, 14, 2}, {96, 15, 2}, {104, 16, 1}, {108, 17, 1}, {112, 18, 1},
                          {116, 19, 1}, {120, 20, 1}, {134, 21, 1}, {138, 22, 1}, {142, 23, 1}, {146, 24, 1}, {150, 25, 1}, {158, 26, 1}};
  if (!first && s >= 8 && s < 24 && s % 2 == 0) return 24 + (s - 8) / 2;
  for (auto& e : t) if (s >= e[0] && s < e[0] + 4 * e[2] && (s - e[0]) % e[2] == 0) return e[1] * 4 + (s - e[0]) / e[2];
  return -1;
}
constexpr int a_park(int s) { return s >= 129 && s < 135 ? s - 129 : (s == 162 || s == 163 ? s - 156 : -1); }
constexpr int a_ld0(bool first) { return first ? 32 : 2; }
constexpr bool a_side(int s, bool first) { return (s >= a_ld0(first) && s < a_ld0(first) + (first ? 12 : 4)) || s == 50 || (s >= 154 && s < 158) || a_park(s) >= 0; }
constexpr int f_rd(int n) { return n % 6 < 4 && n / 6 + 1 < 16 ? (n / 6 + 1) * 4 + n % 6 : -1; }
constexpr int f_in(int n) { return (n + 1) % 6 < 4 && (n + 1) / 6 >= 1 && (n + 1) / 6 + 4 < 16 ? ((n + 1) / 6 + 4) * 4 + (n + 1) % 6 : -1; }
constexpr int f_raw(int s) { return s >= 6 && s < 46 && s % 2 == 0 ? ((s + 2) / 8 + 2) * 4 + ((s + 2) % 8) / 2 : -1; }
struct SchedA {
  int xw[64], bw[192], sw[3], aw, fw, cw, pw, zw, uw, vw, lw[32];
  constexpr SchedA(bool first) : xw{}, bw{}, sw{}, aw(0), fw(0), cw(0), pw(0), zw(0), uw(0), vw(0), lw{} {
    int pos = 0, xpos[64] = {}, wpos[32] = {}, spos[192] = {}, fpos = 0, stpos = 0, ppos = 0, upos = 0, vpos = 0, lpos[34] = {};
    if (first) { xpos[3] = 4; xpos[4] = pos = 8; } else for (int s = 0; s < XL; ++s) xpos[s] = ++pos;
    for (int s = 0; s < 192; ++s) {
      if (s == 128) { pos += 3; stpos = pos; }
      if (a_bar(s)) {
        const int g = (s + 2) / 8;
        bw[s] = wpos[g] ? pos - wpos[g] : 63;
        if (s == 62 && pos - spos[50] < bw[s]) bw[s] = pos - spos[50];
        if (s == 142) { if (pos - stpos < bw[s]) bw[s] = pos - stpos; ++pos; }
      }
      if (s == 102) uw = pos - upos;
      if (s == 118) vw = pos - vpos;
      for (int k = 0; k < 3; ++k) if (s == a_ld0(first) + 14 + 4 * k) sw[k] = pos - spos[a_ld0(first) + 3 + 4 * k];
      if (s == 32) aw = pos - fpos;
      if (s == 128) fw = pos - fpos;
      if (s == 170) cw = pos - spos[157];
      if (s == QC) pw = pos - ppos;
      if (s < 64) xw[s] = first ? (s % 3 == 0 && s / 3 + 1 >= 3 && s / 3 + 1 < 16 ? pos - xpos[s / 3 + 1] : 63) : pos - xpos[s];
      if (first && s < 64 && f_in(2 * s) >= 0) xpos[f_in(2 * s) / 4] = ++pos;
      if (!first && s + XL < 64) xpos[s + XL] = ++pos;
      if ((first && s == 20) || s == 127) fpos = ++pos;
      if (s == QP) ppos = ++pos;
      if (s >= QC) lpos[s - QC] = ++pos;
      if (a_piece(s, first) >= 0) wpos[a_piece(s, first) / 4] = ++pos;
      if (a_side(s, first)) spos[s] = ++pos;
      if (first && s < 64 && f_in(2 * s + 1) >= 0) xpos[f_in(2 * s + 1) / 4] = ++pos;
      if (first && f_raw(s) >= 0) wpos[f_raw(s) / 4] = ++pos;
      if (s == 63) pos += 8;
      if (s == 87) upos = pos;
      if (s == 103) vpos = pos;
      if (s == 177) zw = pos - lpos[1];
    }
    for (int i = 0; i < 32; ++i) {
      lw[i] = pos - lpos[2 + i];
      if (i % 2 == 0 && 192 - QC + i / 2 < 34) lpos[192 - QC + i / 2] = ++pos;
      if (i % 2 == 0 && i >= 16) ++pos;
    }
  }
};
constexpr int cmin(int a, int b) { return a < b ? a : b; }

#define R_OA "v[64:79]"
#define R_OB "v[80:95]"
#define S0_(n, q, m) "v_mfma_f32_32x32x16_bf16 v[" #n ":" #n "+15], v[208:211], v[" #q ":" #q "+3], v[" #m ":" #m "+15]\n\t"
#define S1_(n, q) "v_mfma_f32_32x32x16_bf16 v[" #n ":" #n "+15], v[212:215], v[" #q "+4:" #q "+7], v[" #n ":" #n "+15]\n\t"
#define PV0_(o, p) "v_smfmac_f32_32x32x32_bf16 v[" #o ":" #o "+15], v[216:219], v[" #p ":" #p "+7], v236\n\t"
#define PV1_(o, p) "v_smfmac_f32_32x32x32_bf16 v[" #o ":" #o "+15], v[220:223], v[" #p "+8:" #p "+15], v236\n\t"
#define L0_(l, p) "v_smfmac_f32_16x16x64_bf16 v[" #l ":" #l "+3], v[224:227], v[" #p ":" #p "+7], v236\n\t"
#define L1_(l, p) "v_smfmac_f32_16x16x64_bf16 v[" #l ":" #l "+3], v[224:227], v[" #p "+8:" #p "+15], v236\n\t"
#define E_(e, i) "v_exp_f32 v[" #e "+" #i "], v[" #e "+" #i "]\n\t"
#define RD_(d, off) "ds_read_b128 v[" #d ":" #d "+3], v237 offset:" #off "\n\t"
#define HALF_(e, n, q, m, p, o, l, X0, X1, X2, X3, X4, X5) \
  S0_(n, q, m) X0 E_(e, 0) E_(e, 1) E_(e, 2) S1_(n, q) X1 E_(e, 3) E_(e, 4) E_(e, 5) PV0_(o, p) X2 E_(e, 6) E_(e, 7) E_(e, 8) \
  PV1_(o, p) X3 E_(e, 9) E_(e, 10) E_(e, 11) L0_(l, p) X4 E_(e, 12) E_(e, 13) L1_(l, p) X5 E_(e, 14) E_(e, 15)
#define LG2_ "s_waitcnt lgkmcnt(2)\n\t"
#define LG0_ "s_waitcnt lgkmcnt(0)\n\t"
#define STEP_(b, nb, kv, wo, Y0, Y1) \
  HALF_(128, 160, 200, 112, 176, 80, 232, "", "", RD_(208, b + 2048), RD_(212, b + 3072), RD_(216, b), RD_(220, b + 1024) LG2_) \
  HALF_(160, 144, 192, 96, 128, 64, 228, "", LG0_, "s_add_u32 m0, s44, " #nb "\n\t", "", "global_load_lds_dwordx4 v" #kv ", s[40:41] sc1\n\t", \
        "global_load_lds_dwordx4 v" #kv ", s[40:41] offset:1024 sc1\n\t") \
  HALF_(144, 176, 200, 112, 160, 80, 232, Y0, "", RD_(208, b + 6144), RD_(212, b + 7168), RD_(216, b + 4096), RD_(220, b + 5120) LG2_) \
  HALF_(176, 128, 192, 96, 144, 64, 228, "s_mov_b32 m0, s45\n\t", LG0_, "global_load_lds_dwordx4 v240, s[42:43] offset:" #wo " sc1\n\t", Y1, \
        "s_waitcnt vmcnt(4)\n\t", "s_barrier\n\t")
DI float max16(const f32x16& s) {
  float mx = s[0];
#pragma unroll
  for (int e = 1; e < 16; ++e) mx = fmaxf(mx, s[e]);
  return mx;
}
typedef __attribute__((ext_vector_type(8))) unsigned u32x8;
typedef __attribute__((ext_vector_type(16))) unsigned u32x16;

__global__ __launch_bounds__(256, 1) void k_main(const char* xin, const char* wqkv, const char* wphi, const char* woA, const char* wq, const char* wk, const char* wv,
                                                 const char* woB, char* ws, char* out) {
  __shared__ __attribute__((aligned(16))) char smem_[163840];
  LAS char* const smem = (LAS char*)smem_;
  const unsigned lds0 = (unsigned)(uintptr_t)smem;
  Ctx c;
  int lane = threadIdx.x & 63, w = __builtin_amdgcn_readfirstlane(threadIdx.x >> 6), r = lane & 31, h = lane >> 5, rb, cb;
  block_map<4>(rb, cb);
  int tt = rb * 4 + w, blk = (lane >> 4) & 1, q4 = (lane & 15) >> 2, p4 = lane & 3;
  c.xv = lane * 16; c.p.init(lds0 + c.xv);
  char* flags = ws + WS_FLAG;
  char* col = ws + WS_COL + (size_t)cb * 2 * MB;
  const int ut = rb * 4 + (cb >> 2), ty = cb & 3;
  char* act = ws + (size_t)rb * 2 * MB + w * 65536;
  char* acto = act + cb * 4096;
  char* fact = flags + tt * FL_I;
  char* facto = fact + cb * 8;
  char* stt = ws + WS_ST + tt * 4096;
  char* stto = stt + cb * 256;
  char* kvp = col + C_KVP + w * 32768;
  char* zp = col + C_ZP + w * 2048;
  char* fkvp = flags + (cb * 4 + w) * FL_I + 8 * FL_K;
  char* fwl = flags + cb * 4 * FL_I + 11 * FL_K;
  unsigned kvb = ut * 4096 + (ty < 2 ? ty * 1024 + (w & 1) * 8 : 2048 + (w >> 1) * 1024 + (ty & 1) * 256 + (w & 1) * 512);
  asm volatile("" : "+s"(kvb));
  char* kvf = ws + WS_KVF;
  char* fkv = flags + 12 * FL_K;
  const char* xu_p = ws + (size_t)rb * 2 * MB + 2 * STG + (cb >> 2) * 65536 + w * 16384 + 4096;
  const char* su_p = ws + WS_ST + 256 * KB + ut * 4096;
  const char* wkv = ((cb & 2) ? wv : wk) + w * 256 * 64;
  const char* wph = wphi + cb * 4096 + w * 1024;
  u64 tag = 0;
  u32x4 xr[4] = {};
  char* colw = col + w * 4096;
  unsigned ldsw = lds0 + w * 4096;
  auto wt = [&](int m, int ch) { return colw + C_WT + m * 131072 + ch * SLOT; };
  auto pc_w = [&](int m, int slot, int ch, auto i) { if constexpr (i == 0) dma_c<0>(ldsw + slot * SLOT, c.xv, wt(m, ch)); else dma_n<i * 1024>(c.xv, wt(m, ch)); };
  auto pc_a = [&](auto n) { constexpr int g = n / 4; pc_w(g < 8 ? 1 : (g < 16 ? 2 : (g < 24 ? 0 : 3)), a_slot(g), g % 8, IC(n % 4)); };
  u64 csm = ~0ull;
  float csv[4] = {};
  auto pc_m = [&](auto n) { constexpr int g = n / 4; dma_c<(n % 4) * 1024>(ldsw + a_slot(g) * SLOT, c.xv, wt(2, g % 8)); };
  auto ld_cs = [&](bf16x8& d, int m, auto i) { ldx1m<i * 1024>(d, c.xv, col + C_CS + m * 4096, csm); };
  auto raw_src = [&](int pitch, auto i) AI { const int row = lane >> 3; return (unsigned)((32 * w + 8 * i + row) * pitch + ((lane & 7) ^ (((row >> 1) & 1) << 2)) * 16 - i * 1024); };
  auto raw_in = [&](auto j, int slot, auto i) AI {
    const int hi = rb >> 3, pitch = (j == 0 || (j == 2 && hi)) ? 6144 : 2048;
    const char* src = (j == 0 ? wqkv + (hi ? 0 : 4096) : (j == 1 ? (hi ? wq : woA) : (hi ? wqkv + 2048 : woB))) + cb * 128 + (size_t)(rb & 7) * 128 * pitch;
    dma<i * 1024>(ldsw + slot * SLOT, raw_src(pitch, i), src);
  };
  auto raw_out = [&](auto j, int slot) AI {
    const int hi = rb >> 3, ch = rb & 7, m = j == 0 ? 2 - 2 * hi : (j == 1 ? 3 + hi : 5 - 4 * hi);
    LAS const char* p = smem + slot * SLOT + (w >> 1) * 8192 + (4 * h + q4) * 128 + (((((q4 >> 1) ^ w) & 1) << 2) + 2 * blk + (p4 >> 1)) * 16 + 8 * (p4 & 1);
    char* dst = col + C_WT + m * 131072 + ch * SLOT + (w >> 1) * 8192 + (w & 1) * 1024 + 4096;
    float cs = 0.f;
    sfor<0, 4>([&](auto s) AI {
      const u32x4 f = __builtin_bit_cast(u32x4, cat8(trread(p + s * 2048), trread(p + s * 2048 + 1024)));
      st4_1<s * 2048 - 4096>(f, c.xv, dst);
#pragma unroll
      for (int e = 0; e < 4; ++e) cs = dot2(f[e], 0x3f803f80u, cs);
    });
    st1_1(swap_add(cs), (unsigned)(r * 4), col + C_CS + m * 4096 + (ch * 2 + (w >> 1)) * 256 + (w & 1) * 128);
  };
  const int w0 = w; const unsigned ldsw0 = ldsw;
#define REFRESH() do { \
    w = w0; ldsw = ldsw0; \
    asm volatile("" : "+s"(w), "+s"(tag), "+s"(act), "+s"(acto), "+s"(fact), "+s"(facto), "+s"(stt), "+s"(stto), "+s"(col), "+s"(colw), "+s"(ldsw)); \
    asm volatile("" : "+v"(lane)); \
    r = lane & 31; h = lane >> 5; blk = (lane >> 4) & 1; q4 = (lane & 15) >> 2; p4 = lane & 3; c.xv = lane * 16; c.p.init(lds0 + c.xv); \
  } while (0)
  int l = 0;
  float mean, rstd;
  Red<true> cw;
  f32x16 ra0, ra1;
  bf16x8 park[16];
      constexpr int NA = 58;
      constexpr SchedA SQ(false), S1(true);
      bf16x8 x[64], f0[3], f1[3], wf[4], kp[4], pp[32], zq[2], sel[2], mf0, mf1;
      Red<false> st; Red<true> ck, cv, cq;
      f32x16 acc[6], p[2], kvs;
      u64 fv;
      float mx, mn, e[16], en[16], se, sn, sce;
      f32x4 ks0 = {}, ks1 = {};
      unsigned arow, pk0, ra, xo[4], xs;
      const char* xw = xin + (size_t)tt * 65536;
      LAS char* pk_img = smem + 6 * SLOT;
      LAS char* v_img = smem + 5 * SLOT;
      LAS char* sh = smem + 9 * SLOT + 4096;
      auto issue_x = [&](auto s) { ldx1<(s < NA), (s % 8) * 1024 - 4096>(x[s], c.xv, act + (s / 8) * 8192 + 4096); };
      auto ld_park = [&](auto i) { ldx1<true, (i % 4) * 1024>(park[i], c.xv, wt(4, 2 + i / 4)); };
      auto ldp = [&](auto i) {
        if constexpr (i >= 2) ldx1<true, ((i - 2) % 8) * 1024 - 4096>(pp[i - 2], c.xv, kvp + ((i - 2) / 8) * 8192 + 4096); else ldx1<false, i * 1024>(zq[i], c.xv, zp);
      };
      auto rd_wf = [&](auto k) { LAS const char* wp = smem + 9 * SLOT + (4 * h + q4) * 64 + blk * 32 + p4 * 8 + k * 1024; wf[k] = cat8(trread(wp), trread(wp + 512)); };
      auto rd01 = [&](auto s, auto t, auto fl) {
        if constexpr (s < 192) {
          bf16x8 f;
          if constexpr (fl && s < 64) { LAS const char* q = (LAS const char*)(uintptr_t)(ra ^ (t * 64u)) + ((s / 8 + 3) % 4) * SLOT + (s % 8) * 2048; f = cat8(trread(q), trread(q + 1024)); }
          else f = c.p.template frag<a_slot(s / 8) * SLOT + (s % 8) * 2048 + t * 1024>();
          if constexpr (t == 0) f0[s % 3] = f; else f1[s % 3] = f;
        }
      };
      auto x_in = [&](auto n) { dma<(n % 4) * 1040>(lds0 + w * 20480 + ((n / 4) % 4) * 4160, xs + (n % 4) * (16384 - 1040), xw + (n / 4) * 128); };
      auto x_out = [&](auto s) { ds_read2<(s < NA), ((s / 4) % 4) * 520>(x[s], xo[s % 4]); };
      auto x_fin = [&](auto g, auto n) { waitl<(4 * g < NA), n>(x[4 * g]); sfor<4 * g + 1, 4 * g + 4>([&](auto s) { if constexpr (s < NA) pin_a(x[s]); else x[s] = opq_v(x[s]); }); };
      auto k_in = [&](auto n) { dma<(n % 4) * 1024>(ldsw + (5 + (n / 4 + 3) % 4) * SLOT, raw_src(6144, IC(n % 4)), wqkv + 2048 + cb * 128 + (size_t)(n / 4) * 128 * 6144); };
      auto v_out = [&](auto t, auto hf) {
        *(LAS bf16x8*)(v_img + t * 8192 + w * 2048 + hf * 1024 + lane * 16) = pack8<hf>(acc[2 + t]);
      };
      auto sum_mm = [&](auto i) { waitx<true, SQ.lw[i]>(pp[i]); mm<false, true, i == 0>(kvs, sel[i & 1], pp[i]); };
      auto kmap = [&](auto gi) {
        constexpr int g = gi;
        if constexpr (g == 4) pin2(acc[0], acc[1]);
        if constexpr (g >= 6 && g < 10) mm<false, false, g % 2 == 0>(p[(g - 6) / 2], wf[g - 6], kp[g - 6]);
        if constexpr (g >= 4 && g < 8) kp[g - 4] = opq_v(pack8<g % 2>(acc[(g - 4) / 2]));
        if constexpr (g == 10) mm<true, false, false, true>(acc[2], cv.p[0], mf0);
        if constexpr (g == 11) mm<true, false, false, true>(acc[3], cv.p[0], mf1);
        if constexpr (g >= 12) {
          constexpr int hd = (g - 12) / 58, j = (g - 12) % 58, n = (j - 7) / 2;
          const f32x16& q = p[hd];
          if constexpr (j == 0) { mx = a_max3(q[0], q[1], q[2]); mn = a_min3(q[0], q[1], q[2]); mx = a_max3(mx, q[3], q[3]); mn = a_min3(mn, q[3], q[3]); }
          if constexpr (j >= 1 && j < 4) a_mm4(mx, mn, q[4 * j], q[4 * j + 1], q[4 * j + 2], q[4 * j + 3]);
          if constexpr (j == 4) mx = opq_v(swap_max(mx) * -sce);
          if constexpr (j == 5) mn = opq_v(swap_min(mn) * sce);
          if constexpr (j == 6) { e[0] = a_fma(q[0], sce, mx); en[0] = a_fms(q[0], sce, mn); se = opq_v(0.f); sn = opq_v(0.f); }
          if constexpr (j >= 7 && j < 37) {
            if constexpr (j % 2) a_hedge<false>(e[n], e[n + 1], se, n ? e[n ? n - 1 : 0] : sn, q[n + 1], sce, mx);
            else a_hedge<true>(en[n], en[n + 1], sn, n ? en[n ? n - 1 : 0] : se, q[n + 1], sce, mn);
          }
          if constexpr (j == 37) { a_exp(e[15]); a_add(se, e[14]); }
          if constexpr (j == 38) { a_exp(en[15]); a_add(sn, en[14]); }
          if constexpr (j == 39) se = opq_v(swap_add(se + e[15]));
          if constexpr (j == 40) sn = opq_v(swap_add(sn + en[15]));
          if constexpr (j == 41) { se = opq_v(__builtin_amdgcn_rcpf(se) * rstd); sn = opq_v(__builtin_amdgcn_rcpf(sn) * rstd); }
          if constexpr (j >= 42) {
            constexpr int c8 = (j - 42) / 2, o = 4 * (c8 & 3) + 2 * (j % 2);
            const unsigned u = c8 < 4 ? a_norm2(e[o], e[o + 1], se) : a_norm2(en[o], en[o + 1], sn);
            if constexpr (j % 2 == 0) pk0 = u; else *(LAS u32x2*)((LAS char*)(uintptr_t)(arow ^ (c8 << 4)) + hd * 16384) = u32x2{pk0, u};
          }
        }
      };
      auto aux = [&](auto s, auto i, auto fl) {
        constexpr int g1 = 2 * (s - a_ld0(fl) - 14) + i, g3 = 2 * (s - 128) + i;
        if constexpr (s < 64) {
          if constexpr (fl) { if constexpr (i == 0) a_sum16(ks0, sel[0], f0[s % 3]); else a_sum16(ks1, sel[0], f1[s % 3]); }
          if constexpr (g1 >= 0 && g1 < 11) st.template run<g1>(lane);
          if constexpr (g1 == 11) { mean = st.a; rstd = st.b; sce = opq_v(rstd * LOG2E); }
          if constexpr (g1 == 12) mf0 = opq_v(mean_frag(mean, h == 0));
          if constexpr (g1 == 13) mf1 = opq_v(mean_frag(mean, h == 1));
          if constexpr (fl && g1 >= 8 && g1 < 14) cv.template run<g1 - 8>(lane);
          if constexpr (fl && g1 >= 16 && g1 < 22) cq.template run<g1 - 16>(lane);
          if constexpr (fl && g1 == 18) csv[1] = opq_a(cv.a);
          if constexpr (fl && g1 == 26) csv[2] = opq_a(cq.a);
          if constexpr (g1 == 22) cv.a = csv[1] = opq_a(csv[1]);
          if constexpr (g1 == 22 || g1 == 23) cv.template run<g1 - 14>(lane);
          if constexpr (!fl) {
            if constexpr (g1 == 14) ck.a = opq_v(csv[0]);
            if constexpr (g1 == 14 || g1 == 15) ck.template run<g1 - 6>(lane);
            if constexpr (g1 == 18) mm<false, false, false, true>(acc[0], ck.p[0], mf0);
            if constexpr (g1 == 19) mm<false, false, false, true>(acc[1], ck.p[0], mf1);
          }
          if constexpr (s == 60 && i == 0) *(LAS short*)(sh + 8704 + w * 64 + (r >> 4) * 32 + ((r >> 2) & 1) * 16 + ((r >> 3) & 1) * 8 + (r & 3) * 2) = (short)(__float_as_uint(st.sd) >> 16);
          if constexpr (s == 61 && i == 0) arow = opq_v((unsigned)(uintptr_t)pk_img + (w * 32 + r) * 128 + h * 8 + ((r & 7) << 4));
          if constexpr (s >= 62) rd_wf(IC(2 * (s - 62) + i));
        } else if constexpr (s < 128) {
          kmap(IC(2 * (s - 64) + i));
        } else {
          if constexpr (g3 == 2) cq.a = csv[2] = opq_a(csv[2]);
          if constexpr (g3 == 2 || g3 == 3) cq.template run<g3 + 6>(lane);
          if constexpr (g3 == 6) mm<false, false, false, true>(acc[4], cq.p[0], mf0);
          if constexpr (g3 == 7) mm<false, false, false, true>(acc[5], cq.p[0], mf1);
          if constexpr (g3 == 92) { csv[3] = l == 0 ? cw.a : csv[3]; cw.a = csv[3]; }
          if constexpr (g3 >= 84 && g3 < 94) cw.template run<g3 - 84>(lane);
          if constexpr (g3 >= 32 && g3 < 40) {
            constexpr int n = (g3 - 32) % 4, t = (g3 - 32) / 4;
            const int kr = 16 * t + 8 * (n >> 1) + 4 * h + 2 * (n & 1);
            u32x4 sv = __builtin_bit_cast(u32x4, sel[t]); sv[n] = opq_v(r == kr ? 0x3f80u : (r == kr + 1 ? 0x3f800000u : 0u)); sel[t] = __builtin_bit_cast(bf16x8, sv);
          }
          if constexpr (g3 == 40) { sel[0] = opq_v(sel[0]); sel[1] = opq_v(sel[1]); }
          if constexpr (g3 == 42) *(LAS unsigned*)(sh + 8192 + 256 + lane * 4) = opq_v(0x3f803f80u);
          if constexpr (g3 == 99) {
            wait2<SQ.zw>(zq[0], zq[1]);
#pragma unroll
            for (int n = 0; n < 4; ++n) { e[n] = __builtin_bit_cast(f32x4, zq[0])[n]; a_add(e[n], __builtin_bit_cast(f32x4, zq[1])[n]); }
          }
          if constexpr (g3 == 101) {
            auto a = __builtin_amdgcn_permlane16_swap(__float_as_uint(e[0]), __float_as_uint(e[1]), false, false);
            auto b = __builtin_amdgcn_permlane16_swap(__float_as_uint(e[2]), __float_as_uint(e[3]), false, false);
            e[0] = opq_v(__uint_as_float(a[0]) + __uint_as_float(a[1])); e[1] = opq_v(__uint_as_float(b[0]) + __uint_as_float(b[1]));
          }
          if constexpr (g3 == 103) { auto a = __builtin_amdgcn_permlane32_swap(__float_as_uint(e[0]), __float_as_uint(e[1]), false, false); e[0] = opq_v(__uint_as_float(a[0]) + __uint_as_float(a[1])); }
          if constexpr (g3 == 105) e[1] = lane_get(e[0], lane ^ 8);
          if constexpr (g3 == 111)
            *(LAS short*)(sh + 8192 + w * 64 + (lane & 1) * 32 + ((lane >> 2) & 1) * 16 + ((lane >> 1) & 1) * 8 + (lane >> 4) * 2) = (short)cvtpk(e[0] + e[1], 0.f);
          if constexpr (g3 >= 116 && g3 < 124 && g3 % 2 == 1) rd_wf(IC((g3 - 116) / 2));
        }
      };
      const unsigned fo = (unsigned)((lane & 15) * 8 + (blk ? cb * 4 * FL_I + 10 * FL_K : tt * FL_I));
      auto step = [&](auto s, auto fl) {
        constexpr int ps = s / 64, L0 = a_ld0(fl);
        constexpr bool first = fl;
        if constexpr (s == 102) waitv<SQ.uw>();
        if constexpr (s == 118) waitv<SQ.vw>();
        if constexpr (a_bar(s) && s >= (first ? 14 : 38)) wait_bar<(ps ? cmin(S1.bw[s], SQ.bw[s]) : (first ? S1.bw[s] : SQ.bw[s]))>();
        if constexpr (s == L0 + 14 || (first && (s == L0 + 18 || s == L0 + 22))) waitv<(first ? S1.sw[(s - L0 - 14) / 4] : SQ.sw[0])>();
        if constexpr (first && s == 32) { waitx<false, S1.aw>(fv); if (RARE(!__all(fv == tag))) poll<32>(flags, fo, tag); }
        if constexpr (s == 118) { if (RARE(l == 0 && w == 0)) set_flag(fwl + rb * 8, tag); }
        if constexpr (s == 128) { waitx<false, SQ.fw>(fv); if (RARE(!__all(fv == tag))) poll<16>(fwl, lane * 8, tag); }
        if constexpr (s == 142) set_flag(fkvp + rb * 8, tag);
        if constexpr (s == QC) { waitx<false, SQ.pw>(fv); if (RARE(!__all(fv == tag))) poll<16>(fkvp, lane * 8, tag); }
        if constexpr (s == 170) waitv<SQ.cw>();
        if constexpr (s < 64 && !first) waitx<(s < NA), SQ.xw[s % 64]>(x[s % 64]);
        if constexpr (first && s % 3 == 0) waitv<S1.xw[s % 64]>();
        SBAR();
        mm<ps == 1, (s % 64 < NA), s % 64 == 0>(acc[2 * ps], f0[s % 3], x[s % 64]);
        SBAR();
        if constexpr (first && f_rd(2 * s) >= 0) x_out(IC(f_rd(2 * s)));
        if constexpr (!first) { rd01(IC(s + 2), IC(1), fl); SBAR(); }
        rd01(IC(s + 2), IC(0), fl);
        if constexpr (first && f_in(2 * s) >= 0) x_in(IC(f_in(2 * s)));
        if constexpr (s + XL < 64 && !first) issue_x(IC(s + XL));
        if constexpr (first && s == 20) { fv = tag; rd_flag<32>(fv, fo, flags); }
        if constexpr (s == 127) ld8_1(fv, (lane & 15) * 8, fwl);
        if constexpr (s == QP) { fv = tag; rd_flag<16>(fv, lane * 8, fkvp); }
        if constexpr (s >= QC) ldp(IC(s - QC));
        aux(s, IC(0), fl);
        SBAR();
        mm<ps == 1, (s % 64 < NA), s % 64 == 0>(acc[2 * ps + 1], f1[s % 3], x[s % 64]);
        SBAR();
        if constexpr (first && f_rd(2 * s + 1) >= 0) x_out(IC(f_rd(2 * s + 1)));
        if constexpr (first && s % 3 == 2 && s / 3 + 1 < 16) x_fin(IC(s / 3 + 1), IC(4));
        if constexpr (first) rd01(IC(s + 2), IC(1), fl);
        if constexpr (a_piece(s, first) >= 0) { if constexpr (first) pc_m(IC(a_piece(s, first))); else pc_a(IC(a_piece(s, first))); }
        if constexpr (s >= L0 && s < L0 + 4) ldx1<false, (s - L0) * 1024>(st.p[s - L0], c.xv, stt);
        if constexpr (first && s >= L0 + 4 && s < L0 + 8) ld_cs(cv.p[s - L0 - 4], 2, IC(s - L0 - 4));
        if constexpr (first && s >= L0 + 8 && s < L0 + 12) ld_cs(cq.p[s - L0 - 8], 0, IC(s - L0 - 8));
        if constexpr (s == 50) dma<0>(lds0 + 9 * SLOT + w * 1024, c.xv, wph);
        if constexpr (s >= 154 && s < 158) ld_cs(cw.p[s - 154], 4, IC(s - 154));
        if constexpr (a_park(s) >= 0) ld_park(IC(a_park(s)));
        if constexpr (first && f_in(2 * s + 1) >= 0) x_in(IC(f_in(2 * s + 1)));
        if constexpr (first && f_raw(s) >= 0) k_in(IC(f_raw(s)));
        aux(s, IC(1), fl);
        if constexpr (s == 87) { if (RARE(l == 0)) sfor<0, 4>([&](auto i) AI { raw_in(IC(1), 7, i); }); }
        if constexpr (s == 103) { if (RARE(l == 0)) raw_out(IC(1), 7); }
        if constexpr (s == 133) { if (RARE(l == 0)) sfor<0, 4>([&](auto i) AI { raw_in(IC(2), 8, i); }); }
        SBAR();
      };
      {
        STAMP(30);
        bf16x8 cf, kt[2];
        { const char* pc; unsigned last;
          asm volatile("s_getpc_b64 %0\n1:\n\ts_mov_b32 %1, .Lk_main_tail-1b" : "=s"(pc), "=s"(last));
          const unsigned o = (((blockIdx.x >> 3) * 4 + w) & 63) * 1024;
          ldx0<0>(cf, c.xv, pc + (o < last ? o : last)); }
        bf16x8 xt[4];
        sfor<0, 4>([&](auto i) { ldx0<i * 32>(xt[i], (unsigned)(r * 2048 + h * 16), xw + cb * 128); });
        sfor<0, 2>([&](auto i) { ldx0<0>(kt[i], (unsigned)((lane >> 3) * 6144 + (lane & 7) * 16), wqkv + 2048 + cb * 128 + (size_t)(tt * 16 + i * 8) * 6144); });
        u64 ep; ld8_1(ep, 0u, ws + WS_EPOCH);
        sfor<0, 4>([&](auto i) AI { raw_in(IC(0), 9, i); });
        xs = (unsigned)((lane >> 3) * 2048 + ((lane & 7) ^ (((lane >> 3) & 3) << 1)) * 16);
        sfor<0, 4>([&](auto i) { xo[i] = opq_v(lds0 + w * 20480 + r * 128 + (((r & 3) ^ i) << 5) + h * 8 + (r >> 3) * 16); });
        ra = opq_v(lds0 + 5 * SLOT + (4 * h + q4) * 128 + (((q4 >> 1) << 2) + 2 * blk + (p4 >> 1)) * 16 + 8 * (p4 & 1));
        { const unsigned o = (((lane >> 2) ^ (lane >> 4)) & 1) ? 0u : 0x3f803f80u; sel[0] = opq_v(__builtin_bit_cast(bf16x8, u32x4{o, o, o, o})); ks0 = opq_v(ks0); ks1 = opq_v(ks1); }
        sfor<0, 4>(x_in); sfor<0, 4>(k_in);
        asm volatile("s_waitcnt vmcnt(12)" : "+v"(cf), "+v"(ep), "+v"(xt[0]), "+v"(xt[1]), "+v"(xt[2]), "+v"(xt[3]), "+v"(kt[0]), "+v"(kt[1]));
        sfor<0, 4>([&](auto i) { xr[i] = unperm(__builtin_bit_cast(u32x4, xt[i])); });
        store_stats(r, xr, stto);
        park4(xr);
        { u64 z = ((u64)__builtin_amdgcn_readfirstlane((unsigned)(ep >> 32)) << 32 | __builtin_amdgcn_readfirstlane((unsigned)ep)) + 0x9E3779B97F4A7C15ull;
          z = (z ^ (z >> 30)) * 0xBF58476D1CE4E5B9ull; z = (z ^ (z >> 27)) * 0x94D049BB133111EBull; tag = z ^ (z >> 31); asm volatile("" : "+s"(tag)); }
        wait_bar<9>();
        raw_out(IC(0), 9);
        sfor<4, 12>(x_in); sfor<4, 12>(k_in);
        waitv<22>();
        sfor<0, 4>(x_out);
        wait_bar<0>();
        set_flag(facto, tag);
        if (w == 0) set_flag(fwl - FL_K + rb * 8, tag);
        x_fin(IC(0), IC(0));
        sfor<12, 20>(x_in);
        STAMP(31); STAMP(0); STAMP(1);
        sfor<0, 2>([&](auto s) { rd01(s, IC(0), std::true_type{}); rd01(s, IC(1), std::true_type{}); });
        sfor<0, 64>([&](auto s) { step(s, std::true_type{}); });
        {
          const int gw = blockIdx.x * 4 + w; const unsigned z = 0;
          const unsigned vo = (unsigned)(lane < 5 ? WS_KVF - WS_ST + (gw * 5 + lane) * 128 : WS_FLAG - WS_ST + gw * 128);
          asm volatile("s_mov_b64 exec, 63\n\tglobal_atomic_or %0, %1, %2 sc1\n\ts_mov_b64 exec, -1" ::"v"(vo), "v"(z), "s"(ws + WS_ST) : "memory");
          const unsigned dump = lds0 + 9 * SLOT + 4096 + w * 2048;
          dma<0>(dump, c.xv, woA + gw * 2048); dma<1024>(dump, c.xv, woA + gw * 2048);
          dma<0>(dump, c.xv, wq + gw * 2048); dma<1024>(dump, c.xv, wq + gw * 2048);
          dma<0>(dump, c.xv, woB + gw * 2048); dma<1024>(dump, c.xv, woB + gw * 2048);
          dma<0>(dump, c.xv, ((gw & 64) ? wk : wv) + (gw & 63) * 1024);
        }
        asm volatile("s_nop 7" : "+v"(ks0), "+v"(ks1));
        csv[0] = h ? ks1[0] : ks0[0];
        const bf16x8 kf = cs_frag(csv[0]);
        mm<false, false, false, true>(acc[0], kf, mf0); mm<false, false, false, true>(acc[1], kf, mf1);
      }
  asm volatile("" : "+s"(w), "+s"(tag), "+s"(act), "+s"(acto), "+s"(fact), "+s"(facto), "+s"(stt), "+s"(stto), "+s"(col), "+s"(colw), "+s"(ldsw));
  asm volatile("" : "+s"(kvp), "+s"(zp), "+s"(fkvp), "+s"(fwl), "+s"(rb), "+s"(kvf), "+s"(fkv), "+s"(xu_p), "+s"(su_p), "+s"(wkv), "+s"(csm), "+s"(l));
  for (;;) {
    {
      REFRESH();
      asm volatile("" : "+s"(kvp), "+s"(zp), "+s"(fkvp), "+s"(fwl), "+s"(rb));
      STAMP(2);
      sfor<64, 128>([&](auto s) { step(s, std::false_type{}); });
      STAMP(19);
      mdone(acc[2], acc[3]);
      v_out(IC(0), IC(0)); v_out(IC(0), IC(1)); v_out(IC(1), IC(0)); v_out(IC(1), IC(1));
      lds_bar();
      {
        const int hd = w >> 1, fh = w & 1;
        f32x16 kv, z;
        const int ch = fh * 4 + 2 * blk + (p4 >> 1), r0 = 4 * h + q4;
        LAS const char* pa = pk_img + hd * 16384 + r0 * 128 + ((ch ^ r0) << 4) + 8 * (p4 & 1);
        LAS const char* pb = v_img + hd * 8192 + lane * 16;
        bf16x8 a[8], b[8], d[8];
#pragma unroll
        for (int s = 0; s < 8; ++s) { a[s] = cat8(trread(pa + s * 2048), trread(pa + s * 2048 + 1024)); b[s] = *(LAS const bf16x8*)(pb + s * 1024); }
#pragma unroll
        for (int s = 0; s < 8; ++s) d[s] = *(LAS const bf16x8*)(sh + 8704 + s * 32 + h * 16);
        SBAR();
        sfor<0, 8>([&](auto s) { mm<false, false, s == 0>(kv, a[s], b[s]); });
        sfor<0, 8>([&](auto s) {
          mm<false, false, s == 0>(z, d[s], a[s]);
          if constexpr (s == 1) { mdone(kv); st4_1<0>(__builtin_bit_cast(u32x4, pack8<0>(kv)), c.xv, kvp + rb * 2048); }
          if constexpr (s == 2) { st4_1<1024>(__builtin_bit_cast(u32x4, pack8<1>(kv)), c.xv, kvp + rb * 2048); }
          SBAR();
        });
        mdone(z);
        st1_1(z[0], (unsigned)(r * 4), zp + rb * 128);
      }
      STAMP(3);
      sfor<128, 192>([&](auto s) { step(s, std::false_type{}); });
      mdone(acc[4], acc[5]);
      STAMP(4);
      {
        bf16x8 pq[2][4], ka[8], za[8];
        f32x16 ep, em, nm[4], dn[4];
        float ai[2], bi[2];
        char* dst = acto + STG;
        sfor<0, 4>([&](auto k) { mm<false, false, k % 2 == 0, true>(p[k / 2], wf[k], pack8<k % 2>(acc[4 + k / 2])); });
        mdone(p[0], p[1]);
        bar();
        if (RARE(l == 0)) raw_out(IC(2), 8);
        sfor<0, 2>([&](auto hd) {
          hedge_exp(p[hd], sce, ep, em, [&](auto e) {
            constexpr int i = hd * 16 + e;
            sum_mm(IC(i));
            if constexpr (i % 2 == 0 && 192 - QC + i / 2 < 34) ldp(IC(192 - QC + i / 2));
            if constexpr (i % 2 == 0 && i >= 16) pc_a(IC(108 + (i - 16) / 2));
          });
          if constexpr (hd == 1) {
            mdone(kvs);
            *(LAS bf16x8*)(sh + w * 2048 + lane * 16) = pack8<0>(kvs); *(LAS bf16x8*)(sh + w * 2048 + 1024 + lane * 16) = pack8<1>(kvs);
            SBAR();
          }
          pq[hd][0] = pack8<0>(ep); pq[hd][1] = pack8<1>(ep); pq[hd][2] = pack8<0>(em); pq[hd][3] = pack8<1>(em);
          pin4(pq[hd]);
        });
        STAMP(5);
        lds_bar();
        LAS const char* zb = sh + 8192 + (r & 1) * 256 + h * 32;
#pragma unroll
        for (int k = 0; k < 8; ++k) za[k] = *(LAS const bf16x8*)(zb + (k >> 1) * 64 + (k & 1) * 16);
#pragma unroll
        for (int k = 0; k < 8; ++k) ka[k] = *(LAS const bf16x8*)(sh + k * 1024 + lane * 16);
        SBAR();
        auto coef = [&](auto hd) {
          const float a = __builtin_amdgcn_rcpf(dn[2 * hd][1]), b = __builtin_amdgcn_rcpf(dn[2 * hd + 1][1]);
          const float inv = __builtin_amdgcn_rcpf(dn[2 * hd][0] * a + dn[2 * hd + 1][0] * b + 1e-6f);
          ai[hd] = opq_v(a * inv); bi[hd] = opq_v(b * inv);
        };
        auto fin = [&](auto hd) {
          f32x16 o;
#pragma unroll
          for (int i = 0; i < 16; ++i) o[i] = nm[2 * hd][i] * ai[hd] + nm[2 * hd + 1][i] * bi[hd];
          st4_1<hd * 2048>(__builtin_bit_cast(u32x4, pack8<0>(o)), c.xv, dst);
          st4_1<hd * 2048 + 1024>(__builtin_bit_cast(u32x4, pack8<1>(o)), c.xv, dst);
        };
        sfor<0, 8>([&](auto k) { mm<false, false, k % 2 == 0>(dn[k / 2], za[k], pq[k / 4][k % 4]); if constexpr (k % 2) pc_a(IC(116 + k / 2)); });
        sfor<0, 4>([&](auto k) { mm<false, false, k % 2 == 0>(nm[k / 2], ka[k], pq[0][k]); });
        mdone(dn[0], dn[1], dn[2], dn[3]);
        sfor<4, 8>([&](auto k) { mm<false, false, k % 2 == 0>(nm[k / 2], ka[k], pq[1][k - 4]); if constexpr (k == 4) coef(IC(0)); if constexpr (k == 6) coef(IC(1)); });
        STAMP(6);
        pin2(nm[0], nm[1]);
        fin(IC(0));
        pin2(nm[2], nm[3]);
        fin(IC(1));
        SBAR();
        sfor<120, 124>(pc_a);
        res_to_acc(xr, ra0, ra1);
        waitv<4>();
        set_flag(facto + FL_K, tag);
        sfor<124, 128>(pc_a); sfor<8, 16>(ld_park);
      }
    }
    STAMP(7);
    REFRESH();
    LAS char* reg = smem + 8 * SLOT;
    bf16x8 fu[16], cu;
    {
      f32x16 &a0 = ra0, &a1 = ra1;
      poll<16>(fact + FL_K, lane * 8, tag);
      sfor<0, 16>([&](auto i) { pin_a(park[i]); });
      STAMP(8);
      bf16x8 x[64];
      const char* xb = act + STG;
      sfor<0, XL>([&](auto s) { ld_x<s>(x, c, xb); });
      bar();
      asm volatile("" : "+s"(wkv));
      const unsigned kvv = (unsigned)((lane >> 1) * 64 + (cb & 1) * 32 + (lane & 1) * 16);
      const unsigned wa = ldsw + c.xv, wb = wa + 65536;
      auto unpark = [&](auto i) {
        constexpr int off = a_slot(26 + i / 4) * SLOT + (i % 4) * 1024;
        ds_write_a<(off & 65535)>(off < 65536 ? wa : wb, park[i]);
      };
      gemm64<true, false, HookP3>(c, x, xb, a0, a1, [&](auto s) {
        if constexpr (s == 33) bar();
        if constexpr (s >= 36 && s < 44) unpark(IC(s - 36));
        if constexpr (s == 49) bar();
        if constexpr (s >= 50 && s < 58) unpark(IC(s - 42));
        if constexpr (s >= 52 && s < 60) dma<0>(lds0 + 8 * SLOT + w * 8192 + (s - 52) * 1024, kvv, wkv + (s - 52) * 32 * 64);
        if constexpr (s >= 60) pc_w(4, a_slot(24), 0, IC(s - 60));
      }, [](auto) {});
      STAMP(9);
      store_x(c, r, a0, a1, xr, acto + 2 * STG, stto + 256 * 1024);
      asm volatile("s_waitcnt vmcnt(5) lgkmcnt(0)\n\ts_barrier" ::: "memory");
      { LAS const char* p = reg + w * 8192 + (4 * h + q4) * 32 + p4 * 8;
#pragma unroll
        for (int i = 0; i < 16; ++i) fu[i] = cat8(trread(p + i * 512), trread(p + i * 512 + 256));
        SBAR();
        float t0 = 0.f, t1 = 0.f, t2 = 0.f, t3 = 0.f;
#pragma unroll
        for (int i = 0; i < 16; ++i) {
          const u32x4 f = __builtin_bit_cast(u32x4, fu[i]);
          t0 = dot2(f[0], 0x3f803f80u, t0); t1 = dot2(f[1], 0x3f803f80u, t1); t2 = dot2(f[2], 0x3f803f80u, t2); t3 = dot2(f[3], 0x3f803f80u, t3);
        }
        cu = opq_v(cs_frag(swap_add((t0 + t1) + (t2 + t3)))); }
      waitv<0>();
      set_flag(facto + 2 * FL_K, tag);
      sfor<0, 4>([&](auto i) { pc_w(4, a_slot(25), 1, i); });
    }
    STAMP(10);
    REFRESH();
    f32x16 a0, a1;
    asm volatile("" : "+s"(kvf), "+s"(fkv), "+s"(xu_p), "+s"(su_p));
    {
      poll<16>(fact + 2 * FL_K, lane * 8, tag);
      bar();
      STAMP(11);
      const char* xb = act + 2 * STG; const char* stp = stt + 256 * 1024;
      bf16x8 x[64], xu[16], mf0, mf1;
      Red<false> su, sp;
      sfor<0, 4>([&](auto i) { ldx1<false, i * 1024>(su.p[i], c.xv, su_p); });
      sfor<0, 16>([&](auto i) { ldx1<true, (i % 8) * 1024 - 4096>(xu[i], c.xv, xu_p + (i / 8) * 8192); });
      f32x16 au, av;
      sfor<0, 16>([&](auto i) {
        waitx<true, 15 - i + (i < XL ? i : XL)>(xu[i]);
        SBAR();
        mm<false, true, i == 0>(au, fu[i], xu[i]);
        if constexpr (i < XL) ld_x<i>(x, c, xb);
        mm<true, true, i == 0>(av, fu[i], xu[i]);
        if constexpr (i < 11) su.template run<i>(lane);
        if constexpr (i == 12) mf0 = opq_v(mean_frag(su.a, h == 0));
        if constexpr (i == 14) { mm<false, false, false>(au, cu, mf0); mm<true, false, false>(av, cu, mf0); }
        SBAR();
      });
      STAMP(12);
      u64 fa;
      f32x4 t[4], ru;
      unsigned vo;
      gemm64<true, true, HookP4>(c, x, xb, a0, a1, [&](auto s) {
        if constexpr (s == 1) {
          pin2(au, av);
#pragma unroll
          for (int gq = 0; gq < 6; ++gq) {
            const f32x16& a = gq < 4 ? av : au;
            *(LAS f32x4*)(reg + w * 8192 + gq * 1024 + lane * 16) = f32x4{a[4 * (gq & 3)], a[4 * (gq & 3) + 1], a[4 * (gq & 3) + 2], a[4 * (gq & 3) + 3]};
          }
        }
        if constexpr (s == 3) {
          lds_bar();
#pragma unroll
          for (int j = 0; j < 4; ++j) t[j] = *(LAS const f32x4*)(reg + j * 8192 + (ty < 2 ? 4 + (w & 1) : w) * 1024 + lane * 16);
        }
        if constexpr (s == 6) st2_1(__builtin_bit_cast(f32x2, u32x2{cvtpk(t[0][0], t[0][1]), cvtpk(t[0][2], t[0][3])}), vo, kvf);
        if constexpr (s >= 8 && s < 12) ldx1<false, (s - 8) * 1024>(sp.p[s - 8], c.xv, stp);
        if constexpr (s == 7 + XL) { bar(); if (RARE(w == 0)) set_flag(fkv + rb * 4 * FL_I + cb * 8, tag); }
        if constexpr (s >= 14 && s < 30 && s % 2 == 0) pc_w(4, a_slot(30 + (s - 14) / 8), 6 + (s - 14) / 8, IC(((s - 14) / 2) % 4));
        if constexpr (s == 44) bar();
        if constexpr (s == 52) ld8_1(fa, (lane & 15) * 8 + (w * 4 + (lane >> 4)) * 4 * FL_I, fkv);
      }, [&](auto g) {
        if constexpr (g == 0) {
#pragma unroll
          for (int e = 0; e < 4; ++e) ru[e] = lane_get(su.b, ty < 2 ? r : 8 * w + 4 * h + e);
        }
        if constexpr (g == 4) {
          const unsigned m = (unsigned)(ty >> 1) - 1u;
          vo = opq_v(kvb + (((unsigned)lane * 16 & m) | ((unsigned)((r & 15) * 16 + h * 8) & ~m)));
        }
        if constexpr (g >= 9 && g < 13) {
#pragma unroll
          for (int e = 0; e < 4; ++e) { float v = t[0][e]; if constexpr (g < 12) a_add(v, t[g - 8][e]); else a_mul(v, ru[e]); t[0][e] = v; }
        }
        if constexpr (g >= 48 && g < 59) sp.template run<g - 48>(lane);
        if constexpr (g == 60) mf0 = opq_v(mean_frag(sp.a, h == 0));
        if constexpr (g == 61) mf1 = opq_v(mean_frag(sp.a, h == 1));
        if constexpr (g == 64) mm<false, false, false>(a0, cw.p[0], mf0);
        if constexpr (g == 65) mm<false, false, false>(a1, cw.p[0], mf1);
      });
      rstd = sp.b;
      waitx<false, 0>(fa);
      if (RARE(!__all(fa == tag))) {
#pragma unroll 1
      for (int spin = 0; spin < (1 << 15) && !__all(fa == tag); ++spin) { __builtin_amdgcn_s_sleep(8); ld8_1(fa, (lane & 15) * 8 + (w * 4 + (lane >> 4)) * 4 * FL_I, fkv); waitx<false, 0>(fa); }
      }
      bar();
      STAMP(13);
    }
    STAMP(14);
    {
      u32x16 qq, misc;
      misc[14] = misc[15] = __float_as_uint(rstd * QSCALE);
      int attempt = 0;
#pragma unroll 1
      for (;;) {
        f32x16 oa, ob, t0, t1, t2, t3;
        u32x16 kvr; u32x4 offs;
        offs[0] = c.xv;
        const char* ks = kvf + 2048 + w * 2048; const char* wsr = wt(5, 0);
        unsigned wm0 = ldsw, cnt = 8;
        asm volatile(
            "global_load_dwordx4 v[208:211], v240, %[kv] sc1\n\tglobal_load_dwordx4 v[212:215], v240, %[kv] offset:1024 sc1\n\t"
            "s_mov_b32 m0, s44\n\tv_add_u32 v241, 0x2000, v240\n\t"
            "global_load_lds_dwordx4 v240, s[40:41] sc1\n\tglobal_load_lds_dwordx4 v240, s[40:41] offset:1024 sc1\n\t"
            "s_add_u32 m0, s44, 0x2000\n\tv_add_u32 v242, 0x4000, v240\n\t"
            "global_load_lds_dwordx4 v241, s[40:41] sc1\n\tglobal_load_lds_dwordx4 v241, s[40:41] offset:1024 sc1\n\t"
            "s_add_u32 m0, s44, 0x4000\n\tv_add_u32 v243, 0x6000, v240\n\t"
            "global_load_lds_dwordx4 v242, s[40:41] sc1\n\tglobal_load_lds_dwordx4 v242, s[40:41] offset:1024 sc1\n\t"
            "s_add_u32 s40, s40, 0x6000\n\ts_addc_u32 s41, s41, 0\n\t"
            "s_cmp_lg_u32 %[att], 0\n\ts_cbranch_scc1 2f\n\t"
            "v_pk_mul_f32 v[128:129], v[128:129], v[238:239]\n\t"
            "v_pk_mul_f32 v[130:131], v[130:131], v[238:239]\n\t"
            "v_pk_mul_f32 v[132:133], v[132:133], v[238:239]\n\t"
            "v_pk_mul_f32 v[134:135], v[134:135], v[238:239]\n\t"
            "v_pk_mul_f32 v[136:137], v[136:137], v[238:239]\n\t"
            "v_pk_mul_f32 v[138:139], v[138:139], v[238:239]\n\t"
            "v_pk_mul_f32 v[140:141], v[140:141], v[238:239]\n\t"
            "v_pk_mul_f32 v[142:143], v[142:143], v[238:239]\n\t"
            "v_pk_mul_f32 v[144:145], v[144:145], v[238:239]\n\t"
            "v_pk_mul_f32 v[146:147], v[146:147], v[238:239]\n\t"
            "v_pk_mul_f32 v[148:149], v[148:149], v[238:239]\n\t"
            "v_pk_mul_f32 v[150:151], v[150:151], v[238:239]\n\t"
            "v_pk_mul_f32 v[152:153], v[152:153], v[238:239]\n\t"
            "v_pk_mul_f32 v[154:155], v[154:155], v[238:239]\n\t"
            "v_pk_mul_f32 v[156:157], v[156:157], v[238:239]\n\t"
            "v_pk_mul_f32 v[158:159], v[158:159], v[238:239]\n\t"
            "v_cvt_pk_bf16_f32 v192, v128, v129\n\t"
            "v_cvt_pk_bf16_f32 v193, v130, v131\n\t"
            "v_cvt_pk_bf16_f32 v194, v132, v133\n\t"
            "v_cvt_pk_bf16_f32 v195, v134, v135\n\t"
            "v_cvt_pk_bf16_f32 v196, v136, v137\n\t"
            "v_cvt_pk_bf16_f32 v197, v138, v139\n\t"
            "v_cvt_pk_bf16_f32 v198, v140, v141\n\t"
            "v_cvt_pk_bf16_f32 v199, v142, v143\n\t"
            "v_cvt_pk_bf16_f32 v200, v144, v145\n\t"
            "v_cvt_pk_bf16_f32 v201, v146, v147\n\t"
            "v_cvt_pk_bf16_f32 v202, v148, v149\n\t"
            "v_cvt_pk_bf16_f32 v203, v150, v151\n\t"
            "v_cvt_pk_bf16_f32 v204, v152, v153\n\t"
            "v_cvt_pk_bf16_f32 v205, v154, v155\n\t"
            "v_cvt_pk_bf16_f32 v206, v156, v157\n\t"
            "v_cvt_pk_bf16_f32 v207, v158, v159\n\t"
            "2:\n\t"
            "v_mov_b64 v[64:65], 0\n\t"
            "v_mov_b64 v[66:67], 0\n\t"
            "v_mov_b64 v[68:69], 0\n\t"
            "v_mov_b64 v[70:71], 0\n\t"
            "v_mov_b64 v[72:73], 0\n\t"
            "v_mov_b64 v[74:75], 0\n\t"
            "v_mov_b64 v[76:77], 0\n\t"
            "v_mov_b64 v[78:79], 0\n\t"
            "v_mov_b64 v[80:81], 0\n\t"
            "v_mov_b64 v[82:83], 0\n\t"
            "v_mov_b64 v[84:85], 0\n\t"
            "v_mov_b64 v[86:87], 0\n\t"
            "v_mov_b64 v[88:89], 0\n\t"
            "v_mov_b64 v[90:91], 0\n\t"
            "v_mov_b64 v[92:93], 0\n\t"
            "v_mov_b64 v[94:95], 0\n\t"
            "v_mov_b64 v[176:177], 0\n\t"
            "v_mov_b64 v[178:179], 0\n\t"
            "v_mov_b64 v[180:181], 0\n\t"
            "v_mov_b64 v[182:183], 0\n\t"
            "v_mov_b64 v[184:185], 0\n\t"
            "v_mov_b64 v[186:187], 0\n\t"
            "v_mov_b64 v[188:189], 0\n\t"
            "v_mov_b64 v[190:191], 0\n\t"
            "v_mov_b64 v[216:217], 0\n\t"
            "v_mov_b64 v[218:219], 0\n\t"
            "v_mov_b64 v[220:221], 0\n\t"
            "v_mov_b64 v[222:223], 0\n\t"
            "v_mov_b64 v[228:229], 0\n\t"
            "v_mov_b64 v[230:231], 0\n\t"
            "v_mov_b64 v[232:233], 0\n\t"
            "v_mov_b64 v[234:235], 0\n\t"
            "v_bfe_u32 v226, v240, 6, 1\n\tv_mov_b32 v236, 0xdddddddd\n\tv_mul_u32_u24 v226, 0x3f80, v226\n\tv_add_u32 v237, %[rg], v240\n\t"
            "v_lshl_or_b32 v226, v226, 16, v226\n\tv_mov_b32 v227, v226\n\tv_xor_b32 v224, 0x3f803f80, v226\n\tv_mov_b32 v225, v224\n\t"
            "s_waitcnt vmcnt(6)\n\ts_cbranch_scc1 3f\n\t"
            "v_mfma_f32_32x32x16_bf16 v[128:143], v[208:211], v[192:195], 0\n\tv_mfma_f32_32x32x16_bf16 v[160:175], v[208:211], v[200:203], 0\n\t"
            "v_mfma_f32_32x32x16_bf16 v[128:143], v[212:215], v[196:199], v[128:143]\n\tv_mfma_f32_32x32x16_bf16 v[160:175], v[212:215], v[204:207], v[160:175]\n\t"
            "s_nop 11\n\t"
            "v_max3_f32 v238, v128, v129, v130\n\tv_max3_f32 v239, v160, v161, v162\n\t"
            "v_max3_f32 v238, v238, v131, v132\n\tv_max3_f32 v239, v239, v163, v164\n\t"
            "v_max3_f32 v238, v238, v133, v134\n\tv_max3_f32 v239, v239, v165, v166\n\t"
            "v_max3_f32 v238, v238, v135, v136\n\tv_max3_f32 v239, v239, v167, v168\n\t"
            "v_max3_f32 v238, v238, v137, v138\n\tv_max3_f32 v239, v239, v169, v170\n\t"
            "v_max3_f32 v238, v238, v139, v140\n\tv_max3_f32 v239, v239, v171, v172\n\t"
            "v_max3_f32 v238, v238, v141, v142\n\tv_max3_f32 v239, v239, v173, v174\n\t"
            "v_max3_f32 v238, v238, v143, v143\n\tv_max3_f32 v239, v239, v175, v175\n\t"
            "v_mov_b32 v96, v238\n\tv_mov_b32 v97, v239\n\ts_nop 1\n\tv_permlane32_swap_b32 v238, v96\n\tv_permlane32_swap_b32 v239, v97\n\t"
            "v_max_f32 v238, v238, v96\n\tv_max_f32 v239, v239, v97\n\t"
            "3:\n\t"
            "v_xor_b32 v96, 0x80000000, v238\n\tv_xor_b32 v112, 0x80000000, v239\n\tv_mov_b32 v97, v96\n\tv_mov_b32 v113, v112\n\t"
            "v_mov_b64 v[98:99], v[96:97]\n\tv_mov_b64 v[114:115], v[112:113]\n\t"
            "v_mov_b64 v[100:101], v[96:97]\n\tv_mov_b64 v[116:117], v[112:113]\n\t"
            "v_mov_b64 v[102:103], v[96:97]\n\tv_mov_b64 v[118:119], v[112:113]\n\t"
            "v_mov_b64 v[104:105], v[96:97]\n\tv_mov_b64 v[120:121], v[112:113]\n\t"
            "v_mov_b64 v[106:107], v[96:97]\n\tv_mov_b64 v[122:123], v[112:113]\n\t"
            "v_mov_b64 v[108:109], v[96:97]\n\tv_mov_b64 v[124:125], v[112:113]\n\t"
            "v_mov_b64 v[110:111], v[96:97]\n\tv_mov_b64 v[126:127], v[112:113]\n\t"
            "s_nop 1\n\t" S0_(128, 192, 96) S1_(128, 192)
            "s_waitcnt vmcnt(4)\n\ts_barrier\n\ts_nop 8\n\t"
            "1:\n\t"
            STEP_(0, 24576, 240, 0, "s_sub_u32 %[cnt], %[cnt], 1\n\t", "") STEP_(8192, 0, 241, 1024, "", "") STEP_(16384, 8192, 242, 2048, "", "")
            STEP_(24576, 16384, 243, 3072, "s_add_u32 s40, s40, 0x8000\n\ts_addc_u32 s41, s41, 0\n\t", "s_add_u32 s42, s42, 0x4000\n\ts_addc_u32 s43, s43, 0\n\t")
            "s_add_u32 s45, s45, 0x4000\n\ts_cmp_lg_u32 %[cnt], 0\n\ts_cbranch_scc1 1b\n\t"
            L0_(232, 176) L1_(232, 176) PV0_(80, 176) PV1_(80, 176)
            "s_nop 11\n\t"
            : "=&{v[64:79]}"(oa), "=&{v[80:95]}"(ob), "=&{v[96:111]}"(t0), "=&{v[112:127]}"(t1), "+{v[128:143]}"(a0), "+{v[144:159]}"(a1), "=&{v[160:175]}"(t2),
              "=&{v[176:191]}"(t3), "+{v[192:207]}"(qq), "=&{v[208:223]}"(kvr), "+{v[224:239]}"(misc), "+{v[240:243]}"(offs),
              "+{s[40:41]}"(ks), "+{s[42:43]}"(wsr), "+{s45}"(wm0), [cnt] "+s"(cnt)
            : "{s44}"(lds0 + 8 * SLOT + w * 2048), [kv] "s"(kvf), [att] "s"(attempt), [rg] "s"(lds0 + 8 * SLOT)
            : "scc", "memory");
        const float la = __uint_as_float(misc[4]), lb = __uint_as_float(misc[8]);
        const float ia = __builtin_amdgcn_rcpf(la), ib = __builtin_amdgcn_rcpf(lb);
#pragma unroll
        for (int e = 0; e < 16; ++e) { oa[e] *= ia; ob[e] *= ib; }
        char* dst = acto + 3 * STG;
        st4_1<0>(__builtin_bit_cast(u32x4, pack8<0>(oa)), c.xv, dst); st4_1<1024>(__builtin_bit_cast(u32x4, pack8<1>(oa)), c.xv, dst);
        st4_1<2048>(__builtin_bit_cast(u32x4, pack8<0>(ob)), c.xv, dst); st4_1<3072>(__builtin_bit_cast(u32x4, pack8<1>(ob)), c.xv, dst);
        float chk = 0.f;
#pragma unroll
        for (int e = 0; e < 16; ++e) chk = __builtin_fmaf(oa[e], 0.f, __builtin_fmaf(ob[e], 0.f, chk));
        const bool bad = !(la < 1.2676506e30f) || !(lb < 1.2676506e30f) || !(chk == 0.f);
        wait_bar<4>();
        LAS volatile int* vote = (LAS volatile int*)(smem + 8 * SLOT);
        if (lane == 0) vote[w] = 0;
        if (attempt == 0 && bad) vote[w] = 1;
        lds_bar();
        const int redo = vote[0] | vote[1] | vote[2] | vote[3];
        if (!RARE(__builtin_amdgcn_readfirstlane(redo))) break;
        lds_bar();
        {
          const bf16x8 qa0 = __builtin_bit_cast(bf16x8, __builtin_shufflevector(qq, qq, 0, 1, 2, 3)), qa1 = __builtin_bit_cast(bf16x8, __builtin_shufflevector(qq, qq, 4, 5, 6, 7));
          const bf16x8 qb0 = __builtin_bit_cast(bf16x8, __builtin_shufflevector(qq, qq, 8, 9, 10, 11)), qb1 = __builtin_bit_cast(bf16x8, __builtin_shufflevector(qq, qq, 12, 13, 14, 15));
          float mA = -INFINITY, mB = -INFINITY;
          for (int t = 0; t < 64; ++t) {
            bf16x8 k0, k1;
            ldx1<false, 0>(k0, c.xv, kvf + t * 4096); ldx1<false, 1024>(k1, c.xv, kvf + t * 4096);
            wait2<0>(k0, k1);
            mm<false, false, true>(a0, k0, qa0); mm<false, false, false>(a0, k1, qa1); mm<false, false, true>(a1, k0, qb0); mm<false, false, false>(a1, k1, qb1);
            mdone(a0, a1);
            mA = fmaxf(mA, max16(a0)); mB = fmaxf(mB, max16(a1));
          }
          misc[14] = __float_as_uint(swap_max(mA)); misc[15] = __float_as_uint(swap_max(mB));
        }
        attempt = 1;
      }
      res_to_acc(xr, ra0, ra1);
      waitv<0>();
      set_flag(facto + 3 * FL_K, tag);
    }
    STAMP(15);
    REFRESH();
    {
      f32x16 &a0 = ra0, &a1 = ra1;
      poll<16>(fact + 3 * FL_K, lane * 8, tag);
      STAMP(16);
      bf16x8 x[64];
      const char* xb = act + 3 * STG;
      sfor<0, XL>([&](auto s) { ld_x<s>(x, c, xb); });
      bar();
      gemm64<false, false, HookP6>(c, x, xb, a0, a1, [&](auto s) {
        if constexpr (s == 33) bar();
        if constexpr (s >= 52) pc_a(IC(s - 52));
      }, [](auto) {});
      STAMP(17);
      if (l == 0) {
        store_x(c, r, a0, a1, xr, acto + 4 * STG, stto + 512 * 1024);
        bar();
        sfor<12, 16>(pc_a);
        waitv<4>();
        set_flag(facto + 4 * FL_K, tag);
        sfor<16, 24>(pc_a);
      } else {
        LAS char* im = smem + 8 * SLOT + w * 4096;
        sfor<0, 8>([&](auto n) {
          constexpr int g = n;
          const f32x16& a = g < 4 ? a0 : a1;
          *(LAS u32x2*)(im + r * 128 + ((g ^ (r & 7)) << 4) + h * 8) = u32x2{cvtpk(a[4 * (g & 3)], a[4 * (g & 3) + 1]), cvtpk(a[4 * (g & 3) + 2], a[4 * (g & 3) + 3])};
        });
        char* dst = out + (size_t)tt * 65536 + cb * 128;
        sfor<0, 4>([&](auto i) {
          const u32x4 v = *(LAS const u32x4*)(im + i * 1024 + lane * 16);
          st4_1<0>(v, (unsigned)((i * 8 + (lane >> 3)) * 2048 + (((lane & 7) ^ (lane >> 3)) << 4)), dst);
        });
        if (tt == 0 && cb == 0) set_flag(ws + WS_EPOCH, tag);
      }
    }
    STAMP(18);
    if (l == 1) break;
    csm = 1;
    act += 4 * STG; acto += 4 * STG; fact += 4 * FL_K; facto += 4 * FL_K; stt += 512 * KB; stto += 512 * KB;
    kvp += 131072; zp += 8192; fkvp += FL_K; kvf += 320 * KB; fkv += FL_K; xu_p += 4 * STG; su_p += 512 * KB;
    l = 1;
    REFRESH();
    STAMP(0);
        poll<16>(fact, lane * 8, tag);
        STAMP(1);
        sfor<0, XL>(issue_x);
        wait_bar<XL>();
        sfor<0, 2>([&](auto s) { rd01(s, IC(0), std::false_type{}); rd01(s, IC(1), std::false_type{}); });
        sfor<0, 64>([&](auto s) { step(s, std::false_type{}); });
        asm volatile("s_mov_b32 m0, %0\n\ts_mov_b64 exec, 1\n\t.rept 8\n\tglobal_load_lds_dwordx4 %1, %2\n\t.endr\n\ts_mov_b64 exec, -1\n.Lk_main_tail:" ::"s"(lds0 + 9 * SLOT + 4096), "v"(c.xv), "s"(wph) : "memory");
  }
}

extern "C" void kernel_launch(void* const* d_in, const int* in_sizes, int n_in, void* d_out, int out_size, void* d_ws, size_t ws_size, hipStream_t stream) {
  k_main<<<256, 256, 0, stream>>>((const char*)d_in[0], (const char*)d_in[1], (const char*)d_in[2], (const char*)d_in[3], (const char*)d_in[5], (const char*)d_in[6],
                                  (const char*)d_in[7], (const char*)d_in[8], (char*)d_ws, (char*)d_out);
}
```

```cpp
#include <hip/hip_runtime.h>
#include <hip/hip_bf16.h>
#include <stdint.h>
#include <type_traits>

typedef __attribute__((ext_vector_type(8))) short bf16x8;
typedef __attribute__((ext_vector_type(4))) short s16x4;
typedef __attribute__((ext_vector_type(2))) __bf16 bf2_t;
typedef __attribute__((ext_vector_type(16))) float f32x16;
typedef __attribute__((ext_vector_type(4))) float f32x4;
typedef __attribute__((ext_vector_type(2))) float f32x2;
typedef __attribute__((ext_vector_type(4))) unsigned u32x4;
typedef __attribute__((ext_vector_type(2))) unsigned u32x2;
#define LAS __attribute__((address_space(3)))
#define DI __device__ __forceinline__
#define MFMA32(a, b, c) __builtin_amdgcn_mfma_f32_32x32x16_bf16((a), (b), (c), 0, 0, 0)
#define MFMA16(a, b, c) __builtin_amdgcn_mfma_f32_16x16x32_bf16((a), (b), (c), 0, 0, 0)
#define SBAR() __builtin_amdgcn_sched_barrier(0)

constexpr int S_ = 2048, D_ = 1024;
constexpr float LOG2E = 1.4426950408889634f;
constexpr float QSCALE = 0.17677669529663687f * LOG2E;

constexpr size_t MB = 1u << 20, KB = 1024;
constexpr size_t STG = 256 * KB;
constexpr size_t WS_COL = 32 * MB;
constexpr size_t C_WT = 0;
constexpr size_t C_KVP = 768 * KB;
constexpr size_t C_ZP = 1024 * KB;
constexpr size_t C_CS = 1040 * KB;
constexpr size_t WS_ST = 64 * MB;
constexpr size_t WS_KVF = WS_ST + 1024 * KB;
constexpr size_t WS_FLAG = WS_ST + 1664 * KB;
constexpr size_t WS_EPOCH = WS_ST + 1920 * KB;
constexpr int FL_I = 2048, FL_K = 128;
typedef unsigned long long u64;

template <int I, int N, class F> DI void sfor(F&& f) { if constexpr (I < N) { f(std::integral_constant<int, I>{}); sfor<I + 1, N>(f); } }

DI unsigned cvtpk(float lo, float hi) { f32x2 v = {lo, hi}; return __builtin_bit_cast(unsigned, __builtin_convertvector(v, bf2_t)); }
DI float bflo(unsigned u) { return __uint_as_float(u << 16); }
DI float bfhi(unsigned u) { return __uint_as_float(u & 0xffff0000u); }
DI float rbf(float x) { return bflo(cvtpk(x, x)); }
DI float swap_add(float v) { auto r = __builtin_amdgcn_permlane32_swap(__float_as_uint(v), __float_as_uint(v), false, false); return __uint_as_float(r[0]) + __uint_as_float(r[1]); }
DI float swap_max(float v) { auto r = __builtin_amdgcn_permlane32_swap(__float_as_uint(v), __float_as_uint(v), false, false); return fmaxf(__uint_as_float(r[0]), __uint_as_float(r[1])); }
DI float swap_min(float v) { auto r = __builtin_amdgcn_permlane32_swap(__float_as_uint(v), __float_as_uint(v), false, false); return fminf(__uint_as_float(r[0]), __uint_as_float(r[1])); }
template <int S> DI bf16x8 pack8(const f32x16& v) {
  u32x4 p = {cvtpk(v[8 * S], v[8 * S + 1]), cvtpk(v[8 * S + 2], v[8 * S + 3]), cvtpk(v[8 * S + 4], v[8 * S + 5]), cvtpk(v[8 * S + 6], v[8 * S + 7])};
  return __builtin_bit_cast(bf16x8, p);
}
DI u32x4 unperm(u32x4 v) {
  auto r0 = __builtin_amdgcn_permlane32_swap(v[0], v[2], false, false);
  auto r1 = __builtin_amdgcn_permlane32_swap(v[1], v[3], false, false);
  return u32x4{r0[0], r1[0], r0[1], r1[1]};
}
DI float lane_get(float v, int src) { return __uint_as_float(__builtin_amdgcn_ds_bpermute(src << 2, __float_as_uint(v))); }
DI s16x4 trread(LAS const char* p) { return __builtin_amdgcn_ds_read_tr16_b64_v4i16((LAS s16x4*)p); }
DI bf16x8 cat8(s16x4 a, s16x4 b) { return __builtin_shufflevector(a, b, 0, 1, 2, 3, 4, 5, 6, 7); }
DI float dot2(unsigned a, unsigned b, float c) { return __builtin_amdgcn_fdot2_f32_bf16(__builtin_bit_cast(bf2_t, a), __builtin_bit_cast(bf2_t, b), c, false); }

template <int OFF> DI void dma(unsigned lds, unsigned v, const char* sb) {
  asm volatile("s_mov_b32 m0, %0\n\ts_nop 0\n\tglobal_load_lds_dwordx4 %1, %2 offset:%3" ::"s"(lds), "v"(v), "s"(sb), "i"(OFF) : "memory");
}
template <int OFF> DI void dma_c(unsigned lds, unsigned v, const char* sb) {
  asm volatile("s_mov_b32 m0, %0\n\ts_nop 0\n\tglobal_load_lds_dwordx4 %1, %2 offset:%3 sc1" ::"s"(lds), "v"(v), "s"(sb), "i"(OFF) : "memory");
}
template <int OFF> DI void dma_n(unsigned v, const char* sb) {
  asm volatile("global_load_lds_dwordx4 %0, %1 offset:%2 sc1" ::"v"(v), "s"(sb), "i"(OFF) : "memory");
}
constexpr int cap63(int n) { return n > 63 ? 63 : n; }
template <int N> DI void wait_bar() { asm volatile("s_waitcnt vmcnt(%0)\n\ts_barrier" ::"i"(cap63(N)) : "memory"); }
DI void lds_bar() { asm volatile("s_waitcnt lgkmcnt(0)\n\ts_barrier" ::: "memory"); }
DI void all_bar() { asm volatile("s_waitcnt vmcnt(0) lgkmcnt(0)\n\ts_barrier" ::: "memory"); }
DI void bar() { asm volatile("s_barrier" ::: "memory"); }
template <bool AG, int OFF> DI void ldx1(bf16x8& d, unsigned voff, const char* sb) {
  if constexpr (AG) asm volatile("global_load_dwordx4 %0, %1, %2 offset:%3 sc1" : "=a"(d) : "v"(voff), "s"(sb), "i"(OFF));
  else asm volatile("global_load_dwordx4 %0, %1, %2 offset:%3 sc1" : "=v"(d) : "v"(voff), "s"(sb), "i"(OFF));
}
template <int OFF> DI void ld2_1(f32x2& d, unsigned voff, const char* sb) { asm volatile("global_load_dwordx2 %0, %1, %2 offset:%3 sc1" : "=v"(d) : "v"(voff), "s"(sb), "i"(OFF)); }
DI void ld1_1(unsigned& d, unsigned voff, const void* sb) { asm volatile("global_load_dword %0, %1, %2 sc1" : "=v"(d) : "v"(voff), "s"(sb) : "memory"); }
template <int OFF> DI void st4_1(u32x4 d, unsigned voff, char* sb) { asm volatile("global_store_dwordx4 %0, %1, %2 offset:%3 sc1\n\ts_nop 1" ::"v"(voff), "v"(d), "s"(sb), "i"(OFF) : "memory"); }
DI void st2_1(f32x2 d, unsigned voff, char* sb) { asm volatile("global_store_dwordx2 %0, %1, %2 sc1" ::"v"(voff), "v"(d), "s"(sb) : "memory"); }
DI void st1_1(float d, unsigned voff, char* sb) { asm volatile("global_store_dword %0, %1, %2 sc1" ::"v"(voff), "v"(d), "s"(sb) : "memory"); }
template <int N> DI void waitv() { asm volatile("s_waitcnt vmcnt(%0)" ::"i"(cap63(N)) : "memory"); }
template <bool AG, int N, class T> DI void waitx(T& d) {
  if constexpr (AG) asm volatile("s_waitcnt vmcnt(%1)" : "+a"(d) : "i"(cap63(N))); else asm volatile("s_waitcnt vmcnt(%1)" : "+v"(d) : "i"(cap63(N)));
}
template <bool SW, bool BA, bool Z, bool NOP = false> DI void mm(f32x16& acc, const bf16x8& a, const bf16x8& b) {
#define MM_(OPS, C, OUT, BC) do { if constexpr (NOP) asm volatile("s_nop 1\n\tv_mfma_f32_32x32x16_bf16 %0, " OPS ", " C : OUT(acc) : "v"(a), BC(b)); \
                                  else asm volatile("v_mfma_f32_32x32x16_bf16 %0, " OPS ", " C : OUT(acc) : "v"(a), BC(b)); } while (0)
  if constexpr (Z) {
    if constexpr (SW) { if constexpr (BA) MM_("%2, %1", "0", "=&v", "a"); else MM_("%2, %1", "0", "=&v", "v"); }
    else { if constexpr (BA) MM_("%1, %2", "0", "=&v", "a"); else MM_("%1, %2", "0", "=&v", "v"); }
  } else {
    if constexpr (SW) { if constexpr (BA) MM_("%2, %1", "%0", "+v", "a"); else MM_("%2, %1", "%0", "+v", "v"); }
    else { if constexpr (BA) MM_("%1, %2", "%0", "+v", "a"); else MM_("%1, %2", "%0", "+v", "v"); }
  }
#undef MM_
}
DI void mdone(f32x16& a) { asm volatile("s_nop 11" : "+v"(a)); }
DI void mdone(f32x16& a, f32x16& b) { asm volatile("s_nop 11" : "+v"(a), "+v"(b)); }
DI void mdone(f32x16& a, f32x16& b, f32x16& c, f32x16& d) { asm volatile("s_nop 11" : "+v"(a), "+v"(b), "+v"(c), "+v"(d)); }
template <class T> DI T opq_v(T v) { asm volatile("" : "+v"(v)); return v; }
template <int OFF> DI void ldx0(bf16x8& d, unsigned voff, const char* sb) { asm volatile("global_load_dwordx4 %0, %1, %2 offset:%3" : "=v"(d) : "v"(voff), "s"(sb), "i"(OFF)); }
template <int OFF> DI void ldx1m(bf16x8& d, unsigned voff, const char* sb, unsigned long long m) {
  asm volatile("s_mov_b64 exec, %4\n\tglobal_load_dwordx4 %0, %1, %2 offset:%3 sc1\n\ts_mov_b64 exec, -1" : "=v"(d) : "v"(voff), "s"(sb), "i"(OFF), "s"(m));
}
template <bool AG, int OFF8> DI void ds_read2(bf16x8& d, unsigned addr) {
  addr += OFF8 * 8;
  if constexpr (AG) asm volatile("ds_read2_b64 %0, %1 offset1:2" : "=a"(d) : "v"(addr) : "memory"); else asm volatile("ds_read2_b64 %0, %1 offset1:2" : "=v"(d) : "v"(addr) : "memory");
}
template <bool AG, int N> DI void waitl(bf16x8& d) {
  if constexpr (AG) asm volatile("s_waitcnt lgkmcnt(%1)" : "+a"(d) : "i"(N) : "memory"); else asm volatile("s_waitcnt lgkmcnt(%1)" : "+v"(d) : "i"(N) : "memory");
}
template <int OFF> DI void ds_write_a(unsigned addr, const bf16x8& v) { asm volatile("ds_write_b128 %0, %1 offset:%2" ::"v"(addr), "a"(v), "i"(OFF) : "memory"); }
template <int N, class T> DI void wait2(T& a, T& b) { asm volatile("s_waitcnt vmcnt(%2)" : "+v"(a), "+v"(b) : "i"(cap63(N))); }
DI void set_flag(char* flag, u64 tag) {
  unsigned z = 0;
  asm volatile("s_mov_b64 exec, 1\n\tglobal_atomic_swap_x2 %0, %1, %2 sc1\n\ts_mov_b64 exec, -1" ::"v"(z), "v"(tag), "s"(flag) : "memory");
}
DI void ld8_1(u64& d, unsigned voff, const char* sb) { asm volatile("global_load_dwordx2 %0, %1, %2 sc1" : "=v"(d) : "v"(voff), "s"(sb) : "memory"); }
template <int NL> DI void rd_flag(u64& v, unsigned voff, const char* sb) {
  const u64 zero = 0;
  if constexpr (NL == 64) asm volatile("global_atomic_or_x2 %0, %1, %3, %2 sc0 sc1" : "+v"(v) : "v"(voff), "s"(sb), "v"(zero) : "memory");
  else asm volatile("s_mov_b64 exec, %4\n\tglobal_atomic_or_x2 %0, %1, %3, %2 sc0 sc1\n\ts_mov_b64 exec, -1" : "+v"(v) : "v"(voff), "s"(sb), "v"(zero), "s"((1ull << (NL & 63)) - 1) : "memory");
}
template <int NL> DI void poll(const char* base, unsigned voff, u64 tag) {
  for (int spin = 0; spin < (1 << 15); ++spin) {
    u64 v = tag;
    rd_flag<NL>(v, voff, base);
    waitx<false, 0>(v);
    if (__all(v == tag)) break;
  }
  asm volatile("" ::: "memory");
}

template <int RBX> DI void block_map(int& rb, int& cb) {
  const int x = blockIdx.x & 7, i = blockIdx.x >> 3;
  constexpr int CBX = 32 / RBX, GC = 16 / CBX;
  rb = RBX * (x / GC) + i / CBX; cb = CBX * (x % GC) + i % CBX;
}

DI float a_fma(float a, float b, float c) { float d; asm volatile("v_fma_f32 %0, %1, %2, %3" : "=v"(d) : "v"(a), "v"(b), "v"(c)); return d; }
DI float a_fms(float a, float b, float c) { float d; asm volatile("v_fma_f32 %0, %1, -%2, %3" : "=v"(d) : "v"(a), "v"(b), "v"(c)); return d; }
DI void a_exp(float& x) { asm volatile("v_exp_f32 %0, %0" : "+v"(x)); }
DI void a_add(float& s, float x) { asm volatile("v_add_f32 %0, %0, %1" : "+v"(s) : "v"(x)); }
DI void a_mul(float& x, float s) { asm volatile("v_mul_f32 %0, %0, %1" : "+v"(x) : "v"(s)); }
template <bool NEG> DI void a_hedge(float& c, float& n, float& s, float prev, float p, float sc, float m) {
  if constexpr (NEG) asm volatile("v_exp_f32 %0, %0\n\tv_fma_f32 %1, %4, -%5, %6\n\tv_add_f32 %2, %2, %3" : "+v"(c), "=&v"(n), "+v"(s) : "v"(prev), "v"(p), "v"(sc), "v"(m));
  else asm volatile("v_exp_f32 %0, %0\n\tv_fma_f32 %1, %4, %5, %6\n\tv_add_f32 %2, %2, %3" : "+v"(c), "=&v"(n), "+v"(s) : "v"(prev), "v"(p), "v"(sc), "v"(m));
}
DI void a_mul4(float& a, float& b, float& c, float& d, float s) {
  asm volatile("v_mul_f32 %0, %0, %4\n\tv_mul_f32 %1, %1, %4\n\tv_mul_f32 %2, %2, %4\n\tv_mul_f32 %3, %3, %4" : "+v"(a), "+v"(b), "+v"(c), "+v"(d) : "v"(s));
}
DI void a_sum16(f32x4& s, const bf16x8& sel, const bf16x8& f) { asm volatile("v_mfma_f32_16x16x32_bf16 %0, %1, %2, %0" : "+v"(s) : "v"(sel), "v"(f)); }
DI void a_mm4(float& mx, float& mn, float a, float b, float c, float d) {
  asm volatile("v_max3_f32 %0, %0, %2, %3\n\tv_min3_f32 %1, %1, %2, %3\n\tv_max3_f32 %0, %0, %4, %5\n\tv_min3_f32 %1, %1, %4, %5" : "+v"(mx), "+v"(mn) : "v"(a), "v"(b), "v"(c), "v"(d));
}
DI float a_max3(float a, float b, float c) { float d; asm volatile("v_max3_f32 %0, %1, %2, %3" : "=v"(d) : "v"(a), "v"(b), "v"(c)); return d; }
DI float a_min3(float a, float b, float c) { float d; asm volatile("v_min3_f32 %0, %1, %2, %3" : "=v"(d) : "v"(a), "v"(b), "v"(c)); return d; }

DI float opq_a(float v) { asm volatile("" : "+a"(v)); return v; }
DI void pin_a(bf16x8& a) { asm volatile("" : "+a"(a)); }
DI void pin2(f32x16& a, f32x16& b) { asm volatile("" : "+v"(a), "+v"(b)); }
DI bf16x8 cs_frag(float c) {
  const float hi = rbf(c), mid = rbf(c - hi), lo = (c - hi) - mid;
  return __builtin_bit_cast(bf16x8, u32x4{cvtpk(hi, mid), cvtpk(lo, 0.f), 0u, 0u});
}
template <bool CS> struct Red {
  bf16x8 p[4];
  float t[4], a, b, sd;
  template <int K> DI void run(int lane) {
    if constexpr (K == 0) asm volatile("" : "+v"(p[0]), "+v"(p[1]), "+v"(p[2]), "+v"(p[3]));
    if constexpr (K < 3) {
      const f32x4 u = __builtin_bit_cast(f32x4, p[K + 1]), v = __builtin_bit_cast(f32x4, p[0]);
#pragma unroll
      for (int e = 0; e < 4; ++e) { if (K == 0) t[e] = v[e]; a_add(t[e], u[e]); }
    }
    if constexpr (K == 3) {
      auto x = __builtin_amdgcn_permlane16_swap(__float_as_uint(t[0]), __float_as_uint(t[1]), false, false);
      auto y = __builtin_amdgcn_permlane16_swap(__float_as_uint(t[2]), __float_as_uint(t[3]), false, false);
      a = opq_v(__uint_as_float(x[0]) + __uint_as_float(x[1])); b = opq_v(__uint_as_float(y[0]) + __uint_as_float(y[1]));
    }
    if constexpr (K == 4) { auto x = __builtin_amdgcn_permlane32_swap(__float_as_uint(a), __float_as_uint(b), false, false); a = opq_v(__uint_as_float(x[0]) + __uint_as_float(x[1])); }
    if constexpr (K == 5) {
      if constexpr (CS) a = lane_get(a, (lane >> 2) + ((lane & 3) << 4));
      else { const int src = ((lane & 31) >> 1) + ((lane & 1) << 5); b = lane_get(a, src + 16); a = lane_get(a, src); }
    }
    if constexpr (CS) {
      if constexpr (K == 8) { t[0] = opq_v(rbf(a)); t[1] = opq_v(a - t[0]); }
      if constexpr (K == 9) { const float mid = rbf(t[1]); p[0] = opq_v(__builtin_bit_cast(bf16x8, u32x4{cvtpk(t[0], mid), cvtpk(t[1] - mid, 0.f), 0u, 0u})); }
    } else {
      if constexpr (K == 8) { const float m = a * (1.f / D_); b = opq_v(fmaxf(b * (1.f / D_) - m * m, 0.f)); a = opq_v(rbf(m)); }
      if constexpr (K == 9) b = opq_v(rbf(rbf(b) + 1e-5f));
      if constexpr (K == 10) { sd = opq_v(rbf(__builtin_amdgcn_sqrtf(b))); b = opq_v(__builtin_amdgcn_rcpf(sd)); }
    }
  }
};
DI void pin4(bf16x8 (&p)[4]) { asm volatile("" : "+v"(p[0]), "+v"(p[1]), "+v"(p[2]), "+v"(p[3])); }
DI bf16x8 mean_frag(float mean, bool on) { const float m = on ? -mean : 0.f; return __builtin_bit_cast(bf16x8, u32x4{cvtpk(m, m), cvtpk(m, 0.f), 0u, 0u}); }

#define STAMP(i)

constexpr int SLOT = 16384, XL = 12;
struct WinPtr {
  unsigned b[3];
  DI void init(unsigned a) { sfor<0, 3>([&](auto i) { b[i] = a + i * 65536u; asm volatile("" : "+v"(b[i])); }); }
  template <int OFF> DI bf16x8 frag() const { return *(LAS const bf16x8*)((LAS const char*)(uintptr_t)b[OFF >> 16] + (OFF & 65535)); }
};
struct Ctx { unsigned xv; WinPtr p; };
#define AI __attribute__((always_inline))
#define RARE(c) __builtin_expect(!!(c), 0)
#define IC(n) std::integral_constant<int, (n)>{}

template <int S> DI void ld_x(bf16x8 (&x)[64], const Ctx& c, const char* xb) { ldx1<false, (S % 8) * 1024 - 4096>(x[S], c.xv, xb + (S / 8) * 8192 + 4096); }
template <class H> constexpr int g64_wait(int s) {
  int n = 0;
  const int t0 = s - XL;
  if (t0 < 0) n += XL - 1 - s; else n += H::n(t0);
  for (int t = t0 < 0 ? 0 : t0 + 1; t < s; ++t) n += (t + XL < 64 ? 1 : 0) + H::n(t);
  return n;
}
constexpr int a_slot(int g);
template <bool MAPA, bool ZERO, class H, class FX, class FA>
DI void gemm64(const Ctx& c, bf16x8 (&x)[64], const char* xb, f32x16& acc0, f32x16& acc1, FX&& hook, FA&& aux) {
  bf16x8 f0[3], f1[3];
  auto off = [](int s) constexpr { return (MAPA ? a_slot(24 + s / 8) : s / 8) * SLOT + (s % 8) * 2048; };
  auto rd0 = [&](auto s) { f0[s % 3] = c.p.template frag<off(s)>(); };
  auto rd1 = [&](auto s) { f1[s % 3] = c.p.template frag<off(s) + 1024>(); };
  rd0(IC(0)); rd1(IC(0)); rd0(IC(1)); rd1(IC(1));
  sfor<0, 64>([&](auto s) {
    waitx<false, g64_wait<H>(s)>(x[s]);
    SBAR();
    mm<false, false, ZERO && s == 0>(acc0, f0[s % 3], x[s]);
    SBAR();
    if constexpr (s + 2 < 64) rd0(IC(s + 2));
    if constexpr (s + XL < 64) ld_x<s + XL>(x, c, xb);
    aux(IC(2 * s));
    SBAR();
    mm<false, false, ZERO && s == 0>(acc1, f1[s % 3], x[s]);
    SBAR();
    if constexpr (s + 2 < 64) rd1(IC(s + 2));
    hook(s);
    aux(IC(2 * s + 1));
    SBAR();
  });
  mdone(acc0, acc1);
}
struct HookP6 { static constexpr int n(int s) { return s >= 52 ? 1 : 0; } };
struct HookP3 { static constexpr int n(int s) { return s >= 52 ? 1 : 0; } };
struct HookP4 { static constexpr int n(int s) { return s == 52 || s == 6 || (s >= 8 && s < 12) || (s >= 14 && s < 30 && s % 2 == 0) ? 1 : 0; } };

DI void park4(u32x4 (&v)[4]) { asm volatile("" : "+a"(v[0]), "+a"(v[1]), "+a"(v[2]), "+a"(v[3])); }
DI void res_to_acc(u32x4 (&xr)[4], f32x16& a0, f32x16& a1) {
  park4(xr);
#pragma unroll
  for (int e = 0; e < 4; ++e) {
    a0[2 * e] = bflo(xr[0][e]); a0[2 * e + 1] = bfhi(xr[0][e]); a0[8 + 2 * e] = bflo(xr[1][e]); a0[9 + 2 * e] = bfhi(xr[1][e]);
    a1[2 * e] = bflo(xr[2][e]); a1[2 * e + 1] = bfhi(xr[2][e]); a1[8 + 2 * e] = bflo(xr[3][e]); a1[9 + 2 * e] = bfhi(xr[3][e]);
  }
  asm volatile("" : "+v"(a0), "+v"(a1));
}
DI void store_stats(int r, const u32x4 (&xr)[4], char* stp) {
  float s1 = 0.f, s2 = 0.f, s3 = 0.f, s4 = 0.f;
#pragma unroll
  for (int i = 0; i < 4; ++i)
#pragma unroll
    for (int e = 0; e < 4; e += 2) {
      s1 = dot2(xr[i][e], 0x3f803f80u, s1); s2 = dot2(xr[i][e], xr[i][e], s2);
      s3 = dot2(xr[i][e + 1], 0x3f803f80u, s3); s4 = dot2(xr[i][e + 1], xr[i][e + 1], s4);
    }
  st2_1(f32x2{swap_add(s1 + s3), swap_add(s2 + s4)}, (unsigned)(r * 8), stp);
}
DI void store_x(const Ctx& c, int r, const f32x16& a0, const f32x16& a1, u32x4 (&xr)[4], char* dst, char* stp) {
  SBAR();
  xr[0] = __builtin_bit_cast(u32x4, pack8<0>(a0)); st4_1<0>(xr[0], c.xv, dst); SBAR();
  xr[1] = __builtin_bit_cast(u32x4, pack8<1>(a0)); st4_1<1024>(xr[1], c.xv, dst); SBAR();
  xr[2] = __builtin_bit_cast(u32x4, pack8<0>(a1)); st4_1<2048>(xr[2], c.xv, dst); SBAR();
  xr[3] = __builtin_bit_cast(u32x4, pack8<1>(a1)); st4_1<3072>(xr[3], c.xv, dst); SBAR();
  store_stats(r, xr, stp);
  SBAR();
  park4(xr);
}

template <class FX> DI void hedge_exp(const f32x16& p, float sc, f32x16& ep, f32x16& en, FX&& hook) {
  float mx = fmaxf(p[0], p[1]), mn = fminf(p[0], p[1]);
#pragma unroll
  for (int i = 2; i < 16; i += 2) { mx = fmaxf(fmaxf(mx, p[i]), p[i + 1]); mn = fminf(fminf(mn, p[i]), p[i + 1]); }
  mx = swap_max(mx) * sc; mn = swap_min(mn) * sc;
  sfor<0, 16>([&](auto i) {
    SBAR();
    hook(i);
    constexpr int e = i;
    float a = __builtin_amdgcn_exp2f(__builtin_fmaf(p[e], sc, -mx)), b = __builtin_amdgcn_exp2f(__builtin_fmaf(p[e], -sc, mn));
    asm volatile("" : "+v"(a), "+v"(b));
    ep[e] = a; en[e] = b;
  });
  SBAR();
}

constexpr int a_slot(int g) { return g == 8 || g == 15 ? 8 : (g > 8 && g < 15 ? g - 9 : g % 8); }
constexpr bool a_bar(int s) { return (s + 2) % 8 == 0 && s < 190 && s != 78; }
constexpr int QP = 128 + 24, QC = 128 + 36;
constexpr int a_piece(int s, bool first) {
  constexpr int t[][3] = {{46, 8, 1}, {54, 9, 1}, {58, 10, 1}, {64, 11, 2}, {72, 12, 2}, {80, 13, 2}, {88, 14, 2}, {96, 15, 2}, {104, 16, 1}, {108, 17, 1}, {112, 18, 1},
                          {116, 19, 1}, {120, 20, 1}, {134, 21, 1}, {138, 22, 1}, {142, 23, 1}, {146, 24, 1}, {150, 25, 1}, {158, 26, 1}};
  if (!first && s >= 8 && s < 24 && s % 2 == 0) return 24 + (s - 8) / 2;
  for (auto& e : t) if (s >= e[0] && s < e[0] + 4 * e[2] && (s - e[0]) % e[2] == 0) return e[1] * 4 + (s - e[0]) / e[2];
  return -1;
}
constexpr int a_park(int s) { return s >= 129 && s < 135 ? s - 129 : (s == 162 || s == 163 ? s - 156 : -1); }
constexpr int a_ld0(bool first) { return first ? 32 : 2; }
constexpr bool a_side(int s, bool first) { return (s >= a_ld0(first) && s < a_ld0(first) + (first ? 12 : 4)) || s == 50 || (s >= 154 && s < 158) || a_park(s) >= 0; }
constexpr int f_rd(int n) { return n % 6 < 4 && n / 6 + 1 < 16 ? (n / 6 + 1) * 4 + n % 6 : -1; }
constexpr int f_in(int n) { return (n + 1) % 6 < 4 && (n + 1) / 6 >= 1 && (n + 1) / 6 + 4 < 16 ? ((n + 1) / 6 + 4) * 4 + (n + 1) % 6 : -1; }
constexpr int f_raw(int s) { return s >= 6 && s < 46 && s % 2 == 0 ? ((s + 2) / 8 + 2) * 4 + ((s + 2) % 8) / 2 : -1; }
struct SchedA {
  int xw[64], bw[192], sw[3], aw, fw, cw, pw, zw, uw, vw, lw[32];
  constexpr SchedA(bool first) : xw{}, bw{}, sw{}, aw(0), fw(0), cw(0), pw(0), zw(0), uw(0), vw(0), lw{} {
    int pos = 0, xpos[64] = {}, wpos[32] = {}, spos[192] = {}, fpos = 0, stpos = 0, ppos = 0, upos = 0, vpos = 0, lpos[34] = {};
    if (first) { xpos[1] = 4; xpos[2] = 12; wpos[1] = 18; xpos[3] = 22; wpos[2] = 26; xpos[4] = pos = 30; } else for (int s = 0; s < XL; ++s) xpos[s] = ++pos;
    for (int s = 0; s < 192; ++s) {
      if (s == 128) { pos += 3; stpos = pos; }
      if (a_bar(s)) {
        const int g = (s + 2) / 8 + (s == 70);
        bw[s] = wpos[g] ? pos - wpos[g] : 63;
        if (s == 62 && pos - spos[50] < bw[s]) bw[s] = pos - spos[50];
        if (s == 142) { if (pos - stpos < bw[s]) bw[s] = pos - stpos; ++pos; }
        if (s == 6 && first) ++pos;
      }
      if (s == 102) uw = pos - upos;
      if (s == 118) vw = pos - vpos;
      for (int k = 0; k < 3; ++k) if (s == a_ld0(first) + 14 + 4 * k) sw[k] = pos - spos[a_ld0(first) + 3 + 4 * k];
      if (s == 32) aw = pos - fpos;
      if (s == 128) fw = pos - fpos;
      if (s == 170) cw = pos - spos[157];
      if (s == QC) pw = pos - ppos;
      if (s < 64) xw[s] = first ? (s % 3 == 0 && s / 3 + 1 < 16 ? pos - xpos[s / 3 + 1] : 63) : pos - xpos[s];
      if (first && s < 64 && f_in(2 * s) >= 0) xpos[f_in(2 * s) / 4] = ++pos;
      if (!first && s + XL < 64) xpos[s + XL] = ++pos;
      if ((first && s == 22) || s == 127) fpos = ++pos;
      if (s == QP) ppos = ++pos;
      if (s >= QC) lpos[s - QC] = ++pos;
      if (a_piece(s, first) >= 0) wpos[a_piece(s, first) / 4] = ++pos;
      if (a_side(s, first)) spos[s] = ++pos;
      if (first && s < 64 && f_in(2 * s + 1) >= 0) xpos[f_in(2 * s + 1) / 4] = ++pos;
      if (first && f_raw(s) >= 0) wpos[f_raw(s) / 4] = ++pos;
      if (s == 63) pos += 8;
      if (s == 87) upos = pos;
      if (s == 103) vpos = pos;
      if (s == 177) zw = pos - lpos[1];
    }
    for (int i = 0; i < 32; ++i) {
      lw[i] = pos - lpos[2 + i];
      if (i % 2 == 0 && 192 - QC + i / 2 < 34) lpos[192 - QC + i / 2] = ++pos;
      if (i % 2 == 0 && i >= 16) ++pos;
    }
  }
};
constexpr int cmin(int a, int b) { return a < b ? a : b; }

#define R_OA "v[64:79]"
#define R_OB "v[80:95]"
#define S0_(n, q, m) "v_mfma_f32_32x32x16_bf16 v[" #n ":" #n "+15], v[208:211], v[" #q ":" #q "+3], v[" #m ":" #m "+15]\n\t"
#define S1_(n, q) "v_mfma_f32_32x32x16_bf16 v[" #n ":" #n "+15], v[212:215], v[" #q "+4:" #q "+7], v[" #n ":" #n "+15]\n\t"
#define PV0_(o, p) "v_smfmac_f32_32x32x32_bf16 v[" #o ":" #o "+15], v[216:219], v[" #p ":" #p "+7], v236\n\t"
#define PV1_(o, p) "v_smfmac_f32_32x32x32_bf16 v[" #o ":" #o "+15], v[220:223], v[" #p "+8:" #p "+15], v236\n\t"
#define L0_(l, p) "v_smfmac_f32_16x16x64_bf16 v[" #l ":" #l "+3], v[224:227], v[" #p ":" #p "+7], v236\n\t"
#define L1_(l, p) "v_smfmac_f32_16x16x64_bf16 v[" #l ":" #l "+3], v[224:227], v[" #p "+8:" #p "+15], v236\n\t"
#define E_(e, i) "v_exp_f32 v[" #e "+" #i "], v[" #e "+" #i "]\n\t"
#define RD_(d, off) "ds_read_b128 v[" #d ":" #d "+3], v237 offset:" #off "\n\t"
#define HALF_(e, n, q, m, p, o, l, X0, X1, X2, X3, X4, X5) \
  S0_(n, q, m) X0 E_(e, 0) E_(e, 1) E_(e, 2) S1_(n, q) X1 E_(e, 3) E_(e, 4) E_(e, 5) PV0_(o, p) X2 E_(e, 6) E_(e, 7) E_(e, 8) \
  PV1_(o, p) X3 E_(e, 9) E_(e, 10) E_(e, 11) L0_(l, p) X4 E_(e, 12) E_(e, 13) L1_(l, p) X5 E_(e, 14) E_(e, 15)
#define LG2_ "s_waitcnt lgkmcnt(2)\n\t"
#define LG0_ "s_waitcnt lgkmcnt(0)\n\t"
#define STEP_(b, nb, kv, wo, Y0, Y1) \
  HALF_(128, 160, 200, 112, 176, 80, 232, "", "", RD_(208, b + 2048), RD_(212, b + 3072), RD_(216, b), RD_(220, b + 1024) LG2_) \
  HALF_(160, 144, 192, 96, 128, 64, 228, "", LG0_, "s_add_u32 m0, s44, " #nb "\n\t", "", "global_load_lds_dwordx4 v" #kv ", s[40:41] sc1\n\t", \
        "global_load_lds_dwordx4 v" #kv ", s[40:41] offset:1024 sc1\n\t") \
  HALF_(144, 176, 200, 112, 160, 80, 232, Y0, "", RD_(208, b + 6144), RD_(212, b + 7168), RD_(216, b + 4096), RD_(220, b + 5120) LG2_) \
  HALF_(176, 128, 192, 96, 144, 64, 228, "s_mov_b32 m0, s45\n\t", LG0_, "global_load_lds_dwordx4 v240, s[42:43] offset:" #wo " sc1\n\t", Y1, \
        "s_waitcnt vmcnt(4)\n\t", "s_barrier\n\t")
DI float max16(const f32x16& s) {
  float mx = s[0];
#pragma unroll
  for (int e = 1; e < 16; ++e) mx = fmaxf(mx, s[e]);
  return mx;
}
typedef __attribute__((ext_vector_type(8))) unsigned u32x8;
typedef __attribute__((ext_vector_type(16))) unsigned u32x16;

__global__ __launch_bounds__(256, 1) void k_main(const char* xin, const char* wqkv, const char* wphi, const char* woA, const char* wq, const char* wk, const char* wv,
                                                 const char* woB, char* ws, char* out) {
  __shared__ __attribute__((aligned(16))) char smem_[163840];
  LAS char* const smem = (LAS char*)smem_;
  const unsigned lds0 = (unsigned)(uintptr_t)smem;
  Ctx c;
  int lane = threadIdx.x & 63, w = __builtin_amdgcn_readfirstlane(threadIdx.x >> 6), r = lane & 31, h = lane >> 5, rb, cb;
  bf16x8 cf;
  { const char* pc; unsigned last;
    asm volatile("s_getpc_b64 %0\n1:\n\ts_mov_b32 %1, .Lk_main_tail-1b" : "=s"(pc), "=s"(last));
    const unsigned o = (((blockIdx.x >> 3) * 4 + w) & 63) * 1024;
    ldx0<0>(cf, (unsigned)(lane * 16), pc + (o < last ? o : last)); }
  SBAR();
  block_map<4>(rb, cb);
  int tt = rb * 4 + w, blk = (lane >> 4) & 1, q4 = (lane & 15) >> 2, p4 = lane & 3;
  c.xv = lane * 16; c.p.init(lds0 + c.xv);
  const int ut = rb * 4 + (cb >> 2), ty = cb & 3;
  char *flags, *col, *colw, *act, *acto, *fact, *facto, *stt, *stto, *kvp, *zp, *fkvp, *fwl, *kvf, *fkv;
  const char *xu_p, *su_p, *wkv, *wph;
  unsigned kvb;
  u64 tag = 0;
  u32x4 xr[4] = {};
  unsigned ldsw = lds0 + w * 4096;
  auto wt = [&](int m, int ch) { return colw + C_WT + m * 131072 + ch * SLOT; };
  auto pc_w = [&](int m, int slot, int ch, auto i) { if constexpr (i == 0) dma_c<0>(ldsw + slot * SLOT, c.xv, wt(m, ch)); else dma_n<i * 1024>(c.xv, wt(m, ch)); };
  auto pc_a = [&](auto n) { constexpr int g = n / 4; pc_w(g < 8 ? 1 : (g < 16 ? 2 : (g < 24 ? 0 : 3)), a_slot(g), g % 8, IC(n % 4)); };
  u64 csm = ~0ull;
  float csv[4] = {};
  auto pc_m = [&](auto n) { constexpr int g = n / 4; dma_c<(n % 4) * 1024>(ldsw + a_slot(g) * SLOT, c.xv, wt(2, g % 8)); };
  auto ld_cs = [&](bf16x8& d, int m, auto i) { ldx1m<i * 1024>(d, c.xv, col + C_CS + m * 4096, csm); };
  auto raw_src = [&](int pitch, auto i) AI { const int row = lane >> 3; return (unsigned)((32 * w + 8 * i + row) * pitch + ((lane & 7) ^ (((row >> 1) & 1) << 2)) * 16 - i * 1024); };
  auto raw_in = [&](auto j, int slot, auto i) AI {
    const int hi = rb >> 3, pitch = (j == 0 || (j == 2 && hi)) ? 6144 : 2048;
    const char* src = (j == 0 ? wqkv + (hi ? 0 : 4096) : (j == 1 ? (hi ? wq : woA) : (hi ? wqkv + 2048 : woB))) + cb * 128 + (size_t)(rb & 7) * 128 * pitch;
    dma<i * 1024>(ldsw + slot * SLOT, raw_src(pitch, i), src);
  };
  auto raw_out = [&](auto j, int slot) AI {
    const int hi = rb >> 3, ch = rb & 7, m = j == 0 ? 2 - 2 * hi : (j == 1 ? 3 + hi : 5 - 4 * hi);
    LAS const char* p = smem + slot * SLOT + (w >> 1) * 8192 + (4 * h + q4) * 128 + (((((q4 >> 1) ^ w) & 1) << 2) + 2 * blk + (p4 >> 1)) * 16 + 8 * (p4 & 1);
    char* dst = col + C_WT + m * 131072 + ch * SLOT + (w >> 1) * 8192 + (w & 1) * 1024 + 4096;
    float cs = 0.f;
    sfor<0, 4>([&](auto s) AI {
      const u32x4 f = __builtin_bit_cast(u32x4, cat8(trread(p + s * 2048), trread(p + s * 2048 + 1024)));
      st4_1<s * 2048 - 4096>(f, c.xv, dst);
#pragma unroll
      for (int e = 0; e < 4; ++e) cs = dot2(f[e], 0x3f803f80u, cs);
    });
    st1_1(swap_add(cs), (unsigned)(r * 4), col + C_CS + m * 4096 + (ch * 2 + (w >> 1)) * 256 + (w & 1) * 128);
  };
  const int w0 = w; const unsigned ldsw0 = ldsw;
#define REFRESH() do { \
    w = w0; ldsw = ldsw0; \
    asm volatile("" : "+s"(w), "+s"(tag), "+s"(act), "+s"(acto), "+s"(fact), "+s"(facto), "+s"(stt), "+s"(stto), "+s"(col), "+s"(colw), "+s"(ldsw)); \
    asm volatile("" : "+v"(lane)); \
    r = lane & 31; h = lane >> 5; blk = (lane >> 4) & 1; q4 = (lane & 15) >> 2; p4 = lane & 3; c.xv = lane * 16; c.p.init(lds0 + c.xv); \
  } while (0)
  int l = 0;
  float mean, rstd;
  Red<true> cw;
  f32x16 ra0, ra1;
  bf16x8 park[16];
      constexpr int NA = 58;
      constexpr SchedA SQ(false), S1(true);
      bf16x8 x[64], f0[3], f1[3], wf[4], kp[4], pp[32], zq[2], sel[2], mf0, mf1;
      Red<false> st; Red<true> ck, cv, cq;
      f32x16 acc[6], p[2], kvs;
      u64 fv;
      float mx, mn, e[16], en[16], se, sn, sce;
      f32x4 ks0 = {}, ks1 = {};
      unsigned arow, pk0, ra, xo[4], xs;
      const char* xw = xin + (size_t)tt * 65536;
      LAS char* pk_img = smem + 6 * SLOT;
      LAS char* v_img = smem + 5 * SLOT;
      LAS char* sh = smem + 9 * SLOT + 4096;
      auto issue_x = [&](auto s) { ldx1<(s < NA), (s % 8) * 1024 - 4096>(x[s], c.xv, act + (s / 8) * 8192 + 4096); };
      auto ld_park = [&](auto i) { ldx1<true, (i % 4) * 1024>(park[i], c.xv, wt(4, 2 + i / 4)); };
      auto ldp = [&](auto i) {
        if constexpr (i >= 2) ldx1<true, ((i - 2) % 8) * 1024 - 4096>(pp[i - 2], c.xv, kvp + ((i - 2) / 8) * 8192 + 4096); else ldx1<false, i * 1024>(zq[i], c.xv, zp);
      };
      auto rd_wf = [&](auto k) { LAS const char* wp = smem + 9 * SLOT + (4 * h + q4) * 64 + blk * 32 + p4 * 8 + k * 1024; wf[k] = cat8(trread(wp), trread(wp + 512)); };
      auto rd01 = [&](auto s, auto t, auto fl) {
        if constexpr (s < 192) {
          bf16x8 f;
          if constexpr (fl && s < 64) { LAS const char* q = (LAS const char*)(uintptr_t)(ra ^ (t * 64u)) + ((s / 8 + 3) % 4) * SLOT + (s % 8) * 2048; f = cat8(trread(q), trread(q + 1024)); }
          else f = c.p.template frag<a_slot(s / 8) * SLOT + (s % 8) * 2048 + t * 1024>();
          if constexpr (t == 0) f0[s % 3] = f; else f1[s % 3] = f;
        }
      };
      auto x_in = [&](auto n) { dma<(n % 4) * 1040>(lds0 + w * 20480 + ((n / 4) % 4) * 4160, xs + (n % 4) * (16384 - 1040), xw + (n / 4) * 128); };
      auto x_out = [&](auto s) { ds_read2<(s < NA), ((s / 4) % 4) * 520>(x[s], xo[s % 4]); };
      auto x_fin = [&](auto g, auto n) { waitl<(4 * g < NA), n>(x[4 * g]); sfor<4 * g + 1, 4 * g + 4>([&](auto s) { if constexpr (s < NA) pin_a(x[s]); else x[s] = opq_v(x[s]); }); };
      auto k_in = [&](auto n) { dma<(n % 4) * 1024>(ldsw + (5 + (n / 4 + 3) % 4) * SLOT, raw_src(6144, IC(n % 4)), wqkv + 2048 + cb * 128 + (size_t)(n / 4) * 128 * 6144); };
      auto v_out = [&](auto t, auto hf) {
        *(LAS bf16x8*)(v_img + t * 8192 + w * 2048 + hf * 1024 + lane * 16) = pack8<hf>(acc[2 + t]);
      };
      auto sum_mm = [&](auto i) { waitx<true, SQ.lw[i]>(pp[i]); mm<false, true, i == 0>(kvs, sel[i & 1], pp[i]); };
      auto kmap = [&](auto gi) {
        constexpr int g = gi;
        if constexpr (g == 4) pin2(acc[0], acc[1]);
        if constexpr (g >= 6 && g < 10) mm<false, false, g % 2 == 0>(p[(g - 6) / 2], wf[g - 6], kp[g - 6]);
        if constexpr (g >= 4 && g < 8) kp[g - 4] = opq_v(pack8<g % 2>(acc[(g - 4) / 2]));
        if constexpr (g == 10) mm<true, false, false, true>(acc[2], cv.p[0], mf0);
        if constexpr (g == 11) mm<true, false, false, true>(acc[3], cv.p[0], mf1);
        if constexpr (g >= 12) {
          constexpr int hd = (g - 12) / 58, j = (g - 12) % 58, n = (j - 7) / 2;
          const f32x16& q = p[hd];
          if constexpr (j == 0) { mx = a_max3(q[0], q[1], q[2]); mn = a_min3(q[0], q[1], q[2]); mx = a_max3(mx, q[3], q[3]); mn = a_min3(mn, q[3], q[3]); }
          if constexpr (j >= 1 && j < 4) a_mm4(mx, mn, q[4 * j], q[4 * j + 1], q[4 * j + 2], q[4 * j + 3]);
          if constexpr (j == 4) mx = opq_v(swap_max(mx) * -sce);
          if constexpr (j == 5) mn = opq_v(swap_min(mn) * sce);
          if constexpr (j == 6) { e[0] = a_fma(q[0], sce, mx); en[0] = a_fms(q[0], sce, mn); se = opq_v(0.f); sn = opq_v(0.f); }
          if constexpr (j >= 7 && j < 37) {
            if constexpr (j % 2) a_hedge<false>(e[n], e[n + 1], se, n ? e[n ? n - 1 : 0] : sn, q[n + 1], sce, mx);
            else a_hedge<true>(en[n], en[n + 1], sn, n ? en[n ? n - 1 : 0] : se, q[n + 1], sce, mn);
          }
          if constexpr (j == 37) { a_exp(e[15]); a_add(se, e[14]); }
          if constexpr (j == 38) { a_exp(en[15]); a_add(sn, en[14]); }
          if constexpr (j == 39) se = opq_v(swap_add(se + e[15]));
          if constexpr (j == 40) sn = opq_v(swap_add(sn + en[15]));
          if constexpr (j == 41) { se = opq_v(__builtin_amdgcn_rcpf(se) * rstd); sn = opq_v(__builtin_amdgcn_rcpf(sn) * rstd); }
          if constexpr (j >= 42) {
            constexpr int c8 = (j - 42) / 2, o = 4 * (c8 & 3);
            float* v = c8 < 4 ? e : en;
            if constexpr (j % 2 == 0) { a_mul4(v[o], v[o + 1], v[o + 2], v[o + 3], c8 < 4 ? se : sn); pk0 = opq_v(arow ^ (c8 << 4)); }
            else *(LAS u32x2*)((LAS char*)(uintptr_t)pk0 + hd * 16384) = u32x2{cvtpk(v[o], v[o + 1]), cvtpk(v[o + 2], v[o + 3])};
          }
        }
      };
      auto aux = [&](auto s, auto i, auto fl) {
        constexpr int g1 = 2 * (s - a_ld0(fl) - 14) + i, g3 = 2 * (s - 128) + i;
        if constexpr (s < 64) {
          if constexpr (fl) { if constexpr (i == 0) a_sum16(ks0, sel[0], f0[s % 3]); else a_sum16(ks1, sel[0], f1[s % 3]); }
          if constexpr (g1 >= 0 && g1 < 11) st.template run<g1>(lane);
          if constexpr (g1 == 11) { mean = st.a; rstd = st.b; sce = opq_v(rstd * LOG2E); }
          if constexpr (g1 == 12) mf0 = opq_v(mean_frag(mean, h == 0));
          if constexpr (g1 == 13) mf1 = opq_v(mean_frag(mean, h == 1));
          if constexpr (fl && g1 >= 8 && g1 < 14) cv.template run<g1 - 8>(lane);
          if constexpr (fl && g1 >= 16 && g1 < 22) cq.template run<g1 - 16>(lane);
          if constexpr (fl && g1 == 18) csv[1] = opq_a(cv.a);
          if constexpr (fl && g1 == 26) csv[2] = opq_a(cq.a);
          if constexpr (g1 == 22) cv.a = csv[1] = opq_a(csv[1]);
          if constexpr (g1 == 22 || g1 == 23) cv.template run<g1 - 14>(lane);
          if constexpr (!fl) {
            if constexpr (g1 == 14) ck.a = opq_v(csv[0]);
            if constexpr (g1 == 14 || g1 == 15) ck.template run<g1 - 6>(lane);
            if constexpr (g1 == 18) mm<false, false, false, true>(acc[0], ck.p[0], mf0);
            if constexpr (g1 == 19) mm<false, false, false, true>(acc[1], ck.p[0], mf1);
          }
          if constexpr (s == 60 && i == 0) *(LAS short*)(sh + 8704 + w * 64 + (r >> 4) * 32 + ((r >> 2) & 1) * 16 + ((r >> 3) & 1) * 8 + (r & 3) * 2) = (short)(__float_as_uint(st.sd) >> 16);
          if constexpr (s == 61 && i == 0) arow = opq_v((unsigned)(uintptr_t)pk_img + (w * 32 + r) * 128 + h * 8 + ((r & 7) << 4));
          if constexpr (s >= 62) rd_wf(IC(2 * (s - 62) + i));
        } else if constexpr (s < 128) {
          kmap(IC(2 * (s - 64) + i));
        } else {
          if constexpr (g3 == 2) cq.a = csv[2] = opq_a(csv[2]);
          if constexpr (g3 == 2 || g3 == 3) cq.template run<g3 + 6>(lane);
          if constexpr (g3 == 6) mm<false, false, false, true>(acc[4], cq.p[0], mf0);
          if constexpr (g3 == 7) mm<false, false, false, true>(acc[5], cq.p[0], mf1);
          if constexpr (g3 == 92) { csv[3] = l == 0 ? cw.a : csv[3]; cw.a = csv[3]; }
          if constexpr (g3 >= 84 && g3 < 94) cw.template run<g3 - 84>(lane);
          if constexpr (g3 >= 32 && g3 < 40) {
            constexpr int n = (g3 - 32) % 4, t = (g3 - 32) / 4;
            const int kr = 16 * t + 8 * (n >> 1) + 4 * h + 2 * (n & 1);
            u32x4 sv = __builtin_bit_cast(u32x4, sel[t]); sv[n] = opq_v(r == kr ? 0x3f80u : (r == kr + 1 ? 0x3f800000u : 0u)); sel[t] = __builtin_bit_cast(bf16x8, sv);
          }
          if constexpr (g3 == 40) { sel[0] = opq_v(sel[0]); sel[1] = opq_v(sel[1]); }
          if constexpr (g3 == 42) *(LAS unsigned*)(sh + 8192 + 256 + lane * 4) = opq_v(0x3f803f80u);
          if constexpr (g3 == 99) {
            wait2<SQ.zw>(zq[0], zq[1]);
#pragma unroll
            for (int n = 0; n < 4; ++n) { e[n] = __builtin_bit_cast(f32x4, zq[0])[n]; a_add(e[n], __builtin_bit_cast(f32x4, zq[1])[n]); }
          }
          if constexpr (g3 == 101) {
            auto a = __builtin_amdgcn_permlane16_swap(__float_as_uint(e[0]), __float_as_uint(e[1]), false, false);
            auto b = __builtin_amdgcn_permlane16_swap(__float_as_uint(e[2]), __float_as_uint(e[3]), false, false);
            e[0] = opq_v(__uint_as_float(a[0]) + __uint_as_float(a[1])); e[1] = opq_v(__uint_as_float(b[0]) + __uint_as_float(b[1]));
          }
          if constexpr (g3 == 103) { auto a = __builtin_amdgcn_permlane32_swap(__float_as_uint(e[0]), __float_as_uint(e[1]), false, false); e[0] = opq_v(__uint_as_float(a[0]) + __uint_as_float(a[1])); }
          if constexpr (g3 == 105) e[1] = lane_get(e[0], lane ^ 8);
          if constexpr (g3 == 111)
            *(LAS short*)(sh + 8192 + w * 64 + (lane & 1) * 32 + ((lane >> 2) & 1) * 16 + ((lane >> 1) & 1) * 8 + (lane >> 4) * 2) = (short)cvtpk(e[0] + e[1], 0.f);
          if constexpr (g3 >= 116 && g3 < 124 && g3 % 2 == 1) rd_wf(IC((g3 - 116) / 2));
        }
      };
      const unsigned fo = (unsigned)((lane & 15) * 8 + (blk ? cb * 4 * FL_I + 10 * FL_K : tt * FL_I));
      auto step = [&](auto s, auto fl) {
        constexpr int ps = s / 64, L0 = a_ld0(fl);
        constexpr bool first = fl;
        if constexpr (s == 102) waitv<SQ.uw>();
        if constexpr (s == 118) waitv<SQ.vw>();
        if constexpr (a_bar(s) && s >= (first ? 6 : 38)) wait_bar<(ps ? cmin(S1.bw[s], SQ.bw[s]) : (first ? S1.bw[s] : SQ.bw[s]))>();
        if constexpr (s == L0 + 14 || (first && (s == L0 + 18 || s == L0 + 22))) waitv<(first ? S1.sw[(s - L0 - 14) / 4] : SQ.sw[0])>();
        if constexpr (first && s == 6) { set_flag(facto, tag); if (RARE(w == 0)) set_flag(fwl - FL_K + rb * 8, tag); }
        if constexpr (first && s == 32) { waitx<false, S1.aw>(fv); if (RARE(!__all(fv == tag))) poll<32>(flags, fo, tag); }
        if constexpr (s == 118) { if (RARE(l == 0 && w == 0)) set_flag(fwl + rb * 8, tag); }
        if constexpr (s == 128) { waitx<false, SQ.fw>(fv); if (RARE(!__all(fv == tag))) poll<16>(fwl, lane * 8, tag); }
        if constexpr (s == 142) set_flag(fkvp + rb * 8, tag);
        if constexpr (s == QC) { waitx<false, SQ.pw>(fv); if (RARE(!__all(fv == tag))) poll<16>(fkvp, lane * 8, tag); }
        if constexpr (s == 170) waitv<SQ.cw>();
        if constexpr (s < 64 && !first) waitx<(s < NA), SQ.xw[s % 64]>(x[s % 64]);
        if constexpr (first && s % 3 == 0) waitv<S1.xw[s % 64]>();
        SBAR();
        mm<ps == 1, (s % 64 < NA), s % 64 == 0>(acc[2 * ps], f0[s % 3], x[s % 64]);
        SBAR();
        if constexpr (first && f_rd(2 * s) >= 0) x_out(IC(f_rd(2 * s)));
        if constexpr (!first) { rd01(IC(s + 2), IC(1), fl); SBAR(); }
        rd01(IC(s + 2), IC(0), fl);
        if constexpr (first && f_in(2 * s) >= 0) x_in(IC(f_in(2 * s)));
        if constexpr (s + XL < 64 && !first) issue_x(IC(s + XL));
        if constexpr (first && s == 22) { fv = tag; rd_flag<32>(fv, fo, flags); }
        if constexpr (s == 127) ld8_1(fv, (lane & 15) * 8, fwl);
        if constexpr (s == QP) { fv = tag; rd_flag<16>(fv, lane * 8, fkvp); }
        if constexpr (s >= QC) ldp(IC(s - QC));
        aux(s, IC(0), fl);
        SBAR();
        mm<ps == 1, (s % 64 < NA), s % 64 == 0>(acc[2 * ps + 1], f1[s % 3], x[s % 64]);
        SBAR();
        if constexpr (first && f_rd(2 * s + 1) >= 0) x_out(IC(f_rd(2 * s + 1)));
        if constexpr (first && s % 3 == 2 && s / 3 + 1 < 16) x_fin(IC(s / 3 + 1), IC(4));
        if constexpr (first) rd01(IC(s + 2), IC(1), fl);
        if constexpr (a_piece(s, first) >= 0) { if constexpr (first) pc_m(IC(a_piece(s, first))); else pc_a(IC(a_piece(s, first))); }
        if constexpr (s >= L0 && s < L0 + 4) ldx1<false, (s - L0) * 1024>(st.p[s - L0], c.xv, stt);
        if constexpr (first && s >= L0 + 4 && s < L0 + 8) ld_cs(cv.p[s - L0 - 4], 2, IC(s - L0 - 4));
        if constexpr (first && s >= L0 + 8 && s < L0 + 12) ld_cs(cq.p[s - L0 - 8], 0, IC(s - L0 - 8));
        if constexpr (s == 50) dma<0>(lds0 + 9 * SLOT + w * 1024, c.xv, wph);
        if constexpr (s >= 154 && s < 158) ld_cs(cw.p[s - 154], 4, IC(s - 154));
        if constexpr (a_park(s) >= 0) ld_park(IC(a_park(s)));
        if constexpr (first && f_in(2 * s + 1) >= 0) x_in(IC(f_in(2 * s + 1)));
        if constexpr (first && f_raw(s) >= 0) k_in(IC(f_raw(s)));
        aux(s, IC(1), fl);
        if constexpr (s == 87) { if (RARE(l == 0)) sfor<0, 4>([&](auto i) AI { raw_in(IC(1), 7, i); }); }
        if constexpr (s == 103) { if (RARE(l == 0)) raw_out(IC(1), 7); }
        if constexpr (s == 133) { if (RARE(l == 0)) sfor<0, 4>([&](auto i) AI { raw_in(IC(2), 8, i); }); }
        SBAR();
      };
      {
        STAMP(30);
        bf16x8 kt[2];
        bf16x8 xt[4];
        xs = (unsigned)((lane >> 3) * 2048 + ((lane & 7) ^ (((lane >> 3) & 3) << 1)) * 16);
        sfor<0, 4>([&](auto i) { dma<i * 1040>(lds0 + w * 20480 + 3 * 4160, xs + i * (16384 - 1040), xw + cb * 128); });
        sfor<0, 2>([&](auto i) { ldx0<0>(kt[i], (unsigned)((lane >> 3) * 6144 + (lane & 7) * 16), wqkv + 2048 + cb * 128 + (size_t)(tt * 16 + i * 8) * 6144); });
        u64 ep; ld8_1(ep, 0u, ws + WS_EPOCH);
        sfor<0, 4>([&](auto i) AI { raw_in(IC(0), 9, i); });
        sfor<0, 4>([&](auto i) { xo[i] = opq_v(lds0 + w * 20480 + r * 128 + (((r & 3) ^ i) << 5) + h * 8 + (r >> 3) * 16); });
        ra = opq_v(lds0 + 5 * SLOT + (4 * h + q4) * 128 + (((q4 >> 1) << 2) + 2 * blk + (p4 >> 1)) * 16 + 8 * (p4 & 1));
        { const unsigned o = (((lane >> 2) ^ (lane >> 4)) & 1) ? 0u : 0x3f803f80u; sel[0] = opq_v(__builtin_bit_cast(bf16x8, u32x4{o, o, o, o})); ks0 = opq_v(ks0); ks1 = opq_v(ks1); }
        sfor<0, 4>(x_in); sfor<0, 4>(k_in);
        asm volatile("" : "+s"(ws), "+s"(wk), "+s"(wphi));
        flags = ws + WS_FLAG;
        col = ws + WS_COL + (size_t)cb * 2 * MB; colw = col + w * 4096;
        act = ws + (size_t)rb * 2 * MB + w * 65536;
        acto = act + cb * 4096;
        fact = flags + tt * FL_I;
        facto = fact + cb * 8;
        stt = ws + WS_ST + tt * 4096;
        stto = stt + cb * 256;
        kvp = col + C_KVP + w * 32768; zp = col + C_ZP + w * 2048;
        fkvp = flags + (cb * 4 + w) * FL_I + 8 * FL_K; fwl = flags + cb * 4 * FL_I + 11 * FL_K;
        kvf = ws + WS_KVF; fkv = flags + 12 * FL_K;
        xu_p = ws + (size_t)rb * 2 * MB + 2 * STG + (cb >> 2) * 65536 + w * 16384 + 4096;
        su_p = ws + WS_ST + 256 * KB + ut * 4096;
        wkv = ((cb & 2) ? wv : wk) + w * 256 * 64; wph = wphi + cb * 4096 + w * 1024;
        { const unsigned m = (unsigned)(ty >> 1) - 1u; kvb = ut * 4096 + (((ty * 1024 + (w & 1) * 8) & m) | ((2048 + (w >> 1) * 1024 + (ty & 1) * 256 + (w & 1) * 512) & ~m)); }
        asm volatile("" : "+s"(kvb), "+s"(act), "+s"(acto), "+s"(kvp), "+s"(zp), "+s"(fkvp), "+s"(kvf), "+s"(fkv), "+s"(xu_p), "+s"(su_p), "+s"(wkv), "+s"(wph));
        asm volatile("s_waitcnt vmcnt(12)" : "+v"(cf), "+v"(ep), "+v"(kt[0]), "+v"(kt[1])::"memory");
        sfor<0, 4>([&](auto i) { ds_read2<false, 3 * 520>(xt[i], xo[i]); });
        sfor<4, 8>(x_in); sfor<4, 8>(k_in); sfor<8, 12>(x_in);
        asm volatile("s_waitcnt lgkmcnt(0)" : "+v"(xt[0]), "+v"(xt[1]), "+v"(xt[2]), "+v"(xt[3])::"memory");
        sfor<0, 4>([&](auto i) { xr[i] = __builtin_bit_cast(u32x4, xt[i]); });
        store_stats(r, xr, stto);
        park4(xr);
        { u64 z = ((u64)__builtin_amdgcn_readfirstlane((unsigned)(ep >> 32)) << 32 | __builtin_amdgcn_readfirstlane((unsigned)ep)) + 0x9E3779B97F4A7C15ull;
          z = (z ^ (z >> 30)) * 0xBF58476D1CE4E5B9ull; z = (z ^ (z >> 27)) * 0x94D049BB133111EBull; tag = z ^ (z >> 31); asm volatile("" : "+s"(tag)); }
        wait_bar<13>();
        sfor<0, 4>(x_out);
        raw_out(IC(0), 9);
        sfor<12, 16>(x_in); sfor<8, 12>(k_in);
        x_fin(IC(0), IC(0));
        sfor<16, 20>(x_in);
        STAMP(31); STAMP(0); STAMP(1);
        sfor<0, 2>([&](auto s) { rd01(s, IC(0), std::true_type{}); rd01(s, IC(1), std::true_type{}); });
        sfor<0, 64>([&](auto s) { step(s, std::true_type{}); });
        {
          const int gw = blockIdx.x * 4 + w; const unsigned z = 0;
          const unsigned vo = (unsigned)(lane < 5 ? WS_KVF - WS_ST + (gw * 5 + lane) * 128 : WS_FLAG - WS_ST + gw * 128);
          asm volatile("s_mov_b64 exec, 63\n\tglobal_atomic_or %0, %1, %2 sc1\n\ts_mov_b64 exec, -1" ::"v"(vo), "v"(z), "s"(ws + WS_ST) : "memory");
          const unsigned dump = lds0 + 9 * SLOT + 4096 + w * 2048;
          dma<0>(dump, c.xv, woA + gw * 2048); dma<1024>(dump, c.xv, woA + gw * 2048);
          dma<0>(dump, c.xv, wq + gw * 2048); dma<1024>(dump, c.xv, wq + gw * 2048);
          dma<0>(dump, c.xv, woB + gw * 2048); dma<1024>(dump, c.xv, woB + gw * 2048);
          dma<0>(dump, c.xv, ((gw & 64) ? wk : wv) + (gw & 63) * 1024);
        }
        asm volatile("s_nop 7" : "+v"(ks0), "+v"(ks1));
        csv[0] = h ? ks1[0] : ks0[0];
        const bf16x8 kf = cs_frag(csv[0]);
        mm<false, false, false, true>(acc[0], kf, mf0); mm<false, false, false, true>(acc[1], kf, mf1);
      }
  asm volatile("" : "+s"(w), "+s"(tag), "+s"(act), "+s"(acto), "+s"(fact), "+s"(facto), "+s"(stt), "+s"(stto), "+s"(col), "+s"(colw), "+s"(ldsw));
  asm volatile("" : "+s"(kvp), "+s"(zp), "+s"(fkvp), "+s"(fwl), "+s"(rb), "+s"(kvf), "+s"(fkv), "+s"(xu_p), "+s"(su_p), "+s"(wkv), "+s"(csm), "+s"(l));
  for (;;) {
    {
      REFRESH();
      asm volatile("" : "+s"(kvp), "+s"(zp), "+s"(fkvp), "+s"(fwl), "+s"(rb));
      STAMP(2);
      sfor<64, 128>([&](auto s) { step(s, std::false_type{}); });
      STAMP(19);
      mdone(acc[2], acc[3]);
      v_out(IC(0), IC(0)); v_out(IC(0), IC(1)); v_out(IC(1), IC(0)); v_out(IC(1), IC(1));
      lds_bar();
      {
        const int hd = w >> 1, fh = w & 1;
        f32x16 kv, z;
        const int ch = fh * 4 + 2 * blk + (p4 >> 1), r0 = 4 * h + q4;
        LAS const char* pa = pk_img + hd * 16384 + r0 * 128 + ((ch ^ r0) << 4) + 8 * (p4 & 1);
        LAS const char* pb = v_img + hd * 8192 + lane * 16;
        bf16x8 a[8], b[8], d[8];
#pragma unroll
        for (int s = 0; s < 8; ++s) { a[s] = cat8(trread(pa + s * 2048), trread(pa + s * 2048 + 1024)); b[s] = *(LAS const bf16x8*)(pb + s * 1024); }
#pragma unroll
        for (int s = 0; s < 8; ++s) d[s] = *(LAS const bf16x8*)(sh + 8704 + s * 32 + h * 16);
        SBAR();
        sfor<0, 8>([&](auto s) { mm<false, false, s == 0>(kv, a[s], b[s]); });
        sfor<0, 8>([&](auto s) {
          mm<false, false, s == 0>(z, d[s], a[s]);
          if constexpr (s == 1) { mdone(kv); st4_1<0>(__builtin_bit_cast(u32x4, pack8<0>(kv)), c.xv, kvp + rb * 2048); }
          if constexpr (s == 2) { st4_1<1024>(__builtin_bit_cast(u32x4, pack8<1>(kv)), c.xv, kvp + rb * 2048); }
          SBAR();
        });
        mdone(z);
        st1_1(z[0], (unsigned)(r * 4), zp + rb * 128);
      }
      STAMP(3);
      sfor<128, 192>([&](auto s) { step(s, std::false_type{}); });
      mdone(acc[4], acc[5]);
      STAMP(4);
      {
        bf16x8 pq[2][4], ka[8], za[8];
        f32x16 ep, em, nm[4], dn[4];
        float ai[2], bi[2];
        char* dst = acto + STG;
        sfor<0, 4>([&](auto k) { mm<false, false, k % 2 == 0, true>(p[k / 2], wf[k], pack8<k % 2>(acc[4 + k / 2])); });
        mdone(p[0], p[1]);
        bar();
        if (RARE(l == 0)) raw_out(IC(2), 8);
        sfor<0, 2>([&](auto hd) {
          hedge_exp(p[hd], sce, ep, em, [&](auto e) {
            constexpr int i = hd * 16 + e;
            sum_mm(IC(i));
            if constexpr (i % 2 == 0 && 192 - QC + i / 2 < 34) ldp(IC(192 - QC + i / 2));
            if constexpr (i % 2 == 0 && i >= 16) pc_a(IC(108 + (i - 16) / 2));
          });
          if constexpr (hd == 1) {
            mdone(kvs);
            *(LAS bf16x8*)(sh + w * 2048 + lane * 16) = pack8<0>(kvs); *(LAS bf16x8*)(sh + w * 2048 + 1024 + lane * 16) = pack8<1>(kvs);
            SBAR();
          }
          pq[hd][0] = pack8<0>(ep); pq[hd][1] = pack8<1>(ep); pq[hd][2] = pack8<0>(em); pq[hd][3] = pack8<1>(em);
          pin4(pq[hd]);
        });
        STAMP(5);
        lds_bar();
        LAS const char* zb = sh + 8192 + (r & 1) * 256 + h * 32;
#pragma unroll
        for (int k = 0; k < 8; ++k) za[k] = *(LAS const bf16x8*)(zb + (k >> 1) * 64 + (k & 1) * 16);
#pragma unroll
        for (int k = 0; k < 8; ++k) ka[k] = *(LAS const bf16x8*)(sh + k * 1024 + lane * 16);
        SBAR();
        auto coef = [&](auto hd) {
          const float a = __builtin_amdgcn_rcpf(dn[2 * hd][1]), b = __builtin_amdgcn_rcpf(dn[2 * hd + 1][1]);
          const float inv = __builtin_amdgcn_rcpf(dn[2 * hd][0] * a + dn[2 * hd + 1][0] * b + 1e-6f);
          ai[hd] = opq_v(a * inv); bi[hd] = opq_v(b * inv);
        };
        auto fin = [&](auto hd) {
          f32x16 o;
#pragma unroll
          for (int i = 0; i < 16; ++i) o[i] = nm[2 * hd][i] * ai[hd] + nm[2 * hd + 1][i] * bi[hd];
          st4_1<hd * 2048>(__builtin_bit_cast(u32x4, pack8<0>(o)), c.xv, dst);
          st4_1<hd * 2048 + 1024>(__builtin_bit_cast(u32x4, pack8<1>(o)), c.xv, dst);
        };
        sfor<0, 8>([&](auto k) { mm<false, false, k % 2 == 0>(dn[k / 2], za[k], pq[k / 4][k % 4]); if constexpr (k % 2) pc_a(IC(116 + k / 2)); });
        sfor<0, 4>([&](auto k) { mm<false, false, k % 2 == 0>(nm[k / 2], ka[k], pq[0][k]); });
        mdone(dn[0], dn[1], dn[2], dn[3]);
        sfor<4, 8>([&](auto k) { mm<false, false, k % 2 == 0>(nm[k / 2], ka[k], pq[1][k - 4]); if constexpr (k == 4) coef(IC(0)); if constexpr (k == 6) coef(IC(1)); });
        STAMP(6);
        pin2(nm[0], nm[1]);
        fin(IC(0));
        pin2(nm[2], nm[3]);
        fin(IC(1));
        SBAR();
        sfor<120, 124>(pc_a);
        res_to_acc(xr, ra0, ra1);
        waitv<4>();
        set_flag(facto + FL_K, tag);
        sfor<124, 128>(pc_a); sfor<8, 16>(ld_park);
      }
    }
    STAMP(7);
    REFRESH();
    LAS char* reg = smem + 8 * SLOT;
    bf16x8 fu[16], cu;
    {
      f32x16 &a0 = ra0, &a1 = ra1;
      poll<16>(fact + FL_K, lane * 8, tag);
      sfor<0, 16>([&](auto i) { pin_a(park[i]); });
      STAMP(8);
      bf16x8 x[64];
      const char* xb = act + STG;
      sfor<0, XL>([&](auto s) { ld_x<s>(x, c, xb); });
      bar();
      asm volatile("" : "+s"(wkv));
      const unsigned kvv = (unsigned)((lane >> 1) * 64 + (cb & 1) * 32 + (lane & 1) * 16);
      const unsigned wa = ldsw + c.xv, wb = wa + 65536;
      auto unpark = [&](auto i) {
        constexpr int off = a_slot(26 + i / 4) * SLOT + (i % 4) * 1024;
        ds_write_a<(off & 65535)>(off < 65536 ? wa : wb, park[i]);
      };
      gemm64<true, false, HookP3>(c, x, xb, a0, a1, [&](auto s) {
        if constexpr (s == 33) bar();
        if constexpr (s >= 36 && s < 44) unpark(IC(s - 36));
        if constexpr (s == 49) bar();
        if constexpr (s >= 50 && s < 58) unpark(IC(s - 42));
        if constexpr (s >= 52 && s < 60) dma<0>(lds0 + 8 * SLOT + w * 8192 + (s - 52) * 1024, kvv, wkv + (s - 52) * 32 * 64);
        if constexpr (s >= 60) pc_w(4, a_slot(24), 0, IC(s - 60));
      }, [](auto) {});
      STAMP(9);
      store_x(c, r, a0, a1, xr, acto + 2 * STG, stto + 256 * 1024);
      asm volatile("s_waitcnt vmcnt(5) lgkmcnt(0)\n\ts_barrier" ::: "memory");
      { LAS const char* p = reg + w * 8192 + (4 * h + q4) * 32 + p4 * 8;
#pragma unroll
        for (int i = 0; i < 16; ++i) fu[i] = cat8(trread(p + i * 512), trread(p + i * 512 + 256));
        SBAR();
        float t0 = 0.f, t1 = 0.f, t2 = 0.f, t3 = 0.f;
#pragma unroll
        for (int i = 0; i < 16; ++i) {
          const u32x4 f = __builtin_bit_cast(u32x4, fu[i]);
          t0 = dot2(f[0], 0x3f803f80u, t0); t1 = dot2(f[1], 0x3f803f80u, t1); t2 = dot2(f[2], 0x3f803f80u, t2); t3 = dot2(f[3], 0x3f803f80u, t3);
        }
        cu = opq_v(cs_frag(swap_add((t0 + t1) + (t2 + t3)))); }
      waitv<0>();
      set_flag(facto + 2 * FL_K, tag);
      sfor<0, 4>([&](auto i) { pc_w(4, a_slot(25), 1, i); });
    }
    STAMP(10);
    REFRESH();
    f32x16 a0, a1;
    asm volatile("" : "+s"(kvf), "+s"(fkv), "+s"(xu_p), "+s"(su_p));
    {
      poll<16>(fact + 2 * FL_K, lane * 8, tag);
      bar();
      STAMP(11);
      const char* xb = act + 2 * STG; const char* stp = stt + 256 * 1024;
      bf16x8 x[64], xu[16], mf0, mf1;
      Red<false> su, sp;
      sfor<0, 4>([&](auto i) { ldx1<false, i * 1024>(su.p[i], c.xv, su_p); });
      sfor<0, 16>([&](auto i) { ldx1<true, (i % 8) * 1024 - 4096>(xu[i], c.xv, xu_p + (i / 8) * 8192); });
      f32x16 au, av;
      sfor<0, 16>([&](auto i) {
        waitx<true, 15 - i + (i < XL ? i : XL)>(xu[i]);
        SBAR();
        mm<false, true, i == 0>(au, fu[i], xu[i]);
        if constexpr (i < XL) ld_x<i>(x, c, xb);
        mm<true, true, i == 0>(av, fu[i], xu[i]);
        if constexpr (i < 11) su.template run<i>(lane);
        if constexpr (i == 12) mf0 = opq_v(mean_frag(su.a, h == 0));
        if constexpr (i == 14) { mm<false, false, false>(au, cu, mf0); mm<true, false, false>(av, cu, mf0); }
        SBAR();
      });
      STAMP(12);
      u64 fa;
      f32x4 t[4], ru;
      unsigned vo;
      gemm64<true, true, HookP4>(c, x, xb, a0, a1, [&](auto s) {
        if constexpr (s == 1) {
          pin2(au, av);
#pragma unroll
          for (int gq = 0; gq < 6; ++gq) {
            const f32x16& a = gq < 4 ? av : au;
            *(LAS f32x4*)(reg + w * 8192 + gq * 1024 + lane * 16) = f32x4{a[4 * (gq & 3)], a[4 * (gq & 3) + 1], a[4 * (gq & 3) + 2], a[4 * (gq & 3) + 3]};
          }
        }
        if constexpr (s == 3) {
          lds_bar();
#pragma unroll
          for (int j = 0; j < 4; ++j) t[j] = *(LAS const f32x4*)(reg + j * 8192 + (ty < 2 ? 4 + (w & 1) : w) * 1024 + lane * 16);
        }
        if constexpr (s == 6) st2_1(__builtin_bit_cast(f32x2, u32x2{cvtpk(t[0][0], t[0][1]), cvtpk(t[0][2], t[0][3])}), vo, kvf);
        if constexpr (s >= 8 && s < 12) ldx1<false, (s - 8) * 1024>(sp.p[s - 8], c.xv, stp);
        if constexpr (s == 7 + XL) { bar(); if (RARE(w == 0)) set_flag(fkv + rb * 4 * FL_I + cb * 8, tag); }
        if constexpr (s >= 14 && s < 30 && s % 2 == 0) pc_w(4, a_slot(30 + (s - 14) / 8), 6 + (s - 14) / 8, IC(((s - 14) / 2) % 4));
        if constexpr (s == 44) bar();
        if constexpr (s == 52) ld8_1(fa, (lane & 15) * 8 + (w * 4 + (lane >> 4)) * 4 * FL_I, fkv);
      }, [&](auto g) {
        if constexpr (g == 0) {
#pragma unroll
          for (int e = 0; e < 4; ++e) ru[e] = lane_get(su.b, ty < 2 ? r : 8 * w + 4 * h + e);
        }
        if constexpr (g == 4) {
          const unsigned m = (unsigned)(ty >> 1) - 1u;
          vo = opq_v(kvb + (((unsigned)lane * 16 & m) | ((unsigned)((r & 15) * 16 + h * 8) & ~m)));
        }
        if constexpr (g >= 9 && g < 13) {
#pragma unroll
          for (int e = 0; e < 4; ++e) { float v = t[0][e]; if constexpr (g < 12) a_add(v, t[g - 8][e]); else a_mul(v, ru[e]); t[0][e] = v; }
        }
        if constexpr (g >= 48 && g < 59) sp.template run<g - 48>(lane);
        if constexpr (g == 60) mf0 = opq_v(mean_frag(sp.a, h == 0));
        if constexpr (g == 61) mf1 = opq_v(mean_frag(sp.a, h == 1));
        if constexpr (g == 64) mm<false, false, false>(a0, cw.p[0], mf0);
        if constexpr (g == 65) mm<false, false, false>(a1, cw.p[0], mf1);
      });
      rstd = sp.b;
      waitx<false, 0>(fa);
      if (RARE(!__all(fa == tag))) {
#pragma unroll 1
      for (int spin = 0; spin < (1 << 15) && !__all(fa == tag); ++spin) { __builtin_amdgcn_s_sleep(8); ld8_1(fa, (lane & 15) * 8 + (w * 4 + (lane >> 4)) * 4 * FL_I, fkv); waitx<false, 0>(fa); }
      }
      bar();
      STAMP(13);
    }
    STAMP(14);
    {
      u32x16 qq, misc;
      misc[14] = misc[15] = __float_as_uint(rstd * QSCALE);
      int attempt = 0;
#pragma unroll 1
      for (;;) {
        f32x16 oa, ob, t0, t1, t2, t3;
        u32x16 kvr; u32x4 offs;
        offs[0] = c.xv;
        const char* ks = kvf + 2048 + w * 2048; const char* wsr = wt(5, 0);
        unsigned wm0 = ldsw, cnt = 8;
        asm volatile(
            "global_load_dwordx4 v[208:211], v240, %[kv] sc1\n\tglobal_load_dwordx4 v[212:215], v240, %[kv] offset:1024 sc1\n\t"
            "s_mov_b32 m0, s44\n\tv_add_u32 v241, 0x2000, v240\n\t"
            "global_load_lds_dwordx4 v240, s[40:41] sc1\n\tglobal_load_lds_dwordx4 v240, s[40:41] offset:1024 sc1\n\t"
            "s_add_u32 m0, s44, 0x2000\n\tv_add_u32 v242, 0x4000, v240\n\t"
            "global_load_lds_dwordx4 v241, s[40:41] sc1\n\tglobal_load_lds_dwordx4 v241, s[40:41] offset:1024 sc1\n\t"
            "s_add_u32 m0, s44, 0x4000\n\tv_add_u32 v243, 0x6000, v240\n\t"
            "global_load_lds_dwordx4 v242, s[40:41] sc1\n\tglobal_load_lds_dwordx4 v242, s[40:41] offset:1024 sc1\n\t"
            "s_add_u32 s40, s40, 0x6000\n\ts_addc_u32 s41, s41, 0\n\t"
            "s_cmp_lg_u32 %[att], 0\n\ts_cbranch_scc1 2f\n\t"
            "v_pk_mul_f32 v[128:129], v[128:129], v[238:239]\n\t"
            "v_pk_mul_f32 v[130:131], v[130:131], v[238:239]\n\t"
            "v_pk_mul_f32 v[132:133], v[132:133], v[238:239]\n\t"
            "v_pk_mul_f32 v[134:135], v[134:135], v[238:239]\n\t"
            "v_pk_mul_f32 v[136:137], v[136:137], v[238:239]\n\t"
            "v_pk_mul_f32 v[138:139], v[138:139], v[238:239]\n\t"
            "v_pk_mul_f32 v[140:141], v[140:141], v[238:239]\n\t"
            "v_pk_mul_f32 v[142:143], v[142:143], v[238:239]\n\t"
            "v_pk_mul_f32 v[144:145], v[144:145], v[238:239]\n\t"
            "v_pk_mul_f32 v[146:147], v[146:147], v[238:239]\n\t"
            "v_pk_mul_f32 v[148:149], v[148:149], v[238:239]\n\t"
            "v_pk_mul_f32 v[150:151], v[150:151], v[238:239]\n\t"
            "v_pk_mul_f32 v[152:153], v[152:153], v[238:239]\n\t"
            "v_pk_mul_f32 v[154:155], v[154:155], v[238:239]\n\t"
            "v_pk_mul_f32 v[156:157], v[156:157], v[238:239]\n\t"
            "v_pk_mul_f32 v[158:159], v[158:159], v[238:239]\n\t"
            "v_cvt_pk_bf16_f32 v192, v128, v129\n\t"
            "v_cvt_pk_bf16_f32 v193, v130, v131\n\t"
            "v_cvt_pk_bf16_f32 v194, v132, v133\n\t"
            "v_cvt_pk_bf16_f32 v195, v134, v135\n\t"
            "v_cvt_pk_bf16_f32 v196, v136, v137\n\t"
            "v_cvt_pk_bf16_f32 v197, v138, v139\n\t"
            "v_cvt_pk_bf16_f32 v198, v140, v141\n\t"
            "v_cvt_pk_bf16_f32 v199, v142, v143\n\t"
            "v_cvt_pk_bf16_f32 v200, v144, v145\n\t"
            "v_cvt_pk_bf16_f32 v201, v146, v147\n\t"
            "v_cvt_pk_bf16_f32 v202, v148, v149\n\t"
            "v_cvt_pk_bf16_f32 v203, v150, v151\n\t"
            "v_cvt_pk_bf16_f32 v204, v152, v153\n\t"
            "v_cvt_pk_bf16_f32 v205, v154, v155\n\t"
            "v_cvt_pk_bf16_f32 v206, v156, v157\n\t"
            "v_cvt_pk_bf16_f32 v207, v158, v159\n\t"
            "2:\n\t"
            "v_mov_b64 v[64:65], 0\n\t"
            "v_mov_b64 v[66:67], 0\n\t"
            "v_mov_b64 v[68:69], 0\n\t"
            "v_mov_b64 v[70:71], 0\n\t"
            "v_mov_b64 v[72:73], 0\n\t"
            "v_mov_b64 v[74:75], 0\n\t"
            "v_mov_b64 v[76:77], 0\n\t"
            "v_mov_b64 v[78:79], 0\n\t"
            "v_mov_b64 v[80:81], 0\n\t"
            "v_mov_b64 v[82:83], 0\n\t"
            "v_mov_b64 v[84:85], 0\n\t"
            "v_mov_b64 v[86:87], 0\n\t"
            "v_mov_b64 v[88:89], 0\n\t"
            "v_mov_b64 v[90:91], 0\n\t"
            "v_mov_b64 v[92:93], 0\n\t"
            "v_mov_b64 v[94:95], 0\n\t"
            "v_mov_b64 v[176:177], 0\n\t"
            "v_mov_b64 v[178:179], 0\n\t"
            "v_mov_b64 v[180:181], 0\n\t"
            "v_mov_b64 v[182:183], 0\n\t"
            "v_mov_b64 v[184:185], 0\n\t"
            "v_mov_b64 v[186:187], 0\n\t"
            "v_mov_b64 v[188:189], 0\n\t"
            "v_mov_b64 v[190:191], 0\n\t"
            "v_mov_b64 v[216:217], 0\n\t"
            "v_mov_b64 v[218:219], 0\n\t"
            "v_mov_b64 v[220:221], 0\n\t"
            "v_mov_b64 v[222:223], 0\n\t"
            "v_mov_b64 v[228:229], 0\n\t"
            "v_mov_b64 v[230:231], 0\n\t"
            "v_mov_b64 v[232:233], 0\n\t"
            "v_mov_b64 v[234:235], 0\n\t"
            "v_bfe_u32 v226, v240, 6, 1\n\tv_mov_b32 v236, 0xdddddddd\n\tv_mul_u32_u24 v226, 0x3f80, v226\n\tv_add_u32 v237, %[rg], v240\n\t"
            "v_lshl_or_b32 v226, v226, 16, v226\n\tv_mov_b32 v227, v226\n\tv_xor_b32 v224, 0x3f803f80, v226\n\tv_mov_b32 v225, v224\n\t"
            "s_waitcnt vmcnt(6)\n\ts_cbranch_scc1 3f\n\t"
            "v_mfma_f32_32x32x16_bf16 v[128:143], v[208:211], v[192:195], 0\n\tv_mfma_f32_32x32x16_bf16 v[160:175], v[208:211], v[200:203], 0\n\t"
            "v_mfma_f32_32x32x16_bf16 v[128:143], v[212:215], v[196:199], v[128:143]\n\tv_mfma_f32_32x32x16_bf16 v[160:175], v[212:215], v[204:207], v[160:175]\n\t"
            "s_nop 11\n\t"
            "v_max3_f32 v238, v128, v129, v130\n\tv_max3_f32 v239, v160, v161, v162\n\t"
            "v_max3_f32 v238, v238, v131, v132\n\tv_max3_f32 v239, v239, v163, v164\n\t"
            "v_max3_f32 v238, v238, v133, v134\n\tv_max3_f32 v239, v239, v165, v166\n\t"
            "v_max3_f32 v238, v238, v135, v136\n\tv_max3_f32 v239, v239, v167, v168\n\t"
            "v_max3_f32 v238, v238, v137, v138\n\tv_max3_f32 v239, v239, v169, v170\n\t"
            "v_max3_f32 v238, v238, v139, v140\n\tv_max3_f32 v239, v239, v171, v172\n\t"
            "v_max3_f32 v238, v238, v141, v142\n\tv_max3_f32 v239, v239, v173, v174\n\t"
            "v_max3_f32 v238, v238, v143, v143\n\tv_max3_f32 v239, v239, v175, v175\n\t"
            "v_mov_b32 v96, v238\n\tv_mov_b32 v97, v239\n\ts_nop 1\n\tv_permlane32_swap_b32 v238, v96\n\tv_permlane32_swap_b32 v239, v97\n\t"
            "v_max_f32 v238, v238, v96\n\tv_max_f32 v239, v239, v97\n\t"
            "3:\n\t"
            "v_xor_b32 v96, 0x80000000, v238\n\tv_xor_b32 v112, 0x80000000, v239\n\tv_mov_b32 v97, v96\n\tv_mov_b32 v113, v112\n\t"
            "v_mov_b64 v[98:99], v[96:97]\n\tv_mov_b64 v[114:115], v[112:113]\n\t"
            "v_mov_b64 v[100:101], v[96:97]\n\tv_mov_b64 v[116:117], v[112:113]\n\t"
            "v_mov_b64 v[102:103], v[96:97]\n\tv_mov_b64 v[118:119], v[112:113]\n\t"
            "v_mov_b64 v[104:105], v[96:97]\n\tv_mov_b64 v[120:121], v[112:113]\n\t"
            "v_mov_b64 v[106:107], v[96:97]\n\tv_mov_b64 v[122:123], v[112:113]\n\t"
            "v_mov_b64 v[108:109], v[96:97]\n\tv_mov_b64 v[124:125], v[112:113]\n\t"
            "v_mov_b64 v[110:111], v[96:97]\n\tv_mov_b64 v[126:127], v[112:113]\n\t"
            "s_nop 1\n\t" S0_(128, 192, 96) S1_(128, 192)
            "s_waitcnt vmcnt(4)\n\ts_barrier\n\ts_nop 8\n\t"
            "1:\n\t"
            STEP_(0, 24576, 240, 0, "s_sub_u32 %[cnt], %[cnt], 1\n\t", "") STEP_(8192, 0, 241, 1024, "", "") STEP_(16384, 8192, 242, 2048, "", "")
            STEP_(24576, 16384, 243, 3072, "s_add_u32 s40, s40, 0x8000\n\ts_addc_u32 s41, s41, 0\n\t", "s_add_u32 s42, s42, 0x4000\n\ts_addc_u32 s43, s43, 0\n\t")
            "s_add_u32 s45, s45, 0x4000\n\ts_cmp_lg_u32 %[cnt], 0\n\ts_cbranch_scc1 1b\n\t"
            L0_(232, 176) L1_(232, 176) PV0_(80, 176) PV1_(80, 176)
            "s_nop 11\n\t"
            : "=&{v[64:79]}"(oa), "=&{v[80:95]}"(ob), "=&{v[96:111]}"(t0), "=&{v[112:127]}"(t1), "+{v[128:143]}"(a0), "+{v[144:159]}"(a1), "=&{v[160:175]}"(t2),
              "=&{v[176:191]}"(t3), "+{v[192:207]}"(qq), "=&{v[208:223]}"(kvr), "+{v[224:239]}"(misc), "+{v[240:243]}"(offs),
              "+{s[40:41]}"(ks), "+{s[42:43]}"(wsr), "+{s45}"(wm0), [cnt] "+s"(cnt)
            : "{s44}"(lds0 + 8 * SLOT + w * 2048), [kv] "s"(kvf), [att] "s"(attempt), [rg] "s"(lds0 + 8 * SLOT)
            : "scc", "memory");
        const float la = __uint_as_float(misc[4]), lb = __uint_as_float(misc[8]);
        const float ia = __builtin_amdgcn_rcpf(la), ib = __builtin_amdgcn_rcpf(lb);
#pragma unroll
        for (int e = 0; e < 16; ++e) { oa[e] *= ia; ob[e] *= ib; }
        char* dst = acto + 3 * STG;
        st4_1<0>(__builtin_bit_cast(u32x4, pack8<0>(oa)), c.xv, dst); st4_1<1024>(__builtin_bit_cast(u32x4, pack8<1>(oa)), c.xv, dst);
        st4_1<2048>(__builtin_bit_cast(u32x4, pack8<0>(ob)), c.xv, dst); st4_1<3072>(__builtin_bit_cast(u32x4, pack8<1>(ob)), c.xv, dst);
        float chk = 0.f;
#pragma unroll
        for (int e = 0; e < 16; ++e) chk = __builtin_fmaf(oa[e], 0.f, __builtin_fmaf(ob[e], 0.f, chk));
        const bool bad = !(la < 1.2676506e30f) || !(lb < 1.2676506e30f) || !(chk == 0.f);
        wait_bar<4>();
        LAS volatile int* vote = (LAS volatile int*)(smem + 8 * SLOT);
        if (lane == 0) vote[w] = 0;
        if (attempt == 0 && bad) vote[w] = 1;
        lds_bar();
        const int redo = vote[0] | vote[1] | vote[2] | vote[3];
        if (!RARE(__builtin_amdgcn_readfirstlane(redo))) break;
        lds_bar();
        {
          const bf16x8 qa0 = __builtin_bit_cast(bf16x8, __builtin_shufflevector(qq, qq, 0, 1, 2, 3)), qa1 = __builtin_bit_cast(bf16x8, __builtin_shufflevector(qq, qq, 4, 5, 6, 7));
          const bf16x8 qb0 = __builtin_bit_cast(bf16x8, __builtin_shufflevector(qq, qq, 8, 9, 10, 11)), qb1 = __builtin_bit_cast(bf16x8, __builtin_shufflevector(qq, qq, 12, 13, 14, 15));
          float mA = -INFINITY, mB = -INFINITY;
          for (int t = 0; t < 64; ++t) {
            bf16x8 k0, k1;
            ldx1<false, 0>(k0, c.xv, kvf + t * 4096); ldx1<false, 1024>(k1, c.xv, kvf + t * 4096);
            wait2<0>(k0, k1);
            mm<false, false, true>(a0, k0, qa0); mm<false, false, false>(a0, k1, qa1); mm<false, false, true>(a1, k0, qb0); mm<false, false, false>(a1, k1, qb1);
            mdone(a0, a1);
            mA = fmaxf(mA, max16(a0)); mB = fmaxf(mB, max16(a1));
          }
          misc[14] = __float_as_uint(swap_max(mA)); misc[15] = __float_as_uint(swap_max(mB));
        }
        attempt = 1;
      }
      res_to_acc(xr, ra0, ra1);
      waitv<0>();
      set_flag(facto + 3 * FL_K, tag);
    }
    STAMP(15);
    REFRESH();
    {
      f32x16 &a0 = ra0, &a1 = ra1;
      poll<16>(fact + 3 * FL_K, lane * 8, tag);
      STAMP(16);
      bf16x8 x[64];
      const char* xb = act + 3 * STG;
      sfor<0, XL>([&](auto s) { ld_x<s>(x, c, xb); });
      bar();
      gemm64<false, false, HookP6>(c, x, xb, a0, a1, [&](auto s) {
        if constexpr (s == 33) bar();
        if constexpr (s >= 52) pc_a(IC(s - 52));
      }, [](auto) {});
      STAMP(17);
      if (l == 0) {
        store_x(c, r, a0, a1, xr, acto + 4 * STG, stto + 512 * 1024);
        bar();
        sfor<12, 16>(pc_a);
        waitv<4>();
        set_flag(facto + 4 * FL_K, tag);
        sfor<16, 24>(pc_a);
      } else {
        LAS char* im = smem + 8 * SLOT + w * 4096;
        sfor<0, 8>([&](auto n) {
          constexpr int g = n;
          const f32x16& a = g < 4 ? a0 : a1;
          *(LAS u32x2*)(im + r * 128 + ((g ^ (r & 7)) << 4) + h * 8) = u32x2{cvtpk(a[4 * (g & 3)], a[4 * (g & 3) + 1]), cvtpk(a[4 * (g & 3) + 2], a[4 * (g & 3) + 3])};
        });
        char* dst = out + (size_t)tt * 65536 + cb * 128;
        sfor<0, 4>([&](auto i) {
          const u32x4 v = *(LAS const u32x4*)(im + i * 1024 + lane * 16);
          st4_1<0>(v, (unsigned)((i * 8 + (lane >> 3)) * 2048 + (((lane & 7) ^ (lane >> 3)) << 4)), dst);
        });
        if (tt == 0 && cb == 0) set_flag(ws + WS_EPOCH, tag);
      }
    }
    STAMP(18);
    if (l == 1) break;
    csm = 1;
    act += 4 * STG; acto += 4 * STG; fact += 4 * FL_K; facto += 4 * FL_K; stt += 512 * KB; stto += 512 * KB;
    kvp += 131072; zp += 8192; fkvp += FL_K; kvf += 320 * KB; fkv += FL_K; xu_p += 4 * STG; su_p += 512 * KB;
    l = 1;
    REFRESH();
    STAMP(0);
        poll<16>(fact, lane * 8, tag);
        STAMP(1);
        sfor<0, XL>(issue_x);
        wait_bar<XL>();
        sfor<0, 2>([&](auto s) { rd01(s, IC(0), std::false_type{}); rd01(s, IC(1), std::false_type{}); });
        sfor<0, 64>([&](auto s) { step(s, std::false_type{}); });
        asm volatile("s_mov_b32 m0, %0\n\ts_mov_b64 exec, 1\n\t.rept 8\n\tglobal_load_lds_dwordx4 %1, %2\n\t.endr\n\ts_mov_b64 exec, -1\n.Lk_main_tail:" ::"s"(lds0 + 9 * SLOT + 4096), "v"(c.xv), "s"(wph) : "memory");
  }
}

extern "C" void kernel_launch(void* const* d_in, const int* in_sizes, int n_in, void* d_out, int out_size, void* d_ws, size_t ws_size, hipStream_t stream) {
  k_main<<<256, 256, 0, stream>>>((const char*)d_in[0], (const char*)d_in[1], (const char*)d_in[2], (const char*)d_in[3], (const char*)d_in[5], (const char*)d_in[6],
                                  (const char*)d_in[7], (const char*)d_in[8], (char*)d_ws, (char*)d_out);
}
```

```cpp
#include <hip/hip_runtime.h>
#include <hip/hip_bf16.h>
#include <stdint.h>
#include <type_traits>

typedef __attribute__((ext_vector_type(8))) short bf16x8;
typedef __attribute__((ext_vector_type(4))) short s16x4;
typedef __attribute__((ext_vector_type(2))) __bf16 bf2_t;
typedef __attribute__((ext_vector_type(16))) float f32x16;
typedef __attribute__((ext_vector_type(4))) float f32x4;
typedef __attribute__((ext_vector_type(2))) float f32x2;
typedef __attribute__((ext_vector_type(4))) unsigned u32x4;
typedef __attribute__((ext_vector_type(2))) unsigned u32x2;
#define LAS __attribute__((address_space(3)))
#define DI __device__ __forceinline__
#define MFMA32(a, b, c) __builtin_amdgcn_mfma_f32_32x32x16_bf16((a), (b), (c), 0, 0, 0)
#define MFMA16(a, b, c) __builtin_amdgcn_mfma_f32_16x16x32_bf16((a), (b), (c), 0, 0, 0)
#define SBAR() __builtin_amdgcn_sched_barrier(0)

constexpr int S_ = 2048, D_ = 1024;
constexpr float LOG2E = 1.4426950408889634f;
constexpr float QSCALE = 0.17677669529663687f * LOG2E;

constexpr size_t MB = 1u << 20, KB = 1024;
constexpr size_t STG = 256 * KB;
constexpr size_t WS_COL = 32 * MB;
constexpr size_t C_WT = 0;
constexpr size_t C_KVP = 768 * KB;
constexpr size_t C_ZP = 1024 * KB;
constexpr size_t C_CS = 1040 * KB;
constexpr size_t WS_ST = 64 * MB;
constexpr size_t WS_KVF = WS_ST + 1024 * KB;
constexpr size_t WS_FLAG = WS_ST + 1664 * KB;
constexpr size_t WS_EPOCH = WS_ST + 1920 * KB;
constexpr int FL_I = 2048, FL_K = 128;
typedef unsigned long long u64;

template <int I, int N, class F> DI void sfor(F&& f) { if constexpr (I < N) { f(std::integral_constant<int, I>{}); sfor<I + 1, N>(f); } }

DI unsigned cvtpk(float lo, float hi) { f32x2 v = {lo, hi}; return __builtin_bit_cast(unsigned, __builtin_convertvector(v, bf2_t)); }
DI float bflo(unsigned u) { return __uint_as_float(u << 16); }
DI float bfhi(unsigned u) { return __uint_as_float(u & 0xffff0000u); }
DI float rbf(float x) { return bflo(cvtpk(x, x)); }
DI float swap_add(float v) { auto r = __builtin_amdgcn_permlane32_swap(__float_as_uint(v), __float_as_uint(v), false, false); return __uint_as_float(r[0]) + __uint_as_float(r[1]); }
DI float swap_max(float v) { auto r = __builtin_amdgcn_permlane32_swap(__float_as_uint(v), __float_as_uint(v), false, false); return fmaxf(__uint_as_float(r[0]), __uint_as_float(r[1])); }
DI float swap_min(float v) { auto r = __builtin_amdgcn_permlane32_swap(__float_as_uint(v), __float_as_uint(v), false, false); return fminf(__uint_as_float(r[0]), __uint_as_float(r[1])); }
template <int S> DI bf16x8 pack8(const f32x16& v) {
  u32x4 p = {cvtpk(v[8 * S], v[8 * S + 1]), cvtpk(v[8 * S + 2], v[8 * S + 3]), cvtpk(v[8 * S + 4], v[8 * S + 5]), cvtpk(v[8 * S + 6], v[8 * S + 7])};
  return __builtin_bit_cast(bf16x8, p);
}
DI u32x4 unperm(u32x4 v) {
  auto r0 = __builtin_amdgcn_permlane32_swap(v[0], v[2], false, false);
  auto r1 = __builtin_amdgcn_permlane32_swap(v[1], v[3], false, false);
  return u32x4{r0[0], r1[0], r0[1], r1[1]};
}
DI float lane_get(float v, int src) { return __uint_as_float(__builtin_amdgcn_ds_bpermute(src << 2, __float_as_uint(v))); }
DI s16x4 trread(LAS const char* p) { return __builtin_amdgcn_ds_read_tr16_b64_v4i16((LAS s16x4*)p); }
DI bf16x8 cat8(s16x4 a, s16x4 b) { return __builtin_shufflevector(a, b, 0, 1, 2, 3, 4, 5, 6, 7); }
DI float dot2(unsigned a, unsigned b, float c) { return __builtin_amdgcn_fdot2_f32_bf16(__builtin_bit_cast(bf2_t, a), __builtin_bit_cast(bf2_t, b), c, false); }

template <int OFF> DI void dma(unsigned lds, unsigned v, const char* sb) {
  asm volatile("s_mov_b32 m0, %0\n\ts_nop 0\n\tglobal_load_lds_dwordx4 %1, %2 offset:%3" ::"s"(lds), "v"(v), "s"(sb), "i"(OFF) : "memory");
}
template <int OFF> DI void dma_c(unsigned lds, unsigned v, const char* sb) {
  asm volatile("s_mov_b32 m0, %0\n\ts_nop 0\n\tglobal_load_lds_dwordx4 %1, %2 offset:%3 sc1" ::"s"(lds), "v"(v), "s"(sb), "i"(OFF) : "memory");
}
template <int OFF> DI void dma_n(unsigned v, const char* sb) {
  asm volatile("global_load_lds_dwordx4 %0, %1 offset:%2 sc1" ::"v"(v), "s"(sb), "i"(OFF) : "memory");
}
constexpr int cap63(int n) { return n > 63 ? 63 : n; }
template <int N> DI void wait_bar() { asm volatile("s_waitcnt vmcnt(%0)\n\ts_barrier" ::"i"(cap63(N)) : "memory"); }
DI void lds_bar() { asm volatile("s_waitcnt lgkmcnt(0)\n\ts_barrier" ::: "memory"); }
DI void all_bar() { asm volatile("s_waitcnt vmcnt(0) lgkmcnt(0)\n\ts_barrier" ::: "memory"); }
DI void bar() { asm volatile("s_barrier" ::: "memory"); }
template <bool AG, int OFF> DI void ldx1(bf16x8& d, unsigned voff, const char* sb) {
  if constexpr (AG) asm volatile("global_load_dwordx4 %0, %1, %2 offset:%3 sc1" : "=a"(d) : "v"(voff), "s"(sb), "i"(OFF));
  else asm volatile("global_load_dwordx4 %0, %1, %2 offset:%3 sc1" : "=v"(d) : "v"(voff), "s"(sb), "i"(OFF));
}
template <int OFF> DI void ld2_1(f32x2& d, unsigned voff, const char* sb) { asm volatile("global_load_dwordx2 %0, %1, %2 offset:%3 sc1" : "=v"(d) : "v"(voff), "s"(sb), "i"(OFF)); }
DI void ld1_1(unsigned& d, unsigned voff, const void* sb) { asm volatile("global_load_dword %0, %1, %2 sc1" : "=v"(d) : "v"(voff), "s"(sb) : "memory"); }
template <int OFF> DI void st4_1(u32x4 d, unsigned voff, char* sb) { asm volatile("global_store_dwordx4 %0, %1, %2 offset:%3 sc1\n\ts_nop 1" ::"v"(voff), "v"(d), "s"(sb), "i"(OFF) : "memory"); }
DI void st2_1(f32x2 d, unsigned voff, char* sb) { asm volatile("global_store_dwordx2 %0, %1, %2 sc1" ::"v"(voff), "v"(d), "s"(sb) : "memory"); }
DI void st1_1(float d, unsigned voff, char* sb) { asm volatile("global_store_dword %0, %1, %2 sc1" ::"v"(voff), "v"(d), "s"(sb) : "memory"); }
template <int N> DI void waitv() { asm volatile("s_waitcnt vmcnt(%0)" ::"i"(cap63(N)) : "memory"); }
template <bool AG, int N, class T> DI void waitx(T& d) {
  if constexpr (AG) asm volatile("s_waitcnt vmcnt(%1)" : "+a"(d) : "i"(cap63(N))); else asm volatile("s_waitcnt vmcnt(%1)" : "+v"(d) : "i"(cap63(N)));
}
template <bool SW, bool BA, bool Z, bool NOP = false> DI void mm(f32x16& acc, const bf16x8& a, const bf16x8& b) {
#define MM_(OPS, C, OUT, BC) do { if constexpr (NOP) asm volatile("s_nop 1\n\tv_mfma_f32_32x32x16_bf16 %0, " OPS ", " C : OUT(acc) : "v"(a), BC(b)); \
                                  else asm volatile("v_mfma_f32_32x32x16_bf16 %0, " OPS ", " C : OUT(acc) : "v"(a), BC(b)); } while (0)
  if constexpr (Z) {
    if constexpr (SW) { if constexpr (BA) MM_("%2, %1", "0", "=&v", "a"); else MM_("%2, %1", "0", "=&v", "v"); }
    else { if constexpr (BA) MM_("%1, %2", "0", "=&v", "a"); else MM_("%1, %2", "0", "=&v", "v"); }
  } else {
    if constexpr (SW) { if constexpr (BA) MM_("%2, %1", "%0", "+v", "a"); else MM_("%2, %1", "%0", "+v", "v"); }
    else { if constexpr (BA) MM_("%1, %2", "%0", "+v", "a"); else MM_("%1, %2", "%0", "+v", "v"); }
  }
#undef MM_
}
DI void mdone(f32x16& a) { asm volatile("s_nop 11" : "+v"(a)); }
DI void mdone(f32x16& a, f32x16& b) { asm volatile("s_nop 11" : "+v"(a), "+v"(b)); }
DI void mdone(f32x16& a, f32x16& b, f32x16& c, f32x16& d) { asm volatile("s_nop 11" : "+v"(a), "+v"(b), "+v"(c), "+v"(d)); }
template <class T> DI T opq_v(T v) { asm volatile("" : "+v"(v)); return v; }
template <int OFF> DI void ldx0(bf16x8& d, unsigned voff, const char* sb) { asm volatile("global_load_dwordx4 %0, %1, %2 offset:%3" : "=v"(d) : "v"(voff), "s"(sb), "i"(OFF)); }
template <int OFF> DI void ldx1m(bf16x8& d, unsigned voff, const char* sb, unsigned long long m) {
  asm volatile("s_mov_b64 exec, %4\n\tglobal_load_dwordx4 %0, %1, %2 offset:%3 sc1\n\ts_mov_b64 exec, -1" : "=v"(d) : "v"(voff), "s"(sb), "i"(OFF), "s"(m));
}
template <bool AG, int OFF8> DI void ds_read2(bf16x8& d, unsigned addr) {
  addr += OFF8 * 8;
  if constexpr (AG) asm volatile("ds_read2_b64 %0, %1 offset1:2" : "=a"(d) : "v"(addr) : "memory"); else asm volatile("ds_read2_b64 %0, %1 offset1:2" : "=v"(d) : "v"(addr) : "memory");
}
template <bool AG, int N> DI void waitl(bf16x8& d) {
  if constexpr (AG) asm volatile("s_waitcnt lgkmcnt(%1)" : "+a"(d) : "i"(N) : "memory"); else asm volatile("s_waitcnt lgkmcnt(%1)" : "+v"(d) : "i"(N) : "memory");
}
template <int OFF> DI void ds_write_a(unsigned addr, const bf16x8& v) { asm volatile("ds_write_b128 %0, %1 offset:%2" ::"v"(addr), "a"(v), "i"(OFF) : "memory"); }
template <int N, class T> DI void wait2(T& a, T& b) { asm volatile("s_waitcnt vmcnt(%2)" : "+v"(a), "+v"(b) : "i"(cap63(N))); }
DI void set_flag(char* flag, u64 tag) {
  unsigned z = 0;
  asm volatile("s_mov_b64 exec, 1\n\tglobal_atomic_swap_x2 %0, %1, %2 sc1\n\ts_mov_b64 exec, -1" ::"v"(z), "v"(tag), "s"(flag) : "memory");
}
DI void ld8_1(u64& d, unsigned voff, const char* sb) { asm volatile("global_load_dwordx2 %0, %1, %2 sc1" : "=v"(d) : "v"(voff), "s"(sb) : "memory"); }
template <int NL> DI void rd_flag(u64& v, unsigned voff, const char* sb) {
  const u64 zero = 0;
  if constexpr (NL == 64) asm volatile("global_atomic_or_x2 %0, %1, %3, %2 sc0 sc1" : "+v"(v) : "v"(voff), "s"(sb), "v"(zero) : "memory");
  else asm volatile("s_mov_b64 exec, %4\n\tglobal_atomic_or_x2 %0, %1, %3, %2 sc0 sc1\n\ts_mov_b64 exec, -1" : "+v"(v) : "v"(voff), "s"(sb), "v"(zero), "s"((1ull << (NL & 63)) - 1) : "memory");
}
template <int NL> DI void poll(const char* base, unsigned voff, u64 tag) {
  for (int spin = 0; spin < (1 << 15); ++spin) {
    u64 v = tag;
    rd_flag<NL>(v, voff, base);
    waitx<false, 0>(v);
    if (__all(v == tag)) break;
  }
  asm volatile("" ::: "memory");
}

template <int RBX> DI void block_map(int& rb, int& cb) {
  const int x = blockIdx.x & 7, i = blockIdx.x >> 3;
  constexpr int CBX = 32 / RBX, GC = 16 / CBX;
  rb = RBX * (x / GC) + i / CBX; cb = CBX * (x % GC) + i % CBX;
}

DI float a_fma(float a, float b, float c) { float d; asm volatile("v_fma_f32 %0, %1, %2, %3" : "=v"(d) : "v"(a), "v"(b), "v"(c)); return d; }
DI float a_fms(float a, float b, float c) { float d; asm volatile("v_fma_f32 %0, %1, -%2, %3" : "=v"(d) : "v"(a), "v"(b), "v"(c)); return d; }
DI void a_exp(float& x) { asm volatile("v_exp_f32 %0, %0" : "+v"(x)); }
DI void a_add(float& s, float x) { asm volatile("v_add_f32 %0, %0, %1" : "+v"(s) : "v"(x)); }
DI void a_mul(float& x, float s) { asm volatile("v_mul_f32 %0, %0, %1" : "+v"(x) : "v"(s)); }
template <bool NEG> DI void a_hedge(float& c, float& n, float& s, float prev, float p, float sc, float m) {
  if constexpr (NEG) asm volatile("v_exp_f32 %0, %0\n\tv_fma_f32 %1, %4, -%5, %6\n\tv_add_f32 %2, %2, %3" : "+v"(c), "=&v"(n), "+v"(s) : "v"(prev), "v"(p), "v"(sc), "v"(m));
  else asm volatile("v_exp_f32 %0, %0\n\tv_fma_f32 %1, %4, %5, %6\n\tv_add_f32 %2, %2, %3" : "+v"(c), "=&v"(n), "+v"(s) : "v"(prev), "v"(p), "v"(sc), "v"(m));
}
DI void a_mul4(float& a, float& b, float& c, float& d, float s) {
  asm volatile("v_mul_f32 %0, %0, %4\n\tv_mul_f32 %1, %1, %4\n\tv_mul_f32 %2, %2, %4\n\tv_mul_f32 %3, %3, %4" : "+v"(a), "+v"(b), "+v"(c), "+v"(d) : "v"(s));
}
DI void a_sum16(f32x4& s, const bf16x8& sel, const bf16x8& f) { asm volatile("v_mfma_f32_16x16x32_bf16 %0, %1, %2, %0" : "+v"(s) : "v"(sel), "v"(f)); }
DI void a_mm4(float& mx, float& mn, float a, float b, float c, float d) {
  asm volatile("v_max3_f32 %0, %0, %2, %3\n\tv_min3_f32 %1, %1, %2, %3\n\tv_max3_f32 %0, %0, %4, %5\n\tv_min3_f32 %1, %1, %4, %5" : "+v"(mx), "+v"(mn) : "v"(a), "v"(b), "v"(c), "v"(d));
}
DI float a_max3(float a, float b, float c) { float d; asm volatile("v_max3_f32 %0, %1, %2, %3" : "=v"(d) : "v"(a), "v"(b), "v"(c)); return d; }
DI float a_min3(float a, float b, float c) { float d; asm volatile("v_min3_f32 %0, %1, %2, %3" : "=v"(d) : "v"(a), "v"(b), "v"(c)); return d; }

DI float opq_a(float v) { asm volatile("" : "+a"(v)); return v; }
DI void pin_a(bf16x8& a) { asm volatile("" : "+a"(a)); }
DI void pin2(f32x16& a, f32x16& b) { asm volatile("" : "+v"(a), "+v"(b)); }
DI bf16x8 cs_frag(float c) {
  const float hi = rbf(c), mid = rbf(c - hi), lo = (c - hi) - mid;
  return __builtin_bit_cast(bf16x8, u32x4{cvtpk(hi, mid), cvtpk(lo, 0.f), 0u, 0u});
}
template <bool CS> struct Red {
  bf16x8 p[4];
  float t[4], a, b, sd;
  template <int K> DI void run(int lane) {
    if constexpr (K == 0) asm volatile("" : "+v"(p[0]), "+v"(p[1]), "+v"(p[2]), "+v"(p[3]));
    if constexpr (K < 3) {
      const f32x4 u = __builtin_bit_cast(f32x4, p[K + 1]), v = __builtin_bit_cast(f32x4, p[0]);
#pragma unroll
      for (int e = 0; e < 4; ++e) { if (K == 0) t[e] = v[e]; a_add(t[e], u[e]); }
    }
    if constexpr (K == 3) {
      auto x = __builtin_amdgcn_permlane16_swap(__float_as_uint(t[0]), __float_as_uint(t[1]), false, false);
      auto y = __builtin_amdgcn_permlane16_swap(__float_as_uint(t[2]), __float_as_uint(t[3]), false, false);
      a = opq_v(__uint_as_float(x[0]) + __uint_as_float(x[1])); b = opq_v(__uint_as_float(y[0]) + __uint_as_float(y[1]));
    }
    if constexpr (K == 4) { auto x = __builtin_amdgcn_permlane32_swap(__float_as_uint(a), __float_as_uint(b), false, false); a = opq_v(__uint_as_float(x[0]) + __uint_as_float(x[1])); }
    if constexpr (K == 5) {
      if constexpr (CS) a = lane_get(a, (lane >> 2) + ((lane & 3) << 4));
      else { const int src = ((lane & 31) >> 1) + ((lane & 1) << 5); b = lane_get(a, src + 16); a = lane_get(a, src); }
    }
    if constexpr (CS) {
      if constexpr (K == 8) { t[0] = opq_v(rbf(a)); t[1] = opq_v(a - t[0]); }
      if constexpr (K == 9) { const float mid = rbf(t[1]); p[0] = opq_v(__builtin_bit_cast(bf16x8, u32x4{cvtpk(t[0], mid), cvtpk(t[1] - mid, 0.f), 0u, 0u})); }
    } else {
      if constexpr (K == 8) { const float m = a * (1.f / D_); b = opq_v(fmaxf(b * (1.f / D_) - m * m, 0.f)); a = opq_v(rbf(m)); }
      if constexpr (K == 9) b = opq_v(rbf(rbf(b) + 1e-5f));
      if constexpr (K == 10) { sd = opq_v(rbf(__builtin_amdgcn_sqrtf(b))); b = opq_v(__builtin_amdgcn_rcpf(sd)); }
    }
  }
};
DI void pin4(bf16x8 (&p)[4]) { asm volatile("" : "+v"(p[0]), "+v"(p[1]), "+v"(p[2]), "+v"(p[3])); }
DI bf16x8 mean_frag(float mean, bool on) { const float m = on ? -mean : 0.f; return __builtin_bit_cast(bf16x8, u32x4{cvtpk(m, m), cvtpk(m, 0.f), 0u, 0u}); }

#define STAMP(i)

constexpr int SLOT = 16384, XL = 16;
struct WinPtr {
  unsigned b[3];
  DI void init(unsigned a) { sfor<0, 3>([&](auto i) { b[i] = a + i * 65536u; asm volatile("" : "+v"(b[i])); }); }
  template <int OFF> DI bf16x8 frag() const { return *(LAS const bf16x8*)((LAS const char*)(uintptr_t)b[OFF >> 16] + (OFF & 65535)); }
};
struct Ctx { unsigned xv; WinPtr p; };
#define AI __attribute__((always_inline))
#define RARE(c) __builtin_expect(!!(c), 0)
#define IC(n) std::integral_constant<int, (n)>{}

template <int S> DI void ld_x(bf16x8 (&x)[64], const Ctx& c, const char* xb) { ldx1<false, (S % 8) * 1024 - 4096>(x[S], c.xv, xb + (S / 8) * 8192 + 4096); }
template <class H> constexpr int g64_wait(int s) {
  int n = 0;
  const int t0 = s - XL;
  if (t0 < 0) n += XL - 1 - s; else n += H::n(t0);
  for (int t = t0 < 0 ? 0 : t0 + 1; t < s; ++t) n += (t + XL < 64 ? 1 : 0) + H::n(t);
  return n;
}
constexpr int a_slot(int g);
template <bool MAPA, bool ZERO, class H, class FX, class FA>
DI void gemm64(const Ctx& c, bf16x8 (&x)[64], const char* xb, f32x16& acc0, f32x16& acc1, FX&& hook, FA&& aux) {
  bf16x8 f0[3], f1[3];
  auto off = [](int s) constexpr { return (MAPA ? a_slot(24 + s / 8) : s / 8) * SLOT + (s % 8) * 2048; };
  auto rd0 = [&](auto s) { f0[s % 3] = c.p.template frag<off(s)>(); };
  auto rd1 = [&](auto s) { f1[s % 3] = c.p.template frag<off(s) + 1024>(); };
  rd0(IC(0)); rd1(IC(0)); rd0(IC(1)); rd1(IC(1));
  sfor<0, 64>([&](auto s) {
    waitx<false, g64_wait<H>(s)>(x[s]);
    SBAR();
    mm<false, false, ZERO && s == 0>(acc0, f0[s % 3], x[s]);
    SBAR();
    if constexpr (s + 2 < 64) rd0(IC(s + 2));
    if constexpr (s + XL < 64) ld_x<s + XL>(x, c, xb);
    aux(IC(2 * s));
    SBAR();
    mm<false, false, ZERO && s == 0>(acc1, f1[s % 3], x[s]);
    SBAR();
    if constexpr (s + 2 < 64) rd1(IC(s + 2));
    hook(s);
    aux(IC(2 * s + 1));
    SBAR();
  });
  mdone(acc0, acc1);
}
struct HookP6 { static constexpr int n(int s) { return s >= 52 ? 1 : 0; } };
struct HookP3 { static constexpr int n(int s) { return s >= 52 ? 1 : 0; } };
struct HookP4 { static constexpr int n(int s) { return s == 52 || s == 6 || (s >= 8 && s < 12) || (s >= 14 && s < 30 && s % 2 == 0) ? 1 : 0; } };

DI void park4(u32x4 (&v)[4]) { asm volatile("" : "+a"(v[0]), "+a"(v[1]), "+a"(v[2]), "+a"(v[3])); }
DI void res_to_acc(u32x4 (&xr)[4], f32x16& a0, f32x16& a1) {
  park4(xr);
#pragma unroll
  for (int e = 0; e < 4; ++e) {
    a0[2 * e] = bflo(xr[0][e]); a0[2 * e + 1] = bfhi(xr[0][e]); a0[8 + 2 * e] = bflo(xr[1][e]); a0[9 + 2 * e] = bfhi(xr[1][e]);
    a1[2 * e] = bflo(xr[2][e]); a1[2 * e + 1] = bfhi(xr[2][e]); a1[8 + 2 * e] = bflo(xr[3][e]); a1[9 + 2 * e] = bfhi(xr[3][e]);
  }
  asm volatile("" : "+v"(a0), "+v"(a1));
}
DI void store_stats(int r, const u32x4 (&xr)[4], char* stp) {
  float s1 = 0.f, s2 = 0.f, s3 = 0.f, s4 = 0.f;
#pragma unroll
  for (int i = 0; i < 4; ++i)
#pragma unroll
    for (int e = 0; e < 4; e += 2) {
      s1 = dot2(xr[i][e], 0x3f803f80u, s1); s2 = dot2(xr[i][e], xr[i][e], s2);
      s3 = dot2(xr[i][e + 1], 0x3f803f80u, s3); s4 = dot2(xr[i][e + 1], xr[i][e + 1], s4);
    }
  st2_1(f32x2{swap_add(s1 + s3), swap_add(s2 + s4)}, (unsigned)(r * 8), stp);
}
DI void store_x(const Ctx& c, int r, const f32x16& a0, const f32x16& a1, u32x4 (&xr)[4], char* dst, char* stp) {
  SBAR();
  xr[0] = __builtin_bit_cast(u32x4, pack8<0>(a0)); st4_1<0>(xr[0], c.xv, dst); SBAR();
  xr[1] = __builtin_bit_cast(u32x4, pack8<1>(a0)); st4_1<1024>(xr[1], c.xv, dst); SBAR();
  xr[2] = __builtin_bit_cast(u32x4, pack8<0>(a1)); st4_1<2048>(xr[2], c.xv, dst); SBAR();
  xr[3] = __builtin_bit_cast(u32x4, pack8<1>(a1)); st4_1<3072>(xr[3], c.xv, dst); SBAR();
  store_stats(r, xr, stp);
  SBAR();
  park4(xr);
}

template <class FX> DI void hedge_exp(const f32x16& p, float sc, f32x16& ep, f32x16& en, FX&& hook) {
  float mx = fmaxf(p[0], p[1]), mn = fminf(p[0], p[1]);
#pragma unroll
  for (int i = 2; i < 16; i += 2) { mx = fmaxf(fmaxf(mx, p[i]), p[i + 1]); mn = fminf(fminf(mn, p[i]), p[i + 1]); }
  mx = swap_max(mx) * sc; mn = swap_min(mn) * sc;
  sfor<0, 16>([&](auto i) {
    SBAR();
    hook(i);
    constexpr int e = i;
    float a = __builtin_amdgcn_exp2f(__builtin_fmaf(p[e], sc, -mx)), b = __builtin_amdgcn_exp2f(__builtin_fmaf(p[e], -sc, mn));
    asm volatile("" : "+v"(a), "+v"(b));
    ep[e] = a; en[e] = b;
  });
  SBAR();
}

constexpr int a_slot(int g) { return g == 8 || g == 15 ? 8 : (g > 8 && g < 15 ? g - 9 : g % 8); }
constexpr bool a_bar(int s) { return (s + 2) % 8 == 0 && s < 190 && s != 78; }
constexpr int QP = 128 + 24, QC = 128 + 36;
constexpr int a_piece(int s, bool first) {
  constexpr int t[][3] = {{46, 8, 1}, {54, 9, 1}, {58, 10, 1}, {64, 11, 2}, {72, 12, 2}, {80, 13, 2}, {88, 14, 2}, {96, 15, 2}, {104, 16, 1}, {108, 17, 1}, {112, 18, 1},
                          {116, 19, 1}, {120, 20, 1}, {134, 21, 1}, {138, 22, 1}, {142, 23, 1}, {146, 24, 1}, {150, 25, 1}, {158, 26, 1}};
  if (!first && s >= 8 && s < 24 && s % 2 == 0) return 24 + (s - 8) / 2;
  for (auto& e : t) if (s >= e[0] && s < e[0] + 4 * e[2] && (s - e[0]) % e[2] == 0) return e[1] * 4 + (s - e[0]) / e[2];
  return -1;
}
constexpr int a_park(int s) { return s >= 129 && s < 135 ? s - 129 : (s == 162 || s == 163 ? s - 156 : -1); }
constexpr int a_ld0(bool first) { return first ? 32 : 2; }
constexpr bool a_side(int s, bool first) { return (s >= a_ld0(first) && s < a_ld0(first) + (first ? 12 : 4)) || s == 50 || (s >= 154 && s < 158) || a_park(s) >= 0; }
constexpr int f_rd(int n) { return n % 6 < 4 && n / 6 + 1 < 16 ? (n / 6 + 1) * 4 + n % 6 : -1; }
constexpr int f_in(int n) { return (n + 1) % 6 < 4 && (n + 1) / 6 >= 1 && (n + 1) / 6 + 4 < 16 ? ((n + 1) / 6 + 4) * 4 + (n + 1) % 6 : -1; }
constexpr int f_raw(int s) { return s >= 6 && s < 46 && s % 2 == 0 ? ((s + 2) / 8 + 2) * 4 + ((s + 2) % 8) / 2 : -1; }
struct SchedA {
  int xw[64], bw[192], sw[3], aw, fw, cw, pw, zw, uw, vw, lw[32];
  constexpr SchedA(bool first) : xw{}, bw{}, sw{}, aw(0), fw(0), cw(0), pw(0), zw(0), uw(0), vw(0), lw{} {
    int pos = 0, xpos[64] = {}, wpos[32] = {}, spos[192] = {}, fpos = 0, stpos = 0, ppos = 0, upos = 0, vpos = 0, lpos[34] = {};
    if (first) { xpos[1] = 4; xpos[2] = 12; wpos[1] = 18; xpos[3] = 22; wpos[2] = 26; xpos[4] = pos = 30; } else for (int s = 0; s < XL; ++s) xpos[s] = ++pos;
    for (int s = 0; s < 192; ++s) {
      if (s == 128) { pos += 3; stpos = pos; }
      if (a_bar(s)) {
        const int g = (s + 2) / 8 + (s == 70);
        bw[s] = wpos[g] ? pos - wpos[g] : 63;
        if (s == 62 && pos - spos[50] < bw[s]) bw[s] = pos - spos[50];
        if (s == 142) { if (pos - stpos < bw[s]) bw[s] = pos - stpos; ++pos; }
        if (s == 6 && first) ++pos;
      }
      if (s == 102) uw = pos - upos;
      if (s == 118) vw = pos - vpos;
      for (int k = 0; k < 3; ++k) if (s == a_ld0(first) + 14 + 4 * k) sw[k] = pos - spos[a_ld0(first) + 3 + 4 * k];
      if (s == 32) aw = pos - fpos;
      if (s == 128) fw = pos - fpos;
      if (s == 170) cw = pos - spos[157];
      if (s == QC) pw = pos - ppos;
      if (s < 64) xw[s] = first ? (s % 3 == 0 && s / 3 + 1 < 16 ? pos - xpos[s / 3 + 1] : 63) : pos - xpos[s];
      if (first && s < 64 && f_in(2 * s) >= 0) xpos[f_in(2 * s) / 4] = ++pos;
      if (!first && s + XL < 64) xpos[s + XL] = ++pos;
      if ((first && s == 22) || s == 127) fpos = ++pos;
      if (s == QP) ppos = ++pos;
      if (s >= QC) lpos[s - QC] = ++pos;
      if (a_piece(s, first) >= 0) wpos[a_piece(s, first) / 4] = ++pos;
      if (a_side(s, first)) spos[s] = ++pos;
      if (first && s < 64 && f_in(2 * s + 1) >= 0) xpos[f_in(2 * s + 1) / 4] = ++pos;
      if (first && f_raw(s) >= 0) wpos[f_raw(s) / 4] = ++pos;
      if (s == 63) pos += 8;
      if (s == 87) upos = pos;
      if (s == 103) vpos = pos;
      if (s == 177) zw = pos - lpos[1];
    }
    for (int i = 0; i < 32; ++i) {
      lw[i] = pos - lpos[2 + i];
      if (i % 2 == 0 && 192 - QC + i / 2 < 34) lpos[192 - QC + i / 2] = ++pos;
      if (i % 2 == 0 && i >= 16) ++pos;
    }
  }
};
constexpr int cmin(int a, int b) { return a < b ? a : b; }

#define R_OA "v[64:79]"
#define R_OB "v[80:95]"
#define S0_(n, q, m) "v_mfma_f32_32x32x16_bf16 v[" #n ":" #n "+15], v[208:211], v[" #q ":" #q "+3], v[" #m ":" #m "+15]\n\t"
#define S1_(n, q) "v_mfma_f32_32x32x16_bf16 v[" #n ":" #n "+15], v[212:215], v[" #q "+4:" #q "+7], v[" #n ":" #n "+15]\n\t"
#define PV0_(o, p) "v_smfmac_f32_32x32x32_bf16 v[" #o ":" #o "+15], v[216:219], v[" #p ":" #p "+7], v236\n\t"
#define PV1_(o, p) "v_smfmac_f32_32x32x32_bf16 v[" #o ":" #o "+15], v[220:223], v[" #p "+8:" #p "+15], v236\n\t"
#define L0_(l, p) "v_smfmac_f32_16x16x64_bf16 v[" #l ":" #l "+3], v[224:227], v[" #p ":" #p "+7], v236\n\t"
#define L1_(l, p) "v_smfmac_f32_16x16x64_bf16 v[" #l ":" #l "+3], v[224:227], v[" #p "+8:" #p "+15], v236\n\t"
#define E_(e, i) "v_exp_f32 v[" #e "+" #i "], v[" #e "+" #i "]\n\t"
#define RD_(d, off) "ds_read_b128 v[" #d ":" #d "+3], v237 offset:" #off "\n\t"
#define HALF_(e, n, q, m, p, o, l, X0, X1, X2, X3, X4, X5) \
  S0_(n, q, m) X0 E_(e, 0) E_(e, 1) E_(e, 2) S1_(n, q) X1 E_(e, 3) E_(e, 4) E_(e, 5) PV0_(o, p) X2 E_(e, 6) E_(e, 7) E_(e, 8) \
  PV1_(o, p) X3 E_(e, 9) E_(e, 10) E_(e, 11) L0_(l, p) X4 E_(e, 12) E_(e, 13) L1_(l, p) X5 E_(e, 14) E_(e, 15)
#define LG2_ "s_waitcnt lgkmcnt(2)\n\t"
#define LG0_ "s_waitcnt lgkmcnt(0)\n\t"
#define STEP_(b, nb, kv, wo, Y0, Y1) \
  HALF_(128, 160, 200, 112, 176, 80, 232, "", "", RD_(208, b + 2048), RD_(212, b + 3072), RD_(216, b), RD_(220, b + 1024) LG2_) \
  HALF_(160, 144, 192, 96, 128, 64, 228, "", LG0_, "s_add_u32 m0, s44, " #nb "\n\t", "", "global_load_lds_dwordx4 v" #kv ", s[40:41] sc1\n\t", \
        "global_load_lds_dwordx4 v" #kv ", s[40:41] offset:1024 sc1\n\t") \
  HALF_(144, 176, 200, 112, 160, 80, 232, Y0, "", RD_(208, b + 6144), RD_(212, b + 7168), RD_(216, b + 4096), RD_(220, b + 5120) LG2_) \
  HALF_(176, 128, 192, 96, 144, 64, 228, "s_mov_b32 m0, s45\n\t", LG0_, "global_load_lds_dwordx4 v240, s[42:43] offset:" #wo " sc1\n\t", Y1, \
        "s_waitcnt vmcnt(4)\n\t", "s_barrier\n\t")
DI float max16(const f32x16& s) {
  float mx = s[0];
#pragma unroll
  for (int e = 1; e < 16; ++e) mx = fmaxf(mx, s[e]);
  return mx;
}
typedef __attribute__((ext_vector_type(8))) unsigned u32x8;
typedef __attribute__((ext_vector_type(16))) unsigned u32x16;

__global__ __launch_bounds__(256, 1) void k_main(const char* xin, const char* wqkv, const char* wphi, const char* woA, const char* wq, const char* wk, const char* wv,
                                                 const char* woB, char* ws, char* out) {
  __shared__ __attribute__((aligned(16))) char smem_[163840];
  LAS char* const smem = (LAS char*)smem_;
  const unsigned lds0 = (unsigned)(uintptr_t)smem;
  Ctx c;
  int lane = threadIdx.x & 63, w = __builtin_amdgcn_readfirstlane(threadIdx.x >> 6), r = lane & 31, h = lane >> 5, rb, cb;
  bf16x8 cf;
  { const char* pc; unsigned last;
    asm volatile("s_getpc_b64 %0\n1:\n\ts_mov_b32 %1, .Lk_main_tail-1b" : "=s"(pc), "=s"(last));
    const unsigned o = (((blockIdx.x >> 3) * 4 + w) & 63) * 1024;
    ldx0<0>(cf, (unsigned)(lane * 16), pc + (o < last ? o : last)); }
  SBAR();
  block_map<4>(rb, cb);
  int tt = rb * 4 + w, blk = (lane >> 4) & 1, q4 = (lane & 15) >> 2, p4 = lane & 3;
  c.xv = lane * 16; c.p.init(lds0 + c.xv);
  const int ut = rb * 4 + (cb >> 2), ty = cb & 3;
  char *flags, *col, *colw, *act, *acto, *fact, *facto, *stt, *stto, *kvp, *zp, *fkvp, *fwl, *kvf, *fkv;
  const char *xu_p, *su_p, *wkv, *wph;
  unsigned kvb;
  u64 tag = 0;
  u32x4 xr[4] = {};
  unsigned ldsw = lds0 + w * 4096;
  auto wt = [&](int m, int ch) { return colw + C_WT + m * 131072 + ch * SLOT; };
  auto pc_w = [&](int m, int slot, int ch, auto i) { if constexpr (i == 0) dma_c<0>(ldsw + slot * SLOT, c.xv, wt(m, ch)); else dma_n<i * 1024>(c.xv, wt(m, ch)); };
  auto pc_a = [&](auto n) { constexpr int g = n / 4; pc_w(g < 8 ? 1 : (g < 16 ? 2 : (g < 24 ? 0 : 3)), a_slot(g), g % 8, IC(n % 4)); };
  u64 csm = ~0ull;
  float csv[4] = {};
  auto pc_m = [&](auto n) { constexpr int g = n / 4; dma_c<(n % 4) * 1024>(ldsw + a_slot(g) * SLOT, c.xv, wt(2, g % 8)); };
  auto ld_cs = [&](bf16x8& d, int m, auto i) { ldx1m<i * 1024>(d, c.xv, col + C_CS + m * 4096, csm); };
  auto raw_src = [&](int pitch, auto i) AI { const int row = lane >> 3; return (unsigned)((32 * w + 8 * i + row) * pitch + ((lane & 7) ^ (((row >> 1) & 1) << 2)) * 16 - i * 1024); };
  auto raw_in = [&](auto j, int slot, auto i) AI {
    const int hi = rb >> 3, pitch = (j == 0 || (j == 2 && hi)) ? 6144 : 2048;
    const char* src = (j == 0 ? wqkv + (hi ? 0 : 4096) : (j == 1 ? (hi ? wq : woA) : (hi ? wqkv + 2048 : woB))) + cb * 128 + (size_t)(rb & 7) * 128 * pitch;
    dma<i * 1024>(ldsw + slot * SLOT, raw_src(pitch, i), src);
  };
  auto raw_out = [&](auto j, int slot) AI {
    const int hi = rb >> 3, ch = rb & 7, m = j == 0 ? 2 - 2 * hi : (j == 1 ? 3 + hi : 5 - 4 * hi);
    LAS const char* p = smem + slot * SLOT + (w >> 1) * 8192 + (4 * h + q4) * 128 + (((((q4 >> 1) ^ w) & 1) << 2) + 2 * blk + (p4 >> 1)) * 16 + 8 * (p4 & 1);
    char* dst = col + C_WT + m * 131072 + ch * SLOT + (w >> 1) * 8192 + (w & 1) * 1024 + 4096;
    float cs = 0.f;
    sfor<0, 4>([&](auto s) AI {
      const u32x4 f = __builtin_bit_cast(u32x4, cat8(trread(p + s * 2048), trread(p + s * 2048 + 1024)));
      st4_1<s * 2048 - 4096>(f, c.xv, dst);
#pragma unroll
      for (int e = 0; e < 4; ++e) cs = dot2(f[e], 0x3f803f80u, cs);
    });
    st1_1(swap_add(cs), (unsigned)(r * 4), col + C_CS + m * 4096 + (ch * 2 + (w >> 1)) * 256 + (w & 1) * 128);
  };
  const int w0 = w; const unsigned ldsw0 = ldsw;
#define REFRESH() do { \
    w = w0; ldsw = ldsw0; \
    asm volatile("" : "+s"(w), "+s"(tag), "+s"(act), "+s"(acto), "+s"(fact), "+s"(facto), "+s"(stt), "+s"(stto), "+s"(col), "+s"(colw), "+s"(ldsw)); \
    asm volatile("" : "+v"(lane)); \
    r = lane & 31; h = lane >> 5; blk = (lane >> 4) & 1; q4 = (lane & 15) >> 2; p4 = lane & 3; c.xv = lane * 16; c.p.init(lds0 + c.xv); \
  } while (0)
  int l = 0;
  float mean, rstd;
  Red<true> cw;
  f32x16 ra0, ra1;
  bf16x8 park[16];
      constexpr int NA = 58;
      constexpr SchedA SQ(false), S1(true);
      bf16x8 x[64], f0[3], f1[3], wf[4], kp[4], pp[32], zq[2], sel[2], mf0, mf1;
      Red<false> st; Red<true> ck, cv, cq;
      f32x16 acc[6], p[2], kvs;
      u64 fv;
      float mx, mn, e[16], en[16], se, sn, sce;
      f32x4 ks0 = {}, ks1 = {};
      unsigned arow, pk0, ra, xo[4], xs;
      const char* xw = xin + (size_t)tt * 65536;
      LAS char* pk_img = smem + 6 * SLOT;
      LAS char* v_img = smem + 5 * SLOT;
      LAS char* sh = smem + 9 * SLOT + 4096;
      auto issue_x = [&](auto s) { ldx1<(s < NA), (s % 8) * 1024 - 4096>(x[s], c.xv, act + (s / 8) * 8192 + 4096); };
      auto ld_park = [&](auto i) { ldx1<true, (i % 4) * 1024>(park[i], c.xv, wt(4, 2 + i / 4)); };
      auto ldp = [&](auto i) {
        if constexpr (i >= 2) ldx1<true, ((i - 2) % 8) * 1024 - 4096>(pp[i - 2], c.xv, kvp + ((i - 2) / 8) * 8192 + 4096); else ldx1<false, i * 1024>(zq[i], c.xv, zp);
      };
      auto rd_wf = [&](auto k) { LAS const char* wp = smem + 9 * SLOT + (4 * h + q4) * 64 + blk * 32 + p4 * 8 + k * 1024; wf[k] = cat8(trread(wp), trread(wp + 512)); };
      auto rd01 = [&](auto s, auto t, auto fl) {
        if constexpr (s < 192) {
          bf16x8 f;
          if constexpr (fl && s < 64) { LAS const char* q = (LAS const char*)(uintptr_t)(ra ^ (t * 64u)) + ((s / 8 + 3) % 4) * SLOT + (s % 8) * 2048; f = cat8(trread(q), trread(q + 1024)); }
          else f = c.p.template frag<a_slot(s / 8) * SLOT + (s % 8) * 2048 + t * 1024>();
          if constexpr (t == 0) f0[s % 3] = f; else f1[s % 3] = f;
        }
      };
      auto x_in = [&](auto n) { dma<(n % 4) * 1040>(lds0 + w * 20480 + ((n / 4) % 4) * 4160, xs + (n % 4) * (16384 - 1040), xw + (n / 4) * 128); };
      auto x_out = [&](auto s) { ds_read2<(s < NA), ((s / 4) % 4) * 520>(x[s], xo[s % 4]); };
      auto x_fin = [&](auto g, auto n) { waitl<(4 * g < NA), n>(x[4 * g]); sfor<4 * g + 1, 4 * g + 4>([&](auto s) { if constexpr (s < NA) pin_a(x[s]); else x[s] = opq_v(x[s]); }); };
      auto k_in = [&](auto n) { dma<(n % 4) * 1024>(ldsw + (5 + (n / 4 + 3) % 4) * SLOT, raw_src(6144, IC(n % 4)), wqkv + 2048 + cb * 128 + (size_t)(n / 4) * 128 * 6144); };
      auto v_out = [&](auto t, auto hf) {
        *(LAS bf16x8*)(v_img + t * 8192 + w * 2048 + hf * 1024 + lane * 16) = pack8<hf>(acc[2 + t]);
      };
      auto sum_mm = [&](auto i) { waitx<true, SQ.lw[i]>(pp[i]); mm<false, true, i == 0>(kvs, sel[i & 1], pp[i]); };
      auto kmap = [&](auto gi) {
        constexpr int g = gi;
        if constexpr (g == 4) pin2(acc[0], acc[1]);
        if constexpr (g >= 6 && g < 10) mm<false, false, g % 2 == 0>(p[(g - 6) / 2], wf[g - 6], kp[g - 6]);
        if constexpr (g >= 4 && g < 8) kp[g - 4] = opq_v(pack8<g % 2>(acc[(g - 4) / 2]));
        if constexpr (g == 10) mm<true, false, false, true>(acc[2], cv.p[0], mf0);
        if constexpr (g == 11) mm<true, false, false, true>(acc[3], cv.p[0], mf1);
        if constexpr (g >= 12) {
          constexpr int hd = (g - 12) / 58, j = (g - 12) % 58, n = (j - 7) / 2;
          const f32x16& q = p[hd];
          if constexpr (j == 0) { mx = a_max3(q[0], q[1], q[2]); mn = a_min3(q[0], q[1], q[2]); mx = a_max3(mx, q[3], q[3]); mn = a_min3(mn, q[3], q[3]); }
          if constexpr (j >= 1 && j < 4) a_mm4(mx, mn, q[4 * j], q[4 * j + 1], q[4 * j + 2], q[4 * j + 3]);
          if constexpr (j == 4) mx = opq_v(swap_max(mx) * -sce);
          if constexpr (j == 5) mn = opq_v(swap_min(mn) * sce);
          if constexpr (j == 6) { e[0] = a_fma(q[0], sce, mx); en[0] = a_fms(q[0], sce, mn); se = opq_v(0.f); sn = opq_v(0.f); }
          if constexpr (j >= 7 && j < 37) {
            if constexpr (j % 2) a_hedge<false>(e[n], e[n + 1], se, n ? e[n ? n - 1 : 0] : sn, q[n + 1], sce, mx);
            else a_hedge<true>(en[n], en[n + 1], sn, n ? en[n ? n - 1 : 0] : se, q[n + 1], sce, mn);
          }
          if constexpr (j == 37) { a_exp(e[15]); a_add(se, e[14]); }
          if constexpr (j == 38) { a_exp(en[15]); a_add(sn, en[14]); }
          if constexpr (j == 39) se = opq_v(swap_add(se + e[15]));
          if constexpr (j == 40) sn = opq_v(swap_add(sn + en[15]));
          if constexpr (j == 41) { se = opq_v(__builtin_amdgcn_rcpf(se) * rstd); sn = opq_v(__builtin_amdgcn_rcpf(sn) * rstd); }
          if constexpr (j >= 42) {
            constexpr int c8 = (j - 42) / 2, o = 4 * (c8 & 3);
            float* v = c8 < 4 ? e : en;
            if constexpr (j % 2 == 0) { a_mul4(v[o], v[o + 1], v[o + 2], v[o + 3], c8 < 4 ? se : sn); pk0 = opq_v(arow ^ (c8 << 4)); }
            else *(LAS u32x2*)((LAS char*)(uintptr_t)pk0 + hd * 16384) = u32x2{cvtpk(v[o], v[o + 1]), cvtpk(v[o + 2], v[o + 3])};
          }
        }
      };
      auto aux = [&](auto s, auto i, auto fl) {
        constexpr int g1 = 2 * (s - a_ld0(fl) - 14) + i, g3 = 2 * (s - 128) + i;
        if constexpr (s < 64) {
          if constexpr (fl) { if constexpr (i == 0) a_sum16(ks0, sel[0], f0[s % 3]); else a_sum16(ks1, sel[0], f1[s % 3]); }
          if constexpr (g1 >= 0 && g1 < 11) st.template run<g1>(lane);
          if constexpr (g1 == 11) { mean = st.a; rstd = st.b; sce = opq_v(rstd * LOG2E); }
          if constexpr (g1 == 12) mf0 = opq_v(mean_frag(mean, h == 0));
          if constexpr (g1 == 13) mf1 = opq_v(mean_frag(mean, h == 1));
          if constexpr (fl && g1 >= 8 && g1 < 14) cv.template run<g1 - 8>(lane);
          if constexpr (fl && g1 >= 16 && g1 < 22) cq.template run<g1 - 16>(lane);
          if constexpr (fl && g1 == 18) csv[1] = opq_a(cv.a);
          if constexpr (fl && g1 == 26) csv[2] = opq_a(cq.a);
          if constexpr (g1 == 22) cv.a = csv[1] = opq_a(csv[1]);
          if constexpr (g1 == 22 || g1 == 23) cv.template run<g1 - 14>(lane);
          if constexpr (!fl) {
            if constexpr (g1 == 14) ck.a = opq_v(csv[0]);
            if constexpr (g1 == 14 || g1 == 15) ck.template run<g1 - 6>(lane);
            if constexpr (g1 == 18) mm<false, false, false, true>(acc[0], ck.p[0], mf0);
            if constexpr (g1 == 19) mm<false, false, false, true>(acc[1], ck.p[0], mf1);
          }
          if constexpr (s == 60 && i == 0) *(LAS short*)(sh + 8704 + w * 64 + (r >> 4) * 32 + ((r >> 2) & 1) * 16 + ((r >> 3) & 1) * 8 + (r & 3) * 2) = (short)(__float_as_uint(st.sd) >> 16);
          if constexpr (s == 61 && i == 0) arow = opq_v((unsigned)(uintptr_t)pk_img + (w * 32 + r) * 128 + h * 8 + ((r & 7) << 4));
          if constexpr (s >= 62) rd_wf(IC(2 * (s - 62) + i));
        } else if constexpr (s < 128) {
          kmap(IC(2 * (s - 64) + i));
        } else {
          if constexpr (g3 == 2) cq.a = csv[2] = opq_a(csv[2]);
          if constexpr (g3 == 2 || g3 == 3) cq.template run<g3 + 6>(lane);
          if constexpr (g3 == 6) mm<false, false, false, true>(acc[4], cq.p[0], mf0);
          if constexpr (g3 == 7) mm<false, false, false, true>(acc[5], cq.p[0], mf1);
          if constexpr (g3 == 92) { csv[3] = l == 0 ? cw.a : csv[3]; cw.a = csv[3]; }
          if constexpr (g3 >= 84 && g3 < 94) cw.template run<g3 - 84>(lane);
          if constexpr (g3 >= 32 && g3 < 40) {
            constexpr int n = (g3 - 32) % 4, t = (g3 - 32) / 4;
            const int kr = 16 * t + 8 * (n >> 1) + 4 * h + 2 * (n & 1);
            u32x4 sv = __builtin_bit_cast(u32x4, sel[t]); sv[n] = opq_v(r == kr ? 0x3f80u : (r == kr + 1 ? 0x3f800000u : 0u)); sel[t] = __builtin_bit_cast(bf16x8, sv);
          }
          if constexpr (g3 == 40) { sel[0] = opq_v(sel[0]); sel[1] = opq_v(sel[1]); }
          if constexpr (g3 == 42) *(LAS unsigned*)(sh + 8192 + 256 + lane * 4) = opq_v(0x3f803f80u);
          if constexpr (g3 == 99) {
            wait2<SQ.zw>(zq[0], zq[1]);
#pragma unroll
            for (int n = 0; n < 4; ++n) { e[n] = __builtin_bit_cast(f32x4, zq[0])[n]; a_add(e[n], __builtin_bit_cast(f32x4, zq[1])[n]); }
          }
          if constexpr (g3 == 101) {
            auto a = __builtin_amdgcn_permlane16_swap(__float_as_uint(e[0]), __float_as_uint(e[1]), false, false);
            auto b = __builtin_amdgcn_permlane16_swap(__float_as_uint(e[2]), __float_as_uint(e[3]), false, false);
            e[0] = opq_v(__uint_as_float(a[0]) + __uint_as_float(a[1])); e[1] = opq_v(__uint_as_float(b[0]) + __uint_as_float(b[1]));
          }
          if constexpr (g3 == 103) { auto a = __builtin_amdgcn_permlane32_swap(__float_as_uint(e[0]), __float_as_uint(e[1]), false, false); e[0] = opq_v(__uint_as_float(a[0]) + __uint_as_float(a[1])); }
          if constexpr (g3 == 105) e[1] = lane_get(e[0], lane ^ 8);
          if constexpr (g3 == 111)
            *(LAS short*)(sh + 8192 + w * 64 + (lane & 1) * 32 + ((lane >> 2) & 1) * 16 + ((lane >> 1) & 1) * 8 + (lane >> 4) * 2) = (short)cvtpk(e[0] + e[1], 0.f);
          if constexpr (g3 >= 116 && g3 < 124 && g3 % 2 == 1) rd_wf(IC((g3 - 116) / 2));
        }
      };
      const unsigned fo = (unsigned)((lane & 15) * 8 + (blk ? cb * 4 * FL_I + 10 * FL_K : tt * FL_I));
      auto step = [&](auto s, auto fl) {
        constexpr int ps = s / 64, L0 = a_ld0(fl);
        constexpr bool first = fl;
        if constexpr (s == 102) waitv<SQ.uw>();
        if constexpr (s == 118) waitv<SQ.vw>();
        if constexpr (a_bar(s) && s >= (first ? 6 : 38)) wait_bar<(ps ? cmin(S1.bw[s], SQ.bw[s]) : (first ? S1.bw[s] : SQ.bw[s]))>();
        if constexpr (s == L0 + 14 || (first && (s == L0 + 18 || s == L0 + 22))) waitv<(first ? S1.sw[(s - L0 - 14) / 4] : SQ.sw[0])>();
        if constexpr (first && s == 6) { set_flag(facto, tag); if (RARE(w == 0)) set_flag(fwl - FL_K + rb * 8, tag); }
        if constexpr (first && s == 32) { waitx<false, S1.aw>(fv); if (RARE(!__all(fv == tag))) poll<32>(flags, fo, tag); }
        if constexpr (s == 118) { if (RARE(l == 0 && w == 0)) set_flag(fwl + rb * 8, tag); }
        if constexpr (s == 128) { waitx<false, SQ.fw>(fv); if (RARE(!__all(fv == tag))) poll<16>(fwl, lane * 8, tag); }
        if constexpr (s == 142) set_flag(fkvp + rb * 8, tag);
        if constexpr (s == QC) { waitx<false, SQ.pw>(fv); if (RARE(!__all(fv == tag))) poll<16>(fkvp, lane * 8, tag); }
        if constexpr (s == 170) waitv<SQ.cw>();
        if constexpr (s < 64 && !first) waitx<(s < NA), SQ.xw[s % 64]>(x[s % 64]);
        if constexpr (first && s % 3 == 0) waitv<S1.xw[s % 64]>();
        SBAR();
        mm<ps == 1, (s % 64 < NA), s % 64 == 0>(acc[2 * ps], f0[s % 3], x[s % 64]);
        SBAR();
        if constexpr (first && f_rd(2 * s) >= 0) x_out(IC(f_rd(2 * s)));
        if constexpr (!first) { rd01(IC(s + 2), IC(1), fl); SBAR(); }
        rd01(IC(s + 2), IC(0), fl);
        if constexpr (first && f_in(2 * s) >= 0) x_in(IC(f_in(2 * s)));
        if constexpr (s + XL < 64 && !first) issue_x(IC(s + XL));
        if constexpr (first && s == 22) { fv = tag; rd_flag<32>(fv, fo, flags); }
        if constexpr (s == 127) ld8_1(fv, (lane & 15) * 8, fwl);
        if constexpr (s == QP) { fv = tag; rd_flag<16>(fv, lane * 8, fkvp); }
        if constexpr (s >= QC) ldp(IC(s - QC));
        aux(s, IC(0), fl);
        SBAR();
        mm<ps == 1, (s % 64 < NA), s % 64 == 0>(acc[2 * ps + 1], f1[s % 3], x[s % 64]);
        SBAR();
        if constexpr (first && f_rd(2 * s + 1) >= 0) x_out(IC(f_rd(2 * s + 1)));
        if constexpr (first && s % 3 == 2 && s / 3 + 1 < 16) x_fin(IC(s / 3 + 1), IC(4));
        if constexpr (first) rd01(IC(s + 2), IC(1), fl);
        if constexpr (a_piece(s, first) >= 0) { if constexpr (first) pc_m(IC(a_piece(s, first))); else pc_a(IC(a_piece(s, first))); }
        if constexpr (s >= L0 && s < L0 + 4) ldx1<false, (s - L0) * 1024>(st.p[s - L0], c.xv, stt);
        if constexpr (first && s >= L0 + 4 && s < L0 + 8) ld_cs(cv.p[s - L0 - 4], 2, IC(s - L0 - 4));
        if constexpr (first && s >= L0 + 8 && s < L0 + 12) ld_cs(cq.p[s - L0 - 8], 0, IC(s - L0 - 8));
        if constexpr (s == 50) dma<0>(lds0 + 9 * SLOT + w * 1024, c.xv, wph);
        if constexpr (s >= 154 && s < 158) ld_cs(cw.p[s - 154], 4, IC(s - 154));
        if constexpr (a_park(s) >= 0) ld_park(IC(a_park(s)));
        if constexpr (first && f_in(2 * s + 1) >= 0) x_in(IC(f_in(2 * s + 1)));
        if constexpr (first && f_raw(s) >= 0) k_in(IC(f_raw(s)));
        aux(s, IC(1), fl);
        if constexpr (s == 87) { if (RARE(l == 0)) sfor<0, 4>([&](auto i) AI { raw_in(IC(1), 7, i); }); }
        if constexpr (s == 103) { if (RARE(l == 0)) raw_out(IC(1), 7); }
        if constexpr (s == 133) { if (RARE(l == 0)) sfor<0, 4>([&](auto i) AI { raw_in(IC(2), 8, i); }); }
        SBAR();
      };
      {
        STAMP(30);
        bf16x8 kt[2];
        bf16x8 xt[4];
        xs = (unsigned)((lane >> 3) * 2048 + ((lane & 7) ^ (((lane >> 3) & 3) << 1)) * 16);
        sfor<0, 4>([&](auto i) { dma<i * 1040>(lds0 + w * 20480 + 3 * 4160, xs + i * (16384 - 1040), xw + cb * 128); });
        sfor<0, 2>([&](auto i) { ldx0<0>(kt[i], (unsigned)((lane >> 3) * 6144 + (lane & 7) * 16), wqkv + 2048 + cb * 128 + (size_t)(tt * 16 + i * 8) * 6144); });
        u64 ep; ld8_1(ep, 0u, ws + WS_EPOCH);
        sfor<0, 4>([&](auto i) AI { raw_in(IC(0), 9, i); });
        sfor<0, 4>([&](auto i) { xo[i] = opq_v(lds0 + w * 20480 + r * 128 + (((r & 3) ^ i) << 5) + h * 8 + (r >> 3) * 16); });
        ra = opq_v(lds0 + 5 * SLOT + (4 * h + q4) * 128 + (((q4 >> 1) << 2) + 2 * blk + (p4 >> 1)) * 16 + 8 * (p4 & 1));
        { const unsigned o = (((lane >> 2) ^ (lane >> 4)) & 1) ? 0u : 0x3f803f80u; sel[0] = opq_v(__builtin_bit_cast(bf16x8, u32x4{o, o, o, o})); ks0 = opq_v(ks0); ks1 = opq_v(ks1); }
        sfor<0, 4>(x_in); sfor<0, 4>(k_in);
        asm volatile("" : "+s"(ws), "+s"(wk), "+s"(wphi));
        flags = ws + WS_FLAG;
        col = ws + WS_COL + (size_t)cb * 2 * MB; colw = col + w * 4096;
        act = ws + (size_t)rb * 2 * MB + w * 65536;
        acto = act + cb * 4096;
        fact = flags + tt * FL_I;
        facto = fact + cb * 8;
        stt = ws + WS_ST + tt * 4096;
        stto = stt + cb * 256;
        kvp = col + C_KVP + w * 32768; zp = col + C_ZP + w * 2048;
        fkvp = flags + (cb * 4 + w) * FL_I + 8 * FL_K; fwl = flags + cb * 4 * FL_I + 11 * FL_K;
        kvf = ws + WS_KVF; fkv = flags + 12 * FL_K;
        xu_p = ws + (size_t)rb * 2 * MB + 2 * STG + (cb >> 2) * 65536 + w * 16384 + 4096;
        su_p = ws + WS_ST + 256 * KB + ut * 4096;
        wkv = ((cb & 2) ? wv : wk) + w * 256 * 64; wph = wphi + cb * 4096 + w * 1024;
        { const unsigned m = (unsigned)(ty >> 1) - 1u; kvb = ut * 4096 + (((ty * 1024 + (w & 1) * 8) & m) | ((2048 + (w >> 1) * 1024 + (ty & 1) * 256 + (w & 1) * 512) & ~m)); }
        asm volatile("" : "+s"(kvb), "+s"(act), "+s"(acto), "+s"(kvp), "+s"(zp), "+s"(fkvp), "+s"(kvf), "+s"(fkv), "+s"(xu_p), "+s"(su_p), "+s"(wkv), "+s"(wph));
        asm volatile("s_waitcnt vmcnt(12)" : "+v"(cf), "+v"(ep), "+v"(kt[0]), "+v"(kt[1])::"memory");
        sfor<0, 4>([&](auto i) { ds_read2<false, 3 * 520>(xt[i], xo[i]); });
        sfor<4, 8>(x_in); sfor<4, 8>(k_in); sfor<8, 12>(x_in);
        asm volatile("s_waitcnt lgkmcnt(0)" : "+v"(xt[0]), "+v"(xt[1]), "+v"(xt[2]), "+v"(xt[3])::"memory");
        sfor<0, 4>([&](auto i) { xr[i] = __builtin_bit_cast(u32x4, xt[i]); });
        store_stats(r, xr, stto);
        park4(xr);
        { u64 z = ((u64)__builtin_amdgcn_readfirstlane((unsigned)(ep >> 32)) << 32 | __builtin_amdgcn_readfirstlane((unsigned)ep)) + 0x9E3779B97F4A7C15ull;
          z = (z ^ (z >> 30)) * 0xBF58476D1CE4E5B9ull; z = (z ^ (z >> 27)) * 0x94D049BB133111EBull; tag = z ^ (z >> 31); asm volatile("" : "+s"(tag)); }
        wait_bar<13>();
        sfor<0, 4>(x_out);
        raw_out(IC(0), 9);
        sfor<12, 16>(x_in); sfor<8, 12>(k_in);
        x_fin(IC(0), IC(0));
        sfor<16, 20>(x_in);
        STAMP(31); STAMP(0); STAMP(1);
        sfor<0, 2>([&](auto s) { rd01(s, IC(0), std::true_type{}); rd01(s, IC(1), std::true_type{}); });
        sfor<0, 64>([&](auto s) { step(s, std::true_type{}); });
        {
          const int gw = blockIdx.x * 4 + w; const unsigned z = 0;
          const unsigned vo = (unsigned)(lane < 5 ? WS_KVF - WS_ST + (gw * 5 + lane) * 128 : WS_FLAG - WS_ST + gw * 128);
          asm volatile("s_mov_b64 exec, 63\n\tglobal_atomic_or %0, %1, %2 sc1\n\ts_mov_b64 exec, -1" ::"v"(vo), "v"(z), "s"(ws + WS_ST) : "memory");
          const unsigned dump = lds0 + 9 * SLOT + 4096 + w * 2048;
          dma<0>(dump, c.xv, woA + gw * 2048); dma<1024>(dump, c.xv, woA + gw * 2048);
          dma<0>(dump, c.xv, wq + gw * 2048); dma<1024>(dump, c.xv, wq + gw * 2048);
          dma<0>(dump, c.xv, woB + gw * 2048); dma<1024>(dump, c.xv, woB + gw * 2048);
          dma<0>(dump, c.xv, ((gw & 64) ? wk : wv) + (gw & 63) * 1024);
        }
        asm volatile("s_nop 7" : "+v"(ks0), "+v"(ks1));
        csv[0] = h ? ks1[0] : ks0[0];
        const bf16x8 kf = cs_frag(csv[0]);
        mm<false, false, false, true>(acc[0], kf, mf0); mm<false, false, false, true>(acc[1], kf, mf1);
      }
  asm volatile("" : "+s"(w), "+s"(tag), "+s"(act), "+s"(acto), "+s"(fact), "+s"(facto), "+s"(stt), "+s"(stto), "+s"(col), "+s"(colw), "+s"(ldsw));
  asm volatile("" : "+s"(kvp), "+s"(zp), "+s"(fkvp), "+s"(fwl), "+s"(rb), "+s"(kvf), "+s"(fkv), "+s"(xu_p), "+s"(su_p), "+s"(wkv), "+s"(csm), "+s"(l));
  for (;;) {
    {
      REFRESH();
      asm volatile("" : "+s"(kvp), "+s"(zp), "+s"(fkvp), "+s"(fwl), "+s"(rb));
      STAMP(2);
      sfor<64, 128>([&](auto s) { step(s, std::false_type{}); });
      STAMP(19);
      mdone(acc[2], acc[3]);
      v_out(IC(0), IC(0)); v_out(IC(0), IC(1)); v_out(IC(1), IC(0)); v_out(IC(1), IC(1));
      lds_bar();
      {
        const int hd = w >> 1, fh = w & 1;
        f32x16 kv, z;
        const int ch = fh * 4 + 2 * blk + (p4 >> 1), r0 = 4 * h + q4;
        LAS const char* pa = pk_img + hd * 16384 + r0 * 128 + ((ch ^ r0) << 4) + 8 * (p4 & 1);
        LAS const char* pb = v_img + hd * 8192 + lane * 16;
        bf16x8 a[8], b[8], d[8];
#pragma unroll
        for (int s = 0; s < 8; ++s) { a[s] = cat8(trread(pa + s * 2048), trread(pa + s * 2048 + 1024)); b[s] = *(LAS const bf16x8*)(pb + s * 1024); }
#pragma unroll
        for (int s = 0; s < 8; ++s) d[s] = *(LAS const bf16x8*)(sh + 8704 + s * 32 + h * 16);
        SBAR();
        sfor<0, 8>([&](auto s) { mm<false, false, s == 0>(kv, a[s], b[s]); });
        sfor<0, 8>([&](auto s) {
          mm<false, false, s == 0>(z, d[s], a[s]);
          if constexpr (s == 1) { mdone(kv); st4_1<0>(__builtin_bit_cast(u32x4, pack8<0>(kv)), c.xv, kvp + rb * 2048); }
          if constexpr (s == 2) { st4_1<1024>(__builtin_bit_cast(u32x4, pack8<1>(kv)), c.xv, kvp + rb * 2048); }
          SBAR();
        });
        mdone(z);
        st1_1(z[0], (unsigned)(r * 4), zp + rb * 128);
      }
      STAMP(3);
      sfor<128, 192>([&](auto s) { step(s, std::false_type{}); });
      mdone(acc[4], acc[5]);
      STAMP(4);
      {
        bf16x8 pq[2][4], ka[8], za[8];
        f32x16 ep, em, nm[4], dn[4];
        float ai[2], bi[2];
        char* dst = acto + STG;
        sfor<0, 4>([&](auto k) { mm<false, false, k % 2 == 0, true>(p[k / 2], wf[k], pack8<k % 2>(acc[4 + k / 2])); });
        mdone(p[0], p[1]);
        bar();
        if (RARE(l == 0)) raw_out(IC(2), 8);
        sfor<0, 2>([&](auto hd) {
          hedge_exp(p[hd], sce, ep, em, [&](auto e) {
            constexpr int i = hd * 16 + e;
            sum_mm(IC(i));
            if constexpr (i % 2 == 0 && 192 - QC + i / 2 < 34) ldp(IC(192 - QC + i / 2));
            if constexpr (i % 2 == 0 && i >= 16) pc_a(IC(108 + (i - 16) / 2));
          });
          if constexpr (hd == 1) {
            mdone(kvs);
            *(LAS bf16x8*)(sh + w * 2048 + lane * 16) = pack8<0>(kvs); *(LAS bf16x8*)(sh + w * 2048 + 1024 + lane * 16) = pack8<1>(kvs);
            SBAR();
          }
          pq[hd][0] = pack8<0>(ep); pq[hd][1] = pack8<1>(ep); pq[hd][2] = pack8<0>(em); pq[hd][3] = pack8<1>(em);
          pin4(pq[hd]);
        });
        STAMP(5);
        lds_bar();
        LAS const char* zb = sh + 8192 + (r & 1) * 256 + h * 32;
#pragma unroll
        for (int k = 0; k < 8; ++k) za[k] = *(LAS const bf16x8*)(zb + (k >> 1) * 64 + (k & 1) * 16);
#pragma unroll
        for (int k = 0; k < 8; ++k) ka[k] = *(LAS const bf16x8*)(sh + k * 1024 + lane * 16);
        SBAR();
        auto coef = [&](auto hd) {
          const float a = __builtin_amdgcn_rcpf(dn[2 * hd][1]), b = __builtin_amdgcn_rcpf(dn[2 * hd + 1][1]);
          const float inv = __builtin_amdgcn_rcpf(dn[2 * hd][0] * a + dn[2 * hd + 1][0] * b + 1e-6f);
          ai[hd] = opq_v(a * inv); bi[hd] = opq_v(b * inv);
        };
        auto fin = [&](auto hd) {
          f32x16 o;
#pragma unroll
          for (int i = 0; i < 16; ++i) o[i] = nm[2 * hd][i] * ai[hd] + nm[2 * hd + 1][i] * bi[hd];
          st4_1<hd * 2048>(__builtin_bit_cast(u32x4, pack8<0>(o)), c.xv, dst);
          st4_1<hd * 2048 + 1024>(__builtin_bit_cast(u32x4, pack8<1>(o)), c.xv, dst);
        };
        sfor<0, 8>([&](auto k) { mm<false, false, k % 2 == 0>(dn[k / 2], za[k], pq[k / 4][k % 4]); if constexpr (k % 2) pc_a(IC(116 + k / 2)); });
        sfor<0, 4>([&](auto k) { mm<false, false, k % 2 == 0>(nm[k / 2], ka[k], pq[0][k]); });
        mdone(dn[0], dn[1], dn[2], dn[3]);
        sfor<4, 8>([&](auto k) { mm<false, false, k % 2 == 0>(nm[k / 2], ka[k], pq[1][k - 4]); if constexpr (k == 4) coef(IC(0)); if constexpr (k == 6) coef(IC(1)); });
        STAMP(6);
        pin2(nm[0], nm[1]);
        fin(IC(0));
        pin2(nm[2], nm[3]);
        fin(IC(1));
        SBAR();
        sfor<120, 124>(pc_a);
        res_to_acc(xr, ra0, ra1);
        waitv<4>();
        set_flag(facto + FL_K, tag);
        sfor<124, 128>(pc_a); sfor<8, 16>(ld_park);
      }
    }
    STAMP(7);
    REFRESH();
    LAS char* reg = smem + 8 * SLOT;
    bf16x8 fu[16], cu;
    {
      f32x16 &a0 = ra0, &a1 = ra1;
      poll<16>(fact + FL_K, lane * 8, tag);
      sfor<0, 16>([&](auto i) { pin_a(park[i]); });
      STAMP(8);
      bf16x8 x[64];
      const char* xb = act + STG;
      sfor<0, XL>([&](auto s) { ld_x<s>(x, c, xb); });
      bar();
      asm volatile("" : "+s"(wkv));
      const unsigned kvv = (unsigned)((lane >> 1) * 64 + (cb & 1) * 32 + (lane & 1) * 16);
      const unsigned wa = ldsw + c.xv, wb = wa + 65536;
      auto unpark = [&](auto i) {
        constexpr int off = a_slot(26 + i / 4) * SLOT + (i % 4) * 1024;
        ds_write_a<(off & 65535)>(off < 65536 ? wa : wb, park[i]);
      };
      gemm64<true, false, HookP3>(c, x, xb, a0, a1, [&](auto s) {
        if constexpr (s == 33) bar();
        if constexpr (s >= 36 && s < 44) unpark(IC(s - 36));
        if constexpr (s == 49) bar();
        if constexpr (s >= 50 && s < 58) unpark(IC(s - 42));
        if constexpr (s >= 52 && s < 60) dma<0>(lds0 + 8 * SLOT + w * 8192 + (s - 52) * 1024, kvv, wkv + (s - 52) * 32 * 64);
        if constexpr (s >= 60) pc_w(4, a_slot(24), 0, IC(s - 60));
      }, [](auto) {});
      STAMP(9);
      store_x(c, r, a0, a1, xr, acto + 2 * STG, stto + 256 * 1024);
      asm volatile("s_waitcnt vmcnt(5) lgkmcnt(0)\n\ts_barrier" ::: "memory");
      { LAS const char* p = reg + w * 8192 + (4 * h + q4) * 32 + p4 * 8;
#pragma unroll
        for (int i = 0; i < 16; ++i) fu[i] = cat8(trread(p + i * 512), trread(p + i * 512 + 256));
        SBAR();
        float t0 = 0.f, t1 = 0.f, t2 = 0.f, t3 = 0.f;
#pragma unroll
        for (int i = 0; i < 16; ++i) {
          const u32x4 f = __builtin_bit_cast(u32x4, fu[i]);
          t0 = dot2(f[0], 0x3f803f80u, t0); t1 = dot2(f[1], 0x3f803f80u, t1); t2 = dot2(f[2], 0x3f803f80u, t2); t3 = dot2(f[3], 0x3f803f80u, t3);
        }
        cu = opq_v(cs_frag(swap_add((t0 + t1) + (t2 + t3)))); }
      waitv<0>();
      set_flag(facto + 2 * FL_K, tag);
      sfor<0, 4>([&](auto i) { pc_w(4, a_slot(25), 1, i); });
    }
    STAMP(10);
    REFRESH();
    f32x16 a0, a1;
    asm volatile("" : "+s"(kvf), "+s"(fkv), "+s"(xu_p), "+s"(su_p));
    {
      poll<16>(fact + 2 * FL_K, lane * 8, tag);
      bar();
      STAMP(11);
      const char* xb = act + 2 * STG; const char* stp = stt + 256 * 1024;
      bf16x8 x[64], xu[16], mf0, mf1;
      Red<false> su, sp;
      sfor<0, 4>([&](auto i) { ldx1<false, i * 1024>(su.p[i], c.xv, su_p); });
      sfor<0, 16>([&](auto i) { ldx1<true, (i % 8) * 1024 - 4096>(xu[i], c.xv, xu_p + (i / 8) * 8192); });
      f32x16 au, av;
      sfor<0, 16>([&](auto i) {
        waitx<true, 15 - i + (i < XL ? i : XL)>(xu[i]);
        SBAR();
        mm<false, true, i == 0>(au, fu[i], xu[i]);
        if constexpr (i < XL) ld_x<i>(x, c, xb);
        mm<true, true, i == 0>(av, fu[i], xu[i]);
        if constexpr (i < 11) su.template run<i>(lane);
        if constexpr (i == 12) mf0 = opq_v(mean_frag(su.a, h == 0));
        if constexpr (i == 14) { mm<false, false, false>(au, cu, mf0); mm<true, false, false>(av, cu, mf0); }
        SBAR();
      });
      STAMP(12);
      u64 fa;
      f32x4 t[4], ru;
      unsigned vo;
      gemm64<true, true, HookP4>(c, x, xb, a0, a1, [&](auto s) {
        if constexpr (s == 1) {
          pin2(au, av);
#pragma unroll
          for (int gq = 0; gq < 6; ++gq) {
            const f32x16& a = gq < 4 ? av : au;
            *(LAS f32x4*)(reg + w * 8192 + gq * 1024 + lane * 16) = f32x4{a[4 * (gq & 3)], a[4 * (gq & 3) + 1], a[4 * (gq & 3) + 2], a[4 * (gq & 3) + 3]};
          }
        }
        if constexpr (s == 3) {
          lds_bar();
#pragma unroll
          for (int j = 0; j < 4; ++j) t[j] = *(LAS const f32x4*)(reg + j * 8192 + (ty < 2 ? 4 + (w & 1) : w) * 1024 + lane * 16);
        }
        if constexpr (s == 6) st2_1(__builtin_bit_cast(f32x2, u32x2{cvtpk(t[0][0], t[0][1]), cvtpk(t[0][2], t[0][3])}), vo, kvf);
        if constexpr (s >= 8 && s < 12) ldx1<false, (s - 8) * 1024>(sp.p[s - 8], c.xv, stp);
        if constexpr (s == 7 + XL) { bar(); if (RARE(w == 0)) set_flag(fkv + rb * 4 * FL_I + cb * 8, tag); }
        if constexpr (s >= 14 && s < 30 && s % 2 == 0) pc_w(4, a_slot(30 + (s - 14) / 8), 6 + (s - 14) / 8, IC(((s - 14) / 2) % 4));
        if constexpr (s == 45) bar();
        if constexpr (s == 52) ld8_1(fa, (lane & 15) * 8 + (w * 4 + (lane >> 4)) * 4 * FL_I, fkv);
      }, [&](auto g) {
        if constexpr (g == 0) {
#pragma unroll
          for (int e = 0; e < 4; ++e) ru[e] = lane_get(su.b, ty < 2 ? r : 8 * w + 4 * h + e);
        }
        if constexpr (g == 4) {
          const unsigned m = (unsigned)(ty >> 1) - 1u;
          vo = opq_v(kvb + (((unsigned)lane * 16 & m) | ((unsigned)((r & 15) * 16 + h * 8) & ~m)));
        }
        if constexpr (g >= 9 && g < 13) {
#pragma unroll
          for (int e = 0; e < 4; ++e) { float v = t[0][e]; if constexpr (g < 12) a_add(v, t[g - 8][e]); else a_mul(v, ru[e]); t[0][e] = v; }
        }
        if constexpr (g >= 56 && g < 67) sp.template run<g - 56>(lane);
        if constexpr (g == 68) mf0 = opq_v(mean_frag(sp.a, h == 0));
        if constexpr (g == 69) mf1 = opq_v(mean_frag(sp.a, h == 1));
        if constexpr (g == 72) mm<false, false, false>(a0, cw.p[0], mf0);
        if constexpr (g == 73) mm<false, false, false>(a1, cw.p[0], mf1);
      });
      rstd = sp.b;
      waitx<false, 0>(fa);
      if (RARE(!__all(fa == tag))) {
#pragma unroll 1
      for (int spin = 0; spin < (1 << 15) && !__all(fa == tag); ++spin) { __builtin_amdgcn_s_sleep(8); ld8_1(fa, (lane & 15) * 8 + (w * 4 + (lane >> 4)) * 4 * FL_I, fkv); waitx<false, 0>(fa); }
      }
      bar();
      STAMP(13);
    }
    STAMP(14);
    {
      u32x16 qq, misc;
      misc[14] = misc[15] = __float_as_uint(rstd * QSCALE);
      int attempt = 0;
#pragma unroll 1
      for (;;) {
        f32x16 oa, ob, t0, t1, t2, t3;
        u32x16 kvr; u32x4 offs;
        offs[0] = c.xv;
        const char* ks = kvf + 2048 + w * 2048; const char* wsr = wt(5, 0);
        unsigned wm0 = ldsw, cnt = 8;
        asm volatile(
            "global_load_dwordx4 v[208:211], v240, %[kv] sc1\n\tglobal_load_dwordx4 v[212:215], v240, %[kv] offset:1024 sc1\n\t"
            "s_mov_b32 m0, s44\n\tv_add_u32 v241, 0x2000, v240\n\t"
            "global_load_lds_dwordx4 v240, s[40:41] sc1\n\tglobal_load_lds_dwordx4 v240, s[40:41] offset:1024 sc1\n\t"
            "s_add_u32 m0, s44, 0x2000\n\tv_add_u32 v242, 0x4000, v240\n\t"
            "global_load_lds_dwordx4 v241, s[40:41] sc1\n\tglobal_load_lds_dwordx4 v241, s[40:41] offset:1024 sc1\n\t"
            "s_add_u32 m0, s44, 0x4000\n\tv_add_u32 v243, 0x6000, v240\n\t"
            "global_load_lds_dwordx4 v242, s[40:41] sc1\n\tglobal_load_lds_dwordx4 v242, s[40:41] offset:1024 sc1\n\t"
            "s_add_u32 s40, s40, 0x6000\n\ts_addc_u32 s41, s41, 0\n\t"
            "s_cmp_lg_u32 %[att], 0\n\ts_cbranch_scc1 2f\n\t"
            "v_pk_mul_f32 v[128:129], v[128:129], v[238:239]\n\t"
            "v_pk_mul_f32 v[130:131], v[130:131], v[238:239]\n\t"
            "v_pk_mul_f32 v[132:133], v[132:133], v[238:239]\n\t"
            "v_pk_mul_f32 v[134:135], v[134:135], v[238:239]\n\t"
            "v_pk_mul_f32 v[136:137], v[136:137], v[238:239]\n\t"
            "v_pk_mul_f32 v[138:139], v[138:139], v[238:239]\n\t"
            "v_pk_mul_f32 v[140:141], v[140:141], v[238:239]\n\t"
            "v_pk_mul_f32 v[142:143], v[142:143], v[238:239]\n\t"
            "v_pk_mul_f32 v[144:145], v[144:145], v[238:239]\n\t"
            "v_pk_mul_f32 v[146:147], v[146:147], v[238:239]\n\t"
            "v_pk_mul_f32 v[148:149], v[148:149], v[238:239]\n\t"
            "v_pk_mul_f32 v[150:151], v[150:151], v[238:239]\n\t"
            "v_pk_mul_f32 v[152:153], v[152:153], v[238:239]\n\t"
            "v_pk_mul_f32 v[154:155], v[154:155], v[238:239]\n\t"
            "v_pk_mul_f32 v[156:157], v[156:157], v[238:239]\n\t"
            "v_pk_mul_f32 v[158:159], v[158:159], v[238:239]\n\t"
            "v_cvt_pk_bf16_f32 v192, v128, v129\n\t"
            "v_cvt_pk_bf16_f32 v193, v130, v131\n\t"
            "v_cvt_pk_bf16_f32 v194, v132, v133\n\t"
            "v_cvt_pk_bf16_f32 v195, v134, v135\n\t"
            "v_cvt_pk_bf16_f32 v196, v136, v137\n\t"
            "v_cvt_pk_bf16_f32 v197, v138, v139\n\t"
            "v_cvt_pk_bf16_f32 v198, v140, v141\n\t"
            "v_cvt_pk_bf16_f32 v199, v142, v143\n\t"
            "v_cvt_pk_bf16_f32 v200, v144, v145\n\t"
            "v_cvt_pk_bf16_f32 v201, v146, v147\n\t"
            "v_cvt_pk_bf16_f32 v202, v148, v149\n\t"
            "v_cvt_pk_bf16_f32 v203, v150, v151\n\t"
            "v_cvt_pk_bf16_f32 v204, v152, v153\n\t"
            "v_cvt_pk_bf16_f32 v205, v154, v155\n\t"
            "v_cvt_pk_bf16_f32 v206, v156, v157\n\t"
            "v_cvt_pk_bf16_f32 v207, v158, v159\n\t"
            "2:\n\t"
            "v_mov_b64 v[64:65], 0\n\t"
            "v_mov_b64 v[66:67], 0\n\t"
            "v_mov_b64 v[68:69], 0\n\t"
            "v_mov_b64 v[70:71], 0\n\t"
            "v_mov_b64 v[72:73], 0\n\t"
            "v_mov_b64 v[74:75], 0\n\t"
            "v_mov_b64 v[76:77], 0\n\t"
            "v_mov_b64 v[78:79], 0\n\t"
            "v_mov_b64 v[80:81], 0\n\t"
            "v_mov_b64 v[82:83], 0\n\t"
            "v_mov_b64 v[84:85], 0\n\t"
            "v_mov_b64 v[86:87], 0\n\t"
            "v_mov_b64 v[88:89], 0\n\t"
            "v_mov_b64 v[90:91], 0\n\t"
            "v_mov_b64 v[92:93], 0\n\t"
            "v_mov_b64 v[94:95], 0\n\t"
            "v_mov_b64 v[176:177], 0\n\t"
            "v_mov_b64 v[178:179], 0\n\t"
            "v_mov_b64 v[180:181], 0\n\t"
            "v_mov_b64 v[182:183], 0\n\t"
            "v_mov_b64 v[184:185], 0\n\t"
            "v_mov_b64 v[186:187], 0\n\t"
            "v_mov_b64 v[188:189], 0\n\t"
            "v_mov_b64 v[190:191], 0\n\t"
            "v_mov_b64 v[216:217], 0\n\t"
            "v_mov_b64 v[218:219], 0\n\t"
            "v_mov_b64 v[220:221], 0\n\t"
            "v_mov_b64 v[222:223], 0\n\t"
            "v_mov_b64 v[228:229], 0\n\t"
            "v_mov_b64 v[230:231], 0\n\t"
            "v_mov_b64 v[232:233], 0\n\t"
            "v_mov_b64 v[234:235], 0\n\t"
            "v_bfe_u32 v226, v240, 6, 1\n\tv_mov_b32 v236, 0xdddddddd\n\tv_mul_u32_u24 v226, 0x3f80, v226\n\tv_add_u32 v237, %[rg], v240\n\t"
            "v_lshl_or_b32 v226, v226, 16, v226\n\tv_mov_b32 v227, v226\n\tv_xor_b32 v224, 0x3f803f80, v226\n\tv_mov_b32 v225, v224\n\t"
            "s_waitcnt vmcnt(6)\n\ts_cbranch_scc1 3f\n\t"
            "v_mfma_f32_32x32x16_bf16 v[128:143], v[208:211], v[192:195], 0\n\tv_mfma_f32_32x32x16_bf16 v[160:175], v[208:211], v[200:203], 0\n\t"
            "v_mfma_f32_32x32x16_bf16 v[128:143], v[212:215], v[196:199], v[128:143]\n\tv_mfma_f32_32x32x16_bf16 v[160:175], v[212:215], v[204:207], v[160:175]\n\t"
            "s_nop 11\n\t"
            "v_max3_f32 v238, v128, v129, v130\n\tv_max3_f32 v239, v160, v161, v162\n\t"
            "v_max3_f32 v238, v238, v131, v132\n\tv_max3_f32 v239, v239, v163, v164\n\t"
            "v_max3_f32 v238, v238, v133, v134\n\tv_max3_f32 v239, v239, v165, v166\n\t"
            "v_max3_f32 v238, v238, v135, v136\n\tv_max3_f32 v239, v239, v167, v168\n\t"
            "v_max3_f32 v238, v238, v137, v138\n\tv_max3_f32 v239, v239, v169, v170\n\t"
            "v_max3_f32 v238, v238, v139, v140\n\tv_max3_f32 v239, v239, v171, v172\n\t"
            "v_max3_f32 v238, v238, v141, v142\n\tv_max3_f32 v239, v239, v173, v174\n\t"
            "v_max3_f32 v238, v238, v143, v143\n\tv_max3_f32 v239, v239, v175, v175\n\t"
            "v_mov_b32 v96, v238\n\tv_mov_b32 v97, v239\n\ts_nop 1\n\tv_permlane32_swap_b32 v238, v96\n\tv_permlane32_swap_b32 v239, v97\n\t"
            "v_max_f32 v238, v238, v96\n\tv_max_f32 v239, v239, v97\n\t"
            "3:\n\t"
            "v_xor_b32 v96, 0x80000000, v238\n\tv_xor_b32 v112, 0x80000000, v239\n\tv_mov_b32 v97, v96\n\tv_mov_b32 v113, v112\n\t"
            "v_mov_b64 v[98:99], v[96:97]\n\tv_mov_b64 v[114:115], v[112:113]\n\t"
            "v_mov_b64 v[100:101], v[96:97]\n\tv_mov_b64 v[116:117], v[112:113]\n\t"
            "v_mov_b64 v[102:103], v[96:97]\n\tv_mov_b64 v[118:119], v[112:113]\n\t"
            "v_mov_b64 v[104:105], v[96:97]\n\tv_mov_b64 v[120:121], v[112:113]\n\t"
            "v_mov_b64 v[106:107], v[96:97]\n\tv_mov_b64 v[122:123], v[112:113]\n\t"
            "v_mov_b64 v[108:109], v[96:97]\n\tv_mov_b64 v[124:125], v[112:113]\n\t"
            "v_mov_b64 v[110:111], v[96:97]\n\tv_mov_b64 v[126:127], v[112:113]\n\t"
            "s_nop 1\n\t" S0_(128, 192, 96) S1_(128, 192)
            "s_waitcnt vmcnt(4)\n\ts_barrier\n\ts_nop 8\n\t"
            "1:\n\t"
            STEP_(0, 24576, 240, 0, "s_sub_u32 %[cnt], %[cnt], 1\n\t", "") STEP_(8192, 0, 241, 1024, "", "") STEP_(16384, 8192, 242, 2048, "", "")
            STEP_(24576, 16384, 243, 3072, "s_add_u32 s40, s40, 0x8000\n\ts_addc_u32 s41, s41, 0\n\t", "s_add_u32 s42, s42, 0x4000\n\ts_addc_u32 s43, s43, 0\n\t")
            "s_add_u32 s45, s45, 0x4000\n\ts_cmp_lg_u32 %[cnt], 0\n\ts_cbranch_scc1 1b\n\t"
            L0_(232, 176) L1_(232, 176) PV0_(80, 176) PV1_(80, 176)
            "s_nop 11\n\t"
            : "=&{v[64:79]}"(oa), "=&{v[80:95]}"(ob), "=&{v[96:111]}"(t0), "=&{v[112:127]}"(t1), "+{v[128:143]}"(a0), "+{v[144:159]}"(a1), "=&{v[160:175]}"(t2),
              "=&{v[176:191]}"(t3), "+{v[192:207]}"(qq), "=&{v[208:223]}"(kvr), "+{v[224:239]}"(misc), "+{v[240:243]}"(offs),
              "+{s[40:41]}"(ks), "+{s[42:43]}"(wsr), "+{s45}"(wm0), [cnt] "+s"(cnt)
            : "{s44}"(lds0 + 8 * SLOT + w * 2048), [kv] "s"(kvf), [att] "s"(attempt), [rg] "s"(lds0 + 8 * SLOT)
            : "scc", "memory");
        const float la = __uint_as_float(misc[4]), lb = __uint_as_float(misc[8]);
        const float ia = __builtin_amdgcn_rcpf(la), ib = __builtin_amdgcn_rcpf(lb);
#pragma unroll
        for (int e = 0; e < 16; ++e) { oa[e] *= ia; ob[e] *= ib; }
        char* dst = acto + 3 * STG;
        st4_1<0>(__builtin_bit_cast(u32x4, pack8<0>(oa)), c.xv, dst); st4_1<1024>(__builtin_bit_cast(u32x4, pack8<1>(oa)), c.xv, dst);
        st4_1<2048>(__builtin_bit_cast(u32x4, pack8<0>(ob)), c.xv, dst); st4_1<3072>(__builtin_bit_cast(u32x4, pack8<1>(ob)), c.xv, dst);
        float chk = 0.f;
#pragma unroll
        for (int e = 0; e < 16; ++e) chk = __builtin_fmaf(oa[e], 0.f, __builtin_fmaf(ob[e], 0.f, chk));
        const bool bad = !(la < 1.2676506e30f) || !(lb < 1.2676506e30f) || !(chk == 0.f);
        wait_bar<4>();
        LAS volatile int* vote = (LAS volatile int*)(smem + 8 * SLOT);
        if (lane == 0) vote[w] = 0;
        if (attempt == 0 && bad) vote[w] = 1;
        lds_bar();
        const int redo = vote[0] | vote[1] | vote[2] | vote[3];
        if (!RARE(__builtin_amdgcn_readfirstlane(redo))) break;
        lds_bar();
        {
          const bf16x8 qa0 = __builtin_bit_cast(bf16x8, __builtin_shufflevector(qq, qq, 0, 1, 2, 3)), qa1 = __builtin_bit_cast(bf16x8, __builtin_shufflevector(qq, qq, 4, 5, 6, 7));
          const bf16x8 qb0 = __builtin_bit_cast(bf16x8, __builtin_shufflevector(qq, qq, 8, 9, 10, 11)), qb1 = __builtin_bit_cast(bf16x8, __builtin_shufflevector(qq, qq, 12, 13, 14, 15));
          float mA = -INFINITY, mB = -INFINITY;
          for (int t = 0; t < 64; ++t) {
            bf16x8 k0, k1;
            ldx1<false, 0>(k0, c.xv, kvf + t * 4096); ldx1<false, 1024>(k1, c.xv, kvf + t * 4096);
            wait2<0>(k0, k1);
            mm<false, false, true>(a0, k0, qa0); mm<false, false, false>(a0, k1, qa1); mm<false, false, true>(a1, k0, qb0); mm<false, false, false>(a1, k1, qb1);
            mdone(a0, a1);
            mA = fmaxf(mA, max16(a0)); mB = fmaxf(mB, max16(a1));
          }
          misc[14] = __float_as_uint(swap_max(mA)); misc[15] = __float_as_uint(swap_max(mB));
        }
        attempt = 1;
      }
      res_to_acc(xr, ra0, ra1);
      waitv<0>();
      set_flag(facto + 3 * FL_K, tag);
    }
    STAMP(15);
    REFRESH();
    {
      f32x16 &a0 = ra0, &a1 = ra1;
      poll<16>(fact + 3 * FL_K, lane * 8, tag);
      STAMP(16);
      bf16x8 x[64];
      const char* xb = act + 3 * STG;
      sfor<0, XL>([&](auto s) { ld_x<s>(x, c, xb); });
      bar();
      gemm64<false, false, HookP6>(c, x, xb, a0, a1, [&](auto s) {
        if constexpr (s == 33) bar();
        if constexpr (s >= 52) pc_a(IC(s - 52));
      }, [](auto) {});
      STAMP(17);
      if (l == 0) {
        store_x(c, r, a0, a1, xr, acto + 4 * STG, stto + 512 * 1024);
        bar();
        sfor<12, 16>(pc_a);
        waitv<4>();
        set_flag(facto + 4 * FL_K, tag);
        sfor<16, 24>(pc_a);
      } else {
        LAS char* im = smem + 8 * SLOT + w * 4096;
        sfor<0, 8>([&](auto n) {
          constexpr int g = n;
          const f32x16& a = g < 4 ? a0 : a1;
          *(LAS u32x2*)(im + r * 128 + ((g ^ (r & 7)) << 4) + h * 8) = u32x2{cvtpk(a[4 * (g & 3)], a[4 * (g & 3) + 1]), cvtpk(a[4 * (g & 3) + 2], a[4 * (g & 3) + 3])};
        });
        char* dst = out + (size_t)tt * 65536 + cb * 128;
        sfor<0, 4>([&](auto i) {
          const u32x4 v = *(LAS const u32x4*)(im + i * 1024 + lane * 16);
          st4_1<0>(v, (unsigned)((i * 8 + (lane >> 3)) * 2048 + (((lane & 7) ^ (lane >> 3)) << 4)), dst);
        });
        if (tt == 0 && cb == 0) set_flag(ws + WS_EPOCH, tag);
      }
    }
    STAMP(18);
    if (l == 1) break;
    csm = 1;
    act += 4 * STG; acto += 4 * STG; fact += 4 * FL_K; facto += 4 * FL_K; stt += 512 * KB; stto += 512 * KB;
    kvp += 131072; zp += 8192; fkvp += FL_K; kvf += 320 * KB; fkv += FL_K; xu_p += 4 * STG; su_p += 512 * KB;
    l = 1;
    REFRESH();
    STAMP(0);
        poll<16>(fact, lane * 8, tag);
        STAMP(1);
        sfor<0, XL>(issue_x);
        wait_bar<XL>();
        sfor<0, 2>([&](auto s) { rd01(s, IC(0), std::false_type{}); rd01(s, IC(1), std::false_type{}); });
        sfor<0, 64>([&](auto s) { step(s, std::false_type{}); });
        asm volatile("s_mov_b32 m0, %0\n\ts_mov_b64 exec, 1\n\t.rept 8\n\tglobal_load_lds_dwordx4 %1, %2\n\t.endr\n\ts_mov_b64 exec, -1\n.Lk_main_tail:" ::"s"(lds0 + 9 * SLOT + 4096), "v"(c.xv), "s"(wph) : "memory");
  }
}

extern "C" void kernel_launch(void* const* d_in, const int* in_sizes, int n_in, void* d_out, int out_size, void* d_ws, size_t ws_size, hipStream_t stream) {
  k_main<<<256, 256, 0, stream>>>((const char*)d_in[0], (const char*)d_in[1], (const char*)d_in[2], (const char*)d_in[3], (const char*)d_in[5], (const char*)d_in[6],
                                  (const char*)d_in[7], (const char*)d_in[8], (char*)d_ws, (char*)d_out);
}
```

```cpp
#include <hip/hip_runtime.h>
#include <hip/hip_bf16.h>
#include <stdint.h>
#include <type_traits>

typedef __attribute__((ext_vector_type(8))) short bf16x8;
typedef __attribute__((ext_vector_type(4))) short s16x4;
typedef __attribute__((ext_vector_type(2))) __bf16 bf2_t;
typedef __attribute__((ext_vector_type(16))) float f32x16;
typedef __attribute__((ext_vector_type(4))) float f32x4;
typedef __attribute__((ext_vector_type(2))) float f32x2;
typedef __attribute__((ext_vector_type(4))) unsigned u32x4;
typedef __attribute__((ext_vector_type(2))) unsigned u32x2;
#define LAS __attribute__((address_space(3)))
#define DI __device__ __forceinline__
#define MFMA32(a, b, c) __builtin_amdgcn_mfma_f32_32x32x16_bf16((a), (b), (c), 0, 0, 0)
#define MFMA16(a, b, c) __builtin_amdgcn_mfma_f32_16x16x32_bf16((a), (b), (c), 0, 0, 0)
#define SBAR() __builtin_amdgcn_sched_barrier(0)

constexpr int S_ = 2048, D_ = 1024;
constexpr float LOG2E = 1.4426950408889634f;
constexpr float QSCALE = 0.17677669529663687f * LOG2E;

constexpr size_t MB = 1u << 20, KB = 1024;
constexpr size_t STG = 256 * KB;
constexpr size_t WS_COL = 32 * MB;
constexpr size_t C_WT = 0;
constexpr size_t C_KVP = 768 * KB;
constexpr size_t C_ZP = 1024 * KB;
constexpr size_t C_CS = 1040 * KB;
constexpr size_t WS_ST = 64 * MB;
constexpr size_t WS_KVF = WS_ST + 1024 * KB;
constexpr size_t WS_FLAG = WS_ST + 1664 * KB;
constexpr size_t WS_EPOCH = WS_ST + 1920 * KB;
constexpr int FL_I = 2048, FL_K = 128;
typedef unsigned long long u64;

template <int I, int N, class F> DI void sfor(F&& f) { if constexpr (I < N) { f(std::integral_constant<int, I>{}); sfor<I + 1, N>(f); } }

DI unsigned cvtpk(float lo, float hi) { f32x2 v = {lo, hi}; return __builtin_bit_cast(unsigned, __builtin_convertvector(v, bf2_t)); }
DI float bflo(unsigned u) { return __uint_as_float(u << 16); }
DI float bfhi(unsigned u) { return __uint_as_float(u & 0xffff0000u); }
DI float rbf(float x) { return bflo(cvtpk(x, x)); }
DI float swap_add(float v) { auto r = __builtin_amdgcn_permlane32_swap(__float_as_uint(v), __float_as_uint(v), false, false); return __uint_as_float(r[0]) + __uint_as_float(r[1]); }
DI float swap_max(float v) { auto r = __builtin_amdgcn_permlane32_swap(__float_as_uint(v), __float_as_uint(v), false, false); return fmaxf(__uint_as_float(r[0]), __uint_as_float(r[1])); }
DI float swap_min(float v) { auto r = __builtin_amdgcn_permlane32_swap(__float_as_uint(v), __float_as_uint(v), false, false); return fminf(__uint_as_float(r[0]), __uint_as_float(r[1])); }
template <int S> DI bf16x8 pack8(const f32x16& v) {
  u32x4 p = {cvtpk(v[8 * S], v[8 * S + 1]), cvtpk(v[8 * S + 2], v[8 * S + 3]), cvtpk(v[8 * S + 4], v[8 * S + 5]), cvtpk(v[8 * S + 6], v[8 * S + 7])};
  return __builtin_bit_cast(bf16x8, p);
}
DI u32x4 unperm(u32x4 v) {
  auto r0 = __builtin_amdgcn_permlane32_swap(v[0], v[2], false, false);
  auto r1 = __builtin_amdgcn_permlane32_swap(v[1], v[3], false, false);
  return u32x4{r0[0], r1[0], r0[1], r1[1]};
}
DI float lane_get(float v, int src) { return __uint_as_float(__builtin_amdgcn_ds_bpermute(src << 2, __float_as_uint(v))); }
DI s16x4 trread(LAS const char* p) { return __builtin_amdgcn_ds_read_tr16_b64_v4i16((LAS s16x4*)p); }
DI bf16x8 cat8(s16x4 a, s16x4 b) { return __builtin_shufflevector(a, b, 0, 1, 2, 3, 4, 5, 6, 7); }
DI float dot2(unsigned a, unsigned b, float c) { return __builtin_amdgcn_fdot2_f32_bf16(__builtin_bit_cast(bf2_t, a), __builtin_bit_cast(bf2_t, b), c, false); }

template <int OFF> DI void dma(unsigned lds, unsigned v, const char* sb) {
  asm volatile("s_mov_b32 m0, %0\n\ts_nop 0\n\tglobal_load_lds_dwordx4 %1, %2 offset:%3" ::"s"(lds), "v"(v), "s"(sb), "i"(OFF) : "memory");
}
template <int OFF> DI void dma_c(unsigned lds, unsigned v, const char* sb) {
  asm volatile("s_mov_b32 m0, %0\n\ts_nop 0\n\tglobal_load_lds_dwordx4 %1, %2 offset:%3 sc1" ::"s"(lds), "v"(v), "s"(sb), "i"(OFF) : "memory");
}
template <int OFF> DI void dma_n(unsigned v, const char* sb) {
  asm volatile("global_load_lds_dwordx4 %0, %1 offset:%2 sc1" ::"v"(v), "s"(sb), "i"(OFF) : "memory");
}
constexpr int cap63(int n) { return n > 63 ? 63 : n; }
template <int N> DI void wait_bar() { asm volatile("s_waitcnt vmcnt(%0)\n\ts_barrier" ::"i"(cap63(N)) : "memory"); }
DI void lds_bar() { asm volatile("s_waitcnt lgkmcnt(0)\n\ts_barrier" ::: "memory"); }
DI void all_bar() { asm volatile("s_waitcnt vmcnt(0) lgkmcnt(0)\n\ts_barrier" ::: "memory"); }
DI void bar() { asm volatile("s_barrier" ::: "memory"); }
template <bool AG, int OFF> DI void ldx1(bf16x8& d, unsigned voff, const char* sb) {
  if constexpr (AG) asm volatile("global_load_dwordx4 %0, %1, %2 offset:%3 sc1" : "=a"(d) : "v"(voff), "s"(sb), "i"(OFF));
  else asm volatile("global_load_dwordx4 %0, %1, %2 offset:%3 sc1" : "=v"(d) : "v"(voff), "s"(sb), "i"(OFF));
}
template <int OFF> DI void ld2_1(f32x2& d, unsigned voff, const char* sb) { asm volatile("global_load_dwordx2 %0, %1, %2 offset:%3 sc1" : "=v"(d) : "v"(voff), "s"(sb), "i"(OFF)); }
DI void ld1_1(unsigned& d, unsigned voff, const void* sb) { asm volatile("global_load_dword %0, %1, %2 sc1" : "=v"(d) : "v"(voff), "s"(sb) : "memory"); }
template <int OFF> DI void st4_1(u32x4 d, unsigned voff, char* sb) { asm volatile("global_store_dwordx4 %0, %1, %2 offset:%3 sc1\n\ts_nop 1" ::"v"(voff), "v"(d), "s"(sb), "i"(OFF) : "memory"); }
DI void st2_1(f32x2 d, unsigned voff, char* sb) { asm volatile("global_store_dwordx2 %0, %1, %2 sc1" ::"v"(voff), "v"(d), "s"(sb) : "memory"); }
DI void st1_1(float d, unsigned voff, char* sb) { asm volatile("global_store_dword %0, %1, %2 sc1" ::"v"(voff), "v"(d), "s"(sb) : "memory"); }
template <int N> DI void waitv() { asm volatile("s_waitcnt vmcnt(%0)" ::"i"(cap63(N)) : "memory"); }
template <bool AG, int N, class T> DI void waitx(T& d) {
  if constexpr (AG) asm volatile("s_waitcnt vmcnt(%1)" : "+a"(d) : "i"(cap63(N))); else asm volatile("s_waitcnt vmcnt(%1)" : "+v"(d) : "i"(cap63(N)));
}
template <bool SW, bool BA, bool Z, bool NOP = false> DI void mm(f32x16& acc, const bf16x8& a, const bf16x8& b) {
#define MM_(OPS, C, OUT, BC) do { if constexpr (NOP) asm volatile("s_nop 1\n\tv_mfma_f32_32x32x16_bf16 %0, " OPS ", " C : OUT(acc) : "v"(a), BC(b)); \
                                  else asm volatile("v_mfma_f32_32x32x16_bf16 %0, " OPS ", " C : OUT(acc) : "v"(a), BC(b)); } while (0)
  if constexpr (Z) {
    if constexpr (SW) { if constexpr (BA) MM_("%2, %1", "0", "=&v", "a"); else MM_("%2, %1", "0", "=&v", "v"); }
    else { if constexpr (BA) MM_("%1, %2", "0", "=&v", "a"); else MM_("%1, %2", "0", "=&v", "v"); }
  } else {
    if constexpr (SW) { if constexpr (BA) MM_("%2, %1", "%0", "+v", "a"); else MM_("%2, %1", "%0", "+v", "v"); }
    else { if constexpr (BA) MM_("%1, %2", "%0", "+v", "a"); else MM_("%1, %2", "%0", "+v", "v"); }
  }
#undef MM_
}
DI void mdone(f32x16& a) { asm volatile("s_nop 11" : "+v"(a)); }
DI void mdone(f32x16& a, f32x16& b) { asm volatile("s_nop 11" : "+v"(a), "+v"(b)); }
DI void mdone(f32x16& a, f32x16& b, f32x16& c, f32x16& d) { asm volatile("s_nop 11" : "+v"(a), "+v"(b), "+v"(c), "+v"(d)); }
template <class T> DI T opq_v(T v) { asm volatile("" : "+v"(v)); return v; }
template <int OFF> DI void ldx0(bf16x8& d, unsigned voff, const char* sb) { asm volatile("global_load_dwordx4 %0, %1, %2 offset:%3" : "=v"(d) : "v"(voff), "s"(sb), "i"(OFF)); }
template <int OFF> DI void ldx1m(bf16x8& d, unsigned voff, const char* sb, unsigned long long m) {
  asm volatile("s_mov_b64 exec, %4\n\tglobal_load_dwordx4 %0, %1, %2 offset:%3 sc1\n\ts_mov_b64 exec, -1" : "=v"(d) : "v"(voff), "s"(sb), "i"(OFF), "s"(m));
}
template <bool AG, int OFF8> DI void ds_read2(bf16x8& d, unsigned addr) {
  addr += OFF8 * 8;
  if constexpr (AG) asm volatile("ds_read2_b64 %0, %1 offset1:2" : "=a"(d) : "v"(addr) : "memory"); else asm volatile("ds_read2_b64 %0, %1 offset1:2" : "=v"(d) : "v"(addr) : "memory");
}
template <bool AG, int N> DI void waitl(bf16x8& d) {
  if constexpr (AG) asm volatile("s_waitcnt lgkmcnt(%1)" : "+a"(d) : "i"(N) : "memory"); else asm volatile("s_waitcnt lgkmcnt(%1)" : "+v"(d) : "i"(N) : "memory");
}
template <int OFF> DI void ds_write_a(unsigned addr, const bf16x8& v) { asm volatile("ds_write_b128 %0, %1 offset:%2" ::"v"(addr), "a"(v), "i"(OFF) : "memory"); }
template <int N, class T> DI void wait2(T& a, T& b) { asm volatile("s_waitcnt vmcnt(%2)" : "+v"(a), "+v"(b) : "i"(cap63(N))); }
DI void set_flag(char* flag, u64 tag) {
  unsigned z = 0;
  asm volatile("s_mov_b64 exec, 1\n\tglobal_atomic_swap_x2 %0, %1, %2 sc1\n\ts_mov_b64 exec, -1" ::"v"(z), "v"(tag), "s"(flag) : "memory");
}
DI void ld8_1(u64& d, unsigned voff, const char* sb) { asm volatile("global_load_dwordx2 %0, %1, %2 sc1" : "=v"(d) : "v"(voff), "s"(sb) : "memory"); }
template <int NL> DI void rd_flag(u64& v, unsigned voff, const char* sb) {
  const u64 zero = 0;
  if constexpr (NL == 64) asm volatile("global_atomic_or_x2 %0, %1, %3, %2 sc0 sc1" : "+v"(v) : "v"(voff), "s"(sb), "v"(zero) : "memory");
  else asm volatile("s_mov_b64 exec, %4\n\tglobal_atomic_or_x2 %0, %1, %3, %2 sc0 sc1\n\ts_mov_b64 exec, -1" : "+v"(v) : "v"(voff), "s"(sb), "v"(zero), "s"((1ull << (NL & 63)) - 1) : "memory");
}
template <int NL> DI void poll(const char* base, unsigned voff, u64 tag) {
  for (int spin = 0; spin < (1 << 15); ++spin) {
    u64 v = tag;
    rd_flag<NL>(v, voff, base);
    waitx<false, 0>(v);
    if (__all(v == tag)) break;
  }
  asm volatile("" ::: "memory");
}

template <int RBX> DI void block_map(int& rb, int& cb) {
  const int x = blockIdx.x & 7, i = blockIdx.x >> 3;
  constexpr int CBX = 32 / RBX, GC = 16 / CBX;
  rb = RBX * (x / GC) + i / CBX; cb = CBX * (x % GC) + i % CBX;
}

DI float a_fma(float a, float b, float c) { float d; asm volatile("v_fma_f32 %0, %1, %2, %3" : "=v"(d) : "v"(a), "v"(b), "v"(c)); return d; }
DI float a_fms(float a, float b, float c) { float d; asm volatile("v_fma_f32 %0, %1, -%2, %3" : "=v"(d) : "v"(a), "v"(b), "v"(c)); return d; }
DI void a_exp(float& x) { asm volatile("v_exp_f32 %0, %0" : "+v"(x)); }
DI void a_add(float& s, float x) { asm volatile("v_add_f32 %0, %0, %1" : "+v"(s) : "v"(x)); }
DI void a_mul(float& x, float s) { asm volatile("v_mul_f32 %0, %0, %1" : "+v"(x) : "v"(s)); }
template <bool NEG> DI void a_hedge(float& c, float& n, float& s, float prev, float p, float sc, float m) {
  if constexpr (NEG) asm volatile("v_exp_f32 %0, %0\n\tv_fma_f32 %1, %4, -%5, %6\n\tv_add_f32 %2, %2, %3" : "+v"(c), "=&v"(n), "+v"(s) : "v"(prev), "v"(p), "v"(sc), "v"(m));
  else asm volatile("v_exp_f32 %0, %0\n\tv_fma_f32 %1, %4, %5, %6\n\tv_add_f32 %2, %2, %3" : "+v"(c), "=&v"(n), "+v"(s) : "v"(prev), "v"(p), "v"(sc), "v"(m));
}
DI void a_mul4(float& a, float& b, float& c, float& d, float s) {
  asm volatile("v_mul_f32 %0, %0, %4\n\tv_mul_f32 %1, %1, %4\n\tv_mul_f32 %2, %2, %4\n\tv_mul_f32 %3, %3, %4" : "+v"(a), "+v"(b), "+v"(c), "+v"(d) : "v"(s));
}
DI void a_sum16(f32x4& s, const bf16x8& sel, const bf16x8& f) { asm volatile("v_mfma_f32_16x16x32_bf16 %0, %1, %2, %0" : "+v"(s) : "v"(sel), "v"(f)); }
DI void a_mm4(float& mx, float& mn, float a, float b, float c, float d) {
  asm volatile("v_max3_f32 %0, %0, %2, %3\n\tv_min3_f32 %1, %1, %2, %3\n\tv_max3_f32 %0, %0, %4, %5\n\tv_min3_f32 %1, %1, %4, %5" : "+v"(mx), "+v"(mn) : "v"(a), "v"(b), "v"(c), "v"(d));
}
DI float a_max3(float a, float b, float c) { float d; asm volatile("v_max3_f32 %0, %1, %2, %3" : "=v"(d) : "v"(a), "v"(b), "v"(c)); return d; }
DI float a_min3(float a, float b, float c) { float d; asm volatile("v_min3_f32 %0, %1, %2, %3" : "=v"(d) : "v"(a), "v"(b), "v"(c)); return d; }

DI float opq_a(float v) { asm volatile("" : "+a"(v)); return v; }
DI void pin_a(bf16x8& a) { asm volatile("" : "+a"(a)); }
DI void pin2(f32x16& a, f32x16& b) { asm volatile("" : "+v"(a), "+v"(b)); }
DI bf16x8 cs_frag(float c) {
  const float hi = rbf(c), mid = rbf(c - hi), lo = (c - hi) - mid;
  return __builtin_bit_cast(bf16x8, u32x4{cvtpk(hi, mid), cvtpk(lo, 0.f), 0u, 0u});
}
template <bool CS> struct Red {
  bf16x8 p[4];
  float t[4], a, b, sd;
  template <int K> DI void run(int lane) {
    if constexpr (K == 0) asm volatile("" : "+v"(p[0]), "+v"(p[1]), "+v"(p[2]), "+v"(p[3]));
    if constexpr (K < 3) {
      const f32x4 u = __builtin_bit_cast(f32x4, p[K + 1]), v = __builtin_bit_cast(f32x4, p[0]);
#pragma unroll
      for (int e = 0; e < 4; ++e) { if (K == 0) t[e] = v[e]; a_add(t[e], u[e]); }
    }
    if constexpr (K == 3) {
      auto x = __builtin_amdgcn_permlane16_swap(__float_as_uint(t[0]), __float_as_uint(t[1]), false, false);
      auto y = __builtin_amdgcn_permlane16_swap(__float_as_uint(t[2]), __float_as_uint(t[3]), false, false);
      a = opq_v(__uint_as_float(x[0]) + __uint_as_float(x[1])); b = opq_v(__uint_as_float(y[0]) + __uint_as_float(y[1]));
    }
    if constexpr (K == 4) { auto x = __builtin_amdgcn_permlane32_swap(__float_as_uint(a), __float_as_uint(b), false, false); a = opq_v(__uint_as_float(x[0]) + __uint_as_float(x[1])); }
    if constexpr (K == 5) {
      if constexpr (CS) a = lane_get(a, (lane >> 2) + ((lane & 3) << 4));
      else { const int src = ((lane & 31) >> 1) + ((lane & 1) << 5); b = lane_get(a, src + 16); a = lane_get(a, src); }
    }
    if constexpr (CS) {
      if constexpr (K == 8) { t[0] = opq_v(rbf(a)); t[1] = opq_v(a - t[0]); }
      if constexpr (K == 9) { const float mid = rbf(t[1]); p[0] = opq_v(__builtin_bit_cast(bf16x8, u32x4{cvtpk(t[0], mid), cvtpk(t[1] - mid, 0.f), 0u, 0u})); }
    } else {
      if constexpr (K == 8) { const float m = a * (1.f / D_); b = opq_v(fmaxf(b * (1.f / D_) - m * m, 0.f)); a = opq_v(rbf(m)); }
      if constexpr (K == 9) b = opq_v(rbf(rbf(b) + 1e-5f));
      if constexpr (K == 10) { sd = opq_v(rbf(__builtin_amdgcn_sqrtf(b))); b = opq_v(__builtin_amdgcn_rcpf(sd)); }
    }
  }
};
DI void pin4(bf16x8 (&p)[4]) { asm volatile("" : "+v"(p[0]), "+v"(p[1]), "+v"(p[2]), "+v"(p[3])); }
DI bf16x8 mean_frag(float mean, bool on) { const float m = on ? -mean : 0.f; return __builtin_bit_cast(bf16x8, u32x4{cvtpk(m, m), cvtpk(m, 0.f), 0u, 0u}); }

#define STAMP(i)

constexpr int SLOT = 16384, XL = 12;
struct WinPtr {
  unsigned b[3];
  DI void init(unsigned a) { sfor<0, 3>([&](auto i) { b[i] = a + i * 65536u; asm volatile("" : "+v"(b[i])); }); }
  template <int OFF> DI bf16x8 frag() const { return *(LAS const bf16x8*)((LAS const char*)(uintptr_t)b[OFF >> 16] + (OFF & 65535)); }
};
struct Ctx { unsigned xv; WinPtr p; };
#define AI __attribute__((always_inline))
#define RARE(c) __builtin_expect(!!(c), 0)
#define IC(n) std::integral_constant<int, (n)>{}

template <int S> DI void ld_x(bf16x8 (&x)[64], const Ctx& c, const char* xb) { ldx1<false, (S % 8) * 1024 - 4096>(x[S], c.xv, xb + (S / 8) * 8192 + 4096); }
template <class H> constexpr int g64_wait(int s) {
  int n = 0;
  const int t0 = s - XL;
  if (t0 < 0) n += XL - 1 - s; else n += H::n(t0);
  for (int t = t0 < 0 ? 0 : t0 + 1; t < s; ++t) n += (t + XL < 64 ? 1 : 0) + H::n(t);
  return n;
}
constexpr int a_slot(int g);
template <bool MAPA, bool ZERO, class H, class FX, class FA>
DI void gemm64(const Ctx& c, bf16x8 (&x)[64], const char* xb, f32x16& acc0, f32x16& acc1, FX&& hook, FA&& aux) {
  bf16x8 f0[3], f1[3];
  auto off = [](int s) constexpr { return (MAPA ? a_slot(24 + s / 8) : s / 8) * SLOT + (s % 8) * 2048; };
  auto rd0 = [&](auto s) { f0[s % 3] = c.p.template frag<off(s)>(); };
  auto rd1 = [&](auto s) { f1[s % 3] = c.p.template frag<off(s) + 1024>(); };
  rd0(IC(0)); rd1(IC(0)); rd0(IC(1)); rd1(IC(1));
  sfor<0, 64>([&](auto s) {
    waitx<false, g64_wait<H>(s)>(x[s]);
    SBAR();
    mm<false, false, ZERO && s == 0>(acc0, f0[s % 3], x[s]);
    SBAR();
    if constexpr (s + 2 < 64) rd0(IC(s + 2));
    if constexpr (s + XL < 64) ld_x<s + XL>(x, c, xb);
    aux(IC(2 * s));
    SBAR();
    mm<false, false, ZERO && s == 0>(acc1, f1[s % 3], x[s]);
    SBAR();
    if constexpr (s + 2 < 64) rd1(IC(s + 2));
    hook(s);
    aux(IC(2 * s + 1));
    SBAR();
  });
  mdone(acc0, acc1);
}
struct HookP6 { static constexpr int n(int s) { return s >= 52 ? 1 : 0; } };
struct HookP3 { static constexpr int n(int s) { return s >= 52 ? 1 : 0; } };
struct HookP4 { static constexpr int n(int s) { return s == 52 || s == 6 || (s >= 8 && s < 12) || (s >= 14 && s < 30 && s % 2 == 0) ? 1 : 0; } };

DI void park4(u32x4 (&v)[4]) { asm volatile("" : "+a"(v[0]), "+a"(v[1]), "+a"(v[2]), "+a"(v[3])); }
DI void res_to_acc(u32x4 (&xr)[4], f32x16& a0, f32x16& a1) {
  park4(xr);
#pragma unroll
  for (int e = 0; e < 4; ++e) {
    a0[2 * e] = bflo(xr[0][e]); a0[2 * e + 1] = bfhi(xr[0][e]); a0[8 + 2 * e] = bflo(xr[1][e]); a0[9 + 2 * e] = bfhi(xr[1][e]);
    a1[2 * e] = bflo(xr[2][e]); a1[2 * e + 1] = bfhi(xr[2][e]); a1[8 + 2 * e] = bflo(xr[3][e]); a1[9 + 2 * e] = bfhi(xr[3][e]);
  }
  asm volatile("" : "+v"(a0), "+v"(a1));
}
DI void store_stats(int r, const u32x4 (&xr)[4], char* stp) {
  float s1 = 0.f, s2 = 0.f, s3 = 0.f, s4 = 0.f;
#pragma unroll
  for (int i = 0; i < 4; ++i)
#pragma unroll
    for (int e = 0; e < 4; e += 2) {
      s1 = dot2(xr[i][e], 0x3f803f80u, s1); s2 = dot2(xr[i][e], xr[i][e], s2);
      s3 = dot2(xr[i][e + 1], 0x3f803f80u, s3); s4 = dot2(xr[i][e + 1], xr[i][e + 1], s4);
    }
  st2_1(f32x2{swap_add(s1 + s3), swap_add(s2 + s4)}, (unsigned)(r * 8), stp);
}
DI void store_x(const Ctx& c, int r, const f32x16& a0, const f32x16& a1, u32x4 (&xr)[4], char* dst, char* stp) {
  SBAR();
  xr[0] = __builtin_bit_cast(u32x4, pack8<0>(a0)); st4_1<0>(xr[0], c.xv, dst); SBAR();
  xr[1] = __builtin_bit_cast(u32x4, pack8<1>(a0)); st4_1<1024>(xr[1], c.xv, dst); SBAR();
  xr[2] = __builtin_bit_cast(u32x4, pack8<0>(a1)); st4_1<2048>(xr[2], c.xv, dst); SBAR();
  xr[3] = __builtin_bit_cast(u32x4, pack8<1>(a1)); st4_1<3072>(xr[3], c.xv, dst); SBAR();
  store_stats(r, xr, stp);
  SBAR();
  park4(xr);
}

template <class FX> DI void hedge_exp(const f32x16& p, float sc, f32x16& ep, f32x16& en, FX&& hook) {
  float mx = fmaxf(p[0], p[1]), mn = fminf(p[0], p[1]);
#pragma unroll
  for (int i = 2; i < 16; i += 2) { mx = fmaxf(fmaxf(mx, p[i]), p[i + 1]); mn = fminf(fminf(mn, p[i]), p[i + 1]); }
  mx = swap_max(mx) * sc; mn = swap_min(mn) * sc;
  sfor<0, 16>([&](auto i) {
    SBAR();
    hook(i);
    constexpr int e = i;
    float a = __builtin_amdgcn_exp2f(__builtin_fmaf(p[e], sc, -mx)), b = __builtin_amdgcn_exp2f(__builtin_fmaf(p[e], -sc, mn));
    asm volatile("" : "+v"(a), "+v"(b));
    ep[e] = a; en[e] = b;
  });
  SBAR();
}

constexpr int a_slot(int g) { return g == 8 || g == 15 ? 8 : (g > 8 && g < 15 ? g - 9 : g % 8); }
constexpr bool a_bar(int s) { return (s + 2) % 8 == 0 && s < 190 && s != 78; }
constexpr int QP = 128 + 24, QC = 128 + 36;
constexpr int a_piece(int s, bool first) {
  constexpr int t[][3] = {{46, 8, 1}, {54, 9, 1}, {58, 10, 1}, {64, 11, 2}, {72, 12, 2}, {80, 13, 2}, {88, 14, 2}, {96, 15, 2}, {104, 16, 1}, {108, 17, 1}, {112, 18, 1},
                          {116, 19, 1}, {120, 20, 1}, {134, 21, 1}, {138, 22, 1}, {142, 23, 1}, {146, 24, 1}, {150, 25, 1}, {158, 26, 1}};
  if (!first && s >= 8 && s < 24 && s % 2 == 0) return 24 + (s - 8) / 2;
  for (auto& e : t) if (s >= e[0] && s < e[0] + 4 * e[2] && (s - e[0]) % e[2] == 0) return e[1] * 4 + (s - e[0]) / e[2];
  return -1;
}
constexpr int a_park(int s) { return s >= 129 && s < 135 ? s - 129 : (s == 162 || s == 163 ? s - 156 : -1); }
constexpr int a_ld0(bool first) { return first ? 32 : 2; }
constexpr bool a_side(int s, bool first) { return (s >= a_ld0(first) && s < a_ld0(first) + (first ? 12 : 4)) || s == 50 || (s >= 154 && s < 158) || a_park(s) >= 0; }
constexpr int f_rd(int n) { return n % 6 < 4 && n / 6 + 1 < 16 ? (n / 6 + 1) * 4 + n % 6 : -1; }
constexpr int f_in(int n) { return (n + 1) % 6 < 4 && (n + 1) / 6 >= 1 && (n + 1) / 6 + 4 < 16 ? ((n + 1) / 6 + 4) * 4 + (n + 1) % 6 : -1; }
constexpr int f_raw(int s) { return s >= 6 && s < 46 && s % 2 == 0 ? ((s + 2) / 8 + 2) * 4 + ((s + 2) % 8) / 2 : -1; }
struct SchedA {
  int xw[64], bw[192], sw[3], aw, fw, cw, pw, zw, uw, vw, lw[32];
  constexpr SchedA(bool first) : xw{}, bw{}, sw{}, aw(0), fw(0), cw(0), pw(0), zw(0), uw(0), vw(0), lw{} {
    int pos = 0, xpos[64] = {}, wpos[32] = {}, spos[192] = {}, fpos = 0, stpos = 0, ppos = 0, upos = 0, vpos = 0, lpos[34] = {};
    if (first) { xpos[1] = 4; xpos[2] = 12; wpos[1] = 18; xpos[3] = 22; wpos[2] = 26; xpos[4] = pos = 30; } else for (int s = 0; s < XL; ++s) xpos[s] = ++pos;
    for (int s = 0; s < 192; ++s) {
      if (s == 128) { pos += 3; stpos = pos; }
      if (a_bar(s)) {
        const int g = (s + 2) / 8 + (s == 70);
        bw[s] = wpos[g] ? pos - wpos[g] : 63;
        if (s == 62 && pos - spos[50] < bw[s]) bw[s] = pos - spos[50];
        if (s == 142) { if (pos - stpos < bw[s]) bw[s] = pos - stpos; ++pos; }
        if (s == 6 && first) ++pos;
      }
      if (s == 102) uw = pos - upos;
      if (s == 118) vw = pos - vpos;
      for (int k = 0; k < 3; ++k) if (s == a_ld0(first) + 14 + 4 * k) sw[k] = pos - spos[a_ld0(first) + 3 + 4 * k];
      if (s == 32) aw = pos - fpos;
      if (s == 128) fw = pos - fpos;
      if (s == 170) cw = pos - spos[157];
      if (s == QC) pw = pos - ppos;
      if (s < 64) xw[s] = first ? (s % 3 == 0 && s / 3 + 1 < 16 ? pos - xpos[s / 3 + 1] : 63) : pos - xpos[s];
      if (first && s < 64 && f_in(2 * s) >= 0) xpos[f_in(2 * s) / 4] = ++pos;
      if (!first && s + XL < 64) xpos[s + XL] = ++pos;
      if ((first && s == 22) || s == 127) fpos = ++pos;
      if (s == QP) ppos = ++pos;
      if (s >= QC) lpos[s - QC] = ++pos;
      if (a_piece(s, first) >= 0) wpos[a_piece(s, first) / 4] = ++pos;
      if (a_side(s, first)) spos[s] = ++pos;
      if (first && s < 64 && f_in(2 * s + 1) >= 0) xpos[f_in(2 * s + 1) / 4] = ++pos;
      if (first && f_raw(s) >= 0) wpos[f_raw(s) / 4] = ++pos;
      if (s == 63) pos += 8;
      if (s == 87) upos = pos;
      if (s == 103) vpos = pos;
      if (s == 177) zw = pos - lpos[1];
    }
    for (int i = 0; i < 32; ++i) {
      lw[i] = pos - lpos[2 + i];
      if (i % 2 == 0 && 192 - QC + i / 2 < 34) lpos[192 - QC + i / 2] = ++pos;
      if (i % 2 == 0 && i >= 16) ++pos;
    }
  }
};
constexpr int cmin(int a, int b) { return a < b ? a : b; }

#define R_OA "v[64:79]"
#define R_OB "v[80:95]"
#define S0_(n, q, m) "v_mfma_f32_32x32x16_bf16 v[" #n ":" #n "+15], v[208:211], v[" #q ":" #q "+3], v[" #m ":" #m "+15]\n\t"
#define S1_(n, q) "v_mfma_f32_32x32x16_bf16 v[" #n ":" #n "+15], v[212:215], v[" #q "+4:" #q "+7], v[" #n ":" #n "+15]\n\t"
#define PV0_(o, p) "v_smfmac_f32_32x32x32_bf16 v[" #o ":" #o "+15], v[216:219], v[" #p ":" #p "+7], v236\n\t"
#define PV1_(o, p) "v_smfmac_f32_32x32x32_bf16 v[" #o ":" #o "+15], v[220:223], v[" #p "+8:" #p "+15], v236\n\t"
#define L0_(l, p) "v_smfmac_f32_16x16x64_bf16 v[" #l ":" #l "+3], v[224:227], v[" #p ":" #p "+7], v236\n\t"
#define L1_(l, p) "v_smfmac_f32_16x16x64_bf16 v[" #l ":" #l "+3], v[224:227], v[" #p "+8:" #p "+15], v236\n\t"
#define E_(e, i) "v_exp_f32 v[" #e "+" #i "], v[" #e "+" #i "]\n\t"
#define RD_(d, off) "ds_read_b128 v[" #d ":" #d "+3], v237 offset:" #off "\n\t"
#define HALF_(e, n, q, m, p, o, l, X0, X1, X2, X3, X4, X5) \
  S0_(n, q, m) X0 E_(e, 0) E_(e, 1) E_(e, 2) S1_(n, q) X1 E_(e, 3) E_(e, 4) E_(e, 5) PV0_(o, p) X2 E_(e, 6) E_(e, 7) E_(e, 8) \
  PV1_(o, p) X3 E_(e, 9) E_(e, 10) E_(e, 11) L0_(l, p) X4 E_(e, 12) E_(e, 13) L1_(l, p) X5 E_(e, 14) E_(e, 15)
#define LG2_ "s_waitcnt lgkmcnt(2)\n\t"
#define LG0_ "s_waitcnt lgkmcnt(0)\n\t"
#define STEP_(b, nb, kv, wo, Y0, Y1) \
  HALF_(128, 160, 200, 112, 176, 80, 232, "", "", RD_(208, b + 2048), RD_(212, b + 3072), RD_(216, b), RD_(220, b + 1024) LG2_) \
  HALF_(160, 144, 192, 96, 128, 64, 228, "", LG0_, "s_add_u32 m0, s44, " #nb "\n\t", "", "global_load_lds_dwordx4 v" #kv ", s[40:41] sc1\n\t", \
        "global_load_lds_dwordx4 v" #kv ", s[40:41] offset:1024 sc1\n\t") \
  HALF_(144, 176, 200, 112, 160, 80, 232, Y0, "", RD_(208, b + 6144), RD_(212, b + 7168), RD_(216, b + 4096), RD_(220, b + 5120) LG2_) \
  HALF_(176, 128, 192, 96, 144, 64, 228, "s_mov_b32 m0, s45\n\t", LG0_, "global_load_lds_dwordx4 v240, s[42:43] offset:" #wo " sc1\n\t", Y1, \
        "s_waitcnt vmcnt(4)\n\t", "s_barrier\n\t")
DI float max16(const f32x16& s) {
  float mx = s[0];
#pragma unroll
  for (int e = 1; e < 16; ++e) mx = fmaxf(mx, s[e]);
  return mx;
}
typedef __attribute__((ext_vector_type(8))) unsigned u32x8;
typedef __attribute__((ext_vector_type(16))) unsigned u32x16;

__global__ __launch_bounds__(256, 1) void k_main(const char* xin, const char* wqkv, const char* wphi, const char* woA, const char* wq, const char* wk, const char* wv,
                                                 const char* woB, char* ws, char* out) {
  __shared__ __attribute__((aligned(16))) char smem_[163840];
  LAS char* const smem = (LAS char*)smem_;
  const unsigned lds0 = (unsigned)(uintptr_t)smem;
  Ctx c;
  int lane = threadIdx.x & 63, w = __builtin_amdgcn_readfirstlane(threadIdx.x >> 6), r = lane & 31, h = lane >> 5, rb, cb;
  bf16x8 cf;
  { const char* pc; unsigned last;
    asm volatile("s_getpc_b64 %0\n1:\n\ts_mov_b32 %1, .Lk_main_tail-1b" : "=s"(pc), "=s"(last));
    const unsigned o = (((blockIdx.x >> 3) * 4 + w) & 63) * 1024;
    ldx0<0>(cf, (unsigned)(lane * 16), pc + (o < last ? o : last)); }
  SBAR();
  block_map<4>(rb, cb);
  int tt = rb * 4 + w, blk = (lane >> 4) & 1, q4 = (lane & 15) >> 2, p4 = lane & 3;
  c.xv = lane * 16; c.p.init(lds0 + c.xv);
  const int ut = rb * 4 + (cb >> 2), ty = cb & 3;
  char *flags, *col, *colw, *act, *acto, *fact, *facto, *stt, *stto, *kvp, *zp, *fkvp, *fwl, *kvf, *fkv;
  const char *xu_p, *su_p, *wkv, *wph;
  unsigned kvb;
  u64 tag = 0;
  u32x4 xr[4] = {};
  unsigned ldsw = lds0 + w * 4096;
  auto wt = [&](int m, int ch) { return colw + C_WT + m * 131072 + ch * SLOT; };
  auto pc_w = [&](int m, int slot, int ch, auto i) { if constexpr (i == 0) dma_c<0>(ldsw + slot * SLOT, c.xv, wt(m, ch)); else dma_n<i * 1024>(c.xv, wt(m, ch)); };
  auto pc_a = [&](auto n) { constexpr int g = n / 4; pc_w(g < 8 ? 1 : (g < 16 ? 2 : (g < 24 ? 0 : 3)), a_slot(g), g % 8, IC(n % 4)); };
  u64 csm = ~0ull;
  float csv[4] = {};
  auto pc_m = [&](auto n) { constexpr int g = n / 4; dma_c<(n % 4) * 1024>(ldsw + a_slot(g) * SLOT, c.xv, wt(2, g % 8)); };
  auto ld_cs = [&](bf16x8& d, int m, auto i) { ldx1m<i * 1024>(d, c.xv, col + C_CS + m * 4096, csm); };
  auto raw_src = [&](int pitch, auto i) AI { const int row = lane >> 3; return (unsigned)((32 * w + 8 * i + row) * pitch + ((lane & 7) ^ (((row >> 1) & 1) << 2)) * 16 - i * 1024); };
  auto raw_in = [&](auto j, int slot, auto i) AI {
    const int hi = rb >> 3, pitch = (j == 0 || (j == 2 && hi)) ? 6144 : 2048;
    const char* src = (j == 0 ? wqkv + (hi ? 0 : 4096) : (j == 1 ? (hi ? wq : woA) : (hi ? wqkv + 2048 : woB))) + cb * 128 + (size_t)(rb & 7) * 128 * pitch;
    dma<i * 1024>(ldsw + slot * SLOT, raw_src(pitch, i), src);
  };
  auto raw_out = [&](auto j, int slot) AI {
    const int hi = rb >> 3, ch = rb & 7, m = j == 0 ? 2 - 2 * hi : (j == 1 ? 3 + hi : 5 - 4 * hi);
    LAS const char* p = smem + slot * SLOT + (w >> 1) * 8192 + (4 * h + q4) * 128 + (((((q4 >> 1) ^ w) & 1) << 2) + 2 * blk + (p4 >> 1)) * 16 + 8 * (p4 & 1);
    char* dst = col + C_WT + m * 131072 + ch * SLOT + (w >> 1) * 8192 + (w & 1) * 1024 + 4096;
    float cs = 0.f;
    sfor<0, 4>([&](auto s) AI {
      const u32x4 f = __builtin_bit_cast(u32x4, cat8(trread(p + s * 2048), trread(p + s * 2048 + 1024)));
      st4_1<s * 2048 - 4096>(f, c.xv, dst);
#pragma unroll
      for (int e = 0; e < 4; ++e) cs = dot2(f[e], 0x3f803f80u, cs);
    });
    st1_1(swap_add(cs), (unsigned)(r * 4), col + C_CS + m * 4096 + (ch * 2 + (w >> 1)) * 256 + (w & 1) * 128);
  };
  const int w0 = w; const unsigned ldsw0 = ldsw;
#define REFRESH() do { \
    w = w0; ldsw = ldsw0; \
    asm volatile("" : "+s"(w), "+s"(tag), "+s"(act), "+s"(acto), "+s"(fact), "+s"(facto), "+s"(stt), "+s"(stto), "+s"(col), "+s"(colw), "+s"(ldsw)); \
    asm volatile("" : "+v"(lane)); \
    r = lane & 31; h = lane >> 5; blk = (lane >> 4) & 1; q4 = (lane & 15) >> 2; p4 = lane & 3; c.xv = lane * 16; c.p.init(lds0 + c.xv); \
  } while (0)
  int l = 0;
  float mean, rstd;
  Red<true> cw;
  f32x16 ra0, ra1;
  bf16x8 park[16];
      constexpr int NA = 58;
      constexpr SchedA SQ(false), S1(true);
      bf16x8 x[64], f0[3], f1[3], wf[4], kp[4], pp[32], zq[2], sel[2], mf0, mf1;
      Red<false> st; Red<true> ck, cv, cq;
      f32x16 acc[6], p[2], kvs;
      u64 fv;
      float mx, mn, e[16], en[16], se, sn, sce;
      f32x4 ks0 = {}, ks1 = {};
      unsigned arow, pk0, ra, xo[4], xs;
      const char* xw = xin + (size_t)tt * 65536;
      LAS char* pk_img = smem + 6 * SLOT;
      LAS char* v_img = smem + 5 * SLOT;
      LAS char* sh = smem + 9 * SLOT + 4096;
      auto issue_x = [&](auto s) { ldx1<(s < NA), (s % 8) * 1024 - 4096>(x[s], c.xv, act + (s / 8) * 8192 + 4096); };
      auto ld_park = [&](auto i) { ldx1<true, (i % 4) * 1024>(park[i], c.xv, wt(4, 2 + i / 4)); };
      auto ldp = [&](auto i) {
        if constexpr (i >= 2) ldx1<true, ((i - 2) % 8) * 1024 - 4096>(pp[i - 2], c.xv, kvp + ((i - 2) / 8) * 8192 + 4096); else ldx1<false, i * 1024>(zq[i], c.xv, zp);
      };
      auto rd_wf = [&](auto k) { LAS const char* wp = smem + 9 * SLOT + (4 * h + q4) * 64 + blk * 32 + p4 * 8 + k * 1024; wf[k] = cat8(trread(wp), trread(wp + 512)); };
      auto rd01 = [&](auto s, auto t, auto fl) {
        if constexpr (s < 192) {
          bf16x8 f;
          if constexpr (fl && s < 64) { LAS const char* q = (LAS const char*)(uintptr_t)(ra ^ (t * 64u)) + ((s / 8 + 3) % 4) * SLOT + (s % 8) * 2048; f = cat8(trread(q), trread(q + 1024)); }
          else f = c.p.template frag<a_slot(s / 8) * SLOT + (s % 8) * 2048 + t * 1024>();
          if constexpr (t == 0) f0[s % 3] = f; else f1[s % 3] = f;
        }
      };
      auto x_in = [&](auto n) { dma<(n % 4) * 1040>(lds0 + w * 20480 + ((n / 4) % 4) * 4160, xs + (n % 4) * (16384 - 1040), xw + (n / 4) * 128); };
      auto x_out = [&](auto s) { ds_read2<(s < NA), ((s / 4) % 4) * 520>(x[s], xo[s % 4]); };
      auto x_fin = [&](auto g, auto n) { waitl<(4 * g < NA), n>(x[4 * g]); sfor<4 * g + 1, 4 * g + 4>([&](auto s) { if constexpr (s < NA) pin_a(x[s]); else x[s] = opq_v(x[s]); }); };
      auto k_in = [&](auto n) { dma<(n % 4) * 1024>(ldsw + (5 + (n / 4 + 3) % 4) * SLOT, raw_src(6144, IC(n % 4)), wqkv + 2048 + cb * 128 + (size_t)(n / 4) * 128 * 6144); };
      auto v_out = [&](auto t, auto hf) {
        *(LAS bf16x8*)(v_img + t * 8192 + w * 2048 + hf * 1024 + lane * 16) = pack8<hf>(acc[2 + t]);
      };
      auto sum_mm = [&](auto i) { waitx<true, SQ.lw[i]>(pp[i]); mm<false, true, i == 0>(kvs, sel[i & 1], pp[i]); };
      auto kmap = [&](auto gi) {
        constexpr int g = gi;
        if constexpr (g == 4) pin2(acc[0], acc[1]);
        if constexpr (g >= 6 && g < 10) mm<false, false, g % 2 == 0>(p[(g - 6) / 2], wf[g - 6], kp[g - 6]);
        if constexpr (g >= 4 && g < 8) kp[g - 4] = opq_v(pack8<g % 2>(acc[(g - 4) / 2]));
        if constexpr (g == 10) mm<true, false, false, true>(acc[2], cv.p[0], mf0);
        if constexpr (g == 11) mm<true, false, false, true>(acc[3], cv.p[0], mf1);
        if constexpr (g >= 12) {
          constexpr int hd = (g - 12) / 58, j = (g - 12) % 58, n = (j - 7) / 2;
          const f32x16& q = p[hd];
          if constexpr (j == 0) { mx = a_max3(q[0], q[1], q[2]); mn = a_min3(q[0], q[1], q[2]); mx = a_max3(mx, q[3], q[3]); mn = a_min3(mn, q[3], q[3]); }
          if constexpr (j >= 1 && j < 4) a_mm4(mx, mn, q[4 * j], q[4 * j + 1], q[4 * j + 2], q[4 * j + 3]);
          if constexpr (j == 4) mx = opq_v(swap_max(mx) * -sce);
          if constexpr (j == 5) mn = opq_v(swap_min(mn) * sce);
          if constexpr (j == 6) { e[0] = a_fma(q[0], sce, mx); en[0] = a_fms(q[0], sce, mn); se = opq_v(0.f); sn = opq_v(0.f); }
          if constexpr (j >= 7 && j < 37) {
            if constexpr (j % 2) a_hedge<false>(e[n], e[n + 1], se, n ? e[n ? n - 1 : 0] : sn, q[n + 1], sce, mx);
            else a_hedge<true>(en[n], en[n + 1], sn, n ? en[n ? n - 1 : 0] : se, q[n + 1], sce, mn);
          }
          if constexpr (j == 37) { a_exp(e[15]); a_add(se, e[14]); }
          if constexpr (j == 38) { a_exp(en[15]); a_add(sn, en[14]); }
          if constexpr (j == 39) se = opq_v(swap_add(se + e[15]));
          if constexpr (j == 40) sn = opq_v(swap_add(sn + en[15]));
          if constexpr (j == 41) { se = opq_v(__builtin_amdgcn_rcpf(se) * rstd); sn = opq_v(__builtin_amdgcn_rcpf(sn) * rstd); }
          if constexpr (j >= 42) {
            constexpr int c8 = (j - 42) / 2, o = 4 * (c8 & 3);
            float* v = c8 < 4 ? e : en;
            if constexpr (j % 2 == 0) { a_mul4(v[o], v[o + 1], v[o + 2], v[o + 3], c8 < 4 ? se : sn); pk0 = opq_v(arow ^ (c8 << 4)); }
            else *(LAS u32x2*)((LAS char*)(uintptr_t)pk0 + hd * 16384) = u32x2{cvtpk(v[o], v[o + 1]), cvtpk(v[o + 2], v[o + 3])};
          }
        }
      };
      auto aux = [&](auto s, auto i, auto fl) {
        constexpr int g1 = 2 * (s - a_ld0(fl) - 14) + i, g3 = 2 * (s - 128) + i;
        if constexpr (s < 64) {
          if constexpr (fl) { if constexpr (i == 0) a_sum16(ks0, sel[0], f0[s % 3]); else a_sum16(ks1, sel[0], f1[s % 3]); }
          if constexpr (g1 >= 0 && g1 < 11) st.template run<g1>(lane);
          if constexpr (g1 == 11) { mean = st.a; rstd = st.b; sce = opq_v(rstd * LOG2E); }
          if constexpr (g1 == 12) mf0 = opq_v(mean_frag(mean, h == 0));
          if constexpr (g1 == 13) mf1 = opq_v(mean_frag(mean, h == 1));
          if constexpr (fl && g1 >= 8 && g1 < 14) cv.template run<g1 - 8>(lane);
          if constexpr (fl && g1 >= 16 && g1 < 22) cq.template run<g1 - 16>(lane);
          if constexpr (fl && g1 == 18) csv[1] = opq_a(cv.a);
          if constexpr (fl && g1 == 26) csv[2] = opq_a(cq.a);
          if constexpr (g1 == 22) cv.a = csv[1] = opq_a(csv[1]);
          if constexpr (g1 == 22 || g1 == 23) cv.template run<g1 - 14>(lane);
          if constexpr (!fl) {
            if constexpr (g1 == 14) ck.a = opq_v(csv[0]);
            if constexpr (g1 == 14 || g1 == 15) ck.template run<g1 - 6>(lane);
            if constexpr (g1 == 18) mm<false, false, false, true>(acc[0], ck.p[0], mf0);
            if constexpr (g1 == 19) mm<false, false, false, true>(acc[1], ck.p[0], mf1);
          }
          if constexpr (s == 60 && i == 0) *(LAS short*)(sh + 8704 + w * 64 + (r >> 4) * 32 + ((r >> 2) & 1) * 16 + ((r >> 3) & 1) * 8 + (r & 3) * 2) = (short)(__float_as_uint(st.sd) >> 16);
          if constexpr (s == 61 && i == 0) arow = opq_v((unsigned)(uintptr_t)pk_img + (w * 32 + r) * 128 + h * 8 + ((r & 7) << 4));
          if constexpr (s >= 62) rd_wf(IC(2 * (s - 62) + i));
        } else if constexpr (s < 128) {
          kmap(IC(2 * (s - 64) + i));
        } else {
          if constexpr (g3 == 2) cq.a = csv[2] = opq_a(csv[2]);
          if constexpr (g3 == 2 || g3 == 3) cq.template run<g3 + 6>(lane);
          if constexpr (g3 == 6) mm<false, false, false, true>(acc[4], cq.p[0], mf0);
          if constexpr (g3 == 7) mm<false, false, false, true>(acc[5], cq.p[0], mf1);
          if constexpr (g3 == 92) { csv[3] = l == 0 ? cw.a : csv[3]; cw.a = csv[3]; }
          if constexpr (g3 >= 84 && g3 < 94) cw.template run<g3 - 84>(lane);
          if constexpr (g3 >= 32 && g3 < 40) {
            constexpr int n = (g3 - 32) % 4, t = (g3 - 32) / 4;
            const int kr = 16 * t + 8 * (n >> 1) + 4 * h + 2 * (n & 1);
            u32x4 sv = __builtin_bit_cast(u32x4, sel[t]); sv[n] = opq_v(r == kr ? 0x3f80u : (r == kr + 1 ? 0x3f800000u : 0u)); sel[t] = __builtin_bit_cast(bf16x8, sv);
          }
          if constexpr (g3 == 40) { sel[0] = opq_v(sel[0]); sel[1] = opq_v(sel[1]); }
          if constexpr (g3 == 42) *(LAS unsigned*)(sh + 8192 + 256 + lane * 4) = opq_v(0x3f803f80u);
          if constexpr (g3 == 99) {
            wait2<SQ.zw>(zq[0], zq[1]);
#pragma unroll
            for (int n = 0; n < 4; ++n) { e[n] = __builtin_bit_cast(f32x4, zq[0])[n]; a_add(e[n], __builtin_bit_cast(f32x4, zq[1])[n]); }
          }
          if constexpr (g3 == 101) {
            auto a = __builtin_amdgcn_permlane16_swap(__float_as_uint(e[0]), __float_as_uint(e[1]), false, false);
            auto b = __builtin_amdgcn_permlane16_swap(__float_as_uint(e[2]), __float_as_uint(e[3]), false, false);
            e[0] = opq_v(__uint_as_float(a[0]) + __uint_as_float(a[1])); e[1] = opq_v(__uint_as_float(b[0]) + __uint_as_float(b[1]));
          }
          if constexpr (g3 == 103) { auto a = __builtin_amdgcn_permlane32_swap(__float_as_uint(e[0]), __float_as_uint(e[1]), false, false); e[0] = opq_v(__uint_as_float(a[0]) + __uint_as_float(a[1])); }
          if constexpr (g3 == 105) e[1] = lane_get(e[0], lane ^ 8);
          if constexpr (g3 == 111)
            *(LAS short*)(sh + 8192 + w * 64 + (lane & 1) * 32 + ((lane >> 2) & 1) * 16 + ((lane >> 1) & 1) * 8 + (lane >> 4) * 2) = (short)cvtpk(e[0] + e[1], 0.f);
          if constexpr (g3 >= 116 && g3 < 124 && g3 % 2 == 1) rd_wf(IC((g3 - 116) / 2));
        }
      };
      const unsigned fo = (unsigned)((lane & 15) * 8 + (blk ? cb * 4 * FL_I + 10 * FL_K : tt * FL_I));
      auto step = [&](auto s, auto fl) {
        constexpr int ps = s / 64, L0 = a_ld0(fl);
        constexpr bool first = fl;
        if constexpr (s == 102) waitv<SQ.uw>();
        if constexpr (s == 118) waitv<SQ.vw>();
        if constexpr (a_bar(s) && s >= (first ? 6 : 38)) wait_bar<(ps ? cmin(S1.bw[s], SQ.bw[s]) : (first ? S1.bw[s] : SQ.bw[s]))>();
        if constexpr (s == L0 + 14 || (first && (s == L0 + 18 || s == L0 + 22))) waitv<(first ? S1.sw[(s - L0 - 14) / 4] : SQ.sw[0])>();
        if constexpr (first && s == 6) { set_flag(facto, tag); if (RARE(w == 0)) set_flag(fwl - FL_K + rb * 8, tag); }
        if constexpr (first && s == 32) { waitx<false, S1.aw>(fv); if (RARE(!__all(fv == tag))) poll<32>(flags, fo, tag); }
        if constexpr (s == 118) { if (RARE(l == 0 && w == 0)) set_flag(fwl + rb * 8, tag); }
        if constexpr (s == 128) { waitx<false, SQ.fw>(fv); if (RARE(!__all(fv == tag))) poll<16>(fwl, lane * 8, tag); }
        if constexpr (s == 142) set_flag(fkvp + rb * 8, tag);
        if constexpr (s == QC) { waitx<false, SQ.pw>(fv); if (RARE(!__all(fv == tag))) poll<16>(fkvp, lane * 8, tag); }
        if constexpr (s == 170) waitv<SQ.cw>();
        if constexpr (s < 64 && !first) waitx<(s < NA), SQ.xw[s % 64]>(x[s % 64]);
        if constexpr (first && s % 3 == 0) waitv<S1.xw[s % 64]>();
        SBAR();
        mm<ps == 1, (s % 64 < NA), s % 64 == 0>(acc[2 * ps], f0[s % 3], x[s % 64]);
        SBAR();
        if constexpr (first && f_rd(2 * s) >= 0) x_out(IC(f_rd(2 * s)));
        if constexpr (!first) { rd01(IC(s + 2), IC(1), fl); SBAR(); }
        rd01(IC(s + 2), IC(0), fl);
        if constexpr (first && f_in(2 * s) >= 0) x_in(IC(f_in(2 * s)));
        if constexpr (s + XL < 64 && !first) issue_x(IC(s + XL));
        if constexpr (first && s == 22) { fv = tag; rd_flag<32>(fv, fo, flags); }
        if constexpr (s == 127) ld8_1(fv, (lane & 15) * 8, fwl);
        if constexpr (s == QP) { fv = tag; rd_flag<16>(fv, lane * 8, fkvp); }
        if constexpr (s >= QC) ldp(IC(s - QC));
        aux(s, IC(0), fl);
        SBAR();
        mm<ps == 1, (s % 64 < NA), s % 64 == 0>(acc[2 * ps + 1], f1[s % 3], x[s % 64]);
        SBAR();
        if constexpr (first && f_rd(2 * s + 1) >= 0) x_out(IC(f_rd(2 * s + 1)));
        if constexpr (first && s % 3 == 2 && s / 3 + 1 < 16) x_fin(IC(s / 3 + 1), IC(4));
        if constexpr (first) rd01(IC(s + 2), IC(1), fl);
        if constexpr (a_piece(s, first) >= 0) { if constexpr (first) pc_m(IC(a_piece(s, first))); else pc_a(IC(a_piece(s, first))); }
        if constexpr (s >= L0 && s < L0 + 4) ldx1<false, (s - L0) * 1024>(st.p[s - L0], c.xv, stt);
        if constexpr (first && s >= L0 + 4 && s < L0 + 8) ld_cs(cv.p[s - L0 - 4], 2, IC(s - L0 - 4));
        if constexpr (first && s >= L0 + 8 && s < L0 + 12) ld_cs(cq.p[s - L0 - 8], 0, IC(s - L0 - 8));
        if constexpr (s == 50) dma<0>(lds0 + 9 * SLOT + w * 1024, c.xv, wph);
        if constexpr (s >= 154 && s < 158) ld_cs(cw.p[s - 154], 4, IC(s - 154));
        if constexpr (a_park(s) >= 0) ld_park(IC(a_park(s)));
        if constexpr (first && f_in(2 * s + 1) >= 0) x_in(IC(f_in(2 * s + 1)));
        if constexpr (first && f_raw(s) >= 0) k_in(IC(f_raw(s)));
        aux(s, IC(1), fl);
        if constexpr (s == 87) { if (RARE(l == 0)) sfor<0, 4>([&](auto i) AI { raw_in(IC(1), 7, i); }); }
        if constexpr (s == 103) { if (RARE(l == 0)) raw_out(IC(1), 7); }
        if constexpr (s == 133) { if (RARE(l == 0)) sfor<0, 4>([&](auto i) AI { raw_in(IC(2), 8, i); }); }
        SBAR();
      };
      {
        STAMP(30);
        bf16x8 kt[2];
        bf16x8 xt[4];
        xs = (unsigned)((lane >> 3) * 2048 + ((lane & 7) ^ (((lane >> 3) & 3) << 1)) * 16);
        sfor<0, 4>([&](auto i) { dma<i * 1040>(lds0 + w * 20480 + 3 * 4160, xs + i * (16384 - 1040), xw + cb * 128); });
        sfor<0, 2>([&](auto i) { ldx0<0>(kt[i], (unsigned)((lane >> 3) * 6144 + (lane & 7) * 16), wqkv + 2048 + cb * 128 + (size_t)(tt * 16 + i * 8) * 6144); });
        u64 ep; ld8_1(ep, 0u, ws + WS_EPOCH);
        sfor<0, 4>([&](auto i) AI { raw_in(IC(0), 9, i); });
        sfor<0, 4>([&](auto i) { xo[i] = opq_v(lds0 + w * 20480 + r * 128 + (((r & 3) ^ i) << 5) + h * 8 + (r >> 3) * 16); });
        ra = opq_v(lds0 + 5 * SLOT + (4 * h + q4) * 128 + (((q4 >> 1) << 2) + 2 * blk + (p4 >> 1)) * 16 + 8 * (p4 & 1));
        { const unsigned o = (((lane >> 2) ^ (lane >> 4)) & 1) ? 0u : 0x3f803f80u; sel[0] = opq_v(__builtin_bit_cast(bf16x8, u32x4{o, o, o, o})); ks0 = opq_v(ks0); ks1 = opq_v(ks1); }
        sfor<0, 4>(x_in); sfor<0, 4>(k_in);
        asm volatile("" : "+s"(ws), "+s"(wk), "+s"(wphi));
        flags = ws + WS_FLAG;
        col = ws + WS_COL + (size_t)cb * 2 * MB; colw = col + w * 4096;
        act = ws + (size_t)rb * 2 * MB + w * 65536;
        acto = act + cb * 4096;
        fact = flags + tt * FL_I;
        facto = fact + cb * 8;
        stt = ws + WS_ST + tt * 4096;
        stto = stt + cb * 256;
        kvp = col + C_KVP + w * 32768; zp = col + C_ZP + w * 2048;
        fkvp = flags + (cb * 4 + w) * FL_I + 8 * FL_K; fwl = flags + cb * 4 * FL_I + 11 * FL_K;
        kvf = ws + WS_KVF; fkv = flags + 12 * FL_K;
        xu_p = ws + (size_t)rb * 2 * MB + 2 * STG + (cb >> 2) * 65536 + w * 16384 + 4096;
        su_p = ws + WS_ST + 256 * KB + ut * 4096;
        wkv = ((cb & 2) ? wv : wk) + w * 256 * 64; wph = wphi + cb * 4096 + w * 1024;
        { const unsigned m = (unsigned)(ty >> 1) - 1u; kvb = ut * 4096 + (((ty * 1024 + (w & 1) * 8) & m) | ((2048 + (w >> 1) * 1024 + (ty & 1) * 256 + (w & 1) * 512) & ~m)); }
        asm volatile("" : "+s"(kvb), "+s"(act), "+s"(acto), "+s"(kvp), "+s"(zp), "+s"(fkvp), "+s"(kvf), "+s"(fkv), "+s"(xu_p), "+s"(su_p), "+s"(wkv), "+s"(wph));
        asm volatile("s_waitcnt vmcnt(12)" : "+v"(cf), "+v"(ep), "+v"(kt[0]), "+v"(kt[1])::"memory");
        sfor<0, 4>([&](auto i) { ds_read2<false, 3 * 520>(xt[i], xo[i]); });
        sfor<4, 8>(x_in); sfor<4, 8>(k_in); sfor<8, 12>(x_in);
        asm volatile("s_waitcnt lgkmcnt(0)" : "+v"(xt[0]), "+v"(xt[1]), "+v"(xt[2]), "+v"(xt[3])::"memory");
        sfor<0, 4>([&](auto i) { xr[i] = __builtin_bit_cast(u32x4, xt[i]); });
        store_stats(r, xr, stto);
        park4(xr);
        { u64 z = ((u64)__builtin_amdgcn_readfirstlane((unsigned)(ep >> 32)) << 32 | __builtin_amdgcn_readfirstlane((unsigned)ep)) + 0x9E3779B97F4A7C15ull;
          z = (z ^ (z >> 30)) * 0xBF58476D1CE4E5B9ull; z = (z ^ (z >> 27)) * 0x94D049BB133111EBull; tag = z ^ (z >> 31); asm volatile("" : "+s"(tag)); }
        wait_bar<13>();
        sfor<0, 4>(x_out);
        raw_out(IC(0), 9);
        sfor<12, 16>(x_in); sfor<8, 12>(k_in);
        x_fin(IC(0), IC(0));
        sfor<16, 20>(x_in);
        STAMP(31); STAMP(0); STAMP(1);
        sfor<0, 2>([&](auto s) { rd01(s, IC(0), std::true_type{}); rd01(s, IC(1), std::true_type{}); });
        sfor<0, 64>([&](auto s) { step(s, std::true_type{}); });
        {
          const int gw = blockIdx.x * 4 + w; const unsigned z = 0;
          const unsigned vo = (unsigned)(lane < 5 ? WS_KVF - WS_ST + (gw * 5 + lane) * 128 : WS_FLAG - WS_ST + gw * 128);
          asm volatile("s_mov_b64 exec, 63\n\tglobal_atomic_or %0, %1, %2 sc1\n\ts_mov_b64 exec, -1" ::"v"(vo), "v"(z), "s"(ws + WS_ST) : "memory");
          const unsigned dump = lds0 + 9 * SLOT + 4096 + w * 2048;
          dma<0>(dump, c.xv, woA + gw * 2048); dma<1024>(dump, c.xv, woA + gw * 2048);
          dma<0>(dump, c.xv, wq + gw * 2048); dma<1024>(dump, c.xv, wq + gw * 2048);
          dma<0>(dump, c.xv, woB + gw * 2048); dma<1024>(dump, c.xv, woB + gw * 2048);
          dma<0>(dump, c.xv, ((gw & 64) ? wk : wv) + (gw & 63) * 1024);
        }
        asm volatile("s_nop 7" : "+v"(ks0), "+v"(ks1));
        csv[0] = h ? ks1[0] : ks0[0];
        const bf16x8 kf = cs_frag(csv[0]);
        mm<false, false, false, true>(acc[0], kf, mf0); mm<false, false, false, true>(acc[1], kf, mf1);
      }
  asm volatile("" : "+s"(w), "+s"(tag), "+s"(act), "+s"(acto), "+s"(fact), "+s"(facto), "+s"(stt), "+s"(stto), "+s"(col), "+s"(colw), "+s"(ldsw));
  asm volatile("" : "+s"(kvp), "+s"(zp), "+s"(fkvp), "+s"(fwl), "+s"(rb), "+s"(kvf), "+s"(fkv), "+s"(xu_p), "+s"(su_p), "+s"(wkv), "+s"(csm), "+s"(l));
  for (;;) {
    {
      REFRESH();
      asm volatile("" : "+s"(kvp), "+s"(zp), "+s"(fkvp), "+s"(fwl), "+s"(rb));
      STAMP(2);
      sfor<64, 128>([&](auto s) { step(s, std::false_type{}); });
      STAMP(19);
      mdone(acc[2], acc[3]);
      v_out(IC(0), IC(0)); v_out(IC(0), IC(1)); v_out(IC(1), IC(0)); v_out(IC(1), IC(1));
      lds_bar();
      {
        const int hd = w >> 1, fh = w & 1;
        f32x16 kv, z;
        const int ch = fh * 4 + 2 * blk + (p4 >> 1), r0 = 4 * h + q4;
        LAS const char* pa = pk_img + hd * 16384 + r0 * 128 + ((ch ^ r0) << 4) + 8 * (p4 & 1);
        LAS const char* pb = v_img + hd * 8192 + lane * 16;
        bf16x8 a[8], b[8], d[8];
#pragma unroll
        for (int s = 0; s < 8; ++s) { a[s] = cat8(trread(pa + s * 2048), trread(pa + s * 2048 + 1024)); b[s] = *(LAS const bf16x8*)(pb + s * 1024); }
#pragma unroll
        for (int s = 0; s < 8; ++s) d[s] = *(LAS const bf16x8*)(sh + 8704 + s * 32 + h * 16);
        SBAR();
        sfor<0, 8>([&](auto s) { mm<false, false, s == 0>(kv, a[s], b[s]); });
        sfor<0, 8>([&](auto s) {
          mm<false, false, s == 0>(z, d[s], a[s]);
          if constexpr (s == 1) { mdone(kv); st4_1<0>(__builtin_bit_cast(u32x4, pack8<0>(kv)), c.xv, kvp + rb * 2048); }
          if constexpr (s == 2) { st4_1<1024>(__builtin_bit_cast(u32x4, pack8<1>(kv)), c.xv, kvp + rb * 2048); }
          SBAR();
        });
        mdone(z);
        st1_1(z[0], (unsigned)(r * 4), zp + rb * 128);
      }
      STAMP(3);
      sfor<128, 192>([&](auto s) { step(s, std::false_type{}); });
      mdone(acc[4], acc[5]);
      STAMP(4);
      {
        bf16x8 pq[2][4], ka[8], za[8];
        f32x16 ep, em, nm[4], dn[4];
        float ai[2], bi[2];
        char* dst = acto + STG;
        sfor<0, 4>([&](auto k) { mm<false, false, k % 2 == 0, true>(p[k / 2], wf[k], pack8<k % 2>(acc[4 + k / 2])); });
        mdone(p[0], p[1]);
        bar();
        if (RARE(l == 0)) raw_out(IC(2), 8);
        sfor<0, 2>([&](auto hd) {
          hedge_exp(p[hd], sce, ep, em, [&](auto e) {
            constexpr int i = hd * 16 + e;
            sum_mm(IC(i));
            if constexpr (i % 2 == 0 && 192 - QC + i / 2 < 34) ldp(IC(192 - QC + i / 2));
            if constexpr (i % 2 == 0 && i >= 16) pc_a(IC(108 + (i - 16) / 2));
          });
          if constexpr (hd == 1) {
            mdone(kvs);
            *(LAS bf16x8*)(sh + w * 2048 + lane * 16) = pack8<0>(kvs); *(LAS bf16x8*)(sh + w * 2048 + 1024 + lane * 16) = pack8<1>(kvs);
            SBAR();
          }
          pq[hd][0] = pack8<0>(ep); pq[hd][1] = pack8<1>(ep); pq[hd][2] = pack8<0>(em); pq[hd][3] = pack8<1>(em);
          pin4(pq[hd]);
        });
        STAMP(5);
        lds_bar();
        LAS const char* zb = sh + 8192 + (r & 1) * 256 + h * 32;
#pragma unroll
        for (int k = 0; k < 8; ++k) za[k] = *(LAS const bf16x8*)(zb + (k >> 1) * 64 + (k & 1) * 16);
#pragma unroll
        for (int k = 0; k < 8; ++k) ka[k] = *(LAS const bf16x8*)(sh + k * 1024 + lane * 16);
        SBAR();
        auto coef = [&](auto hd) {
          const float a = __builtin_amdgcn_rcpf(dn[2 * hd][1]), b = __builtin_amdgcn_rcpf(dn[2 * hd + 1][1]);
          const float inv = __builtin_amdgcn_rcpf(dn[2 * hd][0] * a + dn[2 * hd + 1][0] * b + 1e-6f);
          ai[hd] = opq_v(a * inv); bi[hd] = opq_v(b * inv);
        };
        auto fin = [&](auto hd) {
          f32x16 o;
#pragma unroll
          for (int i = 0; i < 16; ++i) o[i] = nm[2 * hd][i] * ai[hd] + nm[2 * hd + 1][i] * bi[hd];
          st4_1<hd * 2048>(__builtin_bit_cast(u32x4, pack8<0>(o)), c.xv, dst);
          st4_1<hd * 2048 + 1024>(__builtin_bit_cast(u32x4, pack8<1>(o)), c.xv, dst);
        };
        sfor<0, 8>([&](auto k) { mm<false, false, k % 2 == 0>(dn[k / 2], za[k], pq[k / 4][k % 4]); if constexpr (k % 2) pc_a(IC(116 + k / 2)); });
        sfor<0, 4>([&](auto k) { mm<false, false, k % 2 == 0>(nm[k / 2], ka[k], pq[0][k]); });
        mdone(dn[0], dn[1], dn[2], dn[3]);
        sfor<4, 8>([&](auto k) { mm<false, false, k % 2 == 0>(nm[k / 2], ka[k], pq[1][k - 4]); if constexpr (k == 4) coef(IC(0)); if constexpr (k == 6) coef(IC(1)); });
        STAMP(6);
        pin2(nm[0], nm[1]);
        fin(IC(0));
        pin2(nm[2], nm[3]);
        fin(IC(1));
        SBAR();
        sfor<120, 124>(pc_a);
        res_to_acc(xr, ra0, ra1);
        waitv<4>();
        set_flag(facto + FL_K, tag);
        sfor<124, 128>(pc_a); sfor<8, 16>(ld_park);
      }
    }
    STAMP(7);
    REFRESH();
    LAS char* reg = smem + 8 * SLOT;
    bf16x8 fu[16], cu;
    {
      f32x16 &a0 = ra0, &a1 = ra1;
      poll<16>(fact + FL_K, lane * 8, tag);
      sfor<0, 16>([&](auto i) { pin_a(park[i]); });
      STAMP(8);
      bf16x8 x[64];
      const char* xb = act + STG;
      sfor<0, XL>([&](auto s) { ld_x<s>(x, c, xb); });
      bar();
      asm volatile("" : "+s"(wkv));
      const unsigned kvv = (unsigned)((lane >> 1) * 64 + (cb & 1) * 32 + (lane & 1) * 16);
      const unsigned wa = ldsw + c.xv, wb = wa + 65536;
      auto unpark = [&](auto i) {
        constexpr int off = a_slot(26 + i / 4) * SLOT + (i % 4) * 1024;
        ds_write_a<(off & 65535)>(off < 65536 ? wa : wb, park[i]);
      };
      gemm64<true, false, HookP3>(c, x, xb, a0, a1, [&](auto s) {
        if constexpr (s == 33) bar();
        if constexpr (s >= 36 && s < 44) unpark(IC(s - 36));
        if constexpr (s == 49) bar();
        if constexpr (s >= 50 && s < 58) unpark(IC(s - 42));
        if constexpr (s >= 52 && s < 60) dma<0>(lds0 + 8 * SLOT + w * 8192 + (s - 52) * 1024, kvv, wkv + (s - 52) * 32 * 64);
        if constexpr (s >= 60) pc_w(4, a_slot(24), 0, IC(s - 60));
      }, [](auto) {});
      STAMP(9);
      store_x(c, r, a0, a1, xr, acto + 2 * STG, stto + 256 * 1024);
      asm volatile("s_waitcnt vmcnt(5) lgkmcnt(0)\n\ts_barrier" ::: "memory");
      { LAS const char* p = reg + w * 8192 + (4 * h + q4) * 32 + p4 * 8;
#pragma unroll
        for (int i = 0; i < 16; ++i) fu[i] = cat8(trread(p + i * 512), trread(p + i * 512 + 256));
        SBAR();
        float t0 = 0.f, t1 = 0.f, t2 = 0.f, t3 = 0.f;
#pragma unroll
        for (int i = 0; i < 16; ++i) {
          const u32x4 f = __builtin_bit_cast(u32x4, fu[i]);
          t0 = dot2(f[0], 0x3f803f80u, t0); t1 = dot2(f[1], 0x3f803f80u, t1); t2 = dot2(f[2], 0x3f803f80u, t2); t3 = dot2(f[3], 0x3f803f80u, t3);
        }
        cu = opq_v(cs_frag(swap_add((t0 + t1) + (t2 + t3)))); }
      waitv<0>();
      set_flag(facto + 2 * FL_K, tag);
      sfor<0, 4>([&](auto i) { pc_w(4, a_slot(25), 1, i); });
    }
    STAMP(10);
    REFRESH();
    f32x16 a0, a1;
    asm volatile("" : "+s"(kvf), "+s"(fkv), "+s"(xu_p), "+s"(su_p));
    {
      poll<16>(fact + 2 * FL_K, lane * 8, tag);
      bar();
      STAMP(11);
      const char* xb = act + 2 * STG; const char* stp = stt + 256 * 1024;
      bf16x8 x[64], xu[16], mf0, mf1;
      Red<false> su, sp;
      sfor<0, 4>([&](auto i) { ldx1<false, i * 1024>(su.p[i], c.xv, su_p); });
      sfor<0, 16>([&](auto i) { ldx1<true, (i % 8) * 1024 - 4096>(xu[i], c.xv, xu_p + (i / 8) * 8192); });
      f32x16 au, av;
      sfor<0, 16>([&](auto i) {
        waitx<true, 15 - i + (i < XL ? i : XL)>(xu[i]);
        SBAR();
        mm<false, true, i == 0>(au, fu[i], xu[i]);
        if constexpr (i < XL) ld_x<i>(x, c, xb);
        mm<true, true, i == 0>(av, fu[i], xu[i]);
        if constexpr (i < 11) su.template run<i>(lane);
        if constexpr (i == 12) mf0 = opq_v(mean_frag(su.a, h == 0));
        if constexpr (i == 14) { mm<false, false, false>(au, cu, mf0); mm<true, false, false>(av, cu, mf0); }
        SBAR();
      });
      STAMP(12);
      u64 fa;
      f32x4 t[4], ru;
      unsigned vo;
      gemm64<true, true, HookP4>(c, x, xb, a0, a1, [&](auto s) {
        if constexpr (s == 1) {
          pin2(au, av);
#pragma unroll
          for (int gq = 0; gq < 6; ++gq) {
            const f32x16& a = gq < 4 ? av : au;
            *(LAS f32x4*)(reg + w * 8192 + gq * 1024 + lane * 16) = f32x4{a[4 * (gq & 3)], a[4 * (gq & 3) + 1], a[4 * (gq & 3) + 2], a[4 * (gq & 3) + 3]};
          }
        }
        if constexpr (s == 3) {
          lds_bar();
#pragma unroll
          for (int j = 0; j < 4; ++j) t[j] = *(LAS const f32x4*)(reg + j * 8192 + (ty < 2 ? 4 + (w & 1) : w) * 1024 + lane * 16);
        }
        if constexpr (s == 6) st2_1(__builtin_bit_cast(f32x2, u32x2{cvtpk(t[0][0], t[0][1]), cvtpk(t[0][2], t[0][3])}), vo, kvf);
        if constexpr (s >= 8 && s < 12) ldx1<false, (s - 8) * 1024>(sp.p[s - 8], c.xv, stp);
        if constexpr (s == 7 + XL) { bar(); if (RARE(w == 0)) set_flag(fkv + rb * 4 * FL_I + cb * 8, tag); }
        if constexpr (s >= 14 && s < 30 && s % 2 == 0) pc_w(4, a_slot(30 + (s - 14) / 8), 6 + (s - 14) / 8, IC(((s - 14) / 2) % 4));
        if constexpr (s == 44) bar();
        if constexpr (s == 52) ld8_1(fa, (lane & 15) * 8 + (w * 4 + (lane >> 4)) * 4 * FL_I, fkv);
      }, [&](auto g) {
        if constexpr (g == 0) {
#pragma unroll
          for (int e = 0; e < 4; ++e) ru[e] = lane_get(su.b, ty < 2 ? r : 8 * w + 4 * h + e);
        }
        if constexpr (g == 4) {
          const unsigned m = (unsigned)(ty >> 1) - 1u;
          vo = opq_v(kvb + (((unsigned)lane * 16 & m) | ((unsigned)((r & 15) * 16 + h * 8) & ~m)));
        }
        if constexpr (g >= 9 && g < 13) {
#pragma unroll
          for (int e = 0; e < 4; ++e) { float v = t[0][e]; if constexpr (g < 12) a_add(v, t[g - 8][e]); else a_mul(v, ru[e]); t[0][e] = v; }
        }
        if constexpr (g >= 48 && g < 59) sp.template run<g - 48>(lane);
        if constexpr (g == 60) mf0 = opq_v(mean_frag(sp.a, h == 0));
        if constexpr (g == 61) mf1 = opq_v(mean_frag(sp.a, h == 1));
        if constexpr (g == 64) mm<false, false, false>(a0, cw.p[0], mf0);
        if constexpr (g == 65) mm<false, false, false>(a1, cw.p[0], mf1);
      });
      rstd = sp.b;
      waitx<false, 0>(fa);
      if (RARE(!__all(fa == tag))) {
#pragma unroll 1
      for (int spin = 0; spin < (1 << 15) && !__all(fa == tag); ++spin) { __builtin_amdgcn_s_sleep(8); ld8_1(fa, (lane & 15) * 8 + (w * 4 + (lane >> 4)) * 4 * FL_I, fkv); waitx<false, 0>(fa); }
      }
      bar();
      STAMP(13);
    }
    STAMP(14);
    {
      u32x16 qq, misc;
      misc[14] = misc[15] = __float_as_uint(rstd * QSCALE);
      int attempt = 0;
#pragma unroll 1
      for (;;) {
        f32x16 oa, ob, t0, t1, t2, t3;
        u32x16 kvr; u32x4 offs;
        offs[0] = c.xv;
        const char* ks = kvf + 2048 + w * 2048; const char* wsr = wt(5, 0);
        unsigned wm0 = ldsw, cnt = 8;
        asm volatile(
            "global_load_dwordx4 v[208:211], v240, %[kv] sc1\n\tglobal_load_dwordx4 v[212:215], v240, %[kv] offset:1024 sc1\n\t"
            "s_mov_b32 m0, s44\n\tv_add_u32 v241, 0x2000, v240\n\t"
            "global_load_lds_dwordx4 v240, s[40:41] sc1\n\tglobal_load_lds_dwordx4 v240, s[40:41] offset:1024 sc1\n\t"
            "s_add_u32 m0, s44, 0x2000\n\tv_add_u32 v242, 0x4000, v240\n\t"
            "global_load_lds_dwordx4 v241, s[40:41] sc1\n\tglobal_load_lds_dwordx4 v241, s[40:41] offset:1024 sc1\n\t"
            "s_add_u32 m0, s44, 0x4000\n\tv_add_u32 v243, 0x6000, v240\n\t"
            "global_load_lds_dwordx4 v242, s[40:41] sc1\n\tglobal_load_lds_dwordx4 v242, s[40:41] offset:1024 sc1\n\t"
            "s_add_u32 s40, s40, 0x6000\n\ts_addc_u32 s41, s41, 0\n\t"
            "s_cmp_lg_u32 %[att], 0\n\ts_cbranch_scc1 2f\n\t"
            "v_pk_mul_f32 v[128:129], v[128:129], v[238:239]\n\t"
            "v_pk_mul_f32 v[130:131], v[130:131], v[238:239]\n\t"
            "v_pk_mul_f32 v[132:133], v[132:133], v[238:239]\n\t"
            "v_pk_mul_f32 v[134:135], v[134:135], v[238:239]\n\t"
            "v_pk_mul_f32 v[136:137], v[136:137], v[238:239]\n\t"
            "v_pk_mul_f32 v[138:139], v[138:139], v[238:239]\n\t"
            "v_pk_mul_f32 v[140:141], v[140:141], v[238:239]\n\t"
            "v_pk_mul_f32 v[142:143], v[142:143], v[238:239]\n\t"
            "v_pk_mul_f32 v[144:145], v[144:145], v[238:239]\n\t"
            "v_pk_mul_f32 v[146:147], v[146:147], v[238:239]\n\t"
            "v_pk_mul_f32 v[148:149], v[148:149], v[238:239]\n\t"
            "v_pk_mul_f32 v[150:151], v[150:151], v[238:239]\n\t"
            "v_pk_mul_f32 v[152:153], v[152:153], v[238:239]\n\t"
            "v_pk_mul_f32 v[154:155], v[154:155], v[238:239]\n\t"
            "v_pk_mul_f32 v[156:157], v[156:157], v[238:239]\n\t"
            "v_pk_mul_f32 v[158:159], v[158:159], v[238:239]\n\t"
            "v_cvt_pk_bf16_f32 v192, v128, v129\n\t"
            "v_cvt_pk_bf16_f32 v193, v130, v131\n\t"
            "v_cvt_pk_bf16_f32 v194, v132, v133\n\t"
            "v_cvt_pk_bf16_f32 v195, v134, v135\n\t"
            "v_cvt_pk_bf16_f32 v196, v136, v137\n\t"
            "v_cvt_pk_bf16_f32 v197, v138, v139\n\t"
            "v_cvt_pk_bf16_f32 v198, v140, v141\n\t"
            "v_cvt_pk_bf16_f32 v199, v142, v143\n\t"
            "v_cvt_pk_bf16_f32 v200, v144, v145\n\t"
            "v_cvt_pk_bf16_f32 v201, v146, v147\n\t"
            "v_cvt_pk_bf16_f32 v202, v148, v149\n\t"
            "v_cvt_pk_bf16_f32 v203, v150, v151\n\t"
            "v_cvt_pk_bf16_f32 v204, v152, v153\n\t"
            "v_cvt_pk_bf16_f32 v205, v154, v155\n\t"
            "v_cvt_pk_bf16_f32 v206, v156, v157\n\t"
            "v_cvt_pk_bf16_f32 v207, v158, v159\n\t"
            "2:\n\t"
            "v_mov_b64 v[64:65], 0\n\t"
            "v_mov_b64 v[66:67], 0\n\t"
            "v_mov_b64 v[68:69], 0\n\t"
            "v_mov_b64 v[70:71], 0\n\t"
            "v_mov_b64 v[72:73], 0\n\t"
            "v_mov_b64 v[74:75], 0\n\t"
            "v_mov_b64 v[76:77], 0\n\t"
            "v_mov_b64 v[78:79], 0\n\t"
            "v_mov_b64 v[80:81], 0\n\t"
            "v_mov_b64 v[82:83], 0\n\t"
            "v_mov_b64 v[84:85], 0\n\t"
            "v_mov_b64 v[86:87], 0\n\t"
            "v_mov_b64 v[88:89], 0\n\t"
            "v_mov_b64 v[90:91], 0\n\t"
            "v_mov_b64 v[92:93], 0\n\t"
            "v_mov_b64 v[94:95], 0\n\t"
            "v_mov_b64 v[176:177], 0\n\t"
            "v_mov_b64 v[178:179], 0\n\t"
            "v_mov_b64 v[180:181], 0\n\t"
            "v_mov_b64 v[182:183], 0\n\t"
            "v_mov_b64 v[184:185], 0\n\t"
            "v_mov_b64 v[186:187], 0\n\t"
            "v_mov_b64 v[188:189], 0\n\t"
            "v_mov_b64 v[190:191], 0\n\t"
            "v_mov_b64 v[216:217], 0\n\t"
            "v_mov_b64 v[218:219], 0\n\t"
            "v_mov_b64 v[220:221], 0\n\t"
            "v_mov_b64 v[222:223], 0\n\t"
            "v_mov_b64 v[228:229], 0\n\t"
            "v_mov_b64 v[230:231], 0\n\t"
            "v_mov_b64 v[232:233], 0\n\t"
            "v_mov_b64 v[234:235], 0\n\t"
            "v_bfe_u32 v226, v240, 6, 1\n\tv_mov_b32 v236, 0xdddddddd\n\tv_mul_u32_u24 v226, 0x3f80, v226\n\tv_add_u32 v237, %[rg], v240\n\t"
            "v_lshl_or_b32 v226, v226, 16, v226\n\tv_mov_b32 v227, v226\n\tv_xor_b32 v224, 0x3f803f80, v226\n\tv_mov_b32 v225, v224\n\t"
            "s_waitcnt vmcnt(6)\n\ts_cbranch_scc1 3f\n\t"
            "v_mfma_f32_32x32x16_bf16 v[128:143], v[208:211], v[192:195], 0\n\tv_mfma_f32_32x32x16_bf16 v[160:175], v[208:211], v[200:203], 0\n\t"
            "v_mfma_f32_32x32x16_bf16 v[128:143], v[212:215], v[196:199], v[128:143]\n\tv_mfma_f32_32x32x16_bf16 v[160:175], v[212:215], v[204:207], v[160:175]\n\t"
            "s_nop 11\n\t"
            "v_max3_f32 v238, v128, v129, v130\n\tv_max3_f32 v239, v160, v161, v162\n\t"
            "v_max3_f32 v238, v238, v131, v132\n\tv_max3_f32 v239, v239, v163, v164\n\t"
            "v_max3_f32 v238, v238, v133, v134\n\tv_max3_f32 v239, v239, v165, v166\n\t"
            "v_max3_f32 v238, v238, v135, v136\n\tv_max3_f32 v239, v239, v167, v168\n\t"
            "v_max3_f32 v238, v238, v137, v138\n\tv_max3_f32 v239, v239, v169, v170\n\t"
            "v_max3_f32 v238, v238, v139, v140\n\tv_max3_f32 v239, v239, v171, v172\n\t"
            "v_max3_f32 v238, v238, v141, v142\n\tv_max3_f32 v239, v239, v173, v174\n\t"
            "v_max3_f32 v238, v238, v143, v143\n\tv_max3_f32 v239, v239, v175, v175\n\t"
            "v_mov_b32 v96, v238\n\tv_mov_b32 v97, v239\n\ts_nop 1\n\tv_permlane32_swap_b32 v238, v96\n\tv_permlane32_swap_b32 v239, v97\n\t"
            "v_max_f32 v238, v238, v96\n\tv_max_f32 v239, v239, v97\n\t"
            "3:\n\t"
            "v_xor_b32 v96, 0x80000000, v238\n\tv_xor_b32 v112, 0x80000000, v239\n\tv_mov_b32 v97, v96\n\tv_mov_b32 v113, v112\n\t"
            "v_mov_b64 v[98:99], v[96:97]\n\tv_mov_b64 v[114:115], v[112:113]\n\t"
            "v_mov_b64 v[100:101], v[96:97]\n\tv_mov_b64 v[116:117], v[112:113]\n\t"
            "v_mov_b64 v[102:103], v[96:97]\n\tv_mov_b64 v[118:119], v[112:113]\n\t"
            "v_mov_b64 v[104:105], v[96:97]\n\tv_mov_b64 v[120:121], v[112:113]\n\t"
            "v_mov_b64 v[106:107], v[96:97]\n\tv_mov_b64 v[122:123], v[112:113]\n\t"
            "v_mov_b64 v[108:109], v[96:97]\n\tv_mov_b64 v[124:125], v[112:113]\n\t"
            "v_mov_b64 v[110:111], v[96:97]\n\tv_mov_b64 v[126:127], v[112:113]\n\t"
            "s_nop 1\n\t" S0_(128, 192, 96) S1_(128, 192)
            "s_waitcnt vmcnt(4)\n\ts_barrier\n\ts_nop 8\n\t"
            "1:\n\t"
            STEP_(0, 24576, 240, 0, "s_sub_u32 %[cnt], %[cnt], 1\n\t", "") STEP_(8192, 0, 241, 1024, "", "") STEP_(16384, 8192, 242, 2048, "", "")
            STEP_(24576, 16384, 243, 3072, "s_add_u32 s40, s40, 0x8000\n\ts_addc_u32 s41, s41, 0\n\t", "s_add_u32 s42, s42, 0x4000\n\ts_addc_u32 s43, s43, 0\n\t")
            "s_add_u32 s45, s45, 0x4000\n\ts_cmp_lg_u32 %[cnt], 0\n\ts_cbranch_scc1 1b\n\t"
            L0_(232, 176) L1_(232, 176) PV0_(80, 176) PV1_(80, 176)
            "s_nop 11\n\t"
            : "=&{v[64:79]}"(oa), "=&{v[80:95]}"(ob), "=&{v[96:111]}"(t0), "=&{v[112:127]}"(t1), "+{v[128:143]}"(a0), "+{v[144:159]}"(a1), "=&{v[160:175]}"(t2),
              "=&{v[176:191]}"(t3), "+{v[192:207]}"(qq), "=&{v[208:223]}"(kvr), "+{v[224:239]}"(misc), "+{v[240:243]}"(offs),
              "+{s[40:41]}"(ks), "+{s[42:43]}"(wsr), "+{s45}"(wm0), [cnt] "+s"(cnt)
            : "{s44}"(lds0 + 8 * SLOT + w * 2048), [kv] "s"(kvf), [att] "s"(attempt), [rg] "s"(lds0 + 8 * SLOT)
            : "scc", "memory");
        const float la = __uint_as_float(misc[4]), lb = __uint_as_float(misc[8]);
        const float ia = __builtin_amdgcn_rcpf(la), ib = __builtin_amdgcn_rcpf(lb);
#pragma unroll
        for (int e = 0; e < 16; ++e) { oa[e] *= ia; ob[e] *= ib; }
        char* dst = acto + 3 * STG;
        st4_1<0>(__builtin_bit_cast(u32x4, pack8<0>(oa)), c.xv, dst); st4_1<1024>(__builtin_bit_cast(u32x4, pack8<1>(oa)), c.xv, dst);
        st4_1<2048>(__builtin_bit_cast(u32x4, pack8<0>(ob)), c.xv, dst); st4_1<3072>(__builtin_bit_cast(u32x4, pack8<1>(ob)), c.xv, dst);
        SBAR();
        f32x2 c0 = {}, c1 = {};
#pragma unroll
        for (int e = 0; e < 16; e += 2) { c0 += f32x2{oa[e], oa[e + 1]} * 0.f; c1 += f32x2{ob[e], ob[e + 1]} * 0.f; }
        c0 += c1;
        const bool bad = !(la < 1.2676506e30f) || !(lb < 1.2676506e30f) || !(c0[0] + c0[1] == 0.f);
        LAS volatile int* vote = (LAS volatile int*)(smem + 8 * SLOT + 24576);
        vote[w] = attempt == 0 && __any(bad);
        lds_bar();
        const u32x4 vt = *(LAS const u32x4*)vote;
        if (!RARE(__builtin_amdgcn_readfirstlane(vt[0] | vt[1] | vt[2] | vt[3]))) break;
        all_bar();
        {
          const bf16x8 qa0 = __builtin_bit_cast(bf16x8, __builtin_shufflevector(qq, qq, 0, 1, 2, 3)), qa1 = __builtin_bit_cast(bf16x8, __builtin_shufflevector(qq, qq, 4, 5, 6, 7));
          const bf16x8 qb0 = __builtin_bit_cast(bf16x8, __builtin_shufflevector(qq, qq, 8, 9, 10, 11)), qb1 = __builtin_bit_cast(bf16x8, __builtin_shufflevector(qq, qq, 12, 13, 14, 15));
          float mA = -INFINITY, mB = -INFINITY;
          for (int t = 0; t < 64; ++t) {
            bf16x8 k0, k1;
            ldx1<false, 0>(k0, c.xv, kvf + t * 4096); ldx1<false, 1024>(k1, c.xv, kvf + t * 4096);
            wait2<0>(k0, k1);
            mm<false, false, true>(a0, k0, qa0); mm<false, false, false>(a0, k1, qa1); mm<false, false, true>(a1, k0, qb0); mm<false, false, false>(a1, k1, qb1);
            mdone(a0, a1);
            mA = fmaxf(mA, max16(a0)); mB = fmaxf(mB, max16(a1));
          }
          misc[14] = __float_as_uint(swap_max(mA)); misc[15] = __float_as_uint(swap_max(mB));
        }
        attempt = 1;
      }
      waitv<0>();
      set_flag(facto + 3 * FL_K, tag);
      res_to_acc(xr, ra0, ra1);
    }
    STAMP(15);
    REFRESH();
    {
      f32x16 &a0 = ra0, &a1 = ra1;
      poll<16>(fact + 3 * FL_K, lane * 8, tag);
      STAMP(16);
      bf16x8 x[64];
      const char* xb = act + 3 * STG;
      sfor<0, XL>([&](auto s) { ld_x<s>(x, c, xb); });
      bar();
      gemm64<false, false, HookP6>(c, x, xb, a0, a1, [&](auto s) {
        if constexpr (s == 33) bar();
        if constexpr (s >= 52) pc_a(IC(s - 52));
      }, [](auto) {});
      STAMP(17);
      if (l == 0) {
        store_x(c, r, a0, a1, xr, acto + 4 * STG, stto + 512 * 1024);
        bar();
        sfor<12, 16>(pc_a);
        waitv<4>();
        set_flag(facto + 4 * FL_K, tag);
        sfor<16, 24>(pc_a);
      } else {
        LAS char* im = smem + 8 * SLOT + w * 4096;
        sfor<0, 8>([&](auto n) {
          constexpr int g = n;
          const f32x16& a = g < 4 ? a0 : a1;
          *(LAS u32x2*)(im + r * 128 + ((g ^ (r & 7)) << 4) + h * 8) = u32x2{cvtpk(a[4 * (g & 3)], a[4 * (g & 3) + 1]), cvtpk(a[4 * (g & 3) + 2], a[4 * (g & 3) + 3])};
        });
        char* dst = out + (size_t)tt * 65536 + cb * 128;
        sfor<0, 4>([&](auto i) {
          const u32x4 v = *(LAS const u32x4*)(im + i * 1024 + lane * 16);
          st4_1<0>(v, (unsigned)((i * 8 + (lane >> 3)) * 2048 + (((lane & 7) ^ (lane >> 3)) << 4)), dst);
        });
        if (tt == 0 && cb == 0) set_flag(ws + WS_EPOCH, tag);
      }
    }
    STAMP(18);
    if (l == 1) break;
    csm = 1;
    act += 4 * STG; acto += 4 * STG; fact += 4 * FL_K; facto += 4 * FL_K; stt += 512 * KB; stto += 512 * KB;
    kvp += 131072; zp += 8192; fkvp += FL_K; kvf += 320 * KB; fkv += FL_K; xu_p += 4 * STG; su_p += 512 * KB;
    l = 1;
    REFRESH();
    STAMP(0);
        poll<16>(fact, lane * 8, tag);
        STAMP(1);
        sfor<0, XL>(issue_x);
        wait_bar<XL>();
        sfor<0, 2>([&](auto s) { rd01(s, IC(0), std::false_type{}); rd01(s, IC(1), std::false_type{}); });
        sfor<0, 64>([&](auto s) { step(s, std::false_type{}); });
        asm volatile("s_mov_b32 m0, %0\n\ts_mov_b64 exec, 1\n\t.rept 8\n\tglobal_load_lds_dwordx4 %1, %2\n\t.endr\n\ts_mov_b64 exec, -1\n.Lk_main_tail:" ::"s"(lds0 + 9 * SLOT + 4096), "v"(c.xv), "s"(wph) : "memory");
  }
}

extern "C" void kernel_launch(void* const* d_in, const int* in_sizes, int n_in, void* d_out, int out_size, void* d_ws, size_t ws_size, hipStream_t stream) {
  k_main<<<256, 256, 0, stream>>>((const char*)d_in[0], (const char*)d_in[1], (const char*)d_in[2], (const char*)d_in[3], (const char*)d_in[5], (const char*)d_in[6],
                                  (const char*)d_in[7], (const char*)d_in[8], (char*)d_ws, (char*)d_out);
}
```

```cpp
#include <hip/hip_runtime.h>
#include <hip/hip_bf16.h>
#include <stdint.h>
#include <type_traits>

typedef __attribute__((ext_vector_type(8))) short bf16x8;
typedef __attribute__((ext_vector_type(4))) short s16x4;
typedef __attribute__((ext_vector_type(2))) __bf16 bf2_t;
typedef __attribute__((ext_vector_type(16))) float f32x16;
typedef __attribute__((ext_vector_type(4))) float f32x4;
typedef __attribute__((ext_vector_type(2))) float f32x2;
typedef __attribute__((ext_vector_type(4))) unsigned u32x4;
typedef __attribute__((ext_vector_type(2))) unsigned u32x2;
#define LAS __attribute__((address_space(3)))
#define DI __device__ __forceinline__
#define MFMA32(a, b, c) __builtin_amdgcn_mfma_f32_32x32x16_bf16((a), (b), (c), 0, 0, 0)
#define MFMA16(a, b, c) __builtin_amdgcn_mfma_f32_16x16x32_bf16((a), (b), (c), 0, 0, 0)
#define SBAR() __builtin_amdgcn_sched_barrier(0)

constexpr int S_ = 2048, D_ = 1024;
constexpr float LOG2E = 1.4426950408889634f;
constexpr float QSCALE = 0.17677669529663687f * LOG2E;

constexpr size_t MB = 1u << 20, KB = 1024;
constexpr size_t STG = 256 * KB;
constexpr size_t WS_COL = 32 * MB;
constexpr size_t C_WT = 0;
constexpr size_t C_KVP = 768 * KB;
constexpr size_t C_ZP = 1024 * KB;
constexpr size_t C_CS = 1040 * KB;
constexpr size_t WS_ST = 64 * MB;
constexpr size_t WS_KVF = WS_ST + 1024 * KB;
constexpr size_t WS_FLAG = WS_ST + 1664 * KB;
constexpr size_t WS_EPOCH = WS_ST + 1920 * KB;
constexpr int FL_I = 2048, FL_K = 128;
typedef unsigned long long u64;

template <int I, int N, class F> DI void sfor(F&& f) { if constexpr (I < N) { f(std::integral_constant<int, I>{}); sfor<I + 1, N>(f); } }

DI unsigned cvtpk(float lo, float hi) { f32x2 v = {lo, hi}; return __builtin_bit_cast(unsigned, __builtin_convertvector(v, bf2_t)); }
DI float bflo(unsigned u) { return __uint_as_float(u << 16); }
DI float bfhi(unsigned u) { return __uint_as_float(u & 0xffff0000u); }
DI float rbf(float x) { return bflo(cvtpk(x, x)); }
DI float swap_add(float v) { auto r = __builtin_amdgcn_permlane32_swap(__float_as_uint(v), __float_as_uint(v), false, false); return __uint_as_float(r[0]) + __uint_as_float(r[1]); }
DI float swap_max(float v) { auto r = __builtin_amdgcn_permlane32_swap(__float_as_uint(v), __float_as_uint(v), false, false); return fmaxf(__uint_as_float(r[0]), __uint_as_float(r[1])); }
DI float swap_min(float v) { auto r = __builtin_amdgcn_permlane32_swap(__float_as_uint(v), __float_as_uint(v), false, false); return fminf(__uint_as_float(r[0]), __uint_as_float(r[1])); }
template <int S> DI bf16x8 pack8(const f32x16& v) {
  u32x4 p = {cvtpk(v[8 * S], v[8 * S + 1]), cvtpk(v[8 * S + 2], v[8 * S + 3]), cvtpk(v[8 * S + 4], v[8 * S + 5]), cvtpk(v[8 * S + 6], v[8 * S + 7])};
  return __builtin_bit_cast(bf16x8, p);
}
DI u32x4 unperm(u32x4 v) {
  auto r0 = __builtin_amdgcn_permlane32_swap(v[0], v[2], false, false);
  auto r1 = __builtin_amdgcn_permlane32_swap(v[1], v[3], false, false);
  return u32x4{r0[0], r1[0], r0[1], r1[1]};
}
DI float lane_get(float v, int src) { return __uint_as_float(__builtin_amdgcn_ds_bpermute(src << 2, __float_as_uint(v))); }
DI s16x4 trread(LAS const char* p) { return __builtin_amdgcn_ds_read_tr16_b64_v4i16((LAS s16x4*)p); }
DI bf16x8 cat8(s16x4 a, s16x4 b) { return __builtin_shufflevector(a, b, 0, 1, 2, 3, 4, 5, 6, 7); }
DI float dot2(unsigned a, unsigned b, float c) { return __builtin_amdgcn_fdot2_f32_bf16(__builtin_bit_cast(bf2_t, a), __builtin_bit_cast(bf2_t, b), c, false); }

template <int OFF> DI void dma(unsigned lds, unsigned v, const char* sb) {
  asm volatile("s_mov_b32 m0, %0\n\ts_nop 0\n\tglobal_load_lds_dwordx4 %1, %2 offset:%3" ::"s"(lds), "v"(v), "s"(sb), "i"(OFF) : "memory");
}
template <int OFF> DI void dma_c(unsigned lds, unsigned v, const char* sb) {
  asm volatile("s_mov_b32 m0, %0\n\ts_nop 0\n\tglobal_load_lds_dwordx4 %1, %2 offset:%3 sc1" ::"s"(lds), "v"(v), "s"(sb), "i"(OFF) : "memory");
}
template <int OFF> DI void dma_n(unsigned v, const char* sb) {
  asm volatile("global_load_lds_dwordx4 %0, %1 offset:%2 sc1" ::"v"(v), "s"(sb), "i"(OFF) : "memory");
}
constexpr int cap63(int n) { return n > 63 ? 63 : n; }
template <int N> DI void wait_bar() { asm volatile("s_waitcnt vmcnt(%0)\n\ts_barrier" ::"i"(cap63(N)) : "memory"); }
DI void lds_bar() { asm volatile("s_waitcnt lgkmcnt(0)\n\ts_barrier" ::: "memory"); }
DI void all_bar() { asm volatile("s_waitcnt vmcnt(0) lgkmcnt(0)\n\ts_barrier" ::: "memory"); }
DI void bar() { asm volatile("s_barrier" ::: "memory"); }
template <bool AG, int OFF> DI void ldx1(bf16x8& d, unsigned voff, const char* sb) {
  if constexpr (AG) asm volatile("global_load_dwordx4 %0, %1, %2 offset:%3 sc1" : "=a"(d) : "v"(voff), "s"(sb), "i"(OFF));
  else asm volatile("global_load_dwordx4 %0, %1, %2 offset:%3 sc1" : "=v"(d) : "v"(voff), "s"(sb), "i"(OFF));
}
template <int OFF> DI void ld2_1(f32x2& d, unsigned voff, const char* sb) { asm volatile("global_load_dwordx2 %0, %1, %2 offset:%3 sc1" : "=v"(d) : "v"(voff), "s"(sb), "i"(OFF)); }
DI void ld1_1(unsigned& d, unsigned voff, const void* sb) { asm volatile("global_load_dword %0, %1, %2 sc1" : "=v"(d) : "v"(voff), "s"(sb) : "memory"); }
template <int OFF> DI void st4_1(u32x4 d, unsigned voff, char* sb) { asm volatile("global_store_dwordx4 %0, %1, %2 offset:%3 sc1\n\ts_nop 1" ::"v"(voff), "v"(d), "s"(sb), "i"(OFF) : "memory"); }
DI void st2_1(f32x2 d, unsigned voff, char* sb) { asm volatile("global_store_dwordx2 %0, %1, %2 sc1" ::"v"(voff), "v"(d), "s"(sb) : "memory"); }
DI void st1_1(float d, unsigned voff, char* sb) { asm volatile("global_store_dword %0, %1, %2 sc1" ::"v"(voff), "v"(d), "s"(sb) : "memory"); }
template <int N> DI void waitv() { asm volatile("s_waitcnt vmcnt(%0)" ::"i"(cap63(N)) : "memory"); }
template <bool AG, int N, class T> DI void waitx(T& d) {
  if constexpr (AG) asm volatile("s_waitcnt vmcnt(%1)" : "+a"(d) : "i"(cap63(N))); else asm volatile("s_waitcnt vmcnt(%1)" : "+v"(d) : "i"(cap63(N)));
}
template <bool SW, bool BA, bool Z, bool NOP = false> DI void mm(f32x16& acc, const bf16x8& a, const bf16x8& b) {
#define MM_(OPS, C, OUT, BC) do { if constexpr (NOP) asm volatile("s_nop 1\n\tv_mfma_f32_32x32x16_bf16 %0, " OPS ", " C : OUT(acc) : "v"(a), BC(b)); \
                                  else asm volatile("v_mfma_f32_32x32x16_bf16 %0, " OPS ", " C : OUT(acc) : "v"(a), BC(b)); } while (0)
  if constexpr (Z) {
    if constexpr (SW) { if constexpr (BA) MM_("%2, %1", "0", "=&v", "a"); else MM_("%2, %1", "0", "=&v", "v"); }
    else { if constexpr (BA) MM_("%1, %2", "0", "=&v", "a"); else MM_("%1, %2", "0", "=&v", "v"); }
  } else {
    if constexpr (SW) { if constexpr (BA) MM_("%2, %1", "%0", "+v", "a"); else MM_("%2, %1", "%0", "+v", "v"); }
    else { if constexpr (BA) MM_("%1, %2", "%0", "+v", "a"); else MM_("%1, %2", "%0", "+v", "v"); }
  }
#undef MM_
}
DI void mdone(f32x16& a) { asm volatile("s_nop 11" : "+v"(a)); }
DI void mdone(f32x16& a, f32x16& b) { asm volatile("s_nop 11" : "+v"(a), "+v"(b)); }
DI void mdone(f32x16& a, f32x16& b, f32x16& c, f32x16& d) { asm volatile("s_nop 11" : "+v"(a), "+v"(b), "+v"(c), "+v"(d)); }
template <class T> DI T opq_v(T v) { asm volatile("" : "+v"(v)); return v; }
template <int OFF> DI void ldx0(bf16x8& d, unsigned voff, const char* sb) { asm volatile("global_load_dwordx4 %0, %1, %2 offset:%3" : "=v"(d) : "v"(voff), "s"(sb), "i"(OFF)); }
template <int OFF> DI void ldx1m(bf16x8& d, unsigned voff, const char* sb, unsigned long long m) {
  asm volatile("s_mov_b64 exec, %4\n\tglobal_load_dwordx4 %0, %1, %2 offset:%3 sc1\n\ts_mov_b64 exec, -1" : "=v"(d) : "v"(voff), "s"(sb), "i"(OFF), "s"(m));
}
template <bool AG, int OFF8> DI void ds_read2(bf16x8& d, unsigned addr) {
  addr += OFF8 * 8;
  if constexpr (AG) asm volatile("ds_read2_b64 %0, %1 offset1:2" : "=a"(d) : "v"(addr) : "memory"); else asm volatile("ds_read2_b64 %0, %1 offset1:2" : "=v"(d) : "v"(addr) : "memory");
}
template <bool AG, int N> DI void waitl(bf16x8& d) {
  if constexpr (AG) asm volatile("s_waitcnt lgkmcnt(%1)" : "+a"(d) : "i"(N) : "memory"); else asm volatile("s_waitcnt lgkmcnt(%1)" : "+v"(d) : "i"(N) : "memory");
}
template <int OFF> DI void ds_write_a(unsigned addr, const bf16x8& v) { asm volatile("ds_write_b128 %0, %1 offset:%2" ::"v"(addr), "a"(v), "i"(OFF) : "memory"); }
template <int N, class T> DI void wait2(T& a, T& b) { asm volatile("s_waitcnt vmcnt(%2)" : "+v"(a), "+v"(b) : "i"(cap63(N))); }
DI void set_flag(char* flag, u64 tag) {
  unsigned z = 0;
  asm volatile("s_mov_b64 exec, 1\n\tglobal_atomic_swap_x2 %0, %1, %2 sc1\n\ts_mov_b64 exec, -1" ::"v"(z), "v"(tag), "s"(flag) : "memory");
}
DI void ld8_1(u64& d, unsigned voff, const char* sb) { asm volatile("global_load_dwordx2 %0, %1, %2 sc1" : "=v"(d) : "v"(voff), "s"(sb) : "memory"); }
template <int NL> DI void rd_flag(u64& v, unsigned voff, const char* sb) {
  const u64 zero = 0;
  if constexpr (NL == 64) asm volatile("global_atomic_or_x2 %0, %1, %3, %2 sc0 sc1" : "+v"(v) : "v"(voff), "s"(sb), "v"(zero) : "memory");
  else asm volatile("s_mov_b64 exec, %4\n\tglobal_atomic_or_x2 %0, %1, %3, %2 sc0 sc1\n\ts_mov_b64 exec, -1" : "+v"(v) : "v"(voff), "s"(sb), "v"(zero), "s"((1ull << (NL & 63)) - 1) : "memory");
}
template <int NL> DI void poll(const char* base, unsigned voff, u64 tag) {
  for (int spin = 0; spin < (1 << 15); ++spin) {
    u64 v = tag;
    rd_flag<NL>(v, voff, base);
    waitx<false, 0>(v);
    if (__all(v == tag)) break;
  }
  asm volatile("" ::: "memory");
}

template <int RBX> DI void block_map(int& rb, int& cb) {
  const int x = blockIdx.x & 7, i = blockIdx.x >> 3;
  constexpr int CBX = 32 / RBX, GC = 16 / CBX;
  rb = RBX * (x / GC) + i / CBX; cb = CBX * (x % GC) + i % CBX;
}

DI float a_fma(float a, float b, float c) { float d; asm volatile("v_fma_f32 %0, %1, %2, %3" : "=v"(d) : "v"(a), "v"(b), "v"(c)); return d; }
DI float a_fms(float a, float b, float c) { float d; asm volatile("v_fma_f32 %0, %1, -%2, %3" : "=v"(d) : "v"(a), "v"(b), "v"(c)); return d; }
DI void a_exp(float& x) { asm volatile("v_exp_f32 %0, %0" : "+v"(x)); }
DI void a_add(float& s, float x) { asm volatile("v_add_f32 %0, %0, %1" : "+v"(s) : "v"(x)); }
DI void a_mul(float& x, float s) { asm volatile("v_mul_f32 %0, %0, %1" : "+v"(x) : "v"(s)); }
template <bool NEG> DI void a_hedge(float& c, float& n, float& s, float prev, float p, float sc, float m) {
  if constexpr (NEG) asm volatile("v_exp_f32 %0, %0\n\tv_fma_f32 %1, %4, -%5, %6\n\tv_add_f32 %2, %2, %3" : "+v"(c), "=&v"(n), "+v"(s) : "v"(prev), "v"(p), "v"(sc), "v"(m));
  else asm volatile("v_exp_f32 %0, %0\n\tv_fma_f32 %1, %4, %5, %6\n\tv_add_f32 %2, %2, %3" : "+v"(c), "=&v"(n), "+v"(s) : "v"(prev), "v"(p), "v"(sc), "v"(m));
}
DI void a_mul4(float& a, float& b, float& c, float& d, float s) {
  asm volatile("v_mul_f32 %0, %0, %4\n\tv_mul_f32 %1, %1, %4\n\tv_mul_f32 %2, %2, %4\n\tv_mul_f32 %3, %3, %4" : "+v"(a), "+v"(b), "+v"(c), "+v"(d) : "v"(s));
}
DI void a_sum16(f32x4& s, const bf16x8& sel, const bf16x8& f) { asm volatile("v_mfma_f32_16x16x32_bf16 %0, %1, %2, %0" : "+v"(s) : "v"(sel), "v"(f)); }
DI void a_mm4(float& mx, float& mn, float a, float b, float c, float d) {
  asm volatile("v_max3_f32 %0, %0, %2, %3\n\tv_min3_f32 %1, %1, %2, %3\n\tv_max3_f32 %0, %0, %4, %5\n\tv_min3_f32 %1, %1, %4, %5" : "+v"(mx), "+v"(mn) : "v"(a), "v"(b), "v"(c), "v"(d));
}
DI float a_max3(float a, float b, float c) { float d; asm volatile("v_max3_f32 %0, %1, %2, %3" : "=v"(d) : "v"(a), "v"(b), "v"(c)); return d; }
DI float a_min3(float a, float b, float c) { float d; asm volatile("v_min3_f32 %0, %1, %2, %3" : "=v"(d) : "v"(a), "v"(b), "v"(c)); return d; }

DI float opq_a(float v) { asm volatile("" : "+a"(v)); return v; }
DI void pin_a(bf16x8& a) { asm volatile("" : "+a"(a)); }
DI void pin2(f32x16& a, f32x16& b) { asm volatile("" : "+v"(a), "+v"(b)); }
DI bf16x8 cs_frag(float c) {
  const float hi = rbf(c), mid = rbf(c - hi), lo = (c - hi) - mid;
  return __builtin_bit_cast(bf16x8, u32x4{cvtpk(hi, mid), cvtpk(lo, 0.f), 0u, 0u});
}
template <bool CS> struct Red {
  bf16x8 p[4];
  float t[4], a, b, sd;
  template <int K> DI void run(int lane) {
    if constexpr (K == 0) asm volatile("" : "+v"(p[0]), "+v"(p[1]), "+v"(p[2]), "+v"(p[3]));
    if constexpr (K < 3) {
      const f32x4 u = __builtin_bit_cast(f32x4, p[K + 1]), v = __builtin_bit_cast(f32x4, p[0]);
#pragma unroll
      for (int e = 0; e < 4; ++e) { if (K == 0) t[e] = v[e]; a_add(t[e], u[e]); }
    }
    if constexpr (K == 3) {
      auto x = __builtin_amdgcn_permlane16_swap(__float_as_uint(t[0]), __float_as_uint(t[1]), false, false);
      auto y = __builtin_amdgcn_permlane16_swap(__float_as_uint(t[2]), __float_as_uint(t[3]), false, false);
      a = opq_v(__uint_as_float(x[0]) + __uint_as_float(x[1])); b = opq_v(__uint_as_float(y[0]) + __uint_as_float(y[1]));
    }
    if constexpr (K == 4) { auto x = __builtin_amdgcn_permlane32_swap(__float_as_uint(a), __float_as_uint(b), false, false); a = opq_v(__uint_as_float(x[0]) + __uint_as_float(x[1])); }
    if constexpr (K == 5) {
      if constexpr (CS) a = lane_get(a, (lane >> 2) + ((lane & 3) << 4));
      else { const int src = ((lane & 31) >> 1) + ((lane & 1) << 5); b = lane_get(a, src + 16); a = lane_get(a, src); }
    }
    if constexpr (CS) {
      if constexpr (K == 8) { t[0] = opq_v(rbf(a)); t[1] = opq_v(a - t[0]); }
      if constexpr (K == 9) { const float mid = rbf(t[1]); p[0] = opq_v(__builtin_bit_cast(bf16x8, u32x4{cvtpk(t[0], mid), cvtpk(t[1] - mid, 0.f), 0u, 0u})); }
    } else {
      if constexpr (K == 8) { const float m = a * (1.f / D_); b = opq_v(fmaxf(b * (1.f / D_) - m * m, 0.f)); a = opq_v(rbf(m)); }
      if constexpr (K == 9) b = opq_v(rbf(rbf(b) + 1e-5f));
      if constexpr (K == 10) { sd = opq_v(rbf(__builtin_amdgcn_sqrtf(b))); b = opq_v(__builtin_amdgcn_rcpf(sd)); }
    }
  }
};
DI void pin4(bf16x8 (&p)[4]) { asm volatile("" : "+v"(p[0]), "+v"(p[1]), "+v"(p[2]), "+v"(p[3])); }
DI bf16x8 mean_frag(float mean, bool on) { const float m = on ? -mean : 0.f; return __builtin_bit_cast(bf16x8, u32x4{cvtpk(m, m), cvtpk(m, 0.f), 0u, 0u}); }

#define STAMP(i)

constexpr int SLOT = 16384, XL = 12;
struct WinPtr {
  unsigned b[3];
  DI void init(unsigned a) { sfor<0, 3>([&](auto i) { b[i] = a + i * 65536u; asm volatile("" : "+v"(b[i])); }); }
  template <int OFF> DI bf16x8 frag() const { return *(LAS const bf16x8*)((LAS const char*)(uintptr_t)b[OFF >> 16] + (OFF & 65535)); }
};
struct Ctx { unsigned xv; WinPtr p; };
#define AI __attribute__((always_inline))
#define RARE(c) __builtin_expect(!!(c), 0)
#define IC(n) std::integral_constant<int, (n)>{}

template <int S> DI void ld_x(bf16x8 (&x)[64], const Ctx& c, const char* xb) { ldx1<false, (S % 8) * 1024 - 4096>(x[S], c.xv, xb + (S / 8) * 8192 + 4096); }
template <class H> constexpr int g64_wait(int s) {
  int n = 0;
  const int t0 = s - XL;
  if (t0 < 0) n += XL - 1 - s; else n += H::n(t0);
  for (int t = t0 < 0 ? 0 : t0 + 1; t < s; ++t) n += (t + XL < 64 ? 1 : 0) + H::n(t);
  return n;
}
constexpr int a_slot(int g);
template <bool MAPA, bool ZERO, class H, class FX, class FA>
DI void gemm64(const Ctx& c, bf16x8 (&x)[64], const char* xb, f32x16& acc0, f32x16& acc1, FX&& hook, FA&& aux) {
  bf16x8 f0[3], f1[3];
  auto off = [](int s) constexpr { return (MAPA ? a_slot(24 + s / 8) : s / 8) * SLOT + (s % 8) * 2048; };
  auto rd0 = [&](auto s) { f0[s % 3] = c.p.template frag<off(s)>(); };
  auto rd1 = [&](auto s) { f1[s % 3] = c.p.template frag<off(s) + 1024>(); };
  rd0(IC(0)); rd1(IC(0)); rd0(IC(1)); rd1(IC(1));
  sfor<0, 64>([&](auto s) {
    waitx<false, g64_wait<H>(s)>(x[s]);
    SBAR();
    mm<false, false, ZERO && s == 0>(acc0, f0[s % 3], x[s]);
    SBAR();
    if constexpr (s + 2 < 64) rd0(IC(s + 2));
    if constexpr (s + XL < 64) ld_x<s + XL>(x, c, xb);
    aux(IC(2 * s));
    SBAR();
    mm<false, false, ZERO && s == 0>(acc1, f1[s % 3], x[s]);
    SBAR();
    if constexpr (s + 2 < 64) rd1(IC(s + 2));
    hook(s);
    aux(IC(2 * s + 1));
    SBAR();
  });
  mdone(acc0, acc1);
}
struct HookP6 { static constexpr int n(int s) { return s >= 52 ? 1 : 0; } };
struct HookP3 { static constexpr int n(int s) { return s >= 52 ? 1 : 0; } };
struct HookP4 { static constexpr int n(int s) { return s == 52 || s == 6 || (s >= 8 && s < 12) || (s >= 14 && s < 30 && s % 2 == 0) ? 1 : 0; } };

DI void park4(u32x4 (&v)[4]) { asm volatile("" : "+a"(v[0]), "+a"(v[1]), "+a"(v[2]), "+a"(v[3])); }
DI void res_to_acc(u32x4 (&xr)[4], f32x16& a0, f32x16& a1) {
  park4(xr);
#pragma unroll
  for (int e = 0; e < 4; ++e) {
    a0[2 * e] = bflo(xr[0][e]); a0[2 * e + 1] = bfhi(xr[0][e]); a0[8 + 2 * e] = bflo(xr[1][e]); a0[9 + 2 * e] = bfhi(xr[1][e]);
    a1[2 * e] = bflo(xr[2][e]); a1[2 * e + 1] = bfhi(xr[2][e]); a1[8 + 2 * e] = bflo(xr[3][e]); a1[9 + 2 * e] = bfhi(xr[3][e]);
  }
  asm volatile("" : "+v"(a0), "+v"(a1));
}
DI void store_stats(int r, const u32x4 (&xr)[4], char* stp) {
  float s1 = 0.f, s2 = 0.f, s3 = 0.f, s4 = 0.f;
#pragma unroll
  for (int i = 0; i < 4; ++i)
#pragma unroll
    for (int e = 0; e < 4; e += 2) {
      s1 = dot2(xr[i][e], 0x3f803f80u, s1); s2 = dot2(xr[i][e], xr[i][e], s2);
      s3 = dot2(xr[i][e + 1], 0x3f803f80u, s3); s4 = dot2(xr[i][e + 1], xr[i][e + 1], s4);
    }
  st2_1(f32x2{swap_add(s1 + s3), swap_add(s2 + s4)}, (unsigned)(r * 8), stp);
}
DI void store_x(const Ctx& c, int r, const f32x16& a0, const f32x16& a1, u32x4 (&xr)[4], char* dst, char* stp) {
  SBAR();
  xr[0] = __builtin_bit_cast(u32x4, pack8<0>(a0)); st4_1<0>(xr[0], c.xv, dst); SBAR();
  xr[1] = __builtin_bit_cast(u32x4, pack8<1>(a0)); st4_1<1024>(xr[1], c.xv, dst); SBAR();
  xr[2] = __builtin_bit_cast(u32x4, pack8<0>(a1)); st4_1<2048>(xr[2], c.xv, dst); SBAR();
  xr[3] = __builtin_bit_cast(u32x4, pack8<1>(a1)); st4_1<3072>(xr[3], c.xv, dst); SBAR();
  store_stats(r, xr, stp);
  SBAR();
  park4(xr);
}

template <class FX> DI void hedge_exp(const f32x16& p, float sc, f32x16& ep, f32x16& en, FX&& hook) {
  float mx = fmaxf(p[0], p[1]), mn = fminf(p[0], p[1]);
#pragma unroll
  for (int i = 2; i < 16; i += 2) { mx = fmaxf(fmaxf(mx, p[i]), p[i + 1]); mn = fminf(fminf(mn, p[i]), p[i + 1]); }
  mx = swap_max(mx) * sc; mn = swap_min(mn) * sc;
  sfor<0, 16>([&](auto i) {
    SBAR();
    hook(i);
    constexpr int e = i;
    float a = __builtin_amdgcn_exp2f(__builtin_fmaf(p[e], sc, -mx)), b = __builtin_amdgcn_exp2f(__builtin_fmaf(p[e], -sc, mn));
    asm volatile("" : "+v"(a), "+v"(b));
    ep[e] = a; en[e] = b;
  });
  SBAR();
}

constexpr int a_slot(int g) { return g == 8 || g == 15 ? 8 : (g > 8 && g < 15 ? g - 9 : g % 8); }
constexpr bool a_bar(int s) { return (s + 2) % 8 == 0 && s < 190 && s != 78; }
constexpr int QP = 128 + 24, QC = 128 + 36;
constexpr int a_piece(int s, bool first) {
  constexpr int t[][3] = {{46, 8, 1}, {54, 9, 1}, {58, 10, 1}, {64, 11, 2}, {72, 12, 2}, {80, 13, 2}, {88, 14, 2}, {96, 15, 2}, {104, 16, 1}, {108, 17, 1}, {112, 18, 1},
                          {116, 19, 1}, {120, 20, 1}, {134, 21, 1}, {138, 22, 1}, {142, 23, 1}, {146, 24, 1}, {150, 25, 1}, {158, 26, 1}};
  if (!first && s >= 8 && s < 24 && s % 2 == 0) return 24 + (s - 8) / 2;
  for (auto& e : t) if (s >= e[0] && s < e[0] + 4 * e[2] && (s - e[0]) % e[2] == 0) return e[1] * 4 + (s - e[0]) / e[2];
  return -1;
}
constexpr int a_park(int s) { return s >= 129 && s < 135 ? s - 129 : (s == 162 || s == 163 ? s - 156 : -1); }
constexpr int a_ld0(bool first) { return first ? 32 : 2; }
constexpr bool a_side(int s, bool first) { return (s >= a_ld0(first) && s < a_ld0(first) + (first ? 12 : 4)) || s == 50 || (s >= 154 && s < 158) || a_park(s) >= 0; }
constexpr int f_rd(int n) { return n % 6 < 4 && n / 6 + 1 < 16 ? (n / 6 + 1) * 4 + n % 6 : -1; }
constexpr int f_in(int n) { return (n + 1) % 6 < 4 && (n + 1) / 6 >= 1 && (n + 1) / 6 + 4 < 16 ? ((n + 1) / 6 + 4) * 4 + (n + 1) % 6 : -1; }
constexpr int f_raw(int s) { return s >= 6 && s < 46 && s % 2 == 0 ? ((s + 2) / 8 + 2) * 4 + ((s + 2) % 8) / 2 : -1; }
struct SchedA {
  int xw[64], bw[192], sw[3], aw, fw, cw, pw, zw, uw, vw, lw[32];
  constexpr SchedA(bool first) : xw{}, bw{}, sw{}, aw(0), fw(0), cw(0), pw(0), zw(0), uw(0), vw(0), lw{} {
    int pos = 0, xpos[64] = {}, wpos[32] = {}, spos[192] = {}, fpos = 0, stpos = 0, ppos = 0, upos = 0, vpos = 0, lpos[34] = {};
    if (first) { xpos[1] = 4; xpos[2] = 12; wpos[1] = 18; xpos[3] = 22; wpos[2] = 26; xpos[4] = pos = 30; } else for (int s = 0; s < XL; ++s) xpos[s] = ++pos;
    for (int s = 0; s < 192; ++s) {
      if (s == 128) { pos += 3; stpos = pos; }
      if (a_bar(s)) {
        const int g = (s + 2) / 8 + (s == 70);
        bw[s] = wpos[g] ? pos - wpos[g] : 63;
        if (s == 62 && pos - spos[50] < bw[s]) bw[s] = pos - spos[50];
        if (s == 142) { if (pos - stpos < bw[s]) bw[s] = pos - stpos; ++pos; }
        if (s == 6 && first) ++pos;
      }
      if (s == 102) uw = pos - upos;
      if (s == 118) vw = pos - vpos;
      for (int k = 0; k < 3; ++k) if (s == a_ld0(first) + 14 + 4 * k) sw[k] = pos - spos[a_ld0(first) + 3 + 4 * k];
      if (s == 32) aw = pos - fpos;
      if (s == 128) fw = pos - fpos;
      if (s == 170) cw = pos - spos[157];
      if (s == QC) pw = pos - ppos;
      if (s < 64) xw[s] = first ? (s % 3 == 0 && s / 3 + 1 < 16 ? pos - xpos[s / 3 + 1] : 63) : pos - xpos[s];
      if (first && s < 64 && f_in(2 * s) >= 0) xpos[f_in(2 * s) / 4] = ++pos;
      if (!first && s + XL < 64) xpos[s + XL] = ++pos;
      if ((first && s == 22) || s == 127) fpos = ++pos;
      if (s == QP) ppos = ++pos;
      if (s >= QC) lpos[s - QC] = ++pos;
      if (a_piece(s, first) >= 0) wpos[a_piece(s, first) / 4] = ++pos;
      if (a_side(s, first)) spos[s] = ++pos;
      if (first && s < 64 && f_in(2 * s + 1) >= 0) xpos[f_in(2 * s + 1) / 4] = ++pos;
      if (first && f_raw(s) >= 0) wpos[f_raw(s) / 4] = ++pos;
      if (s == 63) pos += 8;
      if (s == 87) upos = pos;
      if (s == 103) vpos = pos;
      if (s == 177) zw = pos - lpos[1];
    }
    for (int i = 0; i < 32; ++i) {
      lw[i] = pos - lpos[2 + i];
      if (i % 2 == 0 && 192 - QC + i / 2 < 34) lpos[192 - QC + i / 2] = ++pos;
      if (i % 2 == 0 && i >= 16) ++pos;
    }
  }
};
constexpr int cmin(int a, int b) { return a < b ? a : b; }

#define R_OA "v[64:79]"
#define R_OB "v[80:95]"
#define S0_(n, q, m) "v_mfma_f32_32x32x16_bf16 v[" #n ":" #n "+15], v[208:211], v[" #q ":" #q "+3], v[" #m ":" #m "+15]\n\t"
#define S1_(n, q) "v_mfma_f32_32x32x16_bf16 v[" #n ":" #n "+15], v[212:215], v[" #q "+4:" #q "+7], v[" #n ":" #n "+15]\n\t"
#define PV0_(o, p) "v_smfmac_f32_32x32x32_bf16 v[" #o ":" #o "+15], v[216:219], v[" #p ":" #p "+7], v236\n\t"
#define PV1_(o, p) "v_smfmac_f32_32x32x32_bf16 v[" #o ":" #o "+15], v[220:223], v[" #p "+8:" #p "+15], v236\n\t"
#define L0_(l, p) "v_smfmac_f32_16x16x64_bf16 v[" #l ":" #l "+3], v[224:227], v[" #p ":" #p "+7], v236\n\t"
#define L1_(l, p) "v_smfmac_f32_16x16x64_bf16 v[" #l ":" #l "+3], v[224:227], v[" #p "+8:" #p "+15], v236\n\t"
#define E_(e, i) "v_exp_f32 v[" #e "+" #i "], v[" #e "+" #i "]\n\t"
#define RD_(d, off) "ds_read_b128 v[" #d ":" #d "+3], v237 offset:" #off "\n\t"
#define HALF_(e, n, q, m, p, o, l, X0, X1, X2, X3, X4, X5) \
  S0_(n, q, m) X0 E_(e, 0) E_(e, 1) E_(e, 2) S1_(n, q) X1 E_(e, 3) E_(e, 4) E_(e, 5) PV0_(o, p) X2 E_(e, 6) E_(e, 7) E_(e, 8) \
  PV1_(o, p) X3 E_(e, 9) E_(e, 10) E_(e, 11) L0_(l, p) X4 E_(e, 12) E_(e, 13) L1_(l, p) X5 E_(e, 14) E_(e, 15)
#define LG2_ "s_waitcnt lgkmcnt(2)\n\t"
#define LG0_ "s_waitcnt lgkmcnt(0)\n\t"
#define STEP_(b, nb, kv, wo, Y0, Y1) \
  HALF_(128, 160, 200, 112, 176, 80, 232, "", "", RD_(208, b + 2048), RD_(212, b + 3072), RD_(216, b), RD_(220, b + 1024) LG2_) \
  HALF_(160, 144, 192, 96, 128, 64, 228, "", LG0_, "s_add_u32 m0, s44, " #nb "\n\t", "", "global_load_lds_dwordx4 v" #kv ", s[40:41] sc1\n\t", \
        "global_load_lds_dwordx4 v" #kv ", s[40:41] offset:1024 sc1\n\t") \
  HALF_(144, 176, 200, 112, 160, 80, 232, Y0, "", RD_(208, b + 6144), RD_(212, b + 7168), RD_(216, b + 4096), RD_(220, b + 5120) LG2_) \
  HALF_(176, 128, 192, 96, 144, 64, 228, "s_mov_b32 m0, s45\n\t", LG0_, "global_load_lds_dwordx4 v240, s[42:43] offset:" #wo " sc1\n\t", Y1, \
        "s_waitcnt vmcnt(4)\n\t", "s_barrier\n\t")
DI float max16(const f32x16& s) {
  float mx = s[0];
#pragma unroll
  for (int e = 1; e < 16; ++e) mx = fmaxf(mx, s[e]);
  return mx;
}
typedef __attribute__((ext_vector_type(8))) unsigned u32x8;
typedef __attribute__((ext_vector_type(16))) unsigned u32x16;

__global__ __launch_bounds__(256, 1) void k_main(const char* xin, const char* wqkv, const char* wphi, const char* woA, const char* wq, const char* wk, const char* wv,
                                                 const char* woB, char* ws, char* out) {
  __shared__ __attribute__((aligned(16))) char smem_[163840];
  LAS char* const smem = (LAS char*)smem_;
  const unsigned lds0 = (unsigned)(uintptr_t)smem;
  Ctx c;
  int lane = threadIdx.x & 63, w = __builtin_amdgcn_readfirstlane(threadIdx.x >> 6), r = lane & 31, h = lane >> 5, rb, cb;
  bf16x8 cf;
  { const char* pc; unsigned last;
    asm volatile("s_getpc_b64 %0\n1:\n\ts_mov_b32 %1, .Lk_main_tail-1b" : "=s"(pc), "=s"(last));
    const unsigned o = (((blockIdx.x >> 3) * 4 + w) & 63) * 1024;
    ldx0<0>(cf, (unsigned)(lane * 16), pc + (o < last ? o : last)); }
  SBAR();
  block_map<4>(rb, cb);
  int tt = rb * 4 + w, blk = (lane >> 4) & 1, q4 = (lane & 15) >> 2, p4 = lane & 3;
  c.xv = lane * 16; c.p.init(lds0 + c.xv);
  const int ut = rb * 4 + (cb >> 2), ty = cb & 3;
  char *flags, *col, *colw, *act, *acto, *fact, *facto, *stt, *stto, *kvp, *zp, *fkvp, *fwl, *kvf, *fkv;
  const char *xu_p, *su_p, *wkv, *wph;
  unsigned kvb;
  u64 tag = 0;
  u32x4 xr[4] = {};
  unsigned ldsw = lds0 + w * 4096;
  auto wt = [&](int m, int ch) { return colw + C_WT + m * 131072 + ch * SLOT; };
  auto pc_w = [&](int m, int slot, int ch, auto i) { if constexpr (i == 0) dma_c<0>(ldsw + slot * SLOT, c.xv, wt(m, ch)); else dma_n<i * 1024>(c.xv, wt(m, ch)); };
  auto pc_a = [&](auto n) { constexpr int g = n / 4; pc_w(g < 8 ? 1 : (g < 16 ? 2 : (g < 24 ? 0 : 3)), a_slot(g), g % 8, IC(n % 4)); };
  u64 csm = ~0ull;
  float csv[4] = {};
  auto pc_m = [&](auto n) { constexpr int g = n / 4; dma_c<(n % 4) * 1024>(ldsw + a_slot(g) * SLOT, c.xv, wt(2, g % 8)); };
  auto ld_cs = [&](bf16x8& d, int m, auto i) { ldx1m<i * 1024>(d, c.xv, col + C_CS + m * 4096, csm); };
  auto raw_src = [&](int pitch, auto i) AI { const int row = lane >> 3; return (unsigned)((32 * w + 8 * i + row) * pitch + ((lane & 7) ^ (((row >> 1) & 1) << 2)) * 16 - i * 1024); };
  auto raw_in = [&](auto j, int slot, auto i) AI {
    const int hi = rb >> 3, pitch = (j == 0 || (j == 2 && hi)) ? 6144 : 2048;
    const char* src = (j == 0 ? wqkv + (hi ? 0 : 4096) : (j == 1 ? (hi ? wq : woA) : (hi ? wqkv + 2048 : woB))) + cb * 128 + (size_t)(rb & 7) * 128 * pitch;
    dma<i * 1024>(ldsw + slot * SLOT, raw_src(pitch, i), src);
  };
  auto raw_out = [&](auto j, int slot) AI {
    const int hi = rb >> 3, ch = rb & 7, m = j == 0 ? 2 - 2 * hi : (j == 1 ? 3 + hi : 5 - 4 * hi);
    LAS const char* p = smem + slot * SLOT + (w >> 1) * 8192 + (4 * h + q4) * 128 + (((((q4 >> 1) ^ w) & 1) << 2) + 2 * blk + (p4 >> 1)) * 16 + 8 * (p4 & 1);
    char* dst = col + C_WT + m * 131072 + ch * SLOT + (w >> 1) * 8192 + (w & 1) * 1024 + 4096;
    u32x4 f[4];
    sfor<0, 4>([&](auto s) AI { f[s] = __builtin_bit_cast(u32x4, cat8(trread(p + s * 2048), trread(p + s * 2048 + 1024))); });
    SBAR();
    sfor<0, 4>([&](auto s) AI { st4_1<s * 2048 - 4096>(f[s], c.xv, dst); });
    SBAR();
    float cs[4] = {};
#pragma unroll
    for (int n = 0; n < 16; ++n) cs[n & 3] = dot2(f[n >> 2][n & 3], 0x3f803f80u, cs[n & 3]);
    st1_1(swap_add((cs[0] + cs[1]) + (cs[2] + cs[3])), (unsigned)(r * 4), col + C_CS + m * 4096 + (ch * 2 + (w >> 1)) * 256 + (w & 1) * 128);
  };
  const int w0 = w; const unsigned ldsw0 = ldsw;
#define REFRESH() do { \
    w = w0; ldsw = ldsw0; \
    asm volatile("" : "+s"(w), "+s"(tag), "+s"(act), "+s"(acto), "+s"(fact), "+s"(facto), "+s"(stt), "+s"(stto), "+s"(col), "+s"(colw), "+s"(ldsw)); \
    asm volatile("" : "+v"(lane)); \
    r = lane & 31; h = lane >> 5; blk = (lane >> 4) & 1; q4 = (lane & 15) >> 2; p4 = lane & 3; c.xv = lane * 16; c.p.init(lds0 + c.xv); \
  } while (0)
  int l = 0;
  float mean, rstd;
  Red<true> cw;
  f32x16 ra0, ra1;
  bf16x8 park[16];
      constexpr int NA = 58;
      constexpr SchedA SQ(false), S1(true);
      bf16x8 x[64], f0[3], f1[3], wf[4], kp[4], pp[32], zq[2], sel[2], mf0, mf1;
      Red<false> st; Red<true> ck, cv, cq;
      f32x16 acc[6], p[2], kvs;
      u64 fv;
      float mx, mn, e[16], en[16], se, sn, sce;
      f32x4 ks0 = {}, ks1 = {};
      unsigned arow, pk0, ra, xo[4], xs;
      const char* xw = xin + (size_t)tt * 65536;
      LAS char* pk_img = smem + 6 * SLOT;
      LAS char* v_img = smem + 5 * SLOT;
      LAS char* sh = smem + 9 * SLOT + 4096;
      auto issue_x = [&](auto s) { ldx1<(s < NA), (s % 8) * 1024 - 4096>(x[s], c.xv, act + (s / 8) * 8192 + 4096); };
      auto ld_park = [&](auto i) { ldx1<true, (i % 4) * 1024>(park[i], c.xv, wt(4, 2 + i / 4)); };
      auto ldp = [&](auto i) {
        if constexpr (i >= 2) ldx1<true, ((i - 2) % 8) * 1024 - 4096>(pp[i - 2], c.xv, kvp + ((i - 2) / 8) * 8192 + 4096); else ldx1<false, i * 1024>(zq[i], c.xv, zp);
      };
      auto rd_wf = [&](auto k) { LAS const char* wp = smem + 9 * SLOT + (4 * h + q4) * 64 + blk * 32 + p4 * 8 + k * 1024; wf[k] = cat8(trread(wp), trread(wp + 512)); };
      auto rd01 = [&](auto s, auto t, auto fl) {
        if constexpr (s < 192) {
          bf16x8 f;
          if constexpr (fl && s < 64) { LAS const char* q = (LAS const char*)(uintptr_t)(ra ^ (t * 64u)) + ((s / 8 + 3) % 4) * SLOT + (s % 8) * 2048; f = cat8(trread(q), trread(q + 1024)); }
          else f = c.p.template frag<a_slot(s / 8) * SLOT + (s % 8) * 2048 + t * 1024>();
          if constexpr (t == 0) f0[s % 3] = f; else f1[s % 3] = f;
        }
      };
      auto x_in = [&](auto n) { dma<(n % 4) * 1040>(lds0 + w * 20480 + ((n / 4) % 4) * 4160, xs + (n % 4) * (16384 - 1040), xw + (n / 4) * 128); };
      auto x_out = [&](auto s) { ds_read2<(s < NA), ((s / 4) % 4) * 520>(x[s], xo[s % 4]); };
      auto x_fin = [&](auto g, auto n) { waitl<(4 * g < NA), n>(x[4 * g]); sfor<4 * g + 1, 4 * g + 4>([&](auto s) { if constexpr (s < NA) pin_a(x[s]); else x[s] = opq_v(x[s]); }); };
      auto k_in = [&](auto n) { dma<(n % 4) * 1024>(ldsw + (5 + (n / 4 + 3) % 4) * SLOT, raw_src(6144, IC(n % 4)), wqkv + 2048 + cb * 128 + (size_t)(n / 4) * 128 * 6144); };
      auto v_out = [&](auto t, auto hf) {
        *(LAS bf16x8*)(v_img + t * 8192 + w * 2048 + hf * 1024 + lane * 16) = pack8<hf>(acc[2 + t]);
      };
      auto sum_mm = [&](auto i) { waitx<true, SQ.lw[i]>(pp[i]); mm<false, true, i == 0>(kvs, sel[i & 1], pp[i]); };
      auto kmap = [&](auto gi) {
        constexpr int g = gi;
        if constexpr (g == 4) pin2(acc[0], acc[1]);
        if constexpr (g >= 6 && g < 10) mm<false, false, g % 2 == 0>(p[(g - 6) / 2], wf[g - 6], kp[g - 6]);
        if constexpr (g >= 4 && g < 8) kp[g - 4] = opq_v(pack8<g % 2>(acc[(g - 4) / 2]));
        if constexpr (g == 10) mm<true, false, false, true>(acc[2], cv.p[0], mf0);
        if constexpr (g == 11) mm<true, false, false, true>(acc[3], cv.p[0], mf1);
        if constexpr (g >= 12) {
          constexpr int hd = (g - 12) / 58, j = (g - 12) % 58, n = (j - 7) / 2;
          const f32x16& q = p[hd];
          if constexpr (j == 0) { mx = a_max3(q[0], q[1], q[2]); mn = a_min3(q[0], q[1], q[2]); mx = a_max3(mx, q[3], q[3]); mn = a_min3(mn, q[3], q[3]); }
          if constexpr (j >= 1 && j < 4) a_mm4(mx, mn, q[4 * j], q[4 * j + 1], q[4 * j + 2], q[4 * j + 3]);
          if constexpr (j == 4) mx = opq_v(swap_max(mx) * -sce);
          if constexpr (j == 5) mn = opq_v(swap_min(mn) * sce);
          if constexpr (j == 6) { e[0] = a_fma(q[0], sce, mx); en[0] = a_fms(q[0], sce, mn); se = opq_v(0.f); sn = opq_v(0.f); }
          if constexpr (j >= 7 && j < 37) {
            if constexpr (j % 2) a_hedge<false>(e[n], e[n + 1], se, n ? e[n ? n - 1 : 0] : sn, q[n + 1], sce, mx);
            else a_hedge<true>(en[n], en[n + 1], sn, n ? en[n ? n - 1 : 0] : se, q[n + 1], sce, mn);
          }
          if constexpr (j == 37) { a_exp(e[15]); a_add(se, e[14]); }
          if constexpr (j == 38) { a_exp(en[15]); a_add(sn, en[14]); }
          if constexpr (j == 39) se = opq_v(swap_add(se + e[15]));
          if constexpr (j == 40) sn = opq_v(swap_add(sn + en[15]));
          if constexpr (j == 41) { se = opq_v(__builtin_amdgcn_rcpf(se) * rstd); sn = opq_v(__builtin_amdgcn_rcpf(sn) * rstd); }
          if constexpr (j >= 42) {
            constexpr int c8 = (j - 42) / 2, o = 4 * (c8 & 3);
            float* v = c8 < 4 ? e : en;
            if constexpr (j % 2 == 0) { a_mul4(v[o], v[o + 1], v[o + 2], v[o + 3], c8 < 4 ? se : sn); pk0 = opq_v(arow ^ (c8 << 4)); }
            else *(LAS u32x2*)((LAS char*)(uintptr_t)pk0 + hd * 16384) = u32x2{cvtpk(v[o], v[o + 1]), cvtpk(v[o + 2], v[o + 3])};
          }
        }
      };
      auto aux = [&](auto s, auto i, auto fl) {
        constexpr int g1 = 2 * (s - a_ld0(fl) - 14) + i, g3 = 2 * (s - 128) + i;
        if constexpr (s < 64) {
          if constexpr (fl) { if constexpr (i == 0) a_sum16(ks0, sel[0], f0[s % 3]); else a_sum16(ks1, sel[0], f1[s % 3]); }
          if constexpr (g1 >= 0 && g1 < 11) st.template run<g1>(lane);
          if constexpr (g1 == 11) { mean = st.a; rstd = st.b; sce = opq_v(rstd * LOG2E); }
          if constexpr (g1 == 12) mf0 = opq_v(mean_frag(mean, h == 0));
          if constexpr (g1 == 13) mf1 = opq_v(mean_frag(mean, h == 1));
          if constexpr (fl && g1 >= 8 && g1 < 14) cv.template run<g1 - 8>(lane);
          if constexpr (fl && g1 >= 16 && g1 < 22) cq.template run<g1 - 16>(lane);
          if constexpr (fl && g1 == 18) csv[1] = opq_a(cv.a);
          if constexpr (fl && g1 == 26) csv[2] = opq_a(cq.a);
          if constexpr (g1 == 22) cv.a = csv[1] = opq_a(csv[1]);
          if constexpr (g1 == 22 || g1 == 23) cv.template run<g1 - 14>(lane);
          if constexpr (!fl) {
            if constexpr (g1 == 14) ck.a = opq_v(csv[0]);
            if constexpr (g1 == 14 || g1 == 15) ck.template run<g1 - 6>(lane);
            if constexpr (g1 == 18) mm<false, false, false, true>(acc[0], ck.p[0], mf0);
            if constexpr (g1 == 19) mm<false, false, false, true>(acc[1], ck.p[0], mf1);
          }
          if constexpr (s == 60 && i == 0) *(LAS short*)(sh + 8704 + w * 64 + (r >> 4) * 32 + ((r >> 2) & 1) * 16 + ((r >> 3) & 1) * 8 + (r & 3) * 2) = (short)(__float_as_uint(st.sd) >> 16);
          if constexpr (s == 61 && i == 0) arow = opq_v((unsigned)(uintptr_t)pk_img + (w * 32 + r) * 128 + h * 8 + ((r & 7) << 4));
          if constexpr (s >= 62) rd_wf(IC(2 * (s - 62) + i));
        } else if constexpr (s < 128) {
          kmap(IC(2 * (s - 64) + i));
        } else {
          if constexpr (g3 == 2) cq.a = csv[2] = opq_a(csv[2]);
          if constexpr (g3 == 2 || g3 == 3) cq.template run<g3 + 6>(lane);
          if constexpr (g3 == 6) mm<false, false, false, true>(acc[4], cq.p[0], mf0);
          if constexpr (g3 == 7) mm<false, false, false, true>(acc[5], cq.p[0], mf1);
          if constexpr (g3 == 92) { csv[3] = l == 0 ? cw.a : csv[3]; cw.a = csv[3]; }
          if constexpr (g3 >= 84 && g3 < 94) cw.template run<g3 - 84>(lane);
          if constexpr (g3 >= 32 && g3 < 40) {
            constexpr int n = (g3 - 32) % 4, t = (g3 - 32) / 4;
            const int kr = 16 * t + 8 * (n >> 1) + 4 * h + 2 * (n & 1);
            u32x4 sv = __builtin_bit_cast(u32x4, sel[t]); sv[n] = opq_v(r == kr ? 0x3f80u : (r == kr + 1 ? 0x3f800000u : 0u)); sel[t] = __builtin_bit_cast(bf16x8, sv);
          }
          if constexpr (g3 == 40) { sel[0] = opq_v(sel[0]); sel[1] = opq_v(sel[1]); }
          if constexpr (g3 == 42) *(LAS unsigned*)(sh + 8192 + 256 + lane * 4) = opq_v(0x3f803f80u);
          if constexpr (g3 == 99) {
            wait2<SQ.zw>(zq[0], zq[1]);
#pragma unroll
            for (int n = 0; n < 4; ++n) { e[n] = __builtin_bit_cast(f32x4, zq[0])[n]; a_add(e[n], __builtin_bit_cast(f32x4, zq[1])[n]); }
          }
          if constexpr (g3 == 101) {
            auto a = __builtin_amdgcn_permlane16_swap(__float_as_uint(e[0]), __float_as_uint(e[1]), false, false);
            auto b = __builtin_amdgcn_permlane16_swap(__float_as_uint(e[2]), __float_as_uint(e[3]), false, false);
            e[0] = opq_v(__uint_as_float(a[0]) + __uint_as_float(a[1])); e[1] = opq_v(__uint_as_float(b[0]) + __uint_as_float(b[1]));
          }
          if constexpr (g3 == 103) { auto a = __builtin_amdgcn_permlane32_swap(__float_as_uint(e[0]), __float_as_uint(e[1]), false, false); e[0] = opq_v(__uint_as_float(a[0]) + __uint_as_float(a[1])); }
          if constexpr (g3 == 105) e[1] = lane_get(e[0], lane ^ 8);
          if constexpr (g3 == 111)
            *(LAS short*)(sh + 8192 + w * 64 + (lane & 1) * 32 + ((lane >> 2) & 1) * 16 + ((lane >> 1) & 1) * 8 + (lane >> 4) * 2) = (short)cvtpk(e[0] + e[1], 0.f);
          if constexpr (g3 >= 116 && g3 < 124 && g3 % 2 == 1) rd_wf(IC((g3 - 116) / 2));
        }
      };
      const unsigned fo = (unsigned)((lane & 15) * 8 + (blk ? cb * 4 * FL_I + 10 * FL_K : tt * FL_I));
      auto step = [&](auto s, auto fl) {
        constexpr int ps = s / 64, L0 = a_ld0(fl);
        constexpr bool first = fl;
        if constexpr (s < 64 && !first) waitx<(s < NA), SQ.xw[s % 64]>(x[s % 64]);
        SBAR();
        mm<ps == 1, (s % 64 < NA), s % 64 == 0>(acc[2 * ps], f0[s % 3], x[s % 64]);
        SBAR();
        if constexpr (s == 102) waitv<SQ.uw>();
        if constexpr (s == 118) waitv<SQ.vw>();
        if constexpr (a_bar(s) && s >= (first ? 6 : 38)) wait_bar<(ps ? cmin(S1.bw[s], SQ.bw[s]) : (first ? S1.bw[s] : SQ.bw[s]))>();
        if constexpr (s == L0 + 14 || (first && (s == L0 + 18 || s == L0 + 22))) waitv<(first ? S1.sw[(s - L0 - 14) / 4] : SQ.sw[0])>();
        if constexpr (first && s == 6) { set_flag(facto, tag); if (RARE(w == 0)) set_flag(fwl - FL_K + rb * 8, tag); }
        if constexpr (first && s == 32) { waitx<false, S1.aw>(fv); if (RARE(!__all(fv == tag))) poll<32>(flags, fo, tag); }
        if constexpr (s == 118) { if (RARE(l == 0 && w == 0)) set_flag(fwl + rb * 8, tag); }
        if constexpr (s == 128) { waitx<false, SQ.fw>(fv); if (RARE(!__all(fv == tag))) poll<16>(fwl, lane * 8, tag); }
        if constexpr (s == 142) set_flag(fkvp + rb * 8, tag);
        if constexpr (s == QC) { waitx<false, SQ.pw>(fv); if (RARE(!__all(fv == tag))) poll<16>(fkvp, lane * 8, tag); }
        if constexpr (s == 170) waitv<SQ.cw>();
        if constexpr (first && s % 3 == 0) waitv<S1.xw[s % 64]>();
        SBAR();
        if constexpr (first && f_rd(2 * s) >= 0) x_out(IC(f_rd(2 * s)));
        if constexpr (!first) { rd01(IC(s + 2), IC(1), fl); SBAR(); }
        rd01(IC(s + 2), IC(0), fl);
        if constexpr (first && f_in(2 * s) >= 0) x_in(IC(f_in(2 * s)));
        if constexpr (s + XL < 64 && !first) issue_x(IC(s + XL));
        if constexpr (first && s == 22) { fv = tag; rd_flag<32>(fv, fo, flags); }
        if constexpr (s == 127) ld8_1(fv, (lane & 15) * 8, fwl);
        if constexpr (s == QP) { fv = tag; rd_flag<16>(fv, lane * 8, fkvp); }
        if constexpr (s >= QC) ldp(IC(s - QC));
        aux(s, IC(0), fl);
        SBAR();
        mm<ps == 1, (s % 64 < NA), s % 64 == 0>(acc[2 * ps + 1], f1[s % 3], x[s % 64]);
        SBAR();
        if constexpr (first && f_rd(2 * s + 1) >= 0) x_out(IC(f_rd(2 * s + 1)));
        if constexpr (first && s % 3 == 2 && s / 3 + 1 < 16) x_fin(IC(s / 3 + 1), IC(4));
        if constexpr (first) rd01(IC(s + 2), IC(1), fl);
        if constexpr (a_piece(s, first) >= 0) { if constexpr (first) pc_m(IC(a_piece(s, first))); else pc_a(IC(a_piece(s, first))); }
        if constexpr (s >= L0 && s < L0 + 4) ldx1<false, (s - L0) * 1024>(st.p[s - L0], c.xv, stt);
        if constexpr (first && s >= L0 + 4 && s < L0 + 8) ld_cs(cv.p[s - L0 - 4], 2, IC(s - L0 - 4));
        if constexpr (first && s >= L0 + 8 && s < L0 + 12) ld_cs(cq.p[s - L0 - 8], 0, IC(s - L0 - 8));
        if constexpr (s == 50) dma<0>(lds0 + 9 * SLOT + w * 1024, c.xv, wph);
        if constexpr (s >= 154 && s < 158) ld_cs(cw.p[s - 154], 4, IC(s - 154));
        if constexpr (a_park(s) >= 0) ld_park(IC(a_park(s)));
        if constexpr (first && f_in(2 * s + 1) >= 0) x_in(IC(f_in(2 * s + 1)));
        if constexpr (first && f_raw(s) >= 0) k_in(IC(f_raw(s)));
        aux(s, IC(1), fl);
        if constexpr (s == 87) { if (RARE(l == 0)) sfor<0, 4>([&](auto i) AI { raw_in(IC(1), 7, i); }); }
        if constexpr (s == 103) { if (RARE(l == 0)) raw_out(IC(1), 7); }
        if constexpr (s == 133) { if (RARE(l == 0)) sfor<0, 4>([&](auto i) AI { raw_in(IC(2), 8, i); }); }
        SBAR();
      };
      {
        STAMP(30);
        bf16x8 kt[2];
        bf16x8 xt[4];
        xs = (unsigned)((lane >> 3) * 2048 + ((lane & 7) ^ (((lane >> 3) & 3) << 1)) * 16);
        sfor<0, 4>([&](auto i) { dma<i * 1040>(lds0 + w * 20480 + 3 * 4160, xs + i * (16384 - 1040), xw + cb * 128); });
        sfor<0, 2>([&](auto i) { ldx0<0>(kt[i], (unsigned)((lane >> 3) * 6144 + (lane & 7) * 16), wqkv + 2048 + cb * 128 + (size_t)(tt * 16 + i * 8) * 6144); });
        u64 ep; ld8_1(ep, 0u, ws + WS_EPOCH);
        sfor<0, 4>([&](auto i) AI { raw_in(IC(0), 9, i); });
        sfor<0, 4>([&](auto i) { xo[i] = opq_v(lds0 + w * 20480 + r * 128 + (((r & 3) ^ i) << 5) + h * 8 + (r >> 3) * 16); });
        ra = opq_v(lds0 + 5 * SLOT + (4 * h + q4) * 128 + (((q4 >> 1) << 2) + 2 * blk + (p4 >> 1)) * 16 + 8 * (p4 & 1));
        { const unsigned o = (((lane >> 2) ^ (lane >> 4)) & 1) ? 0u : 0x3f803f80u; sel[0] = opq_v(__builtin_bit_cast(bf16x8, u32x4{o, o, o, o})); ks0 = opq_v(ks0); ks1 = opq_v(ks1); }
        sfor<0, 4>(x_in); sfor<0, 4>(k_in);
        asm volatile("" : "+s"(ws), "+s"(wk), "+s"(wphi));
        flags = ws + WS_FLAG;
        col = ws + WS_COL + (size_t)cb * 2 * MB; colw = col + w * 4096;
        act = ws + (size_t)rb * 2 * MB + w * 65536;
        acto = act + cb * 4096;
        fact = flags + tt * FL_I;
        facto = fact + cb * 8;
        stt = ws + WS_ST + tt * 4096;
        stto = stt + cb * 256;
        kvp = col + C_KVP + w * 32768; zp = col + C_ZP + w * 2048;
        fkvp = flags + (cb * 4 + w) * FL_I + 8 * FL_K; fwl = flags + cb * 4 * FL_I + 11 * FL_K;
        kvf = ws + WS_KVF; fkv = flags + 12 * FL_K;
        xu_p = ws + (size_t)rb * 2 * MB + 2 * STG + (cb >> 2) * 65536 + w * 16384 + 4096;
        su_p = ws + WS_ST + 256 * KB + ut * 4096;
        wkv = ((cb & 2) ? wv : wk) + w * 256 * 64; wph = wphi + cb * 4096 + w * 1024;
        { const unsigned m = (unsigned)(ty >> 1) - 1u; kvb = ut * 4096 + (((ty * 1024 + (w & 1) * 8) & m) | ((2048 + (w >> 1) * 1024 + (ty & 1) * 256 + (w & 1) * 512) & ~m)); }
        asm volatile("" : "+s"(kvb), "+s"(act), "+s"(acto), "+s"(kvp), "+s"(zp), "+s"(fkvp), "+s"(kvf), "+s"(fkv), "+s"(xu_p), "+s"(su_p), "+s"(wkv), "+s"(wph));
        asm volatile("s_waitcnt vmcnt(12)" : "+v"(cf), "+v"(ep), "+v"(kt[0]), "+v"(kt[1])::"memory");
        sfor<0, 4>([&](auto i) { ds_read2<false, 3 * 520>(xt[i], xo[i]); });
        sfor<4, 8>(x_in); sfor<4, 8>(k_in); sfor<8, 12>(x_in);
        asm volatile("s_waitcnt lgkmcnt(0)" : "+v"(xt[0]), "+v"(xt[1]), "+v"(xt[2]), "+v"(xt[3])::"memory");
        sfor<0, 4>([&](auto i) { xr[i] = __builtin_bit_cast(u32x4, xt[i]); });
        store_stats(r, xr, stto);
        park4(xr);
        { u64 z = ((u64)__builtin_amdgcn_readfirstlane((unsigned)(ep >> 32)) << 32 | __builtin_amdgcn_readfirstlane((unsigned)ep)) + 0x9E3779B97F4A7C15ull;
          z = (z ^ (z >> 30)) * 0xBF58476D1CE4E5B9ull; z = (z ^ (z >> 27)) * 0x94D049BB133111EBull; tag = z ^ (z >> 31); asm volatile("" : "+s"(tag)); }
        wait_bar<13>();
        sfor<0, 4>(x_out);
        raw_out(IC(0), 9);
        sfor<12, 16>(x_in); sfor<8, 12>(k_in);
        x_fin(IC(0), IC(0));
        sfor<16, 20>(x_in);
        STAMP(31); STAMP(0); STAMP(1);
        sfor<0, 2>([&](auto s) { rd01(s, IC(0), std::true_type{}); rd01(s, IC(1), std::true_type{}); });
        sfor<0, 64>([&](auto s) { step(s, std::true_type{}); });
        {
          const int gw = blockIdx.x * 4 + w; const unsigned z = 0;
          const unsigned vo = (unsigned)(lane < 5 ? WS_KVF - WS_ST + (gw * 5 + lane) * 128 : WS_FLAG - WS_ST + gw * 128);
          asm volatile("s_mov_b64 exec, 63\n\tglobal_atomic_or %0, %1, %2 sc1\n\ts_mov_b64 exec, -1" ::"v"(vo), "v"(z), "s"(ws + WS_ST) : "memory");
          const unsigned dump = lds0 + 9 * SLOT + 4096 + w * 2048;
          dma<0>(dump, c.xv, woA + gw * 2048); dma<1024>(dump, c.xv, woA + gw * 2048);
          dma<0>(dump, c.xv, wq + gw * 2048); dma<1024>(dump, c.xv, wq + gw * 2048);
          dma<0>(dump, c.xv, woB + gw * 2048); dma<1024>(dump, c.xv, woB + gw * 2048);
          dma<0>(dump, c.xv, ((gw & 64) ? wk : wv) + (gw & 63) * 1024);
        }
        asm volatile("s_nop 7" : "+v"(ks0), "+v"(ks1));
        csv[0] = h ? ks1[0] : ks0[0];
        const bf16x8 kf = cs_frag(csv[0]);
        mm<false, false, false, true>(acc[0], kf, mf0); mm<false, false, false, true>(acc[1], kf, mf1);
      }
  asm volatile("" : "+s"(w), "+s"(tag), "+s"(act), "+s"(acto), "+s"(fact), "+s"(facto), "+s"(stt), "+s"(stto), "+s"(col), "+s"(colw), "+s"(ldsw));
  asm volatile("" : "+s"(kvp), "+s"(zp), "+s"(fkvp), "+s"(fwl), "+s"(rb), "+s"(kvf), "+s"(fkv), "+s"(xu_p), "+s"(su_p), "+s"(wkv), "+s"(csm), "+s"(l));
  for (;;) {
    {
      REFRESH();
      asm volatile("" : "+s"(kvp), "+s"(zp), "+s"(fkvp), "+s"(fwl), "+s"(rb));
      STAMP(2);
      sfor<64, 128>([&](auto s) { step(s, std::false_type{}); });
      STAMP(19);
      mdone(acc[2], acc[3]);
      v_out(IC(0), IC(0)); v_out(IC(0), IC(1)); v_out(IC(1), IC(0)); v_out(IC(1), IC(1));
      lds_bar();
      {
        const int hd = w >> 1, fh = w & 1;
        f32x16 kv, z;
        const int ch = fh * 4 + 2 * blk + (p4 >> 1), r0 = 4 * h + q4;
        LAS const char* pa = pk_img + hd * 16384 + r0 * 128 + ((ch ^ r0) << 4) + 8 * (p4 & 1);
        LAS const char* pb = v_img + hd * 8192 + lane * 16;
        bf16x8 a[8], b[8], d[8];
#pragma unroll
        for (int s = 0; s < 8; ++s) { a[s] = cat8(trread(pa + s * 2048), trread(pa + s * 2048 + 1024)); b[s] = *(LAS const bf16x8*)(pb + s * 1024); }
#pragma unroll
        for (int s = 0; s < 8; ++s) d[s] = *(LAS const bf16x8*)(sh + 8704 + s * 32 + h * 16);
        SBAR();
        sfor<0, 8>([&](auto s) { mm<false, false, s == 0>(kv, a[s], b[s]); });
        sfor<0, 8>([&](auto s) {
          mm<false, false, s == 0>(z, d[s], a[s]);
          if constexpr (s == 1) { mdone(kv); st4_1<0>(__builtin_bit_cast(u32x4, pack8<0>(kv)), c.xv, kvp + rb * 2048); }
          if constexpr (s == 2) { st4_1<1024>(__builtin_bit_cast(u32x4, pack8<1>(kv)), c.xv, kvp + rb * 2048); }
          SBAR();
        });
        mdone(z);
        st1_1(z[0], (unsigned)(r * 4), zp + rb * 128);
      }
      STAMP(3);
      sfor<128, 192>([&](auto s) { step(s, std::false_type{}); });
      mdone(acc[4], acc[5]);
      STAMP(4);
      {
        bf16x8 pq[2][4], ka[8], za[8];
        f32x16 ep, em, nm[4], dn[4];
        float ai[2], bi[2];
        char* dst = acto + STG;
        sfor<0, 4>([&](auto k) { mm<false, false, k % 2 == 0, true>(p[k / 2], wf[k], pack8<k % 2>(acc[4 + k / 2])); });
        mdone(p[0], p[1]);
        bar();
        if (RARE(l == 0)) raw_out(IC(2), 8);
        sfor<0, 2>([&](auto hd) {
          hedge_exp(p[hd], sce, ep, em, [&](auto e) {
            constexpr int i = hd * 16 + e;
            sum_mm(IC(i));
            if constexpr (i % 2 == 0 && 192 - QC + i / 2 < 34) ldp(IC(192 - QC + i / 2));
            if constexpr (i % 2 == 0 && i >= 16) pc_a(IC(108 + (i - 16) / 2));
          });
          if constexpr (hd == 1) {
            mdone(kvs);
            *(LAS bf16x8*)(sh + w * 2048 + lane * 16) = pack8<0>(kvs); *(LAS bf16x8*)(sh + w * 2048 + 1024 + lane * 16) = pack8<1>(kvs);
            SBAR();
          }
          pq[hd][0] = pack8<0>(ep); pq[hd][1] = pack8<1>(ep); pq[hd][2] = pack8<0>(em); pq[hd][3] = pack8<1>(em);
          pin4(pq[hd]);
        });
        STAMP(5);
        lds_bar();
        LAS const char* zb = sh + 8192 + (r & 1) * 256 + h * 32;
#pragma unroll
        for (int k = 0; k < 8; ++k) za[k] = *(LAS const bf16x8*)(zb + (k >> 1) * 64 + (k & 1) * 16);
#pragma unroll
        for (int k = 0; k < 8; ++k) ka[k] = *(LAS const bf16x8*)(sh + k * 1024 + lane * 16);
        SBAR();
        auto coef = [&](auto hd) {
          const float a = __builtin_amdgcn_rcpf(dn[2 * hd][1]), b = __builtin_amdgcn_rcpf(dn[2 * hd + 1][1]);
          const float inv = __builtin_amdgcn_rcpf(dn[2 * hd][0] * a + dn[2 * hd + 1][0] * b + 1e-6f);
          ai[hd] = opq_v(a * inv); bi[hd] = opq_v(b * inv);
        };
        auto fin = [&](auto hd) {
          f32x16 o;
#pragma unroll
          for (int i = 0; i < 16; ++i) o[i] = nm[2 * hd][i] * ai[hd] + nm[2 * hd + 1][i] * bi[hd];
          st4_1<hd * 2048>(__builtin_bit_cast(u32x4, pack8<0>(o)), c.xv, dst);
          st4_1<hd * 2048 + 1024>(__builtin_bit_cast(u32x4, pack8<1>(o)), c.xv, dst);
        };
        sfor<0, 8>([&](auto k) { mm<false, false, k % 2 == 0>(dn[k / 2], za[k], pq[k / 4][k % 4]); if constexpr (k % 2) pc_a(IC(116 + k / 2)); });
        sfor<0, 4>([&](auto k) { mm<false, false, k % 2 == 0>(nm[k / 2], ka[k], pq[0][k]); });
        mdone(dn[0], dn[1], dn[2], dn[3]);
        sfor<4, 8>([&](auto k) { mm<false, false, k % 2 == 0>(nm[k / 2], ka[k], pq[1][k - 4]); if constexpr (k == 4) coef(IC(0)); if constexpr (k == 6) coef(IC(1)); });
        STAMP(6);
        pin2(nm[0], nm[1]);
        fin(IC(0));
        pin2(nm[2], nm[3]);
        fin(IC(1));
        SBAR();
        sfor<120, 124>(pc_a);
        res_to_acc(xr, ra0, ra1);
        waitv<4>();
        set_flag(facto + FL_K, tag);
        sfor<124, 128>(pc_a); sfor<8, 16>(ld_park);
      }
    }
    STAMP(7);
    REFRESH();
    LAS char* reg = smem + 8 * SLOT;
    bf16x8 fu[16], cu;
    {
      f32x16 &a0 = ra0, &a1 = ra1;
      poll<16>(fact + FL_K, lane * 8, tag);
      sfor<0, 16>([&](auto i) { pin_a(park[i]); });
      STAMP(8);
      bf16x8 x[64];
      const char* xb = act + STG;
      sfor<0, XL>([&](auto s) { ld_x<s>(x, c, xb); });
      bar();
      asm volatile("" : "+s"(wkv));
      const unsigned kvv = (unsigned)((lane >> 1) * 64 + (cb & 1) * 32 + (lane & 1) * 16);
      const unsigned wa = ldsw + c.xv, wb = wa + 65536;
      auto unpark = [&](auto i) {
        constexpr int off = a_slot(26 + i / 4) * SLOT + (i % 4) * 1024;
        ds_write_a<(off & 65535)>(off < 65536 ? wa : wb, park[i]);
      };
      gemm64<true, false, HookP3>(c, x, xb, a0, a1, [&](auto s) {
        if constexpr (s == 33) bar();
        if constexpr (s >= 36 && s < 44) unpark(IC(s - 36));
        if constexpr (s == 49) bar();
        if constexpr (s >= 50 && s < 58) unpark(IC(s - 42));
        if constexpr (s >= 52 && s < 60) dma<0>(lds0 + 8 * SLOT + w * 8192 + (s - 52) * 1024, kvv, wkv + (s - 52) * 32 * 64);
        if constexpr (s >= 60) pc_w(4, a_slot(24), 0, IC(s - 60));
      }, [](auto) {});
      STAMP(9);
      store_x(c, r, a0, a1, xr, acto + 2 * STG, stto + 256 * 1024);
      asm volatile("s_waitcnt vmcnt(5) lgkmcnt(0)\n\ts_barrier" ::: "memory");
      { LAS const char* p = reg + w * 8192 + (4 * h + q4) * 32 + p4 * 8;
#pragma unroll
        for (int i = 0; i < 16; ++i) fu[i] = cat8(trread(p + i * 512), trread(p + i * 512 + 256));
        SBAR();
        float t0 = 0.f, t1 = 0.f, t2 = 0.f, t3 = 0.f;
#pragma unroll
        for (int i = 0; i < 16; ++i) {
          const u32x4 f = __builtin_bit_cast(u32x4, fu[i]);
          t0 = dot2(f[0], 0x3f803f80u, t0); t1 = dot2(f[1], 0x3f803f80u, t1); t2 = dot2(f[2], 0x3f803f80u, t2); t3 = dot2(f[3], 0x3f803f80u, t3);
        }
        cu = opq_v(cs_frag(swap_add((t0 + t1) + (t2 + t3)))); }
      waitv<0>();
      set_flag(facto + 2 * FL_K, tag);
      sfor<0, 4>([&](auto i) { pc_w(4, a_slot(25), 1, i); });
    }
    STAMP(10);
    REFRESH();
    f32x16 a0, a1;
    asm volatile("" : "+s"(kvf), "+s"(fkv), "+s"(xu_p), "+s"(su_p));
    {
      poll<16>(fact + 2 * FL_K, lane * 8, tag);
      bar();
      STAMP(11);
      const char* xb = act + 2 * STG; const char* stp = stt + 256 * 1024;
      bf16x8 x[64], xu[16], mf0, mf1;
      Red<false> su, sp;
      sfor<0, 4>([&](auto i) { ldx1<false, i * 1024>(su.p[i], c.xv, su_p); });
      sfor<0, 16>([&](auto i) { ldx1<true, (i % 8) * 1024 - 4096>(xu[i], c.xv, xu_p + (i / 8) * 8192); });
      f32x16 au, av;
      sfor<0, 16>([&](auto i) {
        waitx<true, 15 - i + (i < XL ? i : XL)>(xu[i]);
        SBAR();
        mm<false, true, i == 0>(au, fu[i], xu[i]);
        if constexpr (i < XL) ld_x<i>(x, c, xb);
        mm<true, true, i == 0>(av, fu[i], xu[i]);
        if constexpr (i < 11) su.template run<i>(lane);
        if constexpr (i == 12) mf0 = opq_v(mean_frag(su.a, h == 0));
        if constexpr (i == 14) { mm<false, false, false>(au, cu, mf0); mm<true, false, false>(av, cu, mf0); }
        SBAR();
      });
      STAMP(12);
      u64 fa;
      f32x4 t[4], ru;
      unsigned vo;
      gemm64<true, true, HookP4>(c, x, xb, a0, a1, [&](auto s) {
        if constexpr (s == 1) {
          pin2(au, av);
#pragma unroll
          for (int gq = 0; gq < 6; ++gq) {
            const f32x16& a = gq < 4 ? av : au;
            *(LAS f32x4*)(reg + w * 8192 + gq * 1024 + lane * 16) = f32x4{a[4 * (gq & 3)], a[4 * (gq & 3) + 1], a[4 * (gq & 3) + 2], a[4 * (gq & 3) + 3]};
          }
        }
        if constexpr (s == 3) {
          lds_bar();
#pragma unroll
          for (int j = 0; j < 4; ++j) t[j] = *(LAS const f32x4*)(reg + j * 8192 + (ty < 2 ? 4 + (w & 1) : w) * 1024 + lane * 16);
        }
        if constexpr (s == 6) st2_1(__builtin_bit_cast(f32x2, u32x2{cvtpk(t[0][0], t[0][1]), cvtpk(t[0][2], t[0][3])}), vo, kvf);
        if constexpr (s >= 8 && s < 12) ldx1<false, (s - 8) * 1024>(sp.p[s - 8], c.xv, stp);
        if constexpr (s == 7 + XL) { bar(); if (RARE(w == 0)) set_flag(fkv + rb * 4 * FL_I + cb * 8, tag); }
        if constexpr (s >= 14 && s < 30 && s % 2 == 0) pc_w(4, a_slot(30 + (s - 14) / 8), 6 + (s - 14) / 8, IC(((s - 14) / 2) % 4));
        if constexpr (s == 44) bar();
        if constexpr (s == 52) ld8_1(fa, (lane & 15) * 8 + (w * 4 + (lane >> 4)) * 4 * FL_I, fkv);
      }, [&](auto g) {
        if constexpr (g == 0) {
#pragma unroll
          for (int e = 0; e < 4; ++e) ru[e] = lane_get(su.b, ty < 2 ? r : 8 * w + 4 * h + e);
        }
        if constexpr (g == 4) {
          const unsigned m = (unsigned)(ty >> 1) - 1u;
          vo = opq_v(kvb + (((unsigned)lane * 16 & m) | ((unsigned)((r & 15) * 16 + h * 8) & ~m)));
        }
        if constexpr (g >= 9 && g < 13) {
#pragma unroll
          for (int e = 0; e < 4; ++e) { float v = t[0][e]; if constexpr (g < 12) a_add(v, t[g - 8][e]); else a_mul(v, ru[e]); t[0][e] = v; }
        }
        if constexpr (g >= 48 && g < 59) sp.template run<g - 48>(lane);
        if constexpr (g == 60) mf0 = opq_v(mean_frag(sp.a, h == 0));
        if constexpr (g == 61) mf1 = opq_v(mean_frag(sp.a, h == 1));
        if constexpr (g == 64) mm<false, false, false>(a0, cw.p[0], mf0);
        if constexpr (g == 65) mm<false, false, false>(a1, cw.p[0], mf1);
      });
      rstd = sp.b;
      waitx<false, 0>(fa);
      if (RARE(!__all(fa == tag))) {
#pragma unroll 1
      for (int spin = 0; spin < (1 << 15) && !__all(fa == tag); ++spin) { __builtin_amdgcn_s_sleep(8); ld8_1(fa, (lane & 15) * 8 + (w * 4 + (lane >> 4)) * 4 * FL_I, fkv); waitx<false, 0>(fa); }
      }
      bar();
      STAMP(13);
    }
    STAMP(14);
    {
      u32x16 qq, misc;
      misc[14] = misc[15] = __float_as_uint(rstd * QSCALE);
      int attempt = 0;
#pragma unroll 1
      for (;;) {
        f32x16 oa, ob, t0, t1, t2, t3;
        u32x16 kvr; u32x4 offs;
        offs[0] = c.xv;
        const char* ks = kvf + 2048 + w * 2048; const char* wsr = wt(5, 0);
        unsigned wm0 = ldsw, cnt = 8;
        asm volatile(
            "global_load_dwordx4 v[208:211], v240, %[kv] sc1\n\tglobal_load_dwordx4 v[212:215], v240, %[kv] offset:1024 sc1\n\t"
            "s_mov_b32 m0, s44\n\tv_add_u32 v241, 0x2000, v240\n\t"
            "global_load_lds_dwordx4 v240, s[40:41] sc1\n\tglobal_load_lds_dwordx4 v240, s[40:41] offset:1024 sc1\n\t"
            "s_add_u32 m0, s44, 0x2000\n\tv_add_u32 v242, 0x4000, v240\n\t"
            "global_load_lds_dwordx4 v241, s[40:41] sc1\n\tglobal_load_lds_dwordx4 v241, s[40:41] offset:1024 sc1\n\t"
            "s_add_u32 m0, s44, 0x4000\n\tv_add_u32 v243, 0x6000, v240\n\t"
            "global_load_lds_dwordx4 v242, s[40:41] sc1\n\tglobal_load_lds_dwordx4 v242, s[40:41] offset:1024 sc1\n\t"
            "s_add_u32 s40, s40, 0x6000\n\ts_addc_u32 s41, s41, 0\n\t"
            "s_cmp_lg_u32 %[att], 0\n\ts_cbranch_scc1 2f\n\t"
            "v_pk_mul_f32 v[128:129], v[128:129], v[238:239]\n\t"
            "v_pk_mul_f32 v[130:131], v[130:131], v[238:239]\n\t"
            "v_pk_mul_f32 v[132:133], v[132:133], v[238:239]\n\t"
            "v_pk_mul_f32 v[134:135], v[134:135], v[238:239]\n\t"
            "v_pk_mul_f32 v[136:137], v[136:137], v[238:239]\n\t"
            "v_pk_mul_f32 v[138:139], v[138:139], v[238:239]\n\t"
            "v_pk_mul_f32 v[140:141], v[140:141], v[238:239]\n\t"
            "v_pk_mul_f32 v[142:143], v[142:143], v[238:239]\n\t"
            "v_pk_mul_f32 v[144:145], v[144:145], v[238:239]\n\t"
            "v_pk_mul_f32 v[146:147], v[146:147], v[238:239]\n\t"
            "v_pk_mul_f32 v[148:149], v[148:149], v[238:239]\n\t"
            "v_pk_mul_f32 v[150:151], v[150:151], v[238:239]\n\t"
            "v_pk_mul_f32 v[152:153], v[152:153], v[238:239]\n\t"
            "v_pk_mul_f32 v[154:155], v[154:155], v[238:239]\n\t"
            "v_pk_mul_f32 v[156:157], v[156:157], v[238:239]\n\t"
            "v_pk_mul_f32 v[158:159], v[158:159], v[238:239]\n\t"
            "v_cvt_pk_bf16_f32 v192, v128, v129\n\t"
            "v_cvt_pk_bf16_f32 v193, v130, v131\n\t"
            "v_cvt_pk_bf16_f32 v194, v132, v133\n\t"
            "v_cvt_pk_bf16_f32 v195, v134, v135\n\t"
            "v_cvt_pk_bf16_f32 v196, v136, v137\n\t"
            "v_cvt_pk_bf16_f32 v197, v138, v139\n\t"
            "v_cvt_pk_bf16_f32 v198, v140, v141\n\t"
            "v_cvt_pk_bf16_f32 v199, v142, v143\n\t"
            "v_cvt_pk_bf16_f32 v200, v144, v145\n\t"
            "v_cvt_pk_bf16_f32 v201, v146, v147\n\t"
            "v_cvt_pk_bf16_f32 v202, v148, v149\n\t"
            "v_cvt_pk_bf16_f32 v203, v150, v151\n\t"
            "v_cvt_pk_bf16_f32 v204, v152, v153\n\t"
            "v_cvt_pk_bf16_f32 v205, v154, v155\n\t"
            "v_cvt_pk_bf16_f32 v206, v156, v157\n\t"
            "v_cvt_pk_bf16_f32 v207, v158, v159\n\t"
            "2:\n\t"
            "v_mov_b64 v[64:65], 0\n\t"
            "v_mov_b64 v[66:67], 0\n\t"
            "v_mov_b64 v[68:69], 0\n\t"
            "v_mov_b64 v[70:71], 0\n\t"
            "v_mov_b64 v[72:73], 0\n\t"
            "v_mov_b64 v[74:75], 0\n\t"
            "v_mov_b64 v[76:77], 0\n\t"
            "v_mov_b64 v[78:79], 0\n\t"
            "v_mov_b64 v[80:81], 0\n\t"
            "v_mov_b64 v[82:83], 0\n\t"
            "v_mov_b64 v[84:85], 0\n\t"
            "v_mov_b64 v[86:87], 0\n\t"
            "v_mov_b64 v[88:89], 0\n\t"
            "v_mov_b64 v[90:91], 0\n\t"
            "v_mov_b64 v[92:93], 0\n\t"
            "v_mov_b64 v[94:95], 0\n\t"
            "v_mov_b64 v[176:177], 0\n\t"
            "v_mov_b64 v[178:179], 0\n\t"
            "v_mov_b64 v[180:181], 0\n\t"
            "v_mov_b64 v[182:183], 0\n\t"
            "v_mov_b64 v[184:185], 0\n\t"
            "v_mov_b64 v[186:187], 0\n\t"
            "v_mov_b64 v[188:189], 0\n\t"
            "v_mov_b64 v[190:191], 0\n\t"
            "v_mov_b64 v[216:217], 0\n\t"
            "v_mov_b64 v[218:219], 0\n\t"
            "v_mov_b64 v[220:221], 0\n\t"
            "v_mov_b64 v[222:223], 0\n\t"
            "v_mov_b64 v[228:229], 0\n\t"
            "v_mov_b64 v[230:231], 0\n\t"
            "v_mov_b64 v[232:233], 0\n\t"
            "v_mov_b64 v[234:235], 0\n\t"
            "v_bfe_u32 v226, v240, 6, 1\n\tv_mov_b32 v236, 0xdddddddd\n\tv_mul_u32_u24 v226, 0x3f80, v226\n\tv_add_u32 v237, %[rg], v240\n\t"
            "v_lshl_or_b32 v226, v226, 16, v226\n\tv_mov_b32 v227, v226\n\tv_xor_b32 v224, 0x3f803f80, v226\n\tv_mov_b32 v225, v224\n\t"
            "s_waitcnt vmcnt(6)\n\ts_cbranch_scc1 3f\n\t"
            "v_mfma_f32_32x32x16_bf16 v[128:143], v[208:211], v[192:195], 0\n\tv_mfma_f32_32x32x16_bf16 v[160:175], v[208:211], v[200:203], 0\n\t"
            "v_mfma_f32_32x32x16_bf16 v[128:143], v[212:215], v[196:199], v[128:143]\n\tv_mfma_f32_32x32x16_bf16 v[160:175], v[212:215], v[204:207], v[160:175]\n\t"
            "s_nop 11\n\t"
            "v_max3_f32 v238, v128, v129, v130\n\tv_max3_f32 v239, v160, v161, v162\n\t"
            "v_max3_f32 v238, v238, v131, v132\n\tv_max3_f32 v239, v239, v163, v164\n\t"
            "v_max3_f32 v238, v238, v133, v134\n\tv_max3_f32 v239, v239, v165, v166\n\t"
            "v_max3_f32 v238, v238, v135, v136\n\tv_max3_f32 v239, v239, v167, v168\n\t"
            "v_max3_f32 v238, v238, v137, v138\n\tv_max3_f32 v239, v239, v169, v170\n\t"
            "v_max3_f32 v238, v238, v139, v140\n\tv_max3_f32 v239, v239, v171, v172\n\t"
            "v_max3_f32 v238, v238, v141, v142\n\tv_max3_f32 v239, v239, v173, v174\n\t"
            "v_max3_f32 v238, v238, v143, v143\n\tv_max3_f32 v239, v239, v175, v175\n\t"
            "v_mov_b32 v96, v238\n\tv_mov_b32 v97, v239\n\ts_nop 1\n\tv_permlane32_swap_b32 v238, v96\n\tv_permlane32_swap_b32 v239, v97\n\t"
            "v_max_f32 v238, v238, v96\n\tv_max_f32 v239, v239, v97\n\t"
            "3:\n\t"
            "v_xor_b32 v96, 0x80000000, v238\n\tv_xor_b32 v112, 0x80000000, v239\n\tv_mov_b32 v97, v96\n\tv_mov_b32 v113, v112\n\t"
            "v_mov_b64 v[98:99], v[96:97]\n\tv_mov_b64 v[114:115], v[112:113]\n\t"
            "v_mov_b64 v[100:101], v[96:97]\n\tv_mov_b64 v[116:117], v[112:113]\n\t"
            "v_mov_b64 v[102:103], v[96:97]\n\tv_mov_b64 v[118:119], v[112:113]\n\t"
            "v_mov_b64 v[104:105], v[96:97]\n\tv_mov_b64 v[120:121], v[112:113]\n\t"
            "v_mov_b64 v[106:107], v[96:97]\n\tv_mov_b64 v[122:123], v[112:113]\n\t"
            "v_mov_b64 v[108:109], v[96:97]\n\tv_mov_b64 v[124:125], v[112:113]\n\t"
            "v_mov_b64 v[110:111], v[96:97]\n\tv_mov_b64 v[126:127], v[112:113]\n\t"
            "s_nop 1\n\t" S0_(128, 192, 96) S1_(128, 192)
            "s_waitcnt vmcnt(4)\n\ts_barrier\n\ts_nop 8\n\t"
            "1:\n\t"
            STEP_(0, 24576, 240, 0, "s_sub_u32 %[cnt], %[cnt], 1\n\t", "") STEP_(8192, 0, 241, 1024, "", "") STEP_(16384, 8192, 242, 2048, "", "")
            STEP_(24576, 16384, 243, 3072, "s_add_u32 s40, s40, 0x8000\n\ts_addc_u32 s41, s41, 0\n\t", "s_add_u32 s42, s42, 0x4000\n\ts_addc_u32 s43, s43, 0\n\t")
            "s_add_u32 s45, s45, 0x4000\n\ts_cmp_lg_u32 %[cnt], 0\n\ts_cbranch_scc1 1b\n\t"
            L0_(232, 176) L1_(232, 176) PV0_(80, 176) PV1_(80, 176)
            "s_nop 11\n\t"
            : "=&{v[64:79]}"(oa), "=&{v[80:95]}"(ob), "=&{v[96:111]}"(t0), "=&{v[112:127]}"(t1), "+{v[128:143]}"(a0), "+{v[144:159]}"(a1), "=&{v[160:175]}"(t2),
              "=&{v[176:191]}"(t3), "+{v[192:207]}"(qq), "=&{v[208:223]}"(kvr), "+{v[224:239]}"(misc), "+{v[240:243]}"(offs),
              "+{s[40:41]}"(ks), "+{s[42:43]}"(wsr), "+{s45}"(wm0), [cnt] "+s"(cnt)
            : "{s44}"(lds0 + 8 * SLOT + w * 2048), [kv] "s"(kvf), [att] "s"(attempt), [rg] "s"(lds0 + 8 * SLOT)
            : "scc", "memory");
        const float la = __uint_as_float(misc[4]), lb = __uint_as_float(misc[8]);
        const float ia = __builtin_amdgcn_rcpf(la), ib = __builtin_amdgcn_rcpf(lb);
#pragma unroll
        for (int e = 0; e < 16; ++e) { oa[e] *= ia; ob[e] *= ib; }
        char* dst = acto + 3 * STG;
        st4_1<0>(__builtin_bit_cast(u32x4, pack8<0>(oa)), c.xv, dst); st4_1<1024>(__builtin_bit_cast(u32x4, pack8<1>(oa)), c.xv, dst);
        st4_1<2048>(__builtin_bit_cast(u32x4, pack8<0>(ob)), c.xv, dst); st4_1<3072>(__builtin_bit_cast(u32x4, pack8<1>(ob)), c.xv, dst);
        SBAR();
        f32x2 c0 = {}, c1 = {};
#pragma unroll
        for (int e = 0; e < 16; e += 2) { c0 += f32x2{oa[e], oa[e + 1]} * 0.f; c1 += f32x2{ob[e], ob[e + 1]} * 0.f; }
        c0 += c1;
        const bool bad = !(la < 1.2676506e30f) || !(lb < 1.2676506e30f) || !(c0[0] + c0[1] == 0.f);
        LAS volatile int* vote = (LAS volatile int*)(smem + 8 * SLOT + 24576);
        vote[w] = attempt == 0 && __any(bad);
        lds_bar();
        const u32x4 vt = *(LAS const u32x4*)vote;
        if (!RARE(__builtin_amdgcn_readfirstlane(vt[0] | vt[1] | vt[2] | vt[3]))) break;
        all_bar();
        {
          const bf16x8 qa0 = __builtin_bit_cast(bf16x8, __builtin_shufflevector(qq, qq, 0, 1, 2, 3)), qa1 = __builtin_bit_cast(bf16x8, __builtin_shufflevector(qq, qq, 4, 5, 6, 7));
          const bf16x8 qb0 = __builtin_bit_cast(bf16x8, __builtin_shufflevector(qq, qq, 8, 9, 10, 11)), qb1 = __builtin_bit_cast(bf16x8, __builtin_shufflevector(qq, qq, 12, 13, 14, 15));
          float mA = -INFINITY, mB = -INFINITY;
          for (int t = 0; t < 64; ++t) {
            bf16x8 k0, k1;
            ldx1<false, 0>(k0, c.xv, kvf + t * 4096); ldx1<false, 1024>(k1, c.xv, kvf + t * 4096);
            wait2<0>(k0, k1);
            mm<false, false, true>(a0, k0, qa0); mm<false, false, false>(a0, k1, qa1); mm<false, false, true>(a1, k0, qb0); mm<false, false, false>(a1, k1, qb1);
            mdone(a0, a1);
            mA = fmaxf(mA, max16(a0)); mB = fmaxf(mB, max16(a1));
          }
          misc[14] = __float_as_uint(swap_max(mA)); misc[15] = __float_as_uint(swap_max(mB));
        }
        attempt = 1;
      }
      waitv<0>();
      set_flag(facto + 3 * FL_K, tag);
      res_to_acc(xr, ra0, ra1);
    }
    STAMP(15);
    REFRESH();
    {
      f32x16 &a0 = ra0, &a1 = ra1;
      poll<16>(fact + 3 * FL_K, lane * 8, tag);
      STAMP(16);
      bf16x8 x[64];
      const char* xb = act + 3 * STG;
      sfor<0, XL>([&](auto s) { ld_x<s>(x, c, xb); });
      bar();
      gemm64<false, false, HookP6>(c, x, xb, a0, a1, [&](auto s) {
        if constexpr (s == 33) bar();
        if constexpr (s >= 52) pc_a(IC(s - 52));
      }, [](auto) {});
      STAMP(17);
      if (l == 0) {
        store_x(c, r, a0, a1, xr, acto + 4 * STG, stto + 512 * 1024);
        bar();
        sfor<12, 16>(pc_a);
        waitv<4>();
        set_flag(facto + 4 * FL_K, tag);
        sfor<16, 24>(pc_a);
      } else {
        LAS char* im = smem + 8 * SLOT + w * 4096;
        sfor<0, 8>([&](auto n) {
          constexpr int g = n;
          const f32x16& a = g < 4 ? a0 : a1;
          *(LAS u32x2*)(im + r * 128 + ((g ^ (r & 7)) << 4) + h * 8) = u32x2{cvtpk(a[4 * (g & 3)], a[4 * (g & 3) + 1]), cvtpk(a[4 * (g & 3) + 2], a[4 * (g & 3) + 3])};
        });
        char* dst = out + (size_t)tt * 65536 + cb * 128;
        sfor<0, 4>([&](auto i) {
          const u32x4 v = *(LAS const u32x4*)(im + i * 1024 + lane * 16);
          st4_1<0>(v, (unsigned)((i * 8 + (lane >> 3)) * 2048 + (((lane & 7) ^ (lane >> 3)) << 4)), dst);
        });
        if (tt == 0 && cb == 0) set_flag(ws + WS_EPOCH, tag);
      }
    }
    STAMP(18);
    if (l == 1) break;
    csm = 1;
    act += 4 * STG; acto += 4 * STG; fact += 4 * FL_K; facto += 4 * FL_K; stt += 512 * KB; stto += 512 * KB;
    kvp += 131072; zp += 8192; fkvp += FL_K; kvf += 320 * KB; fkv += FL_K; xu_p += 4 * STG; su_p += 512 * KB;
    l = 1;
    REFRESH();
    STAMP(0);
        poll<16>(fact, lane * 8, tag);
        STAMP(1);
        sfor<0, XL>(issue_x);
        wait_bar<XL>();
        sfor<0, 2>([&](auto s) { rd01(s, IC(0), std::false_type{}); rd01(s, IC(1), std::false_type{}); });
        sfor<0, 64>([&](auto s) { step(s, std::false_type{}); });
        asm volatile("s_mov_b32 m0, %0\n\ts_mov_b64 exec, 1\n\t.rept 8\n\tglobal_load_lds_dwordx4 %1, %2\n\t.endr\n\ts_mov_b64 exec, -1\n.Lk_main_tail:" ::"s"(lds0 + 9 * SLOT + 4096), "v"(c.xv), "s"(wph) : "memory");
  }
}

extern "C" void kernel_launch(void* const* d_in, const int* in_sizes, int n_in, void* d_out, int out_size, void* d_ws, size_t ws_size, hipStream_t stream) {
  k_main<<<256, 256, 0, stream>>>((const char*)d_in[0], (const char*)d_in[1], (const char*)d_in[2], (const char*)d_in[3], (const char*)d_in[5], (const char*)d_in[6],
                                  (const char*)d_in[7], (const char*)d_in[8], (char*)d_ws, (char*)d_out);
}
```
